```python
import torch

def _softmax(x, axis=-1):
    return torch.softmax(x, dim=axis)
def _gelu(x):
    return torch.nn.functional.gelu(x, approximate="tanh")
def _silu(x):
    return torch.nn.functional.silu(x)
def _sigmoid(x):
    return torch.sigmoid(x)
def _layernorm(x, axis=-1, eps=1e-5):
    m = x.mean(dim=axis, keepdim=True)
    v = x.var(dim=axis, keepdim=True, unbiased=False)
    return (x - m) / torch.sqrt(v + eps)
def _scatter_add(tgt, idx, src, axis=0):
    return tgt.clone().index_add_(axis, idx, src)
def _segment_sum(src, seg, n):
    out = torch.zeros((n,) + src.shape[1:], dtype=src.dtype)
    return out.index_add_(0, seg, src)
def _pad(x, pw):
    flat = tuple(v for pair in reversed(pw) for v in pair)
    return torch.nn.functional.pad(x, flat)

kernel_name = 'concat_heads__sliding_local_dilated__compressed_kv_cla__prompt'

def setup_inputs():

    _w = (lambda a: (lambda t: t.to(torch.bfloat16) if t.is_floating_point() else t)(torch.as_tensor(a)))
    def _A_setup():
        B, S, D = 1, 2048, 448
        return {
            "x": _w((lambda *s: torch.randn(*s, dtype=torch.bfloat16) * 0.4)(B, S, D)),
            "wqkv": _w((lambda *s: torch.randn(*s, dtype=torch.bfloat16) * 0.4)(D, 3 * D)),
            "wo": _w((lambda *s: torch.randn(*s, dtype=torch.bfloat16) * 0.05)(D, D)),
        }
    _A_in = _A_setup()

    def _B_setup():
        B, H, Sq, Sk, D, M = 1, 16, 2048, 2048, 256, 448
        Dc = 4 * D
        return {
            "x": _w((lambda *s: torch.randn(*s, dtype=torch.bfloat16) * 0.2)(B, Sq, M)),
            "c_kv": _w((lambda *s: torch.randn(*s, dtype=torch.bfloat16) * 1.6)(B, Sk, Dc)),
            "w_q": _w((lambda *s: torch.randn(*s, dtype=torch.bfloat16) * 3.2)(M, H, D) * (M ** -0.5)),
            "w_uk": _w((lambda *s: torch.randn(*s, dtype=torch.bfloat16) * 3.2)(Dc, H, D) * (Dc ** -0.5)),
            "w_uv": _w((lambda *s: torch.randn(*s, dtype=torch.bfloat16) * 1.6)(Dc, H, D) * (Dc ** -0.5)),
            "w_o": _w((lambda *s: torch.randn(*s, dtype=torch.bfloat16) * 1.6)(H, D, M) * ((H * D) ** -0.5)),
        }
    _B_in = _B_setup()

    return {
        "A_x": _A_in["x"],
        "A_wqkv": _A_in["wqkv"],
        "A_wo": _A_in["wo"],
        "B_x": _B_in["x"],
        "B_c_kv": _B_in["c_kv"],
        "B_w_q": _B_in["w_q"],
        "B_w_uk": _B_in["w_uk"],
        "B_w_uv": _B_in["w_uv"],
        "B_w_o": _B_in["w_o"],
    }

def reference(A_x, A_wqkv, A_wo, B_x, B_c_kv, B_w_q, B_w_uk, B_w_uv, B_w_o):
    def _A_ref(x, wqkv, wo):
        B, S, D = x.shape
        H = 16; Dh = D // H
        W, dil = 8, 2
        qkv = (lambda x, *d: x.permute(*d))((x @ wqkv).reshape(B, S, 3, H, Dh), 2, 0, 3, 1, 4)
        q, k, v = qkv[0], qkv[1], qkv[2]
        scale = Dh ** -0.5
        pos = torch.arange(S)
        off = torch.arange(W) * dil
        idx = pos[:, None] - off[None, :]
        mask = idx >= 0
        idx_c = torch.where(mask, idx, 0).reshape(-1)
        kg = k[:, :, idx_c, :].reshape(B, H, S, W, Dh)
        vg = v[:, :, idx_c, :].reshape(B, H, S, W, Dh)
        logits = torch.einsum("bhsd,bhswd->bhsw", q, kg) * scale
        logits = torch.where(mask[None, None, :, :], logits, -1e9)
        attn = _softmax(logits, axis=-1)
        o = torch.einsum("bhsw,bhswd->bhsd", attn, vg)
        return (lambda x, *d: x.permute(*d))(o, 0, 2, 1, 3).reshape(B, S, D) @ wo

    def _B_ref(x, c_kv, w_q, w_uk, w_uv, w_o):
        B, Sq, M = x.shape[0], x.shape[1], x.shape[2]
        Sk = c_kv.shape[1]
        H, D = w_q.shape[1], w_q.shape[2]
        q = torch.einsum("bqm,mhd->bhqd", x, w_q)
        k = torch.einsum("bkc,chd->bhkd", c_kv, w_uk)
        v = torch.einsum("bkc,chd->bhkd", c_kv, w_uv)
        scores = torch.einsum("bhqd,bhkd->bhqk", q, k) * (D ** -0.5)
        off = Sk - Sq
        qi = torch.arange(Sq)[:, None]
        kj = torch.arange(Sk)[None, :]
        cmask = kj <= (qi + off)
        scores = torch.where(cmask[None, None], scores, -1e9)
        p = _softmax(scores, axis=-1)
        ctx = torch.einsum("bhqk,bhkd->bhqd", p, v)
        return torch.einsum("bhqd,hdm->bqm", ctx, w_o)

    a = _A_ref(A_x, A_wqkv, A_wo)
    b = _B_ref(B_x, B_c_kv, B_w_q, B_w_uk, B_w_uv, B_w_o)
    return torch.concatenate([a, b], axis=-1)
```

```cpp
#include <hip/hip_runtime.h>
#include <hip/hip_bf16.h>
typedef __hip_bfloat16 bf16;
static __device__ __forceinline__ float b2f(bf16 x){ return __bfloat162float(x);}
static __device__ __forceinline__ bf16 f2b(float x){ return __float2bfloat16(x);}

__global__ void gemm_naive(const bf16* A, const bf16* B, bf16* C, int M, int N, int K, int ldc){
  __shared__ float As[64][17];
  __shared__ float Bs[16][65];
  int tx = threadIdx.x & 15, ty = threadIdx.x >> 4;
  int m0 = blockIdx.y*64, n0 = blockIdx.x*64;
  float acc[4][4] = {};
  for(int k0=0;k0<K;k0+=16){
    for(int i=threadIdx.x;i<64*16;i+=256){ int r=i>>4,c=i&15; As[r][c]=b2f(A[(size_t)(m0+r)*K+k0+c]); }
    for(int i=threadIdx.x;i<16*64;i+=256){ int r=i>>6,c=i&63; Bs[r][c]=(n0+c<N)?b2f(B[(size_t)(k0+r)*N+n0+c]):0.f; }
    __syncthreads();
    for(int k=0;k<16;k++){
      float a[4],b[4];
      for(int i=0;i<4;i++) a[i]=As[ty*4+i][k];
      for(int j=0;j<4;j++) b[j]=Bs[k][tx*4+j];
      for(int i=0;i<4;i++) for(int j=0;j<4;j++) acc[i][j]+=a[i]*b[j];
    }
    __syncthreads();
  }
  for(int i=0;i<4;i++) for(int j=0;j<4;j++){ int n=n0+tx*4+j; if(n<N) C[(size_t)(m0+ty*4+i)*ldc+n]=f2b(acc[i][j]); }
}

__global__ void local_attn(const bf16* qkv, bf16* o){
  int idx = blockIdx.x*blockDim.x+threadIdx.x;
  int s = idx>>4, h = idx&15;
  const bf16* q = qkv + (size_t)s*1344 + h*28;
  float lg[8]; float mx=-1e30f;
  #pragma unroll
  for(int w=0;w<8;w++){
    int j=s-2*w; float d=0;
    if(j>=0){ const bf16* k=qkv+(size_t)j*1344+448+h*28; for(int e=0;e<28;e++) d+=b2f(q[e])*b2f(k[e]); d=b2f(f2b(b2f(f2b(d))*0.18898223650461361f)); }
    else d=-1e9f;
    lg[w]=d; mx=fmaxf(mx,d);
  }
  float sum=0;
  #pragma unroll
  for(int w=0;w<8;w++){ lg[w]=__expf(lg[w]-mx); sum+=lg[w]; }
  float inv=1.f/sum;
  for(int e=0;e<28;e++){
    float a=0;
    #pragma unroll
    for(int w=0;w<8;w++){ int j=s-2*w; if(j<0) j=0; a+=lg[w]*inv*b2f(qkv[(size_t)j*1344+896+h*28+e]); }
    o[(size_t)s*448+h*28+e]=f2b(a);
  }
}

__global__ void causal_attn(const bf16* Q, const bf16* Kp, const bf16* V, bf16* ctx){
  __shared__ float qs[256]; __shared__ float p[2048]; __shared__ float red[256];
  int qi=blockIdx.x, h=blockIdx.y, t=threadIdx.x;
  qs[t]=b2f(Q[(size_t)qi*4096+h*256+t]);
  __syncthreads();
  float mx=-1e30f;
  for(int j=t;j<=qi;j+=256){
    const bf16* k=Kp+(size_t)j*4096+h*256; float d=0;
    for(int e=0;e<256;e++) d+=qs[e]*b2f(k[e]);
    d*=0.0625f; p[j]=d; mx=fmaxf(mx,d);
  }
  red[t]=mx; __syncthreads();
  for(int s=128;s>0;s>>=1){ if(t<s) red[t]=fmaxf(red[t],red[t+s]); __syncthreads(); }
  mx=red[0]; __syncthreads();
  float sum=0;
  for(int j=t;j<=qi;j+=256){ float e=__expf(p[j]-mx); p[j]=e; sum+=e; }
  red[t]=sum; __syncthreads();
  for(int s=128;s>0;s>>=1){ if(t<s) red[t]+=red[t+s]; __syncthreads(); }
  float inv=1.f/red[0];
  float a=0;
  for(int j=0;j<=qi;j++) a+=p[j]*b2f(V[(size_t)j*4096+h*256+t]);
  ctx[(size_t)qi*4096+h*256+t]=f2b(a*inv);
}

extern "C" void kernel_launch(void* const* d_in, const int* in_sizes, int n_in,
                              void* d_out, int out_size, void* d_ws, size_t ws_size,
                              hipStream_t stream) {
  const bf16* Ax=(const bf16*)d_in[0]; const bf16* Awqkv=(const bf16*)d_in[1]; const bf16* Awo=(const bf16*)d_in[2];
  const bf16* Bx=(const bf16*)d_in[3]; const bf16* ckv=(const bf16*)d_in[4]; const bf16* wq=(const bf16*)d_in[5];
  const bf16* wuk=(const bf16*)d_in[6]; const bf16* wuv=(const bf16*)d_in[7]; const bf16* wo=(const bf16*)d_in[8];
  bf16* out=(bf16*)d_out; bf16* ws=(bf16*)d_ws;
  bf16* qkv=ws; bf16* oa=qkv+2048*1344; bf16* Q=oa+2048*448; bf16* K=Q+2048*4096; bf16* V=K+2048*4096; bf16* ctx=V+2048*4096;
  gemm_naive<<<dim3(21,32),256,0,stream>>>(Ax,Awqkv,qkv,2048,1344,448,1344);
  local_attn<<<2048*16/256,256,0,stream>>>(qkv,oa);
  gemm_naive<<<dim3(7,32),256,0,stream>>>(oa,Awo,out,2048,448,448,896);
  gemm_naive<<<dim3(64,32),256,0,stream>>>(Bx,wq,Q,2048,4096,448,4096);
  gemm_naive<<<dim3(64,32),256,0,stream>>>(ckv,wuk,K,2048,4096,1024,4096);
  gemm_naive<<<dim3(64,32),256,0,stream>>>(ckv,wuv,V,2048,4096,1024,4096);
  causal_attn<<<dim3(2048,16),256,0,stream>>>(Q,K,V,ctx);
  gemm_naive<<<dim3(7,32),256,0,stream>>>(ctx,wo,out+448,2048,448,4096,896);
}
```

```cpp
#include <hip/hip_runtime.h>
#include <hip/hip_bf16.h>

extern "C" __global__ __launch_bounds__(256, 1) void fused_op(const void* ckv, const void* wuk, const void* wuv, const void* bx, const void* wq,
    const void* ax, const void* awqkv, void* ws, const void* wo, const void* awo, void* out) {
  __shared__ __attribute__((aligned(16))) char lds[163840];
  asm volatile(
".macro CV d,a,b\nv_cvt_pk_bf16_f32 v[\\d], v[\\a], v[\\b]\n.endm\n.macro DP v,r,s,o\n"
"buffer_load_dwordx4 v[\\v], s[\\r:\\r+3], s[\\s] offen offset:\\o lds\n.endm\n.macro DS v,r,s,o\n"
"buffer_load_dwordx4 v[\\v], s[\\r:\\r+3], s[\\s] offen offset:\\o sc1 lds\n.endm\n.macro Maaa d,a,b\n"
"v_mfma_f32_16x16x32_bf16 a[\\d:\\d+3], a[\\a:\\a+3], a[\\b:\\b+3], a[\\d:\\d+3]\n.endm\n.macro Mvaa d,a,b\n"
"v_mfma_f32_16x16x32_bf16 v[\\d:\\d+3], a[\\a:\\a+3], a[\\b:\\b+3], v[\\d:\\d+3]\n.endm\n.macro Mvav d,a,b\n"
"v_mfma_f32_16x16x32_bf16 v[\\d:\\d+3], a[\\a:\\a+3], v[\\b:\\b+3], v[\\d:\\d+3]\n.endm\n.macro Nvaa d,a,b,c\n"
"v_mfma_f32_16x16x32_bf16 v[\\d:\\d+3], a[\\a:\\a+3], a[\\b:\\b+3], v[\\c:\\c+3]\n.endm\n.macro RD d,a,o\nds_read_b128 a[\\d:\\d+3], v[\\a] offset:\\o\n"
".endm\n.macro TR d,a,o\nds_read_b64_tr_b16 a[\\d:\\d+1], v[\\a] offset:\\o\n.endm\n.macro WL n\ns_waitcnt lgkmcnt(\\n)\n.endm\n.macro Zaaa d,a,b\n"
"v_mfma_f32_16x16x32_bf16 a[\\d:\\d+3], a[\\a:\\a+3], a[\\b:\\b+3], 0\n.endm\n.macro Zvaa d,a,b\n"
"v_mfma_f32_16x16x32_bf16 v[\\d:\\d+3], a[\\a:\\a+3], a[\\b:\\b+3], 0\n.endm\n.macro Zvav d,a,b\n"
"v_mfma_f32_16x16x32_bf16 v[\\d:\\d+3], a[\\a:\\a+3], v[\\b:\\b+3], 0\n.endm\n.macro Zvvv d,a,b\n"
"v_mfma_f32_16x16x32_bf16 v[\\d:\\d+3], v[\\a:\\a+3], v[\\b:\\b+3], 0\n.endm\ns_mov_b64 s[8:9],%0\ns_mov_b32 s10,%1\ns_mov_b32 s11,%2\n"
"s_load_dwordx16 s[72:87],s[8:9],0\ns_load_dwordx4 s[88:91],s[8:9],64\ns_load_dwordx2 s[92:93],s[8:9],80\ns_getpc_b64 s[54:55]\nfused_op_pc:\n"
"v_mbcnt_lo_u32_b32 v16,-1,0\nv_mbcnt_hi_u32_b32 v16,-1,v16\nv_lshlrev_b32 v0,4,v16\ns_lshl_b32 s48,s10,13\ns_bfe_u32 s97,s11,0x10006\n"
"s_bfe_u32 s70,s11,0x10003\ns_lshl_b32 s70,s70,2\ns_bfe_u32 s45,s11,0x10004\ns_add_u32 s70,s70,s45\ns_lshr_b32 s58,s11,7\ns_xor_b32 s45,s58,1\n"
"s_lshl_b32 s45,s45,1\ns_add_u32 s70,s70,s45\ns_and_b32 s71,s11,7\ns_lshl_b32 s56,s71,1\ns_bfe_u32 s45,s11,0x10005\ns_add_u32 s71,s56,s45\n"
"v_and_b32 v17,15,v16\nv_lshrrev_b32 v18,4,v16\nv_mul_u32_u24 v19,896,v17\nv_lshl_add_u32 v9,v18,4,v19\nv_lshrrev_b32 v19,1,v17\nv_xor_b32 v19,v19,v18\n"
"v_lshlrev_b32 v19,4,v19\nv_lshl_add_u32 v19,v17,7,v19\ns_lshr_b32 s45,s10,1\ns_lshl_b32 s45,s45,13\nv_add_u32 v3,s45,v19\nv_xor_b32 v4,64,v3\n"
"v_bfe_u32 v19,v16,2,2\nv_and_b32 v20,1,v18\nv_lshl_add_u32 v19,v20,2,v19\nv_lshrrev_b32 v20,1,v19\nv_lshlrev_b32 v20,5,v20\n"
"v_lshl_add_u32 v20,v19,7,v20\nv_and_b32 v21,3,v16\nv_lshl_add_u32 v20,v21,3,v20\nv_lshrrev_b32 v21,1,v18\nv_lshl_add_u32 v10,v21,11,v20\n"
"s_and_b32 s45,s10,1\ns_lshl_b32 s45,s45,13\ns_add_u32 s45,s45,114688\nv_add_u32 v5,s45,v10\nv_xor_b32 v6,32,v5\nv_xor_b32 v7,64,v5\nv_xor_b32 v8,96,v5\n"
"s_mov_b32 s95,0x8000\ns_mov_b32 s96,0xfffe0000\ns_mov_b32 s47,0\nWL 0\ns_lshr_b32 s52,s11,3\ns_and_b32 s53,s52,7\ns_mul_i32 s45,s53,4\n"
"s_mul_i32 s49,s10,1\ns_add_u32 s45,s45,s49\ns_lshl_b32 s45,s45,13\ns_cmp_lt_u32 s45,fused_op_qproj-fused_op_pc\ns_cbranch_scc0 fused_op_cw1\n"
"s_add_u32 s45,s45,fused_op_pc-fused_op_pc\nv_lshlrev_b32 v12,3,v0\nv_add_u32 v12,s45,v12\nglobal_load_dword v11,v12,s[54:55]\nfused_op_cw1:\n"
"s_cmp_lt_u32 s10,2\ns_cbranch_scc1 fused_op_ftw\nv_lshlrev_b32 v13,7,v0\ns_mul_i32 s45,s52,131072\ns_mul_i32 s49,s10,128\ns_add_u32 s45,s45,s49\n"
"s_add_u32 s45,s45,0xffffff00\nv_add_u32 v13,s45,v13\nglobal_load_dword v11,v13,s[72:73]\ns_branch fused_op_ftd\nfused_op_ftw:\nv_lshrrev_b32 v13,7,v0\n"
"v_lshlrev_b32 v13,13,v13\nv_and_b32 v12,0x70,v0\nv_lshl_add_u32 v13,v12,3,v13\ns_bitcmp1_b32 s52,0\ns_cselect_b32 s8,s76,s74\ns_cselect_b32 s9,s77,s75\n"
"s_lshr_b32 s52,s52,1\ns_and_b32 s53,s11,7\ns_mul_i32 s45,s52,65536\ns_mul_i32 s49,s53,1024\ns_add_u32 s45,s45,s49\ns_mul_i32 s49,s10,1048576\n"
"s_add_u32 s45,s45,s49\nv_add_u32 v13,s45,v13\nglobal_load_dword v11,v13,s[8:9]\nfused_op_ftd:\ns_cmp_ge_u32 s10,2\ns_cbranch_scc1 fused_op_wsetup\n"
"v_lshrrev_b32 v17,3,v16\nv_and_b32 v18,7,v16\nv_lshrrev_b32 v19,1,v17\nv_xor_b32 v19,v19,v18\nv_lshlrev_b32 v19,4,v19\nv_lshl_add_u32 v1,v17,11,v19\n"
"v_xor_b32 v2,64,v1\ns_mul_i32 s45,s70,524288\ns_mul_i32 s49,s10,262144\ns_add_u32 s45,s45,s49\ns_mov_b32 s36,0\ns_mov_b32 s37,15360\n"
"s_mov_b32 s38,30720\ns_mov_b32 s39,46080\ns_mov_b32 s40,65536\ns_mov_b32 s41,80896\ns_mov_b32 s42,96256\ns_mov_b32 s43,111616\ns_mov_b32 s34,131072\n"
"s_mov_b32 s35,0xfffe0080\nWL 0\ns_add_u32 s20,s72,s45\ns_addc_u32 s21,s73,0\ns_and_b32 s21,s21,0xffff\ns_sub_u32 s22,4194304,s45\n"
"s_mov_b32 s23,0x20000\ns_add_u32 m0,s48,0\ns_nop 0\nDP 1,20,36,0\nDP 2,20,37,1024\nDP 1,20,38,2048\nDP 2,20,39,3072\ns_add_u32 m0,m0,0x1000\ns_nop 0\n"
"DP 1,20,40,0\nDP 2,20,41,1024\nDP 1,20,42,2048\nDP 2,20,43,3072\nv_add_u32 v1,s34,v1\nv_add_u32 v2,s34,v2\ns_mul_i32 s52,s11,1\ns_mul_i32 s49,s10,1\n"
"s_add_u32 s52,s52,s49\nv_lshrrev_b32 v13,4,v0\nv_min_u32 v13,63,v13\nv_lshlrev_b32 v13,7,v13\ns_mov_b32 s18,0\ns_mov_b64 s[8:9],s[54:55]\n"
"s_cmp_ge_u32 s10,1\ns_cbranch_scc1 fused_op_wsd\ns_cmp_lt_u32 s52,64\ns_cbranch_scc1 fused_op_ws_0\ns_cmp_lt_u32 s52,67\ns_cbranch_scc1 fused_op_ws_1\n"
"s_cmp_lt_u32 s52,68\ns_cbranch_scc1 fused_op_ws_2\ns_cmp_lt_u32 s52,132\ns_cbranch_scc1 fused_op_ws_3\ns_cmp_lt_u32 s52,139\n"
"s_cbranch_scc1 fused_op_ws_4\ns_branch fused_op_wsd\nfused_op_ws_0:\ns_mul_i32 s45,s52,57344\ns_add_u32 s8,s80,s45\ns_addc_u32 s9,s81,0\n"
"s_mov_b32 s18,8192\ns_branch fused_op_wsd\nfused_op_ws_1:\ns_mul_i32 s45,s52,57344\ns_add_u32 s45,s45,0xffc80000\n"
"s_min_u32 s45,s45,fused_op_end-fused_op_pc-57344\ns_add_u32 s8,s54,s45\ns_addc_u32 s9,s55,0\ns_mov_b32 s18,8192\ns_branch fused_op_wsd\nfused_op_ws_2:\n"
"s_mov_b32 s45,fused_op_end-fused_op_pc-57344\ns_add_u32 s8,s54,s45\ns_addc_u32 s9,s55,0\ns_mov_b32 s18,8192\ns_branch fused_op_wsd\nfused_op_ws_3:\n"
"s_mul_i32 s45,s52,57344\ns_add_u32 s45,s45,0xffc48000\ns_add_u32 s8,s88,s45\ns_addc_u32 s9,s89,0\ns_mov_b32 s18,8192\ns_branch fused_op_wsd\n"
"fused_op_ws_4:\ns_mul_i32 s45,s52,57344\ns_add_u32 s45,s45,0xff8c8000\ns_add_u32 s8,s90,s45\ns_addc_u32 s9,s91,0\ns_mov_b32 s18,8192\n"
"s_branch fused_op_wsd\nfused_op_wsd:\ns_cmp_eq_u32 s18,0\ns_cselect_b64 vcc,-1,0\nv_cndmask_b32 v13,v13,0,vcc\ns_branch fused_op_setup_done\n"
"fused_op_wsetup:\nv_lshrrev_b32 v17,7,v0\nv_and_b32 v1,6,v17\nv_lshlrev_b32 v1,4,v1\nv_xor_b32 v1,v1,v0\nv_and_b32 v1,0x70,v1\nv_and_b32 v18,4,v17\n"
"v_add_u32 v17,v17,v18\nv_mul_u32_u24 v17,8192,v17\nv_add_u32 v1,v1,v17\nv_mov_b32 v2,v1\ns_mul_i32 s45,s71,512\ns_mul_i32 s49,s10,256\n"
"s_add_u32 s45,s45,s49\ns_add_u32 s45,s45,0xfffffe00\ns_mov_b32 s36,0\ns_mov_b32 s37,31744\ns_mov_b32 s38,129024\ns_mov_b32 s39,160768\n"
"s_mov_b32 s40,262144\ns_mov_b32 s41,293888\ns_mov_b32 s42,391168\ns_mov_b32 s43,422912\ns_mov_b32 s34,128\ns_mov_b32 s35,524160\nWL 0\n"
"s_cmp_eq_u32 s97,0\ns_cselect_b32 s52,s74,s76\ns_cselect_b32 s53,s75,s77\ns_add_u32 s20,s52,s45\ns_addc_u32 s21,s53,0\ns_and_b32 s21,s21,0xffff\n"
"s_sub_u32 s22,8388608,s45\ns_mov_b32 s23,0x20000\ns_add_u32 m0,s48,98304\ns_nop 0\nDP 1,20,36,0\nDP 2,20,37,1024\nDP 1,20,38,2048\nDP 2,20,39,3072\n"
"s_add_u32 m0,m0,0x1000\ns_nop 0\nDP 1,20,40,0\nDP 2,20,41,1024\nDP 1,20,42,2048\nDP 2,20,43,3072\nv_add_u32 v1,s34,v1\nv_add_u32 v2,s34,v2\n"
"s_add_u32 m0,s48,131072\ns_nop 0\nDP 1,20,36,0\nDP 2,20,37,1024\nDP 1,20,38,2048\nDP 2,20,39,3072\ns_add_u32 m0,m0,0x1000\ns_nop 0\nDP 1,20,40,0\n"
"DP 2,20,41,1024\nDP 1,20,42,2048\nDP 2,20,43,3072\nv_add_u32 v1,s35,v1\nv_add_u32 v2,s35,v2\ns_add_u32 m0,s48,0\ns_nop 0\nDP 1,20,36,0\n"
"DP 2,20,37,1024\nDP 1,20,38,2048\nDP 2,20,39,3072\ns_add_u32 m0,m0,0x1000\ns_nop 0\nDP 1,20,40,0\nDP 2,20,41,1024\nDP 1,20,42,2048\nDP 2,20,43,3072\n"
"v_add_u32 v1,s34,v1\nv_add_u32 v2,s34,v2\ns_mov_b32 s18,0\ns_mov_b64 s[8:9],s[54:55]\nv_mov_b32 v13,0\ns_waitcnt vmcnt(8)\nfused_op_setup_done:\n"
"s_mov_b32 s24,s86\ns_and_b32 s25,s87,0xffff\ns_mov_b32 s26,76546048\ns_mov_b32 s27,0x20000\ns_barrier\nTR 0,5,0\nTR 2,5,1024\nTR 4,5,4096\nTR 6,5,5120\n"
"TR 8,6,0\nTR 10,6,1024\nTR 12,6,4096\nTR 14,6,5120\nTR 16,7,0\nTR 18,7,1024\nTR 20,7,4096\nTR 22,7,5120\nTR 24,8,0\nTR 26,8,1024\nTR 28,8,4096\n"
"TR 30,8,5120\nv_add_u32 v5,0x8000,v5\nv_add_u32 v6,0x8000,v6\nv_add_u32 v7,0x8000,v7\nv_add_u32 v8,0x8000,v8\nWL 8\nTR 32,5,0\nTR 34,5,1024\n"
"TR 36,5,4096\nTR 38,5,5120\nTR 40,6,0\nTR 42,6,1024\nTR 44,6,4096\nTR 46,6,5120\nTR 48,7,0\nTR 50,7,1024\nTR 52,7,4096\nTR 54,7,5120\nTR 56,8,0\n"
"TR 58,8,1024\nTR 60,8,4096\nTR 62,8,5120\nv_add_u32 v5,s96,v5\nv_add_u32 v6,s96,v6\nv_add_u32 v7,s96,v7\nv_add_u32 v8,s96,v8\ns_add_u32 m0,s48,32768\n"
"s_nop 0\nDP 1,20,36,0\nDP 2,20,37,1024\nDP 1,20,38,2048\nDP 2,20,39,3072\ns_add_u32 m0,m0,0x1000\ns_nop 0\nDP 1,20,40,0\nDP 2,20,41,1024\n"
"DP 1,20,42,2048\nDP 2,20,43,3072\nv_add_u32 v1,s35,v1\nv_add_u32 v2,s35,v2\nfused_op_s1done:\ns_waitcnt vmcnt(8) lgkmcnt(0)\ns_barrier\nRD 128,3,0\n"
"RD 132,4,0\nWL 0\ns_cmp_lg_u32 s97,0\ns_cbranch_scc1 fused_op_kvh1\nfused_op_kvh0:\nZaaa 240,0,128\ns_bfe_u32 s45,s11,0x10003\nRD 136,3,2048\n"
"Maaa 240,4,132\ns_add_u32 m0,s48,65536\nRD 140,4,2048\nZvaa 16,12,132\ns_add_u32 s56,s56,s45\nTR 64,5,0\nMvaa 16,8,128\ns_bfe_u32 s57,s11,0x30004\n"
"DP 1,20,36,0\nZaaa 244,16,128\ns_mul_i32 s68,s56,8\nTR 66,5,1024\nMaaa 244,20,132\ns_mul_i32 s49,s57,1\nTR 68,5,4096\nZvaa 20,28,132\n"
"s_add_u32 s68,s68,s49\nTR 70,5,5120\nMvaa 20,24,128\ns_lshl_b32 s45,s57,2\nDP 2,20,37,1024\nZaaa 248,32,128\ns_lshr_b32 s60,0x2468ace,s45\nTR 72,6,0\n"
"Maaa 248,36,132\ns_and_b32 s60,s60,15\nTR 74,6,1024\nZvaa 24,44,132\ns_lshr_b32 s46,0x55558643,s45\nDP 1,20,38,2048\nMvaa 24,40,128\n"
"s_and_b32 s46,s46,15\nTR 76,6,4096\nZaaa 252,48,128\ns_mul_i32 s5,s56,4096\nTR 78,6,5120\nMaaa 252,52,132\ns_mul_i32 s49,s57,512\nDP 2,20,39,3072\n"
"Zvaa 28,60,132\ns_add_u32 s5,s5,s49\nTR 80,7,0\nMvaa 28,56,128\nWL 10\ns_mul_i32 s49,s10,128\nTR 82,7,1024\nZvaa 44,56,136\nWL 10\n"
"s_add_u32 m0,m0,0x1000\nRD 128,3,4096\nMvaa 44,60,140\ns_add_u32 s5,s5,s49\nRD 132,4,4096\nZvaa 156,52,140\ns_add_u32 s5,s5,0x90000\nDP 1,20,40,0\n"
"Mvaa 156,48,136\ns_sub_u32 s45,15,s57\nTR 84,7,4096\nZvaa 40,40,136\ns_cmp_eq_u32 s58,0\ns_cselect_b32 s59,s45,s57\ns_cselect_b32 s31,s60,0\n"
"s_cselect_b32 s46,5,s46\ns_cselect_b32 s5,s5,0x7ff00000\ns_cselect_b32 s52,s82,s78\ns_cselect_b32 s53,s83,s79\nTR 86,7,5120\nMvaa 40,44,140\n"
"s_lshl_b32 s61,s59,1\nDP 2,20,41,1024\nZvaa 152,36,140\ns_add_u32 s61,s61,2\nTR 88,8,0\nMvaa 152,32,136\ns_sub_u32 s61,s61,s31\nTR 90,8,1024\n"
"Zvaa 36,24,136\ns_mov_b32 s69,0\nDP 1,20,42,2048\nMvaa 36,28,140\ns_mul_i32 s66,s56,2097152\nTR 92,8,4096\nZvaa 148,20,140\ns_mul_i32 s49,s10,8192\n"
"TR 94,8,5120\nMvaa 148,16,136\ns_add_u32 s66,s66,s49\nDP 2,20,43,3072\nZvaa 32,8,136\ns_add_u32 m0,s48,98304\nv_add_u32 v1,s34,v1\nv_add_u32 v2,s34,v2\n"
"Mvaa 32,12,140\ns_add_u32 s66,s66,0x100000\nZvaa 144,4,140\ns_mul_i32 s6,s57,-131072\nDP 1,20,36,0\nMvaa 144,0,136\nWL 7\ns_mul_i32 s49,s60,-65536\n"
"Zvaa 160,0,128\nWL 6\ns_add_u32 s6,s6,s49\nRD 136,3,6144\nMvaa 160,4,132\ns_mov_b32 s44,s47\nRD 140,4,6144\nZvaa 48,12,132\ns_add_u32 s47,s47,0x8000\n"
"Mvaa 48,8,128\ns_cmp_eq_u32 s47,0x28000\nZvaa 164,16,128\ns_cselect_b32 s94,s96,s95\nMvaa 164,20,132\ns_cselect_b32 s47,0,s47\nZvaa 52,28,132\n"
"s_add_u32 s6,s6,0x200000\nDP 2,20,37,1024\nMvaa 52,24,128\ns_add_u32 s6,s6,s66\nv_add_u32 v3,s94,v3\nv_add_u32 v4,s94,v4\nZvaa 168,32,128\n"
"s_mov_b32 s64,0\nMvaa 168,36,132\ns_mov_b32 s67,0x41800000\nDP 1,20,38,2048\nZvaa 56,44,132\ns_and_b32 s98,s11,127\nMvaa 56,40,128\n"
"s_mul_i32 s45,s98,14336\nv_add_u32 v5,s94,v5\nv_add_u32 v6,s94,v6\nZvaa 172,48,128\ns_mul_i32 s49,s10,256\nDP 2,20,39,3072\nMvaa 172,52,132\n"
"s_add_u32 s45,s45,s49\nZvaa 60,60,132\ns_add_u32 s12,s52,s45\ns_addc_u32 s13,s53,0\nv_add_u32 v7,s94,v7\nv_add_u32 v8,s94,v8\nMvaa 60,56,128\n"
"s_waitcnt vmcnt(12) lgkmcnt(0)\ns_barrier\nZvaa 76,56,136\ns_and_b32 s13,s13,0xffff\nRD 128,3,0\nMvaa 76,60,140\ns_sub_u32 s14,1835008,s45\nRD 132,4,0\n"
"Zvaa 188,52,140\ns_mov_b32 s15,0x20000\nMvaa 188,48,136\ns_mul_i32 s98,s56,131072\nZvaa 72,40,136\ns_mul_i32 s49,s10,32768\nMvaa 72,44,140\n"
"s_add_u32 s98,s98,s49\nZvaa 184,36,140\ns_add_u32 s98,s98,0x4300000\nMvaa 184,32,136\ns_sub_u32 s99,s98,16384\nZvaa 68,24,136\n"
"s_cmp_eq_u32 s98,70254592\ns_cselect_b32 s99,s98,s99\nMvaa 68,28,140\ns_mul_i32 s30,s71,2097152\nZvaa 180,20,140\ns_mul_i32 s49,s70,262144\n"
"Mvaa 180,16,136\ns_add_u32 s30,s30,s49\nZvaa 64,8,136\ns_add_u32 s30,s30,0x100000\nMvaa 64,12,140\ns_and_b32 s45,s70,3\nZvaa 176,4,140\n"
"s_lshl_b32 s45,s45,1\nMvaa 176,0,136\nWL 1\ns_lshr_b32 s70,s70,2\nZvaa 192,0,128\nWL 0\ns_add_u32 s70,s70,s45\nRD 136,3,2048\nMvaa 192,4,132\n"
"s_add_u32 m0,m0,0x1000\nRD 140,4,2048\nZvaa 80,12,132\ns_mul_i32 s7,s71,8192\nTR 96,5,0\nMvaa 80,8,128\ns_mul_i32 s49,s70,1024\nDP 1,20,40,0\n"
"Zvaa 196,16,128\ns_add_u32 s7,s7,s49\nTR 98,5,1024\nMvaa 196,20,132\ns_mul_i32 s49,s97,512\nTR 100,5,4096\nZvaa 84,28,132\ns_add_u32 s7,s7,s49\n"
"TR 102,5,5120\nMvaa 84,24,128\ns_mul_i32 s49,s10,128\nDP 2,20,41,1024\nZvaa 200,32,128\ns_add_u32 s7,s7,s49\nTR 104,6,0\nMvaa 200,36,132\n"
"s_add_u32 s7,s7,0x10000\nTR 106,6,1024\nZvaa 88,44,132\ns_lshr_b32 s45,s10,1\nDP 1,20,42,2048\nMvaa 88,40,128\ns_lshl_b32 s45,s45,17\nTR 108,6,4096\n"
"Zvaa 204,48,128\ns_add_u32 s30,s30,s45\nTR 110,6,5120\nMvaa 204,52,132\ns_and_b32 s45,s10,1\nDP 2,20,43,3072\nZvaa 92,60,132\ns_lshl_b32 s49,s45,12\n"
"v_add_u32 v1,s35,v1\nv_add_u32 v2,s35,v2\nMvaa 92,56,128\nWL 9\ns_lshl_b32 s45,s45,14\nTR 112,7,0\nZvaa 108,56,136\nWL 9\ns_add_u32 m0,s48,131072\n"
"RD 128,3,4096\nMvaa 108,60,140\ns_add_u32 s45,s45,0x8000\nRD 132,4,4096\nZvaa 220,52,140\ns_mov_b32 s28,4096\nDP 1,20,36,0\nMvaa 220,48,136\n"
"s_cmp_eq_u32 s97,0\ns_cselect_b32 s45,s49,s45\ns_cselect_b32 s28,8192,s28\nTR 114,7,1024\nZvaa 104,40,136\ns_add_u32 s30,s30,s45\nTR 116,7,4096\n"
"Mvaa 104,44,140\ns_mul_i32 s29,s28,3\nDP 2,20,37,1024\nZvaa 216,36,140\ns_sub_u32 s29,65536,s29\nTR 118,7,5120\nMvaa 216,32,136\nTR 120,8,0\n"
"Zvaa 100,24,136\nDP 1,20,38,2048\nMvaa 100,28,140\nTR 122,8,1024\nZvaa 212,20,140\nTR 124,8,4096\nMvaa 212,16,136\nDP 2,20,39,3072\nZvaa 96,8,136\n"
"s_add_u32 m0,m0,0x1000\nTR 126,8,5120\nMvaa 96,12,140\nZvaa 208,4,140\nDP 1,20,40,0\nMvaa 208,0,136\nWL 8\nZvaa 224,0,128\nWL 7\nRD 136,3,6144\n"
"Mvaa 224,4,132\ns_mov_b32 s44,s47\nRD 140,4,6144\nZvaa 112,12,132\ns_add_u32 s47,s47,0x8000\nMvaa 112,8,128\ns_cmp_eq_u32 s47,0x28000\nZvaa 228,16,128\n"
"s_cselect_b32 s94,s96,s95\nMvaa 228,20,132\ns_cselect_b32 s47,0,s47\nZvaa 116,28,132\nDP 2,20,41,1024\nMvaa 116,24,128\nv_add_u32 v3,s94,v3\n"
"v_add_u32 v4,s94,v4\nZvaa 232,32,128\nMvaa 232,36,132\nDP 1,20,42,2048\nZvaa 120,44,132\nMvaa 120,40,128\nv_add_u32 v5,s94,v5\nv_add_u32 v6,s94,v6\n"
"Zvaa 236,48,128\nDP 2,20,43,3072\nMvaa 236,52,132\nv_add_u32 v1,s34,v1\nv_add_u32 v2,s34,v2\nZvaa 124,60,132\nv_add_u32 v7,s94,v7\nv_add_u32 v8,s94,v8\n"
"Mvaa 124,56,128\ns_waitcnt vmcnt(16) lgkmcnt(0)\ns_barrier\nZvaa 140,56,136\nRD 128,3,0\nMvaa 140,60,140\nRD 132,4,0\nZvaa 252,52,140\nMvaa 252,48,136\n"
"Zvaa 136,40,136\nMvaa 136,44,140\nZvaa 248,36,140\nMvaa 248,32,136\nZvaa 132,24,136\nMvaa 132,28,140\nZvaa 244,20,140\nMvaa 244,16,136\nZvaa 128,8,136\n"
"Mvaa 128,12,140\nZvaa 240,4,140\nMvaa 240,0,136\nWL 1\nMaaa 240,64,128\nWL 0\nRD 136,3,2048\nMaaa 240,68,132\ns_add_u32 m0,s48,0\nRD 140,4,2048\n"
"Mvaa 16,76,132\nTR 0,5,0\nMvaa 16,72,128\nDP 1,20,36,0\nMaaa 244,80,128\nTR 2,5,1024\nMaaa 244,84,132\nTR 4,5,4096\nMvaa 20,92,132\nTR 6,5,5120\n"
"Mvaa 20,88,128\nDP 2,20,37,1024\nMaaa 248,96,128\nTR 8,6,0\nMaaa 248,100,132\nTR 10,6,1024\nMvaa 24,108,132\nDP 1,20,38,2048\nMvaa 24,104,128\n"
"TR 12,6,4096\nMaaa 252,112,128\nTR 14,6,5120\nMaaa 252,116,132\nDP 2,20,39,3072\nMvaa 28,124,132\nTR 16,7,0\nMvaa 28,120,128\nWL 10\nTR 18,7,1024\n"
"Mvaa 44,120,136\nWL 10\ns_add_u32 m0,m0,0x1000\nRD 128,3,4096\nMvaa 44,124,140\nRD 132,4,4096\nMvaa 156,116,140\nDP 1,20,40,0\nMvaa 156,112,136\n"
"TR 20,7,4096\nMvaa 40,104,136\nTR 22,7,5120\nMvaa 40,108,140\nDP 2,20,41,1024\nMvaa 152,100,140\nTR 24,8,0\nMvaa 152,96,136\nTR 26,8,1024\n"
"Mvaa 36,88,136\nDP 1,20,42,2048\nMvaa 36,92,140\nTR 28,8,4096\nMvaa 148,84,140\nTR 30,8,5120\nMvaa 148,80,136\nDP 2,20,43,3072\nMvaa 32,72,136\n"
"s_add_u32 m0,s48,32768\nv_add_u32 v1,s35,v1\nv_add_u32 v2,s35,v2\nMvaa 32,76,140\nMvaa 144,68,140\nDP 1,20,36,0\nMvaa 144,64,136\nWL 7\n"
"Mvaa 160,64,128\nWL 6\nRD 136,3,6144\nMvaa 160,68,132\ns_mov_b32 s44,s47\nRD 140,4,6144\nMvaa 48,76,132\ns_add_u32 s47,s47,0x8000\nMvaa 48,72,128\n"
"s_cmp_eq_u32 s47,0x28000\nMvaa 164,80,128\ns_cselect_b32 s94,s96,s95\nMvaa 164,84,132\ns_cselect_b32 s47,0,s47\nMvaa 52,92,132\nDP 2,20,37,1024\n"
"Mvaa 52,88,128\nv_add_u32 v3,s94,v3\nv_add_u32 v4,s94,v4\nMvaa 168,96,128\nMvaa 168,100,132\nDP 1,20,38,2048\nMvaa 56,108,132\nMvaa 56,104,128\n"
"v_add_u32 v5,s94,v5\nv_add_u32 v6,s94,v6\nMvaa 172,112,128\nDP 2,20,39,3072\nMvaa 172,116,132\nMvaa 60,124,132\nv_add_u32 v7,s94,v7\n"
"v_add_u32 v8,s94,v8\nMvaa 60,120,128\ns_waitcnt vmcnt(20) lgkmcnt(0)\ns_barrier\nMvaa 76,120,136\nRD 128,3,0\nMvaa 76,124,140\nRD 132,4,0\n"
"Mvaa 188,116,140\nMvaa 188,112,136\nMvaa 72,104,136\nbuffer_load_dword a239,off,s[24:27],0 offset:0 sc1\nMvaa 72,108,140\nMvaa 184,100,140\n"
"Mvaa 184,96,136\nMvaa 68,88,136\nbuffer_load_dwordx4 a[144:147],v9,s[12:15],0 offen offset:0\nMvaa 68,92,140\nMvaa 180,84,140\nMvaa 180,80,136\n"
"Mvaa 64,72,136\nMvaa 64,76,140\nMvaa 176,68,140\nMvaa 176,64,136\nWL 1\nMvaa 192,64,128\nWL 0\nRD 136,3,2048\nMvaa 192,68,132\ns_add_u32 m0,m0,0x1000\n"
"RD 140,4,2048\nMvaa 80,76,132\nTR 32,5,0\nMvaa 80,72,128\nDP 1,20,40,0\nMvaa 196,80,128\nTR 34,5,1024\nMvaa 196,84,132\nTR 36,5,4096\nMvaa 84,92,132\n"
"TR 38,5,5120\nMvaa 84,88,128\nDP 2,20,41,1024\nMvaa 200,96,128\nTR 40,6,0\nMvaa 200,100,132\nTR 42,6,1024\nMvaa 88,108,132\nDP 1,20,42,2048\n"
"Mvaa 88,104,128\nTR 44,6,4096\nMvaa 204,112,128\nTR 46,6,5120\nMvaa 204,116,132\nDP 2,20,43,3072\nMvaa 92,124,132\nv_add_u32 v1,s34,v1\n"
"v_add_u32 v2,s34,v2\nMvaa 92,120,128\nWL 9\nTR 48,7,0\nMvaa 108,120,136\nWL 9\ns_add_u32 m0,s48,65536\nRD 128,3,4096\nMvaa 108,124,140\nRD 132,4,4096\n"
"Mvaa 220,116,140\nDP 1,20,36,0\nMvaa 220,112,136\nTR 50,7,1024\nMvaa 104,104,136\nTR 52,7,4096\nMvaa 104,108,140\nDP 2,20,37,1024\nMvaa 216,100,140\n"
"TR 54,7,5120\nMvaa 216,96,136\nTR 56,8,0\nMvaa 100,88,136\nDP 1,20,38,2048\nMvaa 100,92,140\nTR 58,8,1024\nMvaa 212,84,140\nTR 60,8,4096\n"
"global_load_dword a238,v13,s[8:9]\nMvaa 212,80,136\ns_add_u32 s8,s8,s18\ns_addc_u32 s9,s9,0\nDP 2,20,39,3072\nMvaa 96,72,136\ns_add_u32 m0,m0,0x1000\n"
"TR 62,8,5120\nMvaa 96,76,140\nMvaa 208,68,140\nDP 1,20,40,0\nMvaa 208,64,136\nWL 8\nMvaa 224,64,128\nWL 7\nRD 136,3,6144\nMvaa 224,68,132\n"
"s_mov_b32 s44,s47\nRD 140,4,6144\nMvaa 112,76,132\ns_add_u32 s47,s47,0x8000\nMvaa 112,72,128\ns_cmp_eq_u32 s47,0x28000\nMvaa 228,80,128\n"
"s_cselect_b32 s94,s96,s95\nMvaa 228,84,132\ns_cselect_b32 s47,0,s47\nMvaa 116,92,132\nDP 2,20,41,1024\nMvaa 116,88,128\nv_add_u32 v3,s94,v3\n"
"v_add_u32 v4,s94,v4\nMvaa 232,96,128\nMvaa 232,100,132\nDP 1,20,42,2048\nMvaa 120,108,132\nMvaa 120,104,128\nv_add_u32 v5,s94,v5\nv_add_u32 v6,s94,v6\n"
"Mvaa 236,112,128\nDP 2,20,43,3072\nMvaa 236,116,132\nv_add_u32 v1,s35,v1\nv_add_u32 v2,s35,v2\nMvaa 124,124,132\nv_add_u32 v7,s94,v7\n"
"v_add_u32 v8,s94,v8\nMvaa 124,120,128\ns_waitcnt vmcnt(27) lgkmcnt(0)\ns_barrier\nMvaa 140,120,136\nRD 128,3,0\nMvaa 140,124,140\nRD 132,4,0\n"
"Mvaa 252,116,140\nMvaa 252,112,136\nMvaa 136,104,136\nMvaa 136,108,140\nMvaa 248,100,140\nMvaa 248,96,136\nMvaa 132,88,136\n"
"buffer_load_dwordx4 a[148:151],v9,s[12:15],0 offen offset:64\nMvaa 132,92,140\nMvaa 244,84,140\nMvaa 244,80,136\nMvaa 128,72,136\nMvaa 128,76,140\n"
"Mvaa 240,68,140\nMvaa 240,64,136\nWL 1\nMaaa 240,0,128\nWL 0\nRD 136,3,2048\nMaaa 240,4,132\nRD 140,4,2048\nMvaa 16,12,132\nTR 64,5,0\nMvaa 16,8,128\n"
"TR 66,5,1024\nMaaa 244,16,128\nTR 68,5,4096\nMaaa 244,20,132\nTR 70,5,5120\nMvaa 20,28,132\nTR 72,6,0\nMvaa 20,24,128\ns_add_u32 m0,s48,s44\n"
"TR 74,6,1024\nMaaa 248,32,128\nTR 76,6,4096\nMaaa 248,36,132\nDP 1,20,36,0\nMvaa 24,44,132\nTR 78,6,5120\nMvaa 24,40,128\nTR 80,7,0\nMaaa 252,48,128\n"
"DP 2,20,37,1024\nMaaa 252,52,132\nTR 82,7,1024\nMvaa 28,60,132\nTR 84,7,4096\nMvaa 28,56,128\nWL 12\nDP 1,20,38,2048\nMvaa 44,56,136\nWL 11\n"
"RD 128,3,4096\nMvaa 44,60,140\nRD 132,4,4096\nMvaa 156,52,140\nTR 86,7,5120\nMvaa 156,48,136\nTR 88,8,0\nMvaa 40,40,136\nTR 90,8,1024\nMvaa 40,44,140\n"
"TR 92,8,4096\nMvaa 152,36,140\nTR 94,8,5120\nMvaa 152,32,136\nMvaa 36,24,136\nMvaa 36,28,140\nDP 2,20,39,3072\nMvaa 148,20,140\ns_add_u32 m0,m0,0x1000\n"
"Mvaa 148,16,136\nMvaa 32,8,136\nDP 1,20,40,0\nMvaa 32,12,140\nMvaa 144,4,140\nbuffer_load_dwordx4 a[152:155],v9,s[12:15],0 offen offset:128\n"
"Mvaa 144,0,136\nWL 6\nDP 2,20,41,1024\nMvaa 160,0,128\nWL 5\nRD 136,3,6144\nMvaa 160,4,132\ns_mov_b32 s44,s47\nRD 140,4,6144\nMvaa 48,12,132\n"
"s_add_u32 s47,s47,0x8000\nMvaa 48,8,128\ns_cmp_eq_u32 s47,0x28000\nMvaa 164,16,128\ns_cselect_b32 s94,s96,s95\nMvaa 164,20,132\n"
"s_cselect_b32 s47,0,s47\nMvaa 52,28,132\nMvaa 52,24,128\nv_add_u32 v3,s94,v3\nv_add_u32 v4,s94,v4\nMvaa 168,32,128\nMvaa 168,36,132\nDP 1,20,42,2048\n"
"Mvaa 56,44,132\nMvaa 56,40,128\nv_add_u32 v5,s94,v5\nv_add_u32 v6,s94,v6\nMvaa 172,48,128\nDP 2,20,43,3072\nMvaa 172,52,132\nv_add_u32 v1,s34,v1\n"
"v_add_u32 v2,s34,v2\nMvaa 60,60,132\nv_add_u32 v7,s94,v7\nv_add_u32 v8,s94,v8\nMvaa 60,56,128\ns_waitcnt vmcnt(29) lgkmcnt(0)\ns_barrier\n"
"Mvaa 76,56,136\ns_mul_hi_u32 s62,s11,0xc30c30d\nRD 128,3,0\nMvaa 76,60,140\ns_mul_i32 s63,s62,21\nRD 132,4,0\nv_lshrrev_b32 v15,7,v0\nMvaa 188,52,140\n"
"s_sub_u32 s63,s11,s63\nv_and_b32 v11,6,v15\nMvaa 188,48,136\ns_mul_hi_u32 s70,s63,0x24924925\nv_lshlrev_b32 v11,4,v11\nMvaa 72,40,136\n"
"s_mul_i32 s71,s70,7\nv_xor_b32 v11,v11,v0\nMvaa 72,44,140\ns_sub_u32 s71,s63,s71\nv_and_b32 v11,0x70,v11\nMvaa 184,36,140\ns_mul_i32 s45,s71,172032\n"
"v_and_b32 v12,4,v15\nMvaa 184,32,136\ns_mul_i32 s49,s10,43008\nv_add_u32 v15,v15,v12\nMvaa 68,24,136\ns_add_u32 s45,s45,s49\n"
"v_mul_u32_u24 v15,2688,v15\nMvaa 68,28,140\ns_mul_i32 s49,s70,896\nv_add_u32 v11,v11,v15\nMvaa 180,20,140\ns_add_u32 s45,s45,s49\nMvaa 180,16,136\n"
"s_mul_i32 s49,s62,112\nMvaa 64,8,136\ns_add_u32 s45,s45,s49\nMvaa 64,12,140\ns_mul_i32 s63,s62,172032\nMvaa 176,4,140\ns_mul_i32 s49,s71,24576\n"
"Mvaa 176,0,136\nWL 1\ns_add_u32 s63,s63,s49\nMvaa 192,0,128\nWL 0\ns_mul_i32 s49,s70,8192\nRD 136,3,2048\nMvaa 192,4,132\ns_add_u32 s63,s63,s49\n"
"RD 140,4,2048\nMvaa 80,12,132\ns_mul_i32 s49,s10,2048\nTR 96,5,0\nMvaa 80,8,128\ns_add_u32 s63,s63,s49\nTR 98,5,1024\nMvaa 196,16,128\n"
"s_add_u32 s63,s63,0x4700000\nTR 100,5,4096\nMvaa 196,20,132\ns_cmp_ge_u32 s11,168\ns_cselect_b32 s45,0x7ff00000,s45\ns_cselect_b32 s63,0x7ff00000,s63\n"
"TR 102,5,5120\nMvaa 84,28,132\ns_add_u32 s62,s45,10752\nTR 104,6,0\nMvaa 84,24,128\ns_add_u32 m0,s48,s44\nTR 106,6,1024\nMvaa 200,32,128\n"
"TR 108,6,4096\nMvaa 200,36,132\nDP 1,20,36,0\nMvaa 88,44,132\nTR 110,6,5120\nMvaa 88,40,128\nTR 112,7,0\nMvaa 204,48,128\nDP 2,20,37,1024\n"
"Mvaa 204,52,132\nTR 114,7,1024\nMvaa 92,60,132\nTR 116,7,4096\nMvaa 92,56,128\nWL 12\nDP 1,20,38,2048\nMvaa 108,56,136\nWL 11\nRD 128,3,4096\n"
"Mvaa 108,60,140\nRD 132,4,4096\nMvaa 220,52,140\nTR 118,7,5120\nMvaa 220,48,136\nTR 120,8,0\nMvaa 104,40,136\nTR 122,8,1024\nMvaa 104,44,140\n"
"TR 124,8,4096\nMvaa 216,36,140\nTR 126,8,5120\nMvaa 216,32,136\nMvaa 100,24,136\nMvaa 100,28,140\nDP 2,20,39,3072\nMvaa 212,20,140\n"
"s_add_u32 m0,m0,0x1000\nMvaa 212,16,136\nMvaa 96,8,136\nDP 1,20,40,0\nMvaa 96,12,140\nMvaa 208,4,140\n"
"buffer_load_dwordx4 a[156:159],v9,s[12:15],0 offen offset:192\nMvaa 208,0,136\nWL 6\nDP 2,20,41,1024\nMvaa 224,0,128\nWL 5\nRD 136,3,6144\n"
"Mvaa 224,4,132\ns_mov_b32 s44,s47\nRD 140,4,6144\nMvaa 112,12,132\ns_add_u32 s47,s47,0x8000\nMvaa 112,8,128\ns_cmp_eq_u32 s47,0x28000\nMvaa 228,16,128\n"
"s_cselect_b32 s94,s96,s95\nMvaa 228,20,132\ns_cselect_b32 s47,0,s47\nMvaa 116,28,132\nMvaa 116,24,128\ns_mov_b32 s12,s84\nv_add_u32 v3,s94,v3\n"
"v_add_u32 v4,s94,v4\nMvaa 232,32,128\ns_and_b32 s13,s85,0xffff\nMvaa 232,36,132\ns_mov_b32 s14,1204224\nDP 1,20,42,2048\nMvaa 120,44,132\n"
"Mvaa 120,40,128\nv_add_u32 v5,s94,v5\nv_add_u32 v6,s94,v6\nMvaa 236,48,128\nDP 2,20,43,3072\nMvaa 236,52,132\nv_add_u32 v1,s35,v1\nv_add_u32 v2,s35,v2\n"
"Mvaa 124,60,132\nv_add_u32 v7,s94,v7\nv_add_u32 v8,s94,v8\nMvaa 124,56,128\ns_waitcnt vmcnt(28) lgkmcnt(0)\ns_barrier\nMvaa 140,56,136\nRD 128,3,0\n"
"Mvaa 140,60,140\nRD 132,4,0\nMvaa 252,52,140\nMvaa 252,48,136\nMvaa 136,40,136\nMvaa 136,44,140\nMvaa 248,36,140\n"
"buffer_load_dwordx4 a[160:163],v11,s[12:15],s45 offen\nMvaa 248,32,136\nMvaa 132,24,136\nMvaa 132,28,140\nMvaa 244,20,140\nMvaa 244,16,136\n"
"Mvaa 128,8,136\nMvaa 128,12,140\nMvaa 240,4,140\nMvaa 240,0,136\nWL 1\nMaaa 240,64,128\nWL 0\nRD 136,3,2048\nMaaa 240,68,132\nRD 140,4,2048\n"
"Mvaa 16,76,132\nTR 0,5,0\nMvaa 16,72,128\nTR 2,5,1024\nMaaa 244,80,128\nTR 4,5,4096\nMaaa 244,84,132\nTR 6,5,5120\nMvaa 20,92,132\nTR 8,6,0\n"
"Mvaa 20,88,128\ns_add_u32 m0,s48,s44\nTR 10,6,1024\nMaaa 248,96,128\nTR 12,6,4096\nMaaa 248,100,132\nDP 1,20,36,0\nMvaa 24,108,132\nTR 14,6,5120\n"
"Mvaa 24,104,128\nTR 16,7,0\nMaaa 252,112,128\nDP 2,20,37,1024\nMaaa 252,116,132\nTR 18,7,1024\nMvaa 28,124,132\nTR 20,7,4096\nMvaa 28,120,128\nWL 12\n"
"DP 1,20,38,2048\nMvaa 44,120,136\nWL 11\nRD 128,3,4096\nMvaa 44,124,140\nRD 132,4,4096\nMvaa 156,116,140\nTR 22,7,5120\nMvaa 156,112,136\nTR 24,8,0\n"
"Mvaa 40,104,136\nTR 26,8,1024\nMvaa 40,108,140\nTR 28,8,4096\nMvaa 152,100,140\nTR 30,8,5120\nMvaa 152,96,136\nMvaa 36,88,136\nMvaa 36,92,140\n"
"DP 2,20,39,3072\nMvaa 148,84,140\ns_add_u32 m0,m0,0x1000\nv_accvgpr_read_b32 v12,a239\nMvaa 148,80,136\nv_readfirstlane_b32 s65,v12\nMvaa 32,72,136\n"
"DP 1,20,40,0\nMvaa 32,76,140\nMvaa 144,68,140\nbuffer_load_dwordx4 a[164:167],v11,s[12:15],s62 offen\nMvaa 144,64,136\nWL 6\nDP 2,20,41,1024\n"
"Mvaa 160,64,128\nWL 5\nRD 136,3,6144\nMvaa 160,68,132\ns_mov_b32 s44,s47\nRD 140,4,6144\nMvaa 48,76,132\ns_add_u32 s47,s47,0x8000\nMvaa 48,72,128\n"
"s_cmp_eq_u32 s47,0x28000\nMvaa 164,80,128\ns_cselect_b32 s94,s96,s95\nMvaa 164,84,132\ns_cselect_b32 s47,0,s47\nMvaa 52,92,132\nMvaa 52,88,128\n"
"v_add_u32 v3,s94,v3\nv_add_u32 v4,s94,v4\nMvaa 168,96,128\nMvaa 168,100,132\nDP 1,20,42,2048\nMvaa 56,108,132\nMvaa 56,104,128\nv_add_u32 v5,s94,v5\n"
"v_add_u32 v6,s94,v6\nMvaa 172,112,128\nDP 2,20,43,3072\nMvaa 172,116,132\nv_add_u32 v1,s34,v1\nv_add_u32 v2,s34,v2\nMvaa 60,124,132\n"
"v_add_u32 v7,s94,v7\nv_add_u32 v8,s94,v8\nMvaa 60,120,128\ns_waitcnt vmcnt(29) lgkmcnt(0)\ns_barrier\nMvaa 76,120,136\nRD 128,3,0\nMvaa 76,124,140\n"
"s_mul_i32 s12,s59,131072\nRD 132,4,0\nMvaa 188,116,140\ns_mul_i32 s49,s10,32768\nMvaa 188,112,136\ns_add_u32 s12,s12,s49\nMvaa 72,104,136\n"
"s_add_u32 s12,s12,0x4500000\nMvaa 72,108,140\ns_mov_b32 s14,0x200000\nMvaa 184,100,140\nMvaa 184,96,136\nMvaa 68,88,136\nMvaa 68,92,140\n"
"Mvaa 180,84,140\nMvaa 180,80,136\nMvaa 64,72,136\nMvaa 64,76,140\nMvaa 176,68,140\nMvaa 176,64,136\nWL 1\nMvaa 192,64,128\nWL 0\nRD 136,3,2048\n"
"Mvaa 192,68,132\nRD 140,4,2048\nMvaa 80,76,132\nTR 32,5,0\nMvaa 80,72,128\nTR 34,5,1024\nMvaa 196,80,128\nTR 36,5,4096\nMvaa 196,84,132\nTR 38,5,5120\n"
"Mvaa 84,92,132\nTR 40,6,0\nMvaa 84,88,128\ns_add_u32 m0,s48,s44\nTR 42,6,1024\nMvaa 200,96,128\nTR 44,6,4096\nMvaa 200,100,132\nDP 1,20,36,0\n"
"Mvaa 88,108,132\nTR 46,6,5120\nMvaa 88,104,128\nTR 48,7,0\nMvaa 204,112,128\nDP 2,20,37,1024\nMvaa 204,116,132\nTR 50,7,1024\nMvaa 92,124,132\n"
"TR 52,7,4096\nMvaa 92,120,128\nWL 12\nDP 1,20,38,2048\nMvaa 108,120,136\nWL 11\nRD 128,3,4096\nMvaa 108,124,140\nRD 132,4,4096\nMvaa 220,116,140\n"
"TR 54,7,5120\nMvaa 220,112,136\nTR 56,8,0\nMvaa 104,104,136\nTR 58,8,1024\nMvaa 104,108,140\nTR 60,8,4096\nMvaa 216,100,140\nTR 62,8,5120\n"
"Mvaa 216,96,136\nMvaa 100,88,136\nMvaa 100,92,140\nDP 2,20,39,3072\nMvaa 212,84,140\ns_add_u32 m0,m0,0x1000\nglobal_load_dword a238,v13,s[8:9]\n"
"Mvaa 212,80,136\ns_add_u32 s8,s8,s18\ns_addc_u32 s9,s9,0\nMvaa 96,72,136\nDP 1,20,40,0\nMvaa 96,76,140\nMvaa 208,68,140\nMvaa 208,64,136\nWL 6\n"
"DP 2,20,41,1024\nMvaa 224,64,128\nWL 5\nRD 136,3,6144\nMvaa 224,68,132\ns_mov_b32 s44,s47\nRD 140,4,6144\nMvaa 112,76,132\ns_add_u32 s47,s47,0x8000\n"
"Mvaa 112,72,128\ns_cmp_eq_u32 s47,0x28000\nMvaa 228,80,128\ns_cselect_b32 s94,s96,s95\nMvaa 228,84,132\ns_cselect_b32 s47,0,s47\nMvaa 116,92,132\n"
"s_add_u32 s65,s65,1\nMvaa 116,88,128\ns_mul_i32 s50,s11,16384\nv_add_u32 v3,s94,v3\nv_add_u32 v4,s94,v4\nMvaa 232,96,128\ns_mul_i32 s49,s10,4096\n"
"Mvaa 232,100,132\ns_add_u32 s50,s50,s49\nDP 1,20,42,2048\nMvaa 120,108,132\ns_add_u32 s50,s50,0x4300000\nMvaa 120,104,128\nv_add_u32 v5,s94,v5\n"
"v_add_u32 v6,s94,v6\nMvaa 236,112,128\nDP 2,20,43,3072\nMvaa 236,116,132\nv_add_u32 v1,s35,v1\nv_add_u32 v2,s35,v2\nMvaa 124,124,132\n"
"v_add_u32 v7,s94,v7\nv_add_u32 v8,s94,v8\nMvaa 124,120,128\ns_waitcnt vmcnt(28) lgkmcnt(0)\ns_barrier\nMvaa 140,120,136\nRD 128,3,0\nMvaa 140,124,140\n"
"RD 132,4,0\nMvaa 252,116,140\nMvaa 252,112,136\nMvaa 136,104,136\nMvaa 136,108,140\nMvaa 248,100,140\nMvaa 248,96,136\nMvaa 132,88,136\n"
"buffer_store_dwordx4 a[144:147],v0,s[24:27],s50 offen offset:0 sc1\nMvaa 132,92,140\nMvaa 244,84,140\nMvaa 244,80,136\nMvaa 128,72,136\n"
"Mvaa 128,76,140\nMvaa 240,68,140\nMvaa 240,64,136\nWL 1\nMaaa 240,0,128\nWL 0\nRD 136,3,2048\nMaaa 240,4,132\nRD 140,4,2048\nMvaa 16,12,132\nTR 64,5,0\n"
"Mvaa 16,8,128\nTR 66,5,1024\nMaaa 244,16,128\nTR 68,5,4096\nMaaa 244,20,132\nTR 70,5,5120\nMvaa 20,28,132\nTR 72,6,0\nMvaa 20,24,128\n"
"s_add_u32 m0,s48,s44\nTR 74,6,1024\nMaaa 248,32,128\nTR 76,6,4096\nMaaa 248,36,132\nDP 1,20,36,0\nMvaa 24,44,132\nTR 78,6,5120\nMvaa 24,40,128\n"
"TR 80,7,0\nMaaa 252,48,128\nDP 2,20,37,1024\nMaaa 252,52,132\nTR 82,7,1024\nMvaa 28,60,132\nTR 84,7,4096\nMvaa 28,56,128\nWL 12\nDP 1,20,38,2048\n"
"Mvaa 44,56,136\nWL 11\nRD 128,3,4096\nMvaa 44,60,140\nRD 132,4,4096\nMvaa 156,52,140\nTR 86,7,5120\nMvaa 156,48,136\nTR 88,8,0\nMvaa 40,40,136\n"
"TR 90,8,1024\nMvaa 40,44,140\nTR 92,8,4096\nMvaa 152,36,140\nTR 94,8,5120\nMvaa 152,32,136\nMvaa 36,24,136\nMvaa 36,28,140\nDP 2,20,39,3072\n"
"Mvaa 148,20,140\ns_add_u32 m0,m0,0x1000\nMvaa 148,16,136\nMvaa 32,8,136\nDP 1,20,40,0\nMvaa 32,12,140\nMvaa 144,4,140\n"
"buffer_store_dwordx4 a[148:151],v0,s[24:27],s50 offen offset:1024 sc1\nMvaa 144,0,136\nWL 6\nDP 2,20,41,1024\nMvaa 160,0,128\nWL 5\nRD 136,3,6144\n"
"Mvaa 160,4,132\ns_mov_b32 s44,s47\nRD 140,4,6144\nMvaa 48,12,132\ns_add_u32 s47,s47,0x8000\nMvaa 48,8,128\ns_cmp_eq_u32 s47,0x28000\nMvaa 164,16,128\n"
"s_cselect_b32 s94,s96,s95\nMvaa 164,20,132\ns_cselect_b32 s47,0,s47\nMvaa 52,28,132\nMvaa 52,24,128\nv_add_u32 v3,s94,v3\nv_add_u32 v4,s94,v4\n"
"Mvaa 168,32,128\nMvaa 168,36,132\nDP 1,20,42,2048\nMvaa 56,44,132\nMvaa 56,40,128\nv_add_u32 v5,s94,v5\nv_add_u32 v6,s94,v6\nMvaa 172,48,128\n"
"DP 2,20,43,3072\nMvaa 172,52,132\nv_add_u32 v1,s34,v1\nv_add_u32 v2,s34,v2\nMvaa 60,60,132\nv_add_u32 v7,s94,v7\nv_add_u32 v8,s94,v8\nMvaa 60,56,128\n"
"s_waitcnt vmcnt(29) lgkmcnt(0)\ns_barrier\nMvaa 76,56,136\nRD 128,3,0\nMvaa 76,60,140\nRD 132,4,0\nMvaa 188,52,140\nMvaa 188,48,136\nMvaa 72,40,136\n"
"Mvaa 72,44,140\nMvaa 184,36,140\nMvaa 184,32,136\nMvaa 68,24,136\nbuffer_store_dwordx4 a[152:155],v0,s[24:27],s50 offen offset:2048 sc1\n"
"Mvaa 68,28,140\nMvaa 180,20,140\nMvaa 180,16,136\nMvaa 64,8,136\nMvaa 64,12,140\nMvaa 176,4,140\nMvaa 176,0,136\nWL 1\nMvaa 192,0,128\nWL 0\n"
"RD 136,3,2048\nMvaa 192,4,132\nRD 140,4,2048\nMvaa 80,12,132\nTR 96,5,0\nMvaa 80,8,128\nTR 98,5,1024\nMvaa 196,16,128\nTR 100,5,4096\nMvaa 196,20,132\n"
"TR 102,5,5120\nMvaa 84,28,132\nTR 104,6,0\nMvaa 84,24,128\ns_add_u32 m0,s48,s44\nTR 106,6,1024\nMvaa 200,32,128\nTR 108,6,4096\nMvaa 200,36,132\n"
"DP 1,20,36,0\nMvaa 88,44,132\nTR 110,6,5120\nMvaa 88,40,128\nTR 112,7,0\nMvaa 204,48,128\nDP 2,20,37,1024\nMvaa 204,52,132\nTR 114,7,1024\n"
"Mvaa 92,60,132\nTR 116,7,4096\nMvaa 92,56,128\nWL 12\nDP 1,20,38,2048\nMvaa 108,56,136\nWL 11\nRD 128,3,4096\nMvaa 108,60,140\nRD 132,4,4096\n"
"Mvaa 220,52,140\nTR 118,7,5120\nMvaa 220,48,136\nTR 120,8,0\nMvaa 104,40,136\nTR 122,8,1024\nMvaa 104,44,140\nTR 124,8,4096\nMvaa 216,36,140\n"
"TR 126,8,5120\nMvaa 216,32,136\nMvaa 100,24,136\nMvaa 100,28,140\nDP 2,20,39,3072\nMvaa 212,20,140\ns_add_u32 m0,m0,0x1000\nMvaa 212,16,136\n"
"Mvaa 96,8,136\nDP 1,20,40,0\nMvaa 96,12,140\nMvaa 208,4,140\nbuffer_store_dwordx4 a[156:159],v0,s[24:27],s50 offen offset:3072 sc1\nMvaa 208,0,136\n"
"WL 6\nDP 2,20,41,1024\nMvaa 224,0,128\nWL 5\nRD 136,3,6144\nMvaa 224,4,132\ns_mov_b32 s44,s47\nRD 140,4,6144\nMvaa 112,12,132\n"
"s_add_u32 s47,s47,0x8000\nMvaa 112,8,128\ns_cmp_eq_u32 s47,0x28000\nMvaa 228,16,128\ns_cselect_b32 s94,s96,s95\nMvaa 228,20,132\n"
"s_cselect_b32 s47,0,s47\nMvaa 116,28,132\nMvaa 116,24,128\nv_add_u32 v3,s94,v3\nv_add_u32 v4,s94,v4\nMvaa 232,32,128\nMvaa 232,36,132\nDP 1,20,42,2048\n"
"Mvaa 120,44,132\nMvaa 120,40,128\nv_add_u32 v5,s94,v5\nv_add_u32 v6,s94,v6\nMvaa 236,48,128\nDP 2,20,43,3072\nMvaa 236,52,132\nv_add_u32 v1,s35,v1\n"
"v_add_u32 v2,s35,v2\nMvaa 124,60,132\nv_add_u32 v7,s94,v7\nv_add_u32 v8,s94,v8\nMvaa 124,56,128\ns_waitcnt vmcnt(29) lgkmcnt(0)\ns_barrier\n"
"Mvaa 140,56,136\nRD 128,3,0\nMvaa 140,60,140\nRD 132,4,0\nMvaa 252,52,140\nMvaa 252,48,136\nMvaa 136,40,136\nMvaa 136,44,140\nMvaa 248,36,140\n"
"Mvaa 248,32,136\nMvaa 132,24,136\nbuffer_store_dwordx4 a[160:163],v0,s[24:27],s63 offen offset:0 sc1\nMvaa 132,28,140\nMvaa 244,20,140\n"
"Mvaa 244,16,136\nMvaa 128,8,136\nMvaa 128,12,140\nMvaa 240,4,140\nMvaa 240,0,136\nWL 1\nMaaa 240,64,128\nWL 0\nRD 136,3,2048\nMaaa 240,68,132\n"
"RD 140,4,2048\nMvaa 16,76,132\nTR 0,5,0\nMvaa 16,72,128\nTR 2,5,1024\nMaaa 244,80,128\nTR 4,5,4096\nMaaa 244,84,132\nTR 6,5,5120\nMvaa 20,92,132\n"
"TR 8,6,0\nMvaa 20,88,128\ns_add_u32 m0,s48,s44\nTR 10,6,1024\nMaaa 248,96,128\nTR 12,6,4096\nMaaa 248,100,132\nDP 1,20,36,0\nMvaa 24,108,132\n"
"TR 14,6,5120\nMvaa 24,104,128\nTR 16,7,0\nMaaa 252,112,128\nDP 2,20,37,1024\nMaaa 252,116,132\nTR 18,7,1024\nMvaa 28,124,132\nTR 20,7,4096\n"
"Mvaa 28,120,128\nWL 12\nDP 1,20,38,2048\nMvaa 44,120,136\nWL 11\nRD 128,3,4096\nMvaa 44,124,140\nRD 132,4,4096\nMvaa 156,116,140\nTR 22,7,5120\n"
"Mvaa 156,112,136\nTR 24,8,0\nMvaa 40,104,136\nTR 26,8,1024\nMvaa 40,108,140\nTR 28,8,4096\nMvaa 152,100,140\nTR 30,8,5120\nMvaa 152,96,136\n"
"Mvaa 36,88,136\nMvaa 36,92,140\nDP 2,20,39,3072\nMvaa 148,84,140\ns_add_u32 m0,m0,0x1000\nMvaa 148,80,136\nMvaa 32,72,136\nDP 1,20,40,0\n"
"Mvaa 32,76,140\nMvaa 144,68,140\nbuffer_store_dwordx4 a[164:167],v0,s[24:27],s63 offen offset:1024 sc1\nMvaa 144,64,136\nWL 6\nDP 2,20,41,1024\n"
"Mvaa 160,64,128\nWL 5\nRD 136,3,6144\nMvaa 160,68,132\ns_mov_b32 s44,s47\nRD 140,4,6144\nMvaa 48,76,132\ns_add_u32 s47,s47,0x8000\nMvaa 48,72,128\n"
"s_cmp_eq_u32 s47,0x28000\nMvaa 164,80,128\ns_cselect_b32 s94,s96,s95\nMvaa 164,84,132\ns_cselect_b32 s47,0,s47\nMvaa 52,92,132\nMvaa 52,88,128\n"
"v_add_u32 v3,s94,v3\nv_add_u32 v4,s94,v4\nMvaa 168,96,128\nMvaa 168,100,132\nDP 1,20,42,2048\nMvaa 56,108,132\nMvaa 56,104,128\nv_add_u32 v5,s94,v5\n"
"v_add_u32 v6,s94,v6\nMvaa 172,112,128\nDP 2,20,43,3072\nMvaa 172,116,132\nv_add_u32 v1,s34,v1\nv_add_u32 v2,s34,v2\nMvaa 60,124,132\n"
"v_add_u32 v7,s94,v7\nv_add_u32 v8,s94,v8\nMvaa 60,120,128\ns_waitcnt vmcnt(30) lgkmcnt(0)\ns_barrier\nMvaa 76,120,136\nRD 128,3,0\nMvaa 76,124,140\n"
"RD 132,4,0\nMvaa 188,116,140\nMvaa 188,112,136\nMvaa 72,104,136\nMvaa 72,108,140\nMvaa 184,100,140\nMvaa 184,96,136\nMvaa 68,88,136\nMvaa 68,92,140\n"
"Mvaa 180,84,140\nMvaa 180,80,136\nMvaa 64,72,136\nMvaa 64,76,140\nMvaa 176,68,140\nMvaa 176,64,136\nWL 1\nMvaa 192,64,128\nWL 0\nRD 136,3,2048\n"
"Mvaa 192,68,132\nRD 140,4,2048\nMvaa 80,76,132\nTR 32,5,0\nMvaa 80,72,128\nTR 34,5,1024\nMvaa 196,80,128\nTR 36,5,4096\nMvaa 196,84,132\nTR 38,5,5120\n"
"Mvaa 84,92,132\nTR 40,6,0\nMvaa 84,88,128\ns_add_u32 m0,s48,s44\nTR 42,6,1024\nMvaa 200,96,128\nTR 44,6,4096\nMvaa 200,100,132\nDP 1,20,36,0\n"
"Mvaa 88,108,132\nTR 46,6,5120\nMvaa 88,104,128\nTR 48,7,0\nMvaa 204,112,128\nDP 2,20,37,1024\nMvaa 204,116,132\nTR 50,7,1024\nMvaa 92,124,132\n"
"TR 52,7,4096\nMvaa 92,120,128\nWL 12\nDP 1,20,38,2048\nMvaa 108,120,136\nWL 11\nRD 128,3,4096\nMvaa 108,124,140\nRD 132,4,4096\nMvaa 220,116,140\n"
"TR 54,7,5120\nMvaa 220,112,136\nTR 56,8,0\nMvaa 104,104,136\nTR 58,8,1024\nMvaa 104,108,140\nTR 60,8,4096\nMvaa 216,100,140\nTR 62,8,5120\n"
"Mvaa 216,96,136\nMvaa 100,88,136\nMvaa 100,92,140\nDP 2,20,39,3072\nMvaa 212,84,140\ns_add_u32 m0,m0,0x1000\nglobal_load_dword a238,v13,s[8:9]\n"
"Mvaa 212,80,136\ns_add_u32 s8,s8,s18\ns_addc_u32 s9,s9,0\nMvaa 96,72,136\nDP 1,20,40,0\nMvaa 96,76,140\nMvaa 208,68,140\nMvaa 208,64,136\nWL 6\n"
"DP 2,20,41,1024\nMvaa 224,64,128\nWL 5\nRD 136,3,6144\nMvaa 224,68,132\ns_mov_b32 s44,s47\nRD 140,4,6144\nMvaa 112,76,132\ns_add_u32 s47,s47,0x8000\n"
"Mvaa 112,72,128\ns_cmp_eq_u32 s47,0x28000\nMvaa 228,80,128\ns_cselect_b32 s94,s96,s95\nMvaa 228,84,132\ns_cselect_b32 s47,0,s47\nMvaa 116,92,132\n"
"Mvaa 116,88,128\nv_add_u32 v3,s94,v3\nv_add_u32 v4,s94,v4\nMvaa 232,96,128\nMvaa 232,100,132\nDP 1,20,42,2048\nMvaa 120,108,132\nMvaa 120,104,128\n"
"v_add_u32 v5,s94,v5\nv_add_u32 v6,s94,v6\nMvaa 236,112,128\nDP 2,20,43,3072\nMvaa 236,116,132\nv_add_u32 v1,s35,v1\nv_add_u32 v2,s35,v2\n"
"Mvaa 124,124,132\nv_add_u32 v7,s94,v7\nv_add_u32 v8,s94,v8\nMvaa 124,120,128\ns_waitcnt vmcnt(29) lgkmcnt(0)\ns_barrier\nMvaa 140,120,136\nRD 128,3,0\n"
"Mvaa 140,124,140\nRD 132,4,0\nMvaa 252,116,140\nMvaa 252,112,136\nMvaa 136,104,136\nMvaa 136,108,140\nMvaa 248,100,140\nMvaa 248,96,136\n"
"Mvaa 132,88,136\nMvaa 132,92,140\nMvaa 244,84,140\nMvaa 244,80,136\nMvaa 128,72,136\nMvaa 128,76,140\nMvaa 240,68,140\nMvaa 240,64,136\nWL 1\n"
"Maaa 240,0,128\nWL 0\nRD 136,3,2048\nMaaa 240,4,132\nRD 140,4,2048\nMvaa 16,12,132\nTR 64,5,0\nMvaa 16,8,128\nTR 66,5,1024\nMaaa 244,16,128\n"
"TR 68,5,4096\nMaaa 244,20,132\nTR 70,5,5120\nMvaa 20,28,132\nTR 72,6,0\nMvaa 20,24,128\ns_add_u32 m0,s48,s44\nTR 74,6,1024\nMaaa 248,32,128\n"
"TR 76,6,4096\nMaaa 248,36,132\nDP 1,20,36,0\nMvaa 24,44,132\nTR 78,6,5120\nMvaa 24,40,128\nTR 80,7,0\nMaaa 252,48,128\nDP 2,20,37,1024\n"
"Maaa 252,52,132\nTR 82,7,1024\nMvaa 28,60,132\nTR 84,7,4096\nMvaa 28,56,128\nWL 12\nDP 1,20,38,2048\nMvaa 44,56,136\nWL 11\nRD 128,3,4096\n"
"Mvaa 44,60,140\nRD 132,4,4096\nMvaa 156,52,140\nTR 86,7,5120\nMvaa 156,48,136\nTR 88,8,0\nMvaa 40,40,136\nTR 90,8,1024\nMvaa 40,44,140\nTR 92,8,4096\n"
"Mvaa 152,36,140\nTR 94,8,5120\nMvaa 152,32,136\nMvaa 36,24,136\nMvaa 36,28,140\nDP 2,20,39,3072\nMvaa 148,20,140\ns_add_u32 m0,m0,0x1000\n"
"Mvaa 148,16,136\nMvaa 32,8,136\nDP 1,20,40,0\nMvaa 32,12,140\nMvaa 144,4,140\nMvaa 144,0,136\nWL 6\nDP 2,20,41,1024\nMvaa 160,0,128\nWL 5\n"
"RD 136,3,6144\nMvaa 160,4,132\ns_mov_b32 s44,s47\nRD 140,4,6144\nMvaa 48,12,132\ns_add_u32 s47,s47,0x8000\nMvaa 48,8,128\ns_cmp_eq_u32 s47,0x28000\n"
"Mvaa 164,16,128\ns_cselect_b32 s94,s96,s95\nMvaa 164,20,132\ns_cselect_b32 s47,0,s47\nMvaa 52,28,132\nMvaa 52,24,128\nv_add_u32 v3,s94,v3\n"
"v_add_u32 v4,s94,v4\nMvaa 168,32,128\nMvaa 168,36,132\nDP 1,20,42,2048\nMvaa 56,44,132\nMvaa 56,40,128\nv_add_u32 v5,s94,v5\nv_add_u32 v6,s94,v6\n"
"Mvaa 172,48,128\nDP 2,20,43,3072\nMvaa 172,52,132\nv_add_u32 v1,s34,v1\nv_add_u32 v2,s34,v2\nMvaa 60,60,132\nv_add_u32 v7,s94,v7\nv_add_u32 v8,s94,v8\n"
"Mvaa 60,56,128\ns_waitcnt vmcnt(27) lgkmcnt(0)\ns_barrier\nMvaa 76,56,136\nRD 128,3,0\nMvaa 76,60,140\nRD 132,4,0\nMvaa 188,52,140\nMvaa 188,48,136\n"
"Mvaa 72,40,136\nMvaa 72,44,140\nMvaa 184,36,140\nMvaa 184,32,136\nMvaa 68,24,136\nMvaa 68,28,140\nMvaa 180,20,140\nMvaa 180,16,136\nMvaa 64,8,136\n"
"Mvaa 64,12,140\nMvaa 176,4,140\nMvaa 176,0,136\nWL 1\nMvaa 192,0,128\nWL 0\nRD 136,3,2048\nMvaa 192,4,132\nRD 140,4,2048\nMvaa 80,12,132\nTR 96,5,0\n"
"Mvaa 80,8,128\nTR 98,5,1024\nMvaa 196,16,128\nTR 100,5,4096\nMvaa 196,20,132\nTR 102,5,5120\nMvaa 84,28,132\nTR 104,6,0\nMvaa 84,24,128\n"
"s_add_u32 m0,s48,s44\nTR 106,6,1024\nMvaa 200,32,128\nTR 108,6,4096\nMvaa 200,36,132\nDP 1,20,36,0\nMvaa 88,44,132\nTR 110,6,5120\nMvaa 88,40,128\n"
"TR 112,7,0\nMvaa 204,48,128\nDP 2,20,37,1024\nMvaa 204,52,132\nTR 114,7,1024\nMvaa 92,60,132\nTR 116,7,4096\nMvaa 92,56,128\nWL 12\nDP 1,20,38,2048\n"
"Mvaa 108,56,136\nWL 11\nRD 128,3,4096\nMvaa 108,60,140\nRD 132,4,4096\nMvaa 220,52,140\nTR 118,7,5120\nMvaa 220,48,136\nTR 120,8,0\nMvaa 104,40,136\n"
"TR 122,8,1024\nMvaa 104,44,140\nTR 124,8,4096\nMvaa 216,36,140\nTR 126,8,5120\nMvaa 216,32,136\nMvaa 100,24,136\nMvaa 100,28,140\nDP 2,20,39,3072\n"
"Mvaa 212,20,140\ns_add_u32 m0,m0,0x1000\nMvaa 212,16,136\nMvaa 96,8,136\nDP 1,20,40,0\nMvaa 96,12,140\nMvaa 208,4,140\nMvaa 208,0,136\nWL 6\n"
"DP 2,20,41,1024\nMvaa 224,0,128\nWL 5\nRD 136,3,6144\nMvaa 224,4,132\ns_mov_b32 s44,s47\nRD 140,4,6144\nMvaa 112,12,132\ns_add_u32 s47,s47,0x8000\n"
"Mvaa 112,8,128\ns_cmp_eq_u32 s47,0x28000\nMvaa 228,16,128\ns_cselect_b32 s94,s96,s95\nMvaa 228,20,132\ns_cselect_b32 s47,0,s47\nMvaa 116,28,132\n"
"s_mul_i32 s15,s11,512\nMvaa 116,24,128\ns_mul_i32 s49,s10,128\nv_add_u32 v3,s94,v3\nv_add_u32 v4,s94,v4\nMvaa 232,32,128\ns_add_u32 s15,s15,s49\n"
"Mvaa 232,36,132\ns_add_u32 s15,s15,0xa0000\nDP 1,20,42,2048\nMvaa 120,44,132\nMvaa 120,40,128\nv_add_u32 v5,s94,v5\nv_add_u32 v6,s94,v6\n"
"Mvaa 236,48,128\nDP 2,20,43,3072\nMvaa 236,52,132\nv_add_u32 v1,s35,v1\nv_add_u32 v2,s35,v2\nMvaa 124,60,132\nv_add_u32 v7,s94,v7\nv_add_u32 v8,s94,v8\n"
"Mvaa 124,56,128\ns_waitcnt vmcnt(25) lgkmcnt(0)\ns_barrier\nMvaa 140,56,136\nRD 128,3,0\nMvaa 140,60,140\nRD 132,4,0\nMvaa 252,52,140\n"
"v_mov_b32 v12,s65\ns_mov_b64 exec,1\nbuffer_store_dword v12,off,s[24:27],s15 sc1\ns_mov_b64 exec,-1\nMvaa 252,48,136\nMvaa 136,40,136\n"
"s_mul_i32 s15,s59,8\nMvaa 136,44,140\ns_mul_i32 s49,s10,2\nMvaa 248,36,140\ns_add_u32 s15,s15,s49\nMvaa 248,32,136\ns_add_u32 s15,s15,0x80\n"
"Mvaa 132,24,136\ns_mul_i32 s70,s56,8\nMvaa 132,28,140\ns_mul_i32 s49,s10,2\nMvaa 244,20,140\ns_add_u32 s70,s70,s49\nMvaa 244,16,136\n"
"s_add_u32 s70,s70,0xfffffffd\nMvaa 128,8,136\ns_mul_i32 s71,s57,21\nMvaa 128,12,140\ns_add_u32 s71,s71,0xfffffffb\nMvaa 240,4,140\n"
"s_mov_b32 s62,655360\nMvaa 240,0,136\nWL 1\nMaaa 240,64,128\nWL 0\nRD 136,3,2048\nMaaa 240,68,132\nRD 140,4,2048\nMvaa 16,76,132\nTR 0,5,0\n"
"Mvaa 16,72,128\nTR 2,5,1024\nMaaa 244,80,128\nTR 4,5,4096\nMaaa 244,84,132\nTR 6,5,5120\nMvaa 20,92,132\nTR 8,6,0\nMvaa 20,88,128\n"
"s_add_u32 m0,s48,s44\nTR 10,6,1024\nMaaa 248,96,128\nTR 12,6,4096\nMaaa 248,100,132\nDP 1,20,36,0\nMvaa 24,108,132\nTR 14,6,5120\nMvaa 24,104,128\n"
"TR 16,7,0\nMaaa 252,112,128\nDP 2,20,37,1024\nMaaa 252,116,132\nTR 18,7,1024\nMvaa 28,124,132\nTR 20,7,4096\nMvaa 28,120,128\nWL 12\nDP 1,20,38,2048\n"
"Mvaa 44,120,136\nWL 11\nRD 128,3,4096\nMvaa 44,124,140\nRD 132,4,4096\nMvaa 156,116,140\nTR 22,7,5120\nMvaa 156,112,136\nTR 24,8,0\nMvaa 40,104,136\n"
"TR 26,8,1024\nMvaa 40,108,140\nTR 28,8,4096\nMvaa 152,100,140\nTR 30,8,5120\nMvaa 152,96,136\nMvaa 36,88,136\nv_lshrrev_b32 v12,4,v0\nMvaa 36,92,140\n"
"DP 2,20,39,3072\nMvaa 148,84,140\ns_add_u32 m0,m0,0x1000\ns_waitcnt vmcnt(25)\nglobal_load_dword a238,v13,s[8:9]\nv_min_u32 v12,25,v12\n"
"Mvaa 148,80,136\ns_add_u32 s8,s8,s18\ns_addc_u32 s9,s9,0\nv_mov_b32 v11,s15\nMvaa 32,72,136\nDP 1,20,40,0\nMvaa 32,76,140\nv_mov_b32 v9,s70\n"
"Mvaa 144,68,140\nv_cmp_gt_u32 vcc,2,v12\nMvaa 144,64,136\nWL 6\nDP 2,20,41,1024\nMvaa 160,64,128\nWL 5\nRD 136,3,6144\nv_mov_b32 v15,s71\n"
"Mvaa 160,68,132\ns_mov_b32 s44,s47\nRD 140,4,6144\nv_cndmask_b32 v11,v9,v11,vcc\nMvaa 48,76,132\ns_add_u32 s47,s47,0x8000\nv_cmp_gt_u32 vcc,5,v12\n"
"Mvaa 48,72,128\ns_cmp_eq_u32 s47,0x28000\nv_cndmask_b32 v11,v15,v11,vcc\nMvaa 164,80,128\ns_cselect_b32 s94,s96,s95\nv_add_u32 v15,v12,v11\n"
"Mvaa 164,84,132\ns_cselect_b32 s47,0,s47\nv_max_i32 v15,0,v15\nMvaa 52,92,132\nv_lshlrev_b32 v15,9,v15\nMvaa 52,88,128\nv_add_u32 v3,s94,v3\n"
"v_add_u32 v4,s94,v4\nMvaa 168,96,128\nMvaa 168,100,132\nDP 1,20,42,2048\nMvaa 56,108,132\nMvaa 56,104,128\nv_add_u32 v5,s94,v5\nv_add_u32 v6,s94,v6\n"
"Mvaa 172,112,128\nDP 2,20,43,3072\nMvaa 172,116,132\nv_add_u32 v1,s34,v1\nv_add_u32 v2,s34,v2\nMvaa 60,124,132\nv_add_u32 v7,s94,v7\n"
"v_add_u32 v8,s94,v8\nMvaa 60,120,128\ns_waitcnt vmcnt(26) lgkmcnt(0)\ns_barrier\nMvaa 76,120,136\nRD 128,3,0\nMvaa 76,124,140\nRD 132,4,0\n"
"Mvaa 188,116,140\nMvaa 188,112,136\nMvaa 72,104,136\nMvaa 72,108,140\nMvaa 184,100,140\nMvaa 184,96,136\nMvaa 68,88,136\nMvaa 68,92,140\n"
"Mvaa 180,84,140\nMvaa 180,80,136\nMvaa 64,72,136\nMvaa 64,76,140\nMvaa 176,68,140\nMvaa 176,64,136\nWL 1\nMvaa 192,64,128\nWL 0\nRD 136,3,2048\n"
"Mvaa 192,68,132\nRD 140,4,2048\nMvaa 80,76,132\nTR 32,5,0\nMvaa 80,72,128\nTR 34,5,1024\nMvaa 196,80,128\nTR 36,5,4096\nMvaa 196,84,132\nTR 38,5,5120\n"
"Mvaa 84,92,132\nTR 40,6,0\nMvaa 84,88,128\ns_add_u32 m0,s48,s44\nTR 42,6,1024\nMvaa 200,96,128\nTR 44,6,4096\nMvaa 200,100,132\nDP 1,20,36,0\n"
"Mvaa 88,108,132\nTR 46,6,5120\nMvaa 88,104,128\nTR 48,7,0\nMvaa 204,112,128\nDP 2,20,37,1024\nMvaa 204,116,132\nTR 50,7,1024\nMvaa 92,124,132\n"
"TR 52,7,4096\nMvaa 92,120,128\nWL 12\nDP 1,20,38,2048\nMvaa 108,120,136\nWL 11\nRD 128,3,4096\nMvaa 108,124,140\nRD 132,4,4096\nMvaa 220,116,140\n"
"TR 54,7,5120\nMvaa 220,112,136\nTR 56,8,0\nMvaa 104,104,136\nTR 58,8,1024\nMvaa 104,108,140\nTR 60,8,4096\nMvaa 216,100,140\nTR 62,8,5120\n"
"Mvaa 216,96,136\nMvaa 100,88,136\nMvaa 100,92,140\nDP 2,20,39,3072\nMvaa 212,84,140\ns_add_u32 m0,m0,0x1000\nMvaa 212,80,136\nMvaa 96,72,136\n"
"DP 1,20,40,0\nMvaa 96,76,140\nMvaa 208,68,140\nMvaa 208,64,136\nWL 6\nDP 2,20,41,1024\nMvaa 224,64,128\nWL 5\nRD 136,3,6144\nMvaa 224,68,132\n"
"s_mov_b32 s44,s47\nRD 140,4,6144\nMvaa 112,76,132\ns_add_u32 s47,s47,0x8000\nMvaa 112,72,128\ns_cmp_eq_u32 s47,0x28000\nMvaa 228,80,128\n"
"s_cselect_b32 s94,s96,s95\nMvaa 228,84,132\ns_cselect_b32 s47,0,s47\nMvaa 116,92,132\nMvaa 116,88,128\nv_add_u32 v3,s94,v3\nv_add_u32 v4,s94,v4\n"
"Mvaa 232,96,128\nMvaa 232,100,132\nDP 1,20,42,2048\nMvaa 120,108,132\nMvaa 120,104,128\nv_add_u32 v5,s94,v5\nv_add_u32 v6,s94,v6\nMvaa 236,112,128\n"
"DP 2,20,43,3072\nMvaa 236,116,132\nv_add_u32 v1,s35,v1\nv_add_u32 v2,s35,v2\nMvaa 124,124,132\nv_add_u32 v7,s94,v7\nv_add_u32 v8,s94,v8\n"
"Mvaa 124,120,128\ns_waitcnt vmcnt(26) lgkmcnt(0)\ns_barrier\nMvaa 140,120,136\nRD 128,3,0\nMvaa 140,124,140\nRD 132,4,0\nMvaa 252,116,140\n"
"Mvaa 252,112,136\nMvaa 136,104,136\nMvaa 136,108,140\nMvaa 248,100,140\nMvaa 248,96,136\nMvaa 132,88,136\nMvaa 132,92,140\nMvaa 244,84,140\n"
"Mvaa 244,80,136\nMvaa 128,72,136\nMvaa 128,76,140\nMvaa 240,68,140\nMvaa 240,64,136\nWL 1\nMaaa 240,0,128\nWL 0\nRD 136,3,2048\nMaaa 240,4,132\n"
"RD 140,4,2048\nMvaa 16,12,132\nTR 64,5,0\nMvaa 16,8,128\nTR 66,5,1024\nMaaa 244,16,128\nTR 68,5,4096\nMaaa 244,20,132\nTR 70,5,5120\nMvaa 20,28,132\n"
"TR 72,6,0\nMvaa 20,24,128\ns_add_u32 m0,s48,s44\nTR 74,6,1024\nMaaa 248,32,128\nTR 76,6,4096\nMaaa 248,36,132\nDP 1,20,36,0\nMvaa 24,44,132\n"
"TR 78,6,5120\nMvaa 24,40,128\nTR 80,7,0\nMaaa 252,48,128\nDP 2,20,37,1024\nMaaa 252,52,132\nTR 82,7,1024\nMvaa 28,60,132\nTR 84,7,4096\nMvaa 28,56,128\n"
"WL 12\nDP 1,20,38,2048\nMvaa 44,56,136\nWL 11\nRD 128,3,4096\nMvaa 44,60,140\nRD 132,4,4096\nMvaa 156,52,140\nTR 86,7,5120\nMvaa 156,48,136\nTR 88,8,0\n"
"Mvaa 40,40,136\nTR 90,8,1024\nMvaa 40,44,140\nTR 92,8,4096\nMvaa 152,36,140\nTR 94,8,5120\nMvaa 152,32,136\nMvaa 36,24,136\nMvaa 36,28,140\n"
"DP 2,20,39,3072\nMvaa 148,20,140\ns_add_u32 m0,m0,0x1000\nMvaa 148,16,136\nMvaa 32,8,136\nDP 1,20,40,0\nMvaa 32,12,140\nMvaa 144,4,140\n"
"buffer_load_dword v14,v15,s[24:27],s62 offen sc1\nMvaa 144,0,136\nWL 6\nDP 2,20,41,1024\nMvaa 160,0,128\nWL 5\nRD 136,3,6144\nMvaa 160,4,132\n"
"s_mov_b32 s44,s47\nRD 140,4,6144\nMvaa 48,12,132\ns_add_u32 s47,s47,0x8000\nMvaa 48,8,128\ns_cmp_eq_u32 s47,0x28000\nMvaa 164,16,128\n"
"s_cselect_b32 s94,s96,s95\nMvaa 164,20,132\ns_cselect_b32 s47,0,s47\nMvaa 52,28,132\nMvaa 52,24,128\nv_add_u32 v3,s94,v3\nv_add_u32 v4,s94,v4\n"
"Mvaa 168,32,128\nMvaa 168,36,132\nDP 1,20,42,2048\nMvaa 56,44,132\nMvaa 56,40,128\nv_add_u32 v5,s94,v5\nv_add_u32 v6,s94,v6\nMvaa 172,48,128\n"
"DP 2,20,43,3072\nMvaa 172,52,132\nv_add_u32 v1,s34,v1\nv_add_u32 v2,s34,v2\nMvaa 60,60,132\nv_add_u32 v7,s94,v7\nv_add_u32 v8,s94,v8\nMvaa 60,56,128\n"
"s_waitcnt vmcnt(27) lgkmcnt(0)\ns_barrier\nMvaa 76,56,136\nRD 128,3,0\nMvaa 76,60,140\nRD 132,4,0\nMvaa 188,52,140\nMvaa 188,48,136\nMvaa 72,40,136\n"
"Mvaa 72,44,140\nMvaa 184,36,140\nMvaa 184,32,136\nMvaa 68,24,136\nMvaa 68,28,140\nMvaa 180,20,140\nMvaa 180,16,136\nMvaa 64,8,136\nMvaa 64,12,140\n"
"Mvaa 176,4,140\nMvaa 176,0,136\nWL 1\nMvaa 192,0,128\nWL 0\nRD 136,3,2048\nMvaa 192,4,132\nRD 140,4,2048\nMvaa 80,12,132\nTR 96,5,0\nMvaa 80,8,128\n"
"TR 98,5,1024\nMvaa 196,16,128\nTR 100,5,4096\nMvaa 196,20,132\nTR 102,5,5120\nMvaa 84,28,132\nTR 104,6,0\nMvaa 84,24,128\ns_add_u32 m0,s48,s44\n"
"TR 106,6,1024\nMvaa 200,32,128\nTR 108,6,4096\nMvaa 200,36,132\nDP 1,20,36,0\nMvaa 88,44,132\nTR 110,6,5120\nMvaa 88,40,128\nTR 112,7,0\n"
"Mvaa 204,48,128\nDP 2,20,37,1024\nMvaa 204,52,132\nTR 114,7,1024\nMvaa 92,60,132\nTR 116,7,4096\nMvaa 92,56,128\nWL 12\nDP 1,20,38,2048\n"
"Mvaa 108,56,136\nWL 11\nRD 128,3,4096\nMvaa 108,60,140\nRD 132,4,4096\nMvaa 220,52,140\nTR 118,7,5120\nMvaa 220,48,136\nTR 120,8,0\nMvaa 104,40,136\n"
"TR 122,8,1024\nMvaa 104,44,140\nTR 124,8,4096\nMvaa 216,36,140\nTR 126,8,5120\nMvaa 216,32,136\nMvaa 100,24,136\nMvaa 100,28,140\nDP 2,20,39,3072\n"
"Mvaa 212,20,140\ns_add_u32 m0,m0,0x1000\nMvaa 212,16,136\nMvaa 96,8,136\nDP 1,20,40,0\nMvaa 96,12,140\nMvaa 208,4,140\nMvaa 208,0,136\nWL 6\n"
"DP 2,20,41,1024\nMvaa 224,0,128\nWL 5\nRD 136,3,6144\nMvaa 224,4,132\ns_mov_b32 s44,s47\nRD 140,4,6144\nMvaa 112,12,132\ns_add_u32 s47,s47,0x8000\n"
"Mvaa 112,8,128\ns_cmp_eq_u32 s47,0x28000\nMvaa 228,16,128\ns_cselect_b32 s94,s96,s95\nMvaa 228,20,132\ns_cselect_b32 s47,0,s47\nMvaa 116,28,132\n"
"Mvaa 116,24,128\nv_add_u32 v3,s94,v3\nv_add_u32 v4,s94,v4\nMvaa 232,32,128\nMvaa 232,36,132\nDP 1,20,42,2048\nMvaa 120,44,132\nMvaa 120,40,128\n"
"v_add_u32 v5,s94,v5\nv_add_u32 v6,s94,v6\nMvaa 236,48,128\nDP 2,20,43,3072\nMvaa 236,52,132\nv_add_u32 v1,s35,v1\nv_add_u32 v2,s35,v2\nMvaa 124,60,132\n"
"v_add_u32 v7,s94,v7\nv_add_u32 v8,s94,v8\nMvaa 124,56,128\ns_waitcnt vmcnt(25) lgkmcnt(0)\ns_barrier\nMvaa 140,56,136\nRD 128,3,0\ns_waitcnt vmcnt(11)\n"
"v_cmp_ne_u32 vcc,s65,v14\ns_nop 0\ns_cbranch_vccnz fused_op_sp2\nfused_op_spb3:\nMvaa 140,60,140\nRD 132,4,0\nMvaa 252,52,140\ns_add_u32 s15,s12,0\n"
"Mvaa 252,48,136\nbuffer_load_dwordx4 a[144:147],v0,s[24:27],s15 offen offset:0 sc1\nMvaa 136,40,136\nMvaa 136,44,140\nMvaa 248,36,140\nMvaa 248,32,136\n"
"Mvaa 132,24,136\ns_add_u32 s15,s12,16384\nMvaa 132,28,140\nbuffer_load_dwordx4 a[148:151],v0,s[24:27],s15 offen offset:0 sc1\nMvaa 244,20,140\n"
"Mvaa 244,16,136\nMvaa 128,8,136\nMvaa 128,12,140\nMvaa 240,4,140\nMvaa 240,0,136\nWL 1\nMaaa 240,64,128\nWL 0\nRD 136,3,2048\nMaaa 240,68,132\n"
"RD 140,4,2048\nMvaa 16,76,132\nTR 0,5,0\nMvaa 16,72,128\nTR 2,5,1024\nMaaa 244,80,128\nTR 4,5,4096\nMaaa 244,84,132\nTR 6,5,5120\nMvaa 20,92,132\n"
"TR 8,6,0\nMvaa 20,88,128\ns_add_u32 m0,s48,s44\nTR 10,6,1024\nMaaa 248,96,128\nTR 12,6,4096\nMaaa 248,100,132\nDP 1,20,36,0\nMvaa 24,108,132\n"
"TR 14,6,5120\nMvaa 24,104,128\nTR 16,7,0\nMaaa 252,112,128\nDP 2,20,37,1024\nMaaa 252,116,132\nTR 18,7,1024\nMvaa 28,124,132\nTR 20,7,4096\n"
"Mvaa 28,120,128\nWL 12\nDP 1,20,38,2048\nMvaa 44,120,136\nWL 11\nRD 128,3,4096\nMvaa 44,124,140\nRD 132,4,4096\nMvaa 156,116,140\nTR 22,7,5120\n"
"Mvaa 156,112,136\nTR 24,8,0\nMvaa 40,104,136\nTR 26,8,1024\nMvaa 40,108,140\nTR 28,8,4096\nMvaa 152,100,140\nTR 30,8,5120\nMvaa 152,96,136\n"
"Mvaa 36,88,136\nMvaa 36,92,140\nDP 2,20,39,3072\nMvaa 148,84,140\ns_add_u32 m0,m0,0x1000\nglobal_load_dword a238,v13,s[8:9]\nMvaa 148,80,136\n"
"s_add_u32 s8,s8,s18\ns_addc_u32 s9,s9,0\nMvaa 32,72,136\nDP 1,20,40,0\nMvaa 32,76,140\nMvaa 144,68,140\nMvaa 144,64,136\nWL 6\nDP 2,20,41,1024\n"
"Mvaa 160,64,128\nWL 5\nRD 136,3,6144\nMvaa 160,68,132\ns_mov_b32 s44,s47\nRD 140,4,6144\nMvaa 48,76,132\ns_add_u32 s47,s47,0x8000\nMvaa 48,72,128\n"
"s_cmp_eq_u32 s47,0x28000\nMvaa 164,80,128\ns_cselect_b32 s94,s96,s95\nMvaa 164,84,132\ns_cselect_b32 s47,0,s47\nMvaa 52,92,132\nMvaa 52,88,128\n"
"v_add_u32 v3,s94,v3\nv_add_u32 v4,s94,v4\nMvaa 168,96,128\nMvaa 168,100,132\nDP 1,20,42,2048\nMvaa 56,108,132\nMvaa 56,104,128\nv_add_u32 v5,s94,v5\n"
"v_add_u32 v6,s94,v6\nMvaa 172,112,128\nDP 2,20,43,3072\nMvaa 172,116,132\nv_add_u32 v1,s34,v1\nv_add_u32 v2,s34,v2\nMvaa 60,124,132\n"
"v_add_u32 v7,s94,v7\nv_add_u32 v8,s94,v8\nMvaa 60,120,128\ns_waitcnt vmcnt(28) lgkmcnt(0)\ns_barrier\nMvaa 76,120,136\nRD 128,3,0\nMvaa 76,124,140\n"
"RD 132,4,0\nMvaa 188,116,140\ns_add_u32 s15,s12,0\nMvaa 188,112,136\nbuffer_load_dwordx4 a[152:155],v0,s[24:27],s15 offen offset:1024 sc1\n"
"Mvaa 72,104,136\nMvaa 72,108,140\nMvaa 184,100,140\nMvaa 184,96,136\nMvaa 68,88,136\ns_add_u32 s15,s12,16384\nMvaa 68,92,140\n"
"buffer_load_dwordx4 a[156:159],v0,s[24:27],s15 offen offset:1024 sc1\nMvaa 180,84,140\nMvaa 180,80,136\nMvaa 64,72,136\nMvaa 64,76,140\n"
"Mvaa 176,68,140\nMvaa 176,64,136\nWL 1\nMvaa 192,64,128\nWL 0\nRD 136,3,2048\nMvaa 192,68,132\nRD 140,4,2048\nMvaa 80,76,132\nTR 32,5,0\n"
"Mvaa 80,72,128\nTR 34,5,1024\nMvaa 196,80,128\nTR 36,5,4096\nMvaa 196,84,132\nTR 38,5,5120\nMvaa 84,92,132\nTR 40,6,0\nMvaa 84,88,128\n"
"s_add_u32 m0,s48,s44\nTR 42,6,1024\nMvaa 200,96,128\nTR 44,6,4096\nMvaa 200,100,132\nDP 1,20,36,0\nMvaa 88,108,132\nTR 46,6,5120\nMvaa 88,104,128\n"
"TR 48,7,0\nMvaa 204,112,128\nDP 2,20,37,1024\nMvaa 204,116,132\nTR 50,7,1024\nMvaa 92,124,132\nTR 52,7,4096\nMvaa 92,120,128\nWL 12\nDP 1,20,38,2048\n"
"Mvaa 108,120,136\nWL 11\nRD 128,3,4096\nMvaa 108,124,140\nRD 132,4,4096\nMvaa 220,116,140\nTR 54,7,5120\nMvaa 220,112,136\nTR 56,8,0\nMvaa 104,104,136\n"
"TR 58,8,1024\nMvaa 104,108,140\nTR 60,8,4096\nMvaa 216,100,140\nTR 62,8,5120\nMvaa 216,96,136\nMvaa 100,88,136\nMvaa 100,92,140\nDP 2,20,39,3072\n"
"Mvaa 212,84,140\ns_add_u32 m0,m0,0x1000\nMvaa 212,80,136\nMvaa 96,72,136\nDP 1,20,40,0\nMvaa 96,76,140\nMvaa 208,68,140\nMvaa 208,64,136\nWL 6\n"
"DP 2,20,41,1024\nMvaa 224,64,128\nWL 5\nRD 136,3,6144\nMvaa 224,68,132\ns_mov_b32 s44,s47\nRD 140,4,6144\nMvaa 112,76,132\ns_add_u32 s47,s47,0x8000\n"
"Mvaa 112,72,128\ns_cmp_eq_u32 s47,0x28000\nMvaa 228,80,128\ns_cselect_b32 s94,s96,s95\nMvaa 228,84,132\ns_cselect_b32 s47,0,s47\nMvaa 116,92,132\n"
"Mvaa 116,88,128\nv_add_u32 v3,s94,v3\nv_add_u32 v4,s94,v4\nMvaa 232,96,128\nMvaa 232,100,132\nDP 1,20,42,2048\nMvaa 120,108,132\nMvaa 120,104,128\n"
"v_add_u32 v5,s94,v5\nv_add_u32 v6,s94,v6\nMvaa 236,112,128\nDP 2,20,43,3072\nMvaa 236,116,132\nv_add_u32 v1,s35,v1\nv_add_u32 v2,s35,v2\n"
"Mvaa 124,124,132\nv_add_u32 v7,s94,v7\nv_add_u32 v8,s94,v8\nMvaa 124,120,128\ns_waitcnt vmcnt(29) lgkmcnt(0)\ns_barrier\nMvaa 140,120,136\nRD 128,3,0\n"
"Mvaa 140,124,140\nRD 132,4,0\nMvaa 252,116,140\ns_add_u32 s15,s12,0\nMvaa 252,112,136\n"
"buffer_load_dwordx4 a[160:163],v0,s[24:27],s15 offen offset:2048 sc1\nMvaa 136,104,136\nMvaa 136,108,140\nMvaa 248,100,140\nMvaa 248,96,136\n"
"Mvaa 132,88,136\ns_add_u32 s15,s12,16384\nMvaa 132,92,140\nbuffer_load_dwordx4 a[164:167],v0,s[24:27],s15 offen offset:2048 sc1\nMvaa 244,84,140\n"
"Mvaa 244,80,136\nMvaa 128,72,136\nMvaa 128,76,140\nMvaa 240,68,140\nMvaa 240,64,136\nWL 1\nMaaa 240,0,128\nWL 0\nRD 136,3,2048\nMaaa 240,4,132\n"
"RD 140,4,2048\nMvaa 16,12,132\nTR 64,5,0\nMvaa 16,8,128\nTR 66,5,1024\nMaaa 244,16,128\nTR 68,5,4096\nMaaa 244,20,132\nTR 70,5,5120\nMvaa 20,28,132\n"
"TR 72,6,0\nMvaa 20,24,128\ns_add_u32 m0,s48,s44\nTR 74,6,1024\nMaaa 248,32,128\nTR 76,6,4096\nMaaa 248,36,132\nDP 1,20,36,0\nMvaa 24,44,132\n"
"TR 78,6,5120\nMvaa 24,40,128\nTR 80,7,0\nMaaa 252,48,128\nDP 2,20,37,1024\nMaaa 252,52,132\nTR 82,7,1024\nMvaa 28,60,132\nTR 84,7,4096\nMvaa 28,56,128\n"
"WL 12\nDP 1,20,38,2048\nMvaa 44,56,136\nWL 11\nRD 128,3,4096\nMvaa 44,60,140\nRD 132,4,4096\nMvaa 156,52,140\nTR 86,7,5120\nMvaa 156,48,136\nTR 88,8,0\n"
"Mvaa 40,40,136\nTR 90,8,1024\nMvaa 40,44,140\nTR 92,8,4096\nMvaa 152,36,140\nTR 94,8,5120\nMvaa 152,32,136\nMvaa 36,24,136\nMvaa 36,28,140\n"
"DP 2,20,39,3072\nMvaa 148,20,140\ns_add_u32 m0,m0,0x1000\nMvaa 148,16,136\nMvaa 32,8,136\nDP 1,20,40,0\nMvaa 32,12,140\nMvaa 144,4,140\nMvaa 144,0,136\n"
"WL 6\nDP 2,20,41,1024\nMvaa 160,0,128\nWL 5\nRD 136,3,6144\nMvaa 160,4,132\ns_mov_b32 s44,s47\nRD 140,4,6144\nMvaa 48,12,132\ns_add_u32 s47,s47,0x8000\n"
"Mvaa 48,8,128\ns_cmp_eq_u32 s47,0x28000\nMvaa 164,16,128\ns_cselect_b32 s94,s96,s95\nMvaa 164,20,132\ns_cselect_b32 s47,0,s47\nMvaa 52,28,132\n"
"Mvaa 52,24,128\nv_add_u32 v3,s94,v3\nv_add_u32 v4,s94,v4\nMvaa 168,32,128\nMvaa 168,36,132\nDP 1,20,42,2048\nMvaa 56,44,132\nMvaa 56,40,128\n"
"v_add_u32 v5,s94,v5\nv_add_u32 v6,s94,v6\nMvaa 172,48,128\nDP 2,20,43,3072\nMvaa 172,52,132\nv_add_u32 v1,s34,v1\nv_add_u32 v2,s34,v2\nMvaa 60,60,132\n"
"v_add_u32 v7,s94,v7\nv_add_u32 v8,s94,v8\nMvaa 60,56,128\ns_waitcnt vmcnt(31) lgkmcnt(0)\ns_barrier\nMvaa 76,56,136\nRD 128,3,0\nMvaa 76,60,140\n"
"RD 132,4,0\nMvaa 188,52,140\ns_add_u32 s15,s12,0\nMvaa 188,48,136\nbuffer_load_dwordx4 a[168:171],v0,s[24:27],s15 offen offset:3072 sc1\n"
"Mvaa 72,40,136\nMvaa 72,44,140\nMvaa 184,36,140\nMvaa 184,32,136\nMvaa 68,24,136\ns_add_u32 s15,s12,16384\nMvaa 68,28,140\n"
"buffer_load_dwordx4 a[172:175],v0,s[24:27],s15 offen offset:3072 sc1\nMvaa 180,20,140\nMvaa 180,16,136\nMvaa 64,8,136\nMvaa 64,12,140\nMvaa 176,4,140\n"
"Mvaa 176,0,136\nWL 1\nMvaa 192,0,128\nWL 0\nRD 136,3,2048\nMvaa 192,4,132\nRD 140,4,2048\nMvaa 80,12,132\nTR 96,5,0\nMvaa 80,8,128\nTR 98,5,1024\n"
"Mvaa 196,16,128\nTR 100,5,4096\nMvaa 196,20,132\nTR 102,5,5120\nMvaa 84,28,132\nTR 104,6,0\nMvaa 84,24,128\ns_add_u32 m0,s48,s44\nTR 106,6,1024\n"
"Mvaa 200,32,128\nTR 108,6,4096\nMvaa 200,36,132\nDP 1,20,36,0\nMvaa 88,44,132\nTR 110,6,5120\nMvaa 88,40,128\nTR 112,7,0\nMvaa 204,48,128\n"
"DP 2,20,37,1024\nMvaa 204,52,132\nTR 114,7,1024\nMvaa 92,60,132\nTR 116,7,4096\nMvaa 92,56,128\nWL 12\nDP 1,20,38,2048\nMvaa 108,56,136\nWL 11\n"
"RD 128,3,4096\nMvaa 108,60,140\nRD 132,4,4096\nMvaa 220,52,140\nTR 118,7,5120\nMvaa 220,48,136\nTR 120,8,0\nMvaa 104,40,136\nTR 122,8,1024\n"
"Mvaa 104,44,140\nTR 124,8,4096\nMvaa 216,36,140\nTR 126,8,5120\nMvaa 216,32,136\nMvaa 100,24,136\nMvaa 100,28,140\nDP 2,20,39,3072\nMvaa 212,20,140\n"
"s_add_u32 m0,m0,0x1000\nMvaa 212,16,136\nMvaa 96,8,136\nDP 1,20,40,0\nMvaa 96,12,140\nMvaa 208,4,140\nMvaa 208,0,136\nWL 6\nDP 2,20,41,1024\n"
"Mvaa 224,0,128\nWL 5\nRD 136,3,6144\nMvaa 224,4,132\ns_mov_b32 s44,s47\nRD 140,4,6144\nMvaa 112,12,132\ns_add_u32 s47,s47,0x8000\nMvaa 112,8,128\n"
"s_cmp_eq_u32 s47,0x28000\nMvaa 228,16,128\ns_cselect_b32 s94,s96,s95\nMvaa 228,20,132\ns_cselect_b32 s47,0,s47\nMvaa 116,28,132\nMvaa 116,24,128\n"
"v_add_u32 v3,s94,v3\nv_add_u32 v4,s94,v4\nMvaa 232,32,128\nMvaa 232,36,132\nDP 1,20,42,2048\nMvaa 120,44,132\nMvaa 120,40,128\nv_add_u32 v5,s94,v5\n"
"v_add_u32 v6,s94,v6\nMvaa 236,48,128\nDP 2,20,43,3072\nMvaa 236,52,132\nv_add_u32 v1,s35,v1\nv_add_u32 v2,s35,v2\nMvaa 124,60,132\nv_add_u32 v7,s94,v7\n"
"v_add_u32 v8,s94,v8\nMvaa 124,56,128\ns_waitcnt vmcnt(30) lgkmcnt(0)\ns_barrier\nMvaa 140,56,136\nRD 128,3,0\nMvaa 140,60,140\nRD 132,4,0\n"
"Mvaa 252,52,140\ns_add_u32 s15,s12,4096\nMvaa 252,48,136\nbuffer_load_dwordx4 a[176:179],v0,s[24:27],s15 offen offset:0 sc1\nMvaa 136,40,136\n"
"Mvaa 136,44,140\nMvaa 248,36,140\nMvaa 248,32,136\nMvaa 132,24,136\ns_add_u32 s15,s12,20480\nMvaa 132,28,140\n"
"buffer_load_dwordx4 a[180:183],v0,s[24:27],s15 offen offset:0 sc1\nMvaa 244,20,140\nMvaa 244,16,136\nMvaa 128,8,136\nMvaa 128,12,140\nMvaa 240,4,140\n"
"Mvaa 240,0,136\nWL 1\nMaaa 240,64,128\nWL 0\nRD 136,3,2048\nMaaa 240,68,132\nRD 140,4,2048\nMvaa 16,76,132\nTR 0,5,0\nMvaa 16,72,128\nTR 2,5,1024\n"
"Maaa 244,80,128\nTR 4,5,4096\nMaaa 244,84,132\nTR 6,5,5120\nMvaa 20,92,132\nTR 8,6,0\nMvaa 20,88,128\ns_add_u32 m0,s48,s44\nTR 10,6,1024\n"
"Maaa 248,96,128\nTR 12,6,4096\nMaaa 248,100,132\nDP 1,20,36,0\nMvaa 24,108,132\nTR 14,6,5120\nMvaa 24,104,128\nTR 16,7,0\nMaaa 252,112,128\n"
"DP 2,20,37,1024\nMaaa 252,116,132\nTR 18,7,1024\nMvaa 28,124,132\nTR 20,7,4096\nMvaa 28,120,128\nWL 12\nDP 1,20,38,2048\nMvaa 44,120,136\nWL 11\n"
"RD 128,3,4096\nMvaa 44,124,140\nRD 132,4,4096\nMvaa 156,116,140\nTR 22,7,5120\nMvaa 156,112,136\nTR 24,8,0\nMvaa 40,104,136\nTR 26,8,1024\n"
"Mvaa 40,108,140\nTR 28,8,4096\nMvaa 152,100,140\nTR 30,8,5120\nMvaa 152,96,136\nMvaa 36,88,136\nMvaa 36,92,140\nDP 2,20,39,3072\nMvaa 148,84,140\n"
"s_add_u32 m0,m0,0x1000\nglobal_load_dword a238,v13,s[8:9]\nMvaa 148,80,136\ns_add_u32 s8,s8,s18\ns_addc_u32 s9,s9,0\nMvaa 32,72,136\nDP 1,20,40,0\n"
"Mvaa 32,76,140\nMvaa 144,68,140\nMvaa 144,64,136\nWL 6\nDP 2,20,41,1024\nMvaa 160,64,128\nWL 5\nRD 136,3,6144\nMvaa 160,68,132\ns_mov_b32 s44,s47\n"
"RD 140,4,6144\nMvaa 48,76,132\ns_add_u32 s47,s47,0x8000\nMvaa 48,72,128\ns_cmp_eq_u32 s47,0x28000\nMvaa 164,80,128\ns_cselect_b32 s94,s96,s95\n"
"Mvaa 164,84,132\ns_cselect_b32 s47,0,s47\nMvaa 52,92,132\nMvaa 52,88,128\nv_add_u32 v3,s94,v3\nv_add_u32 v4,s94,v4\nMvaa 168,96,128\nMvaa 168,100,132\n"
"DP 1,20,42,2048\nMvaa 56,108,132\nMvaa 56,104,128\nv_add_u32 v5,s94,v5\nv_add_u32 v6,s94,v6\nMvaa 172,112,128\nDP 2,20,43,3072\nMvaa 172,116,132\n"
"v_add_u32 v1,s34,v1\nv_add_u32 v2,s34,v2\nMvaa 60,124,132\nv_add_u32 v7,s94,v7\nv_add_u32 v8,s94,v8\nMvaa 60,120,128\ns_waitcnt vmcnt(31) lgkmcnt(0)\n"
"s_barrier\nMvaa 76,120,136\nRD 128,3,0\nMvaa 76,124,140\nRD 132,4,0\nMvaa 188,116,140\ns_add_u32 s15,s12,4096\nMvaa 188,112,136\n"
"buffer_load_dwordx4 a[184:187],v0,s[24:27],s15 offen offset:1024 sc1\nMvaa 72,104,136\nMvaa 72,108,140\nMvaa 184,100,140\nMvaa 184,96,136\n"
"Mvaa 68,88,136\ns_add_u32 s15,s12,20480\nMvaa 68,92,140\nbuffer_load_dwordx4 a[188:191],v0,s[24:27],s15 offen offset:1024 sc1\nMvaa 180,84,140\n"
"Mvaa 180,80,136\nMvaa 64,72,136\nMvaa 64,76,140\nMvaa 176,68,140\nMvaa 176,64,136\nWL 1\nMvaa 192,64,128\nWL 0\nRD 136,3,2048\nMvaa 192,68,132\n"
"RD 140,4,2048\nMvaa 80,76,132\nTR 32,5,0\nMvaa 80,72,128\nTR 34,5,1024\nMvaa 196,80,128\nTR 36,5,4096\nMvaa 196,84,132\nTR 38,5,5120\nMvaa 84,92,132\n"
"TR 40,6,0\nMvaa 84,88,128\ns_add_u32 m0,s48,s44\nTR 42,6,1024\nMvaa 200,96,128\nTR 44,6,4096\nMvaa 200,100,132\nDP 1,20,36,0\nMvaa 88,108,132\n"
"TR 46,6,5120\nMvaa 88,104,128\nTR 48,7,0\nMvaa 204,112,128\nDP 2,20,37,1024\nMvaa 204,116,132\nTR 50,7,1024\nMvaa 92,124,132\nTR 52,7,4096\n"
"Mvaa 92,120,128\nWL 12\nDP 1,20,38,2048\nMvaa 108,120,136\nWL 11\nRD 128,3,4096\nMvaa 108,124,140\nRD 132,4,4096\nMvaa 220,116,140\nTR 54,7,5120\n"
"Mvaa 220,112,136\nTR 56,8,0\nMvaa 104,104,136\nTR 58,8,1024\nMvaa 104,108,140\nTR 60,8,4096\nMvaa 216,100,140\nTR 62,8,5120\nMvaa 216,96,136\n"
"Mvaa 100,88,136\nMvaa 100,92,140\nDP 2,20,39,3072\nMvaa 212,84,140\ns_add_u32 m0,m0,0x1000\nMvaa 212,80,136\nMvaa 96,72,136\nDP 1,20,40,0\n"
"Mvaa 96,76,140\nMvaa 208,68,140\nMvaa 208,64,136\nWL 6\nDP 2,20,41,1024\nMvaa 224,64,128\nWL 5\nRD 136,3,6144\nMvaa 224,68,132\ns_mov_b32 s44,s47\n"
"RD 140,4,6144\nMvaa 112,76,132\ns_add_u32 s47,s47,0x8000\nMvaa 112,72,128\ns_cmp_eq_u32 s47,0x28000\nMvaa 228,80,128\ns_cselect_b32 s94,s96,s95\n"
"Mvaa 228,84,132\ns_cselect_b32 s47,0,s47\nMvaa 116,92,132\nMvaa 116,88,128\nv_add_u32 v3,s94,v3\nv_add_u32 v4,s94,v4\nMvaa 232,96,128\n"
"Mvaa 232,100,132\nDP 1,20,42,2048\nMvaa 120,108,132\nMvaa 120,104,128\nv_add_u32 v5,s94,v5\nv_add_u32 v6,s94,v6\nMvaa 236,112,128\nDP 2,20,43,3072\n"
"Mvaa 236,116,132\nv_add_u32 v1,s35,v1\nv_add_u32 v2,s35,v2\nMvaa 124,124,132\nv_add_u32 v7,s94,v7\nv_add_u32 v8,s94,v8\nMvaa 124,120,128\n"
"s_waitcnt vmcnt(31) lgkmcnt(0)\ns_barrier\nMvaa 140,120,136\nRD 128,3,0\nMvaa 140,124,140\nRD 132,4,0\nMvaa 252,116,140\ns_add_u32 s15,s12,4096\n"
"Mvaa 252,112,136\nbuffer_load_dwordx4 a[192:195],v0,s[24:27],s15 offen offset:2048 sc1\nMvaa 136,104,136\nMvaa 136,108,140\nMvaa 248,100,140\n"
"Mvaa 248,96,136\nMvaa 132,88,136\ns_add_u32 s15,s12,20480\nMvaa 132,92,140\nbuffer_load_dwordx4 a[196:199],v0,s[24:27],s15 offen offset:2048 sc1\n"
"Mvaa 244,84,140\nMvaa 244,80,136\nMvaa 128,72,136\nMvaa 128,76,140\nMvaa 240,68,140\nMvaa 240,64,136\nWL 1\nMaaa 240,0,128\nWL 0\nRD 136,3,2048\n"
"Maaa 240,4,132\nRD 140,4,2048\nMvaa 16,12,132\nTR 64,5,0\nMvaa 16,8,128\nTR 66,5,1024\nMaaa 244,16,128\nTR 68,5,4096\nMaaa 244,20,132\nTR 70,5,5120\n"
"Mvaa 20,28,132\nTR 72,6,0\nMvaa 20,24,128\ns_add_u32 m0,s48,s44\nTR 74,6,1024\nMaaa 248,32,128\nTR 76,6,4096\nMaaa 248,36,132\nDP 1,20,36,0\n"
"Mvaa 24,44,132\nTR 78,6,5120\nMvaa 24,40,128\nTR 80,7,0\nMaaa 252,48,128\nDP 2,20,37,1024\nMaaa 252,52,132\nTR 82,7,1024\nMvaa 28,60,132\nTR 84,7,4096\n"
"Mvaa 28,56,128\nWL 12\nDP 1,20,38,2048\nMvaa 44,56,136\nWL 11\nRD 128,3,4096\nMvaa 44,60,140\nRD 132,4,4096\nMvaa 156,52,140\nTR 86,7,5120\n"
"Mvaa 156,48,136\nTR 88,8,0\nMvaa 40,40,136\nTR 90,8,1024\nMvaa 40,44,140\nTR 92,8,4096\nMvaa 152,36,140\nTR 94,8,5120\nMvaa 152,32,136\nMvaa 36,24,136\n"
"Mvaa 36,28,140\nDP 2,20,39,3072\nMvaa 148,20,140\ns_add_u32 m0,m0,0x1000\nMvaa 148,16,136\nMvaa 32,8,136\nDP 1,20,40,0\nMvaa 32,12,140\nMvaa 144,4,140\n"
"Mvaa 144,0,136\nWL 6\nDP 2,20,41,1024\nMvaa 160,0,128\nWL 5\nRD 136,3,6144\nMvaa 160,4,132\ns_mov_b32 s44,s47\nRD 140,4,6144\nMvaa 48,12,132\n"
"s_add_u32 s47,s47,0x8000\nMvaa 48,8,128\ns_cmp_eq_u32 s47,0x28000\nMvaa 164,16,128\ns_cselect_b32 s94,s96,s95\nMvaa 164,20,132\n"
"s_cselect_b32 s47,0,s47\nMvaa 52,28,132\nMvaa 52,24,128\nv_add_u32 v3,s94,v3\nv_add_u32 v4,s94,v4\nMvaa 168,32,128\nMvaa 168,36,132\nDP 1,20,42,2048\n"
"Mvaa 56,44,132\nMvaa 56,40,128\nv_add_u32 v5,s94,v5\nv_add_u32 v6,s94,v6\nMvaa 172,48,128\nDP 2,20,43,3072\nMvaa 172,52,132\nv_add_u32 v1,s34,v1\n"
"v_add_u32 v2,s34,v2\nMvaa 60,60,132\nv_add_u32 v7,s94,v7\nv_add_u32 v8,s94,v8\nMvaa 60,56,128\ns_waitcnt vmcnt(31) lgkmcnt(0)\ns_barrier\n"
"Mvaa 76,56,136\nRD 128,3,0\nMvaa 76,60,140\nRD 132,4,0\nMvaa 188,52,140\ns_add_u32 s15,s12,4096\nMvaa 188,48,136\n"
"buffer_load_dwordx4 a[200:203],v0,s[24:27],s15 offen offset:3072 sc1\nMvaa 72,40,136\nMvaa 72,44,140\nMvaa 184,36,140\nMvaa 184,32,136\nMvaa 68,24,136\n"
"s_add_u32 s15,s12,20480\nMvaa 68,28,140\nbuffer_load_dwordx4 a[204:207],v0,s[24:27],s15 offen offset:3072 sc1\nMvaa 180,20,140\nMvaa 180,16,136\n"
"Mvaa 64,8,136\nMvaa 64,12,140\nMvaa 176,4,140\nMvaa 176,0,136\nWL 1\nMvaa 192,0,128\nWL 0\nRD 136,3,2048\nMvaa 192,4,132\nRD 140,4,2048\n"
"Mvaa 80,12,132\nTR 96,5,0\nMvaa 80,8,128\nTR 98,5,1024\nMvaa 196,16,128\nTR 100,5,4096\nMvaa 196,20,132\nTR 102,5,5120\nMvaa 84,28,132\nTR 104,6,0\n"
"Mvaa 84,24,128\ns_add_u32 m0,s48,s44\nTR 106,6,1024\nMvaa 200,32,128\nTR 108,6,4096\nMvaa 200,36,132\nDP 1,20,36,0\nMvaa 88,44,132\nTR 110,6,5120\n"
"Mvaa 88,40,128\nTR 112,7,0\nMvaa 204,48,128\nDP 2,20,37,1024\nMvaa 204,52,132\nTR 114,7,1024\nMvaa 92,60,132\nTR 116,7,4096\nMvaa 92,56,128\nWL 12\n"
"DP 1,20,38,2048\nMvaa 108,56,136\nWL 11\nRD 128,3,4096\nMvaa 108,60,140\nRD 132,4,4096\nMvaa 220,52,140\nTR 118,7,5120\nMvaa 220,48,136\nTR 120,8,0\n"
"Mvaa 104,40,136\nTR 122,8,1024\nMvaa 104,44,140\nTR 124,8,4096\nMvaa 216,36,140\nTR 126,8,5120\nMvaa 216,32,136\nMvaa 100,24,136\nMvaa 100,28,140\n"
"DP 2,20,39,3072\nMvaa 212,20,140\ns_add_u32 m0,m0,0x1000\nMvaa 212,16,136\nMvaa 96,8,136\nDP 1,20,40,0\nMvaa 96,12,140\nMvaa 208,4,140\nMvaa 208,0,136\n"
"WL 6\nDP 2,20,41,1024\nMvaa 224,0,128\nWL 5\nRD 136,3,6144\nMvaa 224,4,132\ns_mov_b32 s44,s47\nRD 140,4,6144\nMvaa 112,12,132\n"
"s_add_u32 s47,s47,0x8000\nMvaa 112,8,128\ns_cmp_eq_u32 s47,0x28000\nMvaa 228,16,128\ns_cselect_b32 s94,s96,s95\nMvaa 228,20,132\n"
"s_cselect_b32 s47,0,s47\nMvaa 116,28,132\nMvaa 116,24,128\nv_add_u32 v3,s94,v3\nv_add_u32 v4,s94,v4\nMvaa 232,32,128\nMvaa 232,36,132\nDP 1,20,42,2048\n"
"Mvaa 120,44,132\nMvaa 120,40,128\nv_add_u32 v5,s94,v5\nv_add_u32 v6,s94,v6\nMvaa 236,48,128\nDP 2,20,43,3072\nMvaa 236,52,132\nv_add_u32 v1,s35,v1\n"
"v_add_u32 v2,s35,v2\nMvaa 124,60,132\nv_add_u32 v7,s94,v7\nv_add_u32 v8,s94,v8\nMvaa 124,56,128\ns_waitcnt vmcnt(30) lgkmcnt(0)\ns_barrier\n"
"Mvaa 140,56,136\nRD 128,3,0\nMvaa 140,60,140\nRD 132,4,0\nMvaa 252,52,140\ns_add_u32 s15,s12,8192\ns_lshr_b32 s70,s11,3\ns_mul_i32 s45,s70,4\n"
"s_mul_i32 s49,s10,1\ns_add_u32 s45,s45,s49\ns_lshl_b32 s45,s45,13\ns_cmp_lt_u32 s45,fused_op_end-fused_op_qproj-8191\ns_cbranch_scc0 fused_op_cw4\n"
"s_add_u32 s45,s45,fused_op_qproj-fused_op_pc\nv_lshlrev_b32 v12,3,v0\nv_add_u32 v12,s45,v12\nglobal_load_dword v11,v12,s[54:55]\nfused_op_cw4:\n"
"Mvaa 252,48,136\nbuffer_load_dwordx4 a[208:211],v0,s[24:27],s15 offen offset:0 sc1\nMvaa 136,40,136\nMvaa 136,44,140\nMvaa 248,36,140\nMvaa 248,32,136\n"
"Mvaa 132,24,136\ns_add_u32 s15,s12,24576\nMvaa 132,28,140\nbuffer_load_dwordx4 a[212:215],v0,s[24:27],s15 offen offset:0 sc1\nMvaa 244,20,140\n"
"Mvaa 244,16,136\nMvaa 128,8,136\nMvaa 128,12,140\nMvaa 240,4,140\nMvaa 240,0,136\nWL 1\nMaaa 240,64,128\nWL 0\nRD 136,3,2048\nMaaa 240,68,132\n"
"RD 140,4,2048\nMvaa 16,76,132\nTR 0,5,0\nMvaa 16,72,128\nTR 2,5,1024\nMaaa 244,80,128\nTR 4,5,4096\nMaaa 244,84,132\nTR 6,5,5120\nMvaa 20,92,132\n"
"TR 8,6,0\nMvaa 20,88,128\ns_add_u32 m0,s48,s44\nTR 10,6,1024\nMaaa 248,96,128\nTR 12,6,4096\nMaaa 248,100,132\nDP 1,20,36,0\nMvaa 24,108,132\n"
"TR 14,6,5120\nMvaa 24,104,128\nTR 16,7,0\nMaaa 252,112,128\nDP 2,20,37,1024\nMaaa 252,116,132\nTR 18,7,1024\nMvaa 28,124,132\nTR 20,7,4096\n"
"Mvaa 28,120,128\nWL 12\nDP 1,20,38,2048\nMvaa 44,120,136\nWL 11\nRD 128,3,4096\nMvaa 44,124,140\nRD 132,4,4096\nMvaa 156,116,140\nTR 22,7,5120\n"
"Mvaa 156,112,136\nTR 24,8,0\nMvaa 40,104,136\nTR 26,8,1024\nMvaa 40,108,140\nTR 28,8,4096\nMvaa 152,100,140\nTR 30,8,5120\nMvaa 152,96,136\n"
"Mvaa 36,88,136\nMvaa 36,92,140\nDP 2,20,39,3072\nMvaa 148,84,140\ns_add_u32 m0,m0,0x1000\nglobal_load_dword v9,v13,s[8:9]\nMvaa 148,80,136\n"
"s_add_u32 s8,s8,s18\ns_addc_u32 s9,s9,0\nMvaa 32,72,136\nDP 1,20,40,0\nMvaa 32,76,140\nMvaa 144,68,140\nMvaa 144,64,136\nWL 6\nDP 2,20,41,1024\n"
"Mvaa 160,64,128\nWL 5\nRD 136,3,6144\nMvaa 160,68,132\ns_mov_b32 s44,s47\nRD 140,4,6144\nMvaa 48,76,132\ns_add_u32 s47,s47,0x8000\nMvaa 48,72,128\n"
"s_cmp_eq_u32 s47,0x28000\nMvaa 164,80,128\ns_cselect_b32 s94,s96,s95\nMvaa 164,84,132\ns_cselect_b32 s47,0,s47\nMvaa 52,92,132\nMvaa 52,88,128\n"
"v_add_u32 v3,s94,v3\nv_add_u32 v4,s94,v4\nMvaa 168,96,128\nMvaa 168,100,132\nDP 1,20,42,2048\nMvaa 56,108,132\nMvaa 56,104,128\nv_add_u32 v5,s94,v5\n"
"v_add_u32 v6,s94,v6\nMvaa 172,112,128\nDP 2,20,43,3072\nMvaa 172,116,132\nv_add_u32 v1,s34,v1\nv_add_u32 v2,s34,v2\nMvaa 60,124,132\n"
"v_add_u32 v7,s94,v7\nv_add_u32 v8,s94,v8\nMvaa 60,120,128\ns_waitcnt vmcnt(31) lgkmcnt(0)\ns_barrier\nMvaa 76,120,136\nRD 128,3,0\nMvaa 76,124,140\n"
"RD 132,4,0\nMvaa 188,116,140\ns_add_u32 s15,s12,8192\nMvaa 188,112,136\nbuffer_load_dwordx4 a[216:219],v0,s[24:27],s15 offen offset:1024 sc1\n"
"Mvaa 72,104,136\nMvaa 72,108,140\nMvaa 184,100,140\nMvaa 184,96,136\nMvaa 68,88,136\ns_add_u32 s15,s12,24576\nMvaa 68,92,140\n"
"buffer_load_dwordx4 a[220:223],v0,s[24:27],s15 offen offset:1024 sc1\nMvaa 180,84,140\nMvaa 180,80,136\nMvaa 64,72,136\nMvaa 64,76,140\n"
"Mvaa 176,68,140\nMvaa 176,64,136\nWL 1\nMvaa 192,64,128\nWL 0\nRD 136,3,2048\nMvaa 192,68,132\nRD 140,4,2048\nMvaa 80,76,132\nTR 32,5,0\n"
"Mvaa 80,72,128\nTR 34,5,1024\nMvaa 196,80,128\nTR 36,5,4096\nMvaa 196,84,132\nTR 38,5,5120\nMvaa 84,92,132\nTR 40,6,0\nMvaa 84,88,128\n"
"s_add_u32 m0,s48,s44\nTR 42,6,1024\nMvaa 200,96,128\nTR 44,6,4096\nMvaa 200,100,132\nDP 1,20,36,0\nMvaa 88,108,132\nTR 46,6,5120\nMvaa 88,104,128\n"
"TR 48,7,0\nMvaa 204,112,128\nDP 2,20,37,1024\nMvaa 204,116,132\nTR 50,7,1024\nMvaa 92,124,132\nTR 52,7,4096\nMvaa 92,120,128\nWL 12\nDP 1,20,38,2048\n"
"Mvaa 108,120,136\nWL 11\nRD 128,3,4096\nMvaa 108,124,140\nRD 132,4,4096\nMvaa 220,116,140\nTR 54,7,5120\nMvaa 220,112,136\nTR 56,8,0\nMvaa 104,104,136\n"
"TR 58,8,1024\nMvaa 104,108,140\nTR 60,8,4096\nMvaa 216,100,140\nTR 62,8,5120\nMvaa 216,96,136\nMvaa 100,88,136\nMvaa 100,92,140\nDP 2,20,39,3072\n"
"Mvaa 212,84,140\ns_add_u32 m0,m0,0x1000\nMvaa 212,80,136\nMvaa 96,72,136\nDP 1,20,40,0\nMvaa 96,76,140\nMvaa 208,68,140\nMvaa 208,64,136\nWL 6\n"
"DP 2,20,41,1024\nMvaa 224,64,128\nWL 5\nRD 136,3,6144\nMvaa 224,68,132\ns_mov_b32 s44,s47\nRD 140,4,6144\nMvaa 112,76,132\ns_add_u32 s47,s47,0x8000\n"
"Mvaa 112,72,128\ns_cmp_eq_u32 s47,0x28000\nMvaa 228,80,128\ns_cselect_b32 s94,s96,s95\nMvaa 228,84,132\ns_cselect_b32 s47,0,s47\nMvaa 116,92,132\n"
"Mvaa 116,88,128\nv_add_u32 v3,s94,v3\nv_add_u32 v4,s94,v4\nMvaa 232,96,128\nMvaa 232,100,132\nDP 1,20,42,2048\nMvaa 120,108,132\nMvaa 120,104,128\n"
"v_add_u32 v5,s94,v5\nv_add_u32 v6,s94,v6\nMvaa 236,112,128\nDP 2,20,43,3072\nMvaa 236,116,132\nv_add_u32 v1,s35,v1\nv_add_u32 v2,s35,v2\n"
"Mvaa 124,124,132\nv_add_u32 v7,s94,v7\nv_add_u32 v8,s94,v8\nMvaa 124,120,128\ns_waitcnt vmcnt(31) lgkmcnt(0)\ns_barrier\nMvaa 140,120,136\nRD 128,3,0\n"
"Mvaa 140,124,140\nRD 132,4,0\nMvaa 252,116,140\ns_add_u32 s15,s12,8192\nMvaa 252,112,136\n"
"buffer_load_dwordx4 a[224:227],v0,s[24:27],s15 offen offset:2048 sc1\nMvaa 136,104,136\nMvaa 136,108,140\nMvaa 248,100,140\nMvaa 248,96,136\n"
"Mvaa 132,88,136\ns_add_u32 s15,s12,24576\nMvaa 132,92,140\nbuffer_load_dwordx4 a[228:231],v0,s[24:27],s15 offen offset:2048 sc1\nMvaa 244,84,140\n"
"Mvaa 244,80,136\nMvaa 128,72,136\nMvaa 128,76,140\nMvaa 240,68,140\nMvaa 240,64,136\nWL 1\nMaaa 240,0,128\nWL 0\ns_mul_i32 s45,s56,512\nRD 136,3,2048\n"
"v_lshrrev_b32 v13,7,v0\nMaaa 240,4,132\ns_mul_i32 s49,s10,128\nRD 140,4,2048\nv_and_b32 v1,6,v13\nMvaa 16,12,132\ns_add_u32 s45,s45,s49\nTR 64,5,0\n"
"v_lshlrev_b32 v1,4,v1\nMvaa 16,8,128\ns_add_u32 s20,s80,s45\ns_addc_u32 s21,s81,0\nTR 66,5,1024\nMaaa 244,16,128\ns_and_b32 s21,s21,0xffff\n"
"TR 68,5,4096\nv_xor_b32 v1,v1,v0\nMaaa 244,20,132\ns_sub_u32 s22,3670016,s45\nTR 70,5,5120\nv_and_b32 v1,0x70,v1\nMvaa 20,28,132\n"
"s_mov_b32 s23,0x20000\nTR 72,6,0\nv_and_b32 v12,4,v13\nMvaa 20,24,128\ns_mov_b32 s36,0\nTR 74,6,1024\nv_add_u32 v13,v13,v12\nMaaa 248,32,128\n"
"s_mov_b32 s37,31744\nTR 76,6,4096\nv_mul_u32_u24 v13,8192,v13\nMaaa 248,36,132\ns_mov_b32 s38,129024\nTR 78,6,5120\nv_add_u32 v1,v1,v13\n"
"Mvaa 24,44,132\ns_mov_b32 s39,160768\nTR 80,7,0\nMvaa 24,40,128\ns_mov_b32 s40,262144\nTR 82,7,1024\nMaaa 252,48,128\ns_mov_b32 s41,293888\n"
"TR 84,7,4096\nMaaa 252,52,132\ns_mov_b32 s42,391168\nTR 86,7,5120\nMvaa 28,60,132\ns_mov_b32 s43,422912\nTR 88,8,0\nMvaa 28,56,128\nWL 14\n"
"TR 90,8,1024\nMvaa 44,56,136\nWL 14\nRD 128,3,4096\nMvaa 44,60,140\nRD 132,4,4096\nMvaa 156,52,140\nTR 92,8,4096\nMvaa 156,48,136\nTR 94,8,5120\n"
"Mvaa 40,40,136\nMvaa 40,44,140\nMvaa 152,36,140\nMvaa 152,32,136\ns_add_u32 m0,s48,s44\nMvaa 36,24,136\nMvaa 36,28,140\nDP 1,20,36,0\nMvaa 148,20,140\n"
"Mvaa 148,16,136\nDP 1,20,37,1024\nMvaa 32,8,136\nMvaa 32,12,140\nDP 1,20,38,2048\nMvaa 144,4,140\nMvaa 144,0,136\nWL 3\nDP 1,20,39,3072\n"
"Mvaa 160,0,128\nWL 2\ns_add_u32 m0,m0,0x1000\nRD 136,3,6144\nMvaa 160,4,132\ns_mov_b32 s44,s47\nRD 140,4,6144\nMvaa 48,12,132\n"
"s_add_u32 s47,s47,0x8000\nMvaa 48,8,128\ns_cmp_eq_u32 s47,0x28000\nMvaa 164,16,128\ns_cselect_b32 s94,s96,s95\nMvaa 164,20,132\n"
"s_cselect_b32 s47,0,s47\nMvaa 52,28,132\nDP 1,20,40,0\nMvaa 52,24,128\nv_add_u32 v3,s94,v3\nv_add_u32 v4,s94,v4\nMvaa 168,32,128\nDP 1,20,41,1024\n"
"Mvaa 168,36,132\nMvaa 56,44,132\nDP 1,20,42,2048\nMvaa 56,40,128\nv_add_u32 v5,s94,v5\nv_add_u32 v6,s94,v6\nMvaa 172,48,128\nDP 1,20,43,3072\n"
"Mvaa 172,52,132\nv_add_u32 v1,0x80000,v1\nMvaa 60,60,132\nv_add_u32 v7,s94,v7\nv_add_u32 v8,s94,v8\nMvaa 60,56,128\ns_waitcnt vmcnt(31) lgkmcnt(0)\n"
"s_barrier\nMvaa 76,56,136\nRD 128,3,0\nMvaa 76,60,140\nRD 132,4,0\nMvaa 188,52,140\ns_add_u32 s15,s12,8192\nMvaa 188,48,136\n"
"buffer_load_dwordx4 a[232:235],v0,s[24:27],s15 offen offset:3072 sc1\nMvaa 72,40,136\nMvaa 72,44,140\nMvaa 184,36,140\nMvaa 184,32,136\nMvaa 68,24,136\n"
"s_add_u32 s15,s12,24576\nMvaa 68,28,140\nbuffer_load_dwordx4 a[236:239],v0,s[24:27],s15 offen offset:3072 sc1\nMvaa 180,20,140\nMvaa 180,16,136\n"
"Mvaa 64,8,136\nMvaa 64,12,140\nMvaa 176,4,140\nMvaa 176,0,136\nWL 1\nMvaa 192,0,128\nWL 0\nRD 136,3,2048\nMvaa 192,4,132\nRD 140,4,2048\n"
"Mvaa 80,12,132\nTR 96,5,0\nMvaa 80,8,128\nTR 98,5,1024\nMvaa 196,16,128\nTR 100,5,4096\nMvaa 196,20,132\nTR 102,5,5120\nMvaa 84,28,132\nTR 104,6,0\n"
"Mvaa 84,24,128\ns_add_u32 m0,s48,s44\nTR 106,6,1024\nMvaa 200,32,128\nTR 108,6,4096\nMvaa 200,36,132\nDP 1,20,36,0\nMvaa 88,44,132\nTR 110,6,5120\n"
"Mvaa 88,40,128\nTR 112,7,0\nMvaa 204,48,128\nDP 1,20,37,1024\nMvaa 204,52,132\nTR 114,7,1024\nMvaa 92,60,132\nTR 116,7,4096\nMvaa 92,56,128\nWL 12\n"
"DP 1,20,38,2048\nMvaa 108,56,136\nWL 11\nRD 128,3,4096\nMvaa 108,60,140\nRD 132,4,4096\nMvaa 220,52,140\nTR 118,7,5120\nMvaa 220,48,136\nTR 120,8,0\n"
"Mvaa 104,40,136\nTR 122,8,1024\nMvaa 104,44,140\nTR 124,8,4096\nMvaa 216,36,140\nTR 126,8,5120\nMvaa 216,32,136\nMvaa 100,24,136\nMvaa 100,28,140\n"
"DP 1,20,39,3072\nMvaa 212,20,140\ns_add_u32 m0,m0,0x1000\nMvaa 212,16,136\nMvaa 96,8,136\nDP 1,20,40,0\nMvaa 96,12,140\nMvaa 208,4,140\nMvaa 208,0,136\n"
"WL 6\nDP 1,20,41,1024\nMvaa 224,0,128\nWL 5\nRD 136,3,6144\nMvaa 224,4,132\ns_mov_b32 s44,s47\nRD 140,4,6144\nMvaa 112,12,132\n"
"s_add_u32 s47,s47,0x8000\nMvaa 112,8,128\ns_cmp_eq_u32 s47,0x28000\nMvaa 228,16,128\ns_cselect_b32 s94,s96,s95\nMvaa 228,20,132\n"
"s_cselect_b32 s47,0,s47\nMvaa 116,28,132\nMvaa 116,24,128\nv_add_u32 v3,s94,v3\nv_add_u32 v4,s94,v4\nMvaa 232,32,128\nMvaa 232,36,132\nDP 1,20,42,2048\n"
"Mvaa 120,44,132\nMvaa 120,40,128\nv_add_u32 v5,s94,v5\nv_add_u32 v6,s94,v6\nMvaa 236,48,128\nDP 1,20,43,3072\nMvaa 236,52,132\nv_add_u32 v1,0x80000,v1\n"
"Mvaa 124,60,132\nv_add_u32 v7,s94,v7\nv_add_u32 v8,s94,v8\nMvaa 124,56,128\ns_waitcnt vmcnt(30) lgkmcnt(0)\ns_barrier\nMvaa 140,56,136\nRD 128,3,0\n"
"Mvaa 140,60,140\nRD 132,4,0\nMvaa 252,52,140\nMvaa 252,48,136\nMvaa 136,40,136\nMvaa 136,44,140\nMvaa 248,36,140\nMvaa 248,32,136\nMvaa 132,24,136\n"
"Mvaa 132,28,140\nMvaa 244,20,140\nMvaa 244,16,136\nMvaa 128,8,136\nMvaa 128,12,140\nMvaa 240,4,140\nMvaa 240,0,136\nWL 1\nMaaa 240,64,128\nWL 0\n"
"RD 136,3,2048\nMaaa 240,68,132\nRD 140,4,2048\nMvaa 16,76,132\nMvaa 16,72,128\nMaaa 244,80,128\nMaaa 244,84,132\nMvaa 20,92,132\nMvaa 20,88,128\n"
"s_add_u32 m0,s48,s44\nMaaa 248,96,128\nMaaa 248,100,132\nDP 1,20,36,0\nMvaa 24,108,132\nMvaa 24,104,128\nMaaa 252,112,128\nDP 1,20,37,1024\n"
"Maaa 252,116,132\nMvaa 28,124,132\nMvaa 28,120,128\nWL 1\nDP 1,20,38,2048\nMvaa 44,120,136\nWL 0\nRD 128,3,4096\nMvaa 44,124,140\nRD 132,4,4096\n"
"Mvaa 156,116,140\nMvaa 156,112,136\nMvaa 40,104,136\nMvaa 40,108,140\nMvaa 152,100,140\nMvaa 152,96,136\nMvaa 36,88,136\nv_accvgpr_read_b32 v12,a240\n"
"v_accvgpr_read_b32 v13,a241\nMvaa 36,92,140\nDP 1,20,39,3072\nMvaa 148,84,140\ns_add_u32 m0,m0,0x1000\nv_accvgpr_read_b32 v14,a242\n"
"v_accvgpr_read_b32 v15,a243\nMvaa 148,80,136\nCV 12,12,13\nCV 13,14,15\nMvaa 32,72,136\nDP 1,20,40,0\nMvaa 32,76,140\nCV 14,16,17\nCV 15,18,19\n"
"Mvaa 144,68,140\nbuffer_store_dwordx4 v[12:15],v0,s[24:27],s30 offen offset:0 sc1\nMvaa 144,64,136\nWL 1\nDP 1,20,41,1024\nMvaa 160,64,128\nWL 0\n"
"RD 136,3,6144\nCV 156,156,157\nMvaa 160,68,132\ns_mov_b32 s44,s47\nRD 140,4,6144\nCV 157,158,159\nMvaa 48,76,132\ns_add_u32 s47,s47,0x8000\n"
"CV 158,44,45\nCV 159,46,47\nMvaa 48,72,128\ns_cmp_eq_u32 s47,0x28000\nCV 152,152,153\nCV 153,154,155\nMvaa 164,80,128\ns_cselect_b32 s94,s96,s95\n"
"CV 154,40,41\nCV 155,42,43\nMvaa 164,84,132\ns_cselect_b32 s47,0,s47\nv_accvgpr_read_b32 v12,a244\nv_accvgpr_read_b32 v13,a245\nMvaa 52,92,132\n"
"v_accvgpr_read_b32 v14,a246\nv_accvgpr_read_b32 v15,a247\nMvaa 52,88,128\nv_add_u32 v3,s94,v3\nv_add_u32 v4,s94,v4\nMvaa 168,96,128\nCV 12,12,13\n"
"CV 13,14,15\nMvaa 168,100,132\nDP 1,20,42,2048\nMvaa 56,108,132\nCV 14,20,21\nCV 15,22,23\nMvaa 56,104,128\nv_add_u32 v5,s94,v5\nv_add_u32 v6,s94,v6\n"
"Mvaa 172,112,128\nDP 1,20,43,3072\nMvaa 172,116,132\nbuffer_store_dwordx4 v[12:15],v0,s[24:27],s30 offen offset:1024 sc1\nv_add_u32 v1,0x80000,v1\n"
"Mvaa 60,124,132\nv_add_u32 v7,s94,v7\nv_add_u32 v8,s94,v8\nMvaa 60,120,128\ns_waitcnt vmcnt(30) lgkmcnt(0)\ns_barrier\nCV 148,148,149\nMvaa 76,120,136\n"
"RD 128,3,0\nCV 149,150,151\nMvaa 76,124,140\nRD 132,4,0\nCV 150,36,37\nMvaa 188,116,140\nCV 151,38,39\nCV 144,144,145\nMvaa 188,112,136\n"
"CV 145,146,147\nCV 160,160,161\nMvaa 72,104,136\nv_accvgpr_read_b32 v12,a248\nv_accvgpr_read_b32 v13,a249\nMvaa 72,108,140\n"
"v_accvgpr_read_b32 v14,a250\nv_accvgpr_read_b32 v15,a251\nMvaa 184,100,140\nCV 12,12,13\nCV 13,14,15\nMvaa 184,96,136\nCV 14,24,25\nCV 15,26,27\n"
"Mvaa 68,88,136\nbuffer_store_dwordx4 v[12:15],v0,s[24:27],s30 offen offset:2048 sc1\nMvaa 68,92,140\nCV 146,32,33\nCV 147,34,35\nMvaa 180,84,140\n"
"CV 161,162,163\nCV 164,164,165\nMvaa 180,80,136\nCV 162,48,49\nCV 163,50,51\nMvaa 64,72,136\nCV 165,166,167\nCV 168,168,169\nMvaa 64,76,140\n"
"CV 166,52,53\nCV 167,54,55\nMvaa 176,68,140\nCV 169,170,171\nCV 172,172,173\nMvaa 176,64,136\nWL 1\nv_accvgpr_read_b32 v12,a252\n"
"v_accvgpr_read_b32 v13,a253\nMvaa 192,64,128\nWL 0\nRD 136,3,2048\nv_accvgpr_read_b32 v14,a254\nMvaa 192,68,132\nRD 140,4,2048\n"
"v_accvgpr_read_b32 v15,a255\nMvaa 80,76,132\nCV 12,12,13\nCV 13,14,15\nMvaa 80,72,128\nCV 14,28,29\nCV 15,30,31\nMvaa 196,80,128\n"
"buffer_store_dwordx4 v[12:15],v0,s[24:27],s30 offen offset:3072 sc1\nMvaa 196,84,132\ns_add_u32 s30,s30,s28\nCV 170,56,57\nCV 171,58,59\n"
"Mvaa 84,92,132\nCV 173,174,175\nCV 188,188,189\nMvaa 84,88,128\ns_add_u32 m0,s48,s44\nCV 174,60,61\nCV 175,62,63\nMvaa 200,96,128\nCV 189,190,191\n"
"CV 184,184,185\nMvaa 200,100,132\nDP 1,20,36,0\nMvaa 88,108,132\nCV 190,76,77\nCV 191,78,79\nMvaa 88,104,128\nCV 185,186,187\nCV 180,180,181\n"
"Mvaa 204,112,128\nDP 1,20,37,1024\nMvaa 204,116,132\nbuffer_store_dwordx4 v[144:147],v0,s[24:27],s30 offen offset:0 sc1\nMvaa 92,124,132\nCV 186,72,73\n"
"CV 187,74,75\nMvaa 92,120,128\nWL 1\nDP 1,20,38,2048\nMvaa 108,120,136\nWL 0\nRD 128,3,4096\nCV 181,182,183\nMvaa 108,124,140\nRD 132,4,4096\n"
"CV 182,68,69\nMvaa 220,116,140\nCV 183,70,71\nCV 176,176,177\nMvaa 220,112,136\nCV 177,178,179\nCV 192,192,193\nMvaa 104,104,136\nCV 178,64,65\n"
"CV 179,66,67\nMvaa 104,108,140\nCV 193,194,195\nCV 196,196,197\nMvaa 216,100,140\n"
"buffer_store_dwordx4 v[148:151],v0,s[24:27],s30 offen offset:1024 sc1\nMvaa 216,96,136\nCV 194,80,81\nCV 195,82,83\nMvaa 100,88,136\nCV 197,198,199\n"
"CV 200,200,201\nMvaa 100,92,140\nDP 1,20,39,3072\nMvaa 212,84,140\ns_add_u32 m0,m0,0x1000\nCV 198,84,85\nCV 199,86,87\nMvaa 212,80,136\nCV 201,202,203\n"
"CV 204,204,205\nMvaa 96,72,136\nDP 1,20,40,0\nMvaa 96,76,140\nCV 202,88,89\nCV 203,90,91\nMvaa 208,68,140\nCV 205,206,207\nCV 220,220,221\n"
"Mvaa 208,64,136\nWL 1\nDP 1,20,41,1024\nMvaa 224,64,128\nWL 0\nRD 136,3,6144\nbuffer_store_dwordx4 v[152:155],v0,s[24:27],s30 offen offset:2048 sc1\n"
"Mvaa 224,68,132\ns_mov_b32 s44,s47\nRD 140,4,6144\nCV 206,92,93\nMvaa 112,76,132\ns_add_u32 s47,s47,0x8000\nCV 207,94,95\nCV 221,222,223\n"
"Mvaa 112,72,128\ns_cmp_eq_u32 s47,0x28000\nCV 222,108,109\nCV 223,110,111\nMvaa 228,80,128\ns_cselect_b32 s47,0,s47\nCV 216,216,217\nCV 217,218,219\n"
"Mvaa 228,84,132\nCV 218,104,105\nCV 219,106,107\nMvaa 116,92,132\nCV 212,212,213\nCV 213,214,215\nMvaa 116,88,128\nv_add_u32 v5,s47,v10\n"
"v_xor_b32 v6,32,v5\nMvaa 232,96,128\nCV 214,100,101\nCV 215,102,103\nMvaa 232,100,132\nDP 1,20,42,2048\nMvaa 120,108,132\n"
"buffer_store_dwordx4 v[156:159],v0,s[24:27],s30 offen offset:3072 sc1\nMvaa 120,104,128\ns_add_u32 s30,s30,s28\nv_xor_b32 v7,64,v5\nv_xor_b32 v8,96,v5\n"
"Mvaa 236,112,128\nDP 1,20,43,3072\nMvaa 236,116,132\nv_add_u32 v1,0x80000,v1\nCV 208,208,209\nMvaa 124,124,132\nCV 209,210,211\nCV 224,224,225\n"
"Mvaa 124,120,128\ns_waitcnt vmcnt(34) lgkmcnt(0)\ns_barrier\nCV 210,96,97\nMvaa 140,120,136\nTR 0,5,0\nCV 211,98,99\nMvaa 140,124,140\nTR 2,5,1024\n"
"CV 225,226,227\nMvaa 252,116,140\nTR 4,5,4096\nCV 226,112,113\nMvaa 252,112,136\nTR 6,5,5120\nCV 227,114,115\nMvaa 136,104,136\nTR 8,6,0\n"
"CV 228,228,229\nMvaa 136,108,140\nTR 10,6,1024\nCV 229,230,231\nMvaa 248,100,140\nTR 12,6,4096\nCV 230,116,117\nMvaa 248,96,136\nTR 14,6,5120\n"
"CV 231,118,119\nMvaa 132,88,136\nTR 16,7,0\nCV 232,232,233\nMvaa 132,92,140\nTR 18,7,1024\nCV 233,234,235\nMvaa 244,84,140\nTR 20,7,4096\n"
"CV 234,120,121\nMvaa 244,80,136\nTR 22,7,5120\nCV 235,122,123\nMvaa 128,72,136\nTR 24,8,0\nCV 236,236,237\nMvaa 128,76,140\nTR 26,8,1024\n"
"CV 237,238,239\nMvaa 240,68,140\nTR 28,8,4096\nCV 238,124,125\nMvaa 240,64,136\nTR 30,8,5120\nCV 239,126,127\nCV 252,252,253\nCV 253,254,255\n"
"CV 254,140,141\nCV 255,142,143\nCV 248,248,249\nCV 249,250,251\nCV 250,136,137\nCV 251,138,139\nCV 244,244,245\nCV 245,246,247\nCV 246,132,133\n"
"CV 247,134,135\nCV 240,240,241\nCV 241,242,243\nCV 242,128,129\nCV 243,130,131\ns_branch fused_op_qproj\nfused_op_sp2:\ns_sub_u32 s14,s14,1\n"
"s_cmp_eq_u32 s14,0\ns_cbranch_scc1 fused_op_giveup0\ns_sleep 4\nbuffer_load_dword v14,v15,s[24:27],s62 offen sc1\ns_waitcnt vmcnt(0)\n"
"v_cmp_ne_u32 vcc,s65,v14\ns_nop 1\ns_cbranch_vccnz fused_op_sp2\ns_branch fused_op_spb3\nfused_op_sp5:\ns_sub_u32 s14,s14,1\ns_cmp_eq_u32 s14,0\n"
"s_cbranch_scc1 fused_op_giveup0\ns_sleep 4\nbuffer_load_dword v14,v15,s[24:27],s62 offen sc1\ns_waitcnt vmcnt(0)\nv_cmp_ne_u32 vcc,s65,v14\ns_nop 1\n"
"s_cbranch_vccnz fused_op_sp5\ns_branch fused_op_spb6\nfused_op_giveup0:\ns_endpgm\nfused_op_kvh1:\nZaaa 240,128,0\ns_bfe_u32 s45,s11,0x10003\n"
"RD 136,3,2048\nMaaa 240,132,4\ns_add_u32 m0,s48,65536\nRD 140,4,2048\nZaaa 248,132,12\ns_add_u32 s56,s56,s45\nTR 64,5,0\nMaaa 248,128,8\n"
"s_bfe_u32 s57,s11,0x30004\nDP 1,20,36,0\nZvaa 144,128,16\ns_mul_i32 s68,s56,8\nTR 66,5,1024\nMvaa 144,132,20\ns_mul_i32 s49,s57,1\nTR 68,5,4096\n"
"Zvaa 152,132,28\ns_add_u32 s68,s68,s49\nTR 70,5,5120\nMvaa 152,128,24\ns_lshl_b32 s45,s57,2\nDP 2,20,37,1024\nZvaa 160,128,32\n"
"s_lshr_b32 s60,0x2468ace,s45\nTR 72,6,0\nMvaa 160,132,36\ns_and_b32 s60,s60,15\nTR 74,6,1024\nZvaa 168,132,44\ns_lshr_b32 s46,0x55558643,s45\n"
"DP 1,20,38,2048\nMvaa 168,128,40\ns_and_b32 s46,s46,15\nTR 76,6,4096\nZvaa 176,128,48\ns_mul_i32 s5,s56,4096\nTR 78,6,5120\nMvaa 176,132,52\n"
"s_mul_i32 s49,s57,512\nDP 2,20,39,3072\nZvaa 184,132,60\ns_add_u32 s5,s5,s49\nTR 80,7,0\nMvaa 184,128,56\nWL 10\ns_mul_i32 s49,s10,128\nTR 82,7,1024\n"
"Zvaa 72,136,56\nWL 10\ns_add_u32 m0,m0,0x1000\nRD 128,3,4096\nMvaa 72,140,60\ns_add_u32 s5,s5,s49\nRD 132,4,4096\nZvaa 64,140,52\n"
"s_add_u32 s5,s5,0x90000\nDP 1,20,40,0\nMvaa 64,136,48\ns_sub_u32 s45,15,s57\nTR 84,7,4096\nZvaa 56,136,40\ns_cmp_eq_u32 s58,0\n"
"s_cselect_b32 s59,s45,s57\ns_cselect_b32 s31,s60,0\ns_cselect_b32 s46,5,s46\ns_cselect_b32 s5,s5,0x7ff00000\ns_cselect_b32 s52,s82,s78\n"
"s_cselect_b32 s53,s83,s79\nTR 86,7,5120\nMvaa 56,140,44\ns_lshl_b32 s61,s59,1\nDP 2,20,41,1024\nZvaa 48,140,36\ns_add_u32 s61,s61,2\nTR 88,8,0\n"
"Mvaa 48,136,32\ns_sub_u32 s61,s61,s31\nTR 90,8,1024\nZvaa 40,136,24\ns_mov_b32 s69,0\nDP 1,20,42,2048\nMvaa 40,140,28\ns_mul_i32 s66,s56,2097152\n"
"TR 92,8,4096\nZvaa 32,140,20\ns_mul_i32 s49,s10,8192\nTR 94,8,5120\nMvaa 32,136,16\ns_add_u32 s66,s66,s49\nDP 2,20,43,3072\nZvaa 24,136,8\n"
"s_add_u32 m0,s48,98304\nv_add_u32 v1,s34,v1\nv_add_u32 v2,s34,v2\nMvaa 24,140,12\ns_add_u32 s66,s66,0x100000\nZvaa 16,140,4\ns_mul_i32 s6,s57,-131072\n"
"DP 1,20,36,0\nMvaa 16,136,0\nWL 7\ns_mul_i32 s49,s60,-65536\nZaaa 244,128,0\nWL 6\ns_add_u32 s6,s6,s49\nRD 136,3,6144\nMaaa 244,132,4\n"
"s_mov_b32 s44,s47\nRD 140,4,6144\nZaaa 252,132,12\ns_add_u32 s47,s47,0x8000\nMaaa 252,128,8\ns_cmp_eq_u32 s47,0x28000\nZvaa 148,128,16\n"
"s_cselect_b32 s94,s96,s95\nMvaa 148,132,20\ns_cselect_b32 s47,0,s47\nZvaa 156,132,28\ns_add_u32 s6,s6,0x200000\nDP 2,20,37,1024\nMvaa 156,128,24\n"
"s_add_u32 s6,s6,s66\nv_add_u32 v3,s94,v3\nv_add_u32 v4,s94,v4\nZvaa 164,128,32\ns_mov_b32 s64,0\nMvaa 164,132,36\ns_mov_b32 s67,0x41800000\n"
"DP 1,20,38,2048\nZvaa 172,132,44\ns_and_b32 s98,s11,127\nMvaa 172,128,40\ns_mul_i32 s45,s98,14336\nv_add_u32 v5,s94,v5\nv_add_u32 v6,s94,v6\n"
"Zvaa 180,128,48\ns_mul_i32 s49,s10,256\nDP 2,20,39,3072\nMvaa 180,132,52\ns_add_u32 s45,s45,s49\nZvaa 188,132,60\ns_add_u32 s12,s52,s45\n"
"s_addc_u32 s13,s53,0\nv_add_u32 v7,s94,v7\nv_add_u32 v8,s94,v8\nMvaa 188,128,56\ns_waitcnt vmcnt(12) lgkmcnt(0)\ns_barrier\nZvaa 76,136,56\n"
"s_and_b32 s13,s13,0xffff\nRD 128,3,0\nMvaa 76,140,60\ns_sub_u32 s14,1835008,s45\nRD 132,4,0\nZvaa 68,140,52\ns_mov_b32 s15,0x20000\nMvaa 68,136,48\n"
"s_mul_i32 s98,s56,131072\nZvaa 60,136,40\ns_mul_i32 s49,s10,32768\nMvaa 60,140,44\ns_add_u32 s98,s98,s49\nZvaa 52,140,36\ns_add_u32 s98,s98,0x4300000\n"
"Mvaa 52,136,32\ns_sub_u32 s99,s98,16384\nZvaa 44,136,24\ns_cmp_eq_u32 s98,70254592\ns_cselect_b32 s99,s98,s99\nMvaa 44,140,28\n"
"s_mul_i32 s30,s71,2097152\nZvaa 36,140,20\ns_mul_i32 s49,s70,262144\nMvaa 36,136,16\ns_add_u32 s30,s30,s49\nZvaa 28,136,8\ns_add_u32 s30,s30,0x100000\n"
"Mvaa 28,140,12\ns_and_b32 s45,s70,3\nZvaa 20,140,4\ns_lshl_b32 s45,s45,1\nMvaa 20,136,0\nWL 1\ns_lshr_b32 s70,s70,2\nZvaa 192,128,0\nWL 0\n"
"s_add_u32 s70,s70,s45\nRD 136,3,2048\nMvaa 192,132,4\ns_add_u32 m0,m0,0x1000\nRD 140,4,2048\nZvaa 200,132,12\ns_mul_i32 s7,s71,8192\nTR 96,5,0\n"
"Mvaa 200,128,8\ns_mul_i32 s49,s70,1024\nDP 1,20,40,0\nZvaa 208,128,16\ns_add_u32 s7,s7,s49\nTR 98,5,1024\nMvaa 208,132,20\ns_mul_i32 s49,s97,512\n"
"TR 100,5,4096\nZvaa 216,132,28\ns_add_u32 s7,s7,s49\nTR 102,5,5120\nMvaa 216,128,24\ns_mul_i32 s49,s10,128\nDP 2,20,41,1024\nZvaa 224,128,32\n"
"s_add_u32 s7,s7,s49\nTR 104,6,0\nMvaa 224,132,36\ns_add_u32 s7,s7,0x10000\nTR 106,6,1024\nZvaa 232,132,44\ns_lshr_b32 s45,s10,1\nDP 1,20,42,2048\n"
"Mvaa 232,128,40\ns_lshl_b32 s45,s45,17\nTR 108,6,4096\nZvaa 240,128,48\ns_add_u32 s30,s30,s45\nTR 110,6,5120\nMvaa 240,132,52\ns_and_b32 s45,s10,1\n"
"DP 2,20,43,3072\nZvaa 248,132,60\ns_lshl_b32 s49,s45,12\nv_add_u32 v1,s35,v1\nv_add_u32 v2,s35,v2\nMvaa 248,128,56\nWL 9\ns_lshl_b32 s45,s45,14\n"
"TR 112,7,0\nZvaa 136,136,56\nWL 9\ns_add_u32 m0,s48,131072\nRD 128,3,4096\nMvaa 136,140,60\ns_add_u32 s45,s45,0x8000\nRD 132,4,4096\nZvaa 128,140,52\n"
"s_mov_b32 s28,4096\nDP 1,20,36,0\nMvaa 128,136,48\ns_cmp_eq_u32 s97,0\ns_cselect_b32 s45,s49,s45\ns_cselect_b32 s28,8192,s28\nTR 114,7,1024\n"
"Zvaa 120,136,40\ns_add_u32 s30,s30,s45\nTR 116,7,4096\nMvaa 120,140,44\ns_mul_i32 s29,s28,3\nDP 2,20,37,1024\nZvaa 112,140,36\ns_sub_u32 s29,65536,s29\n"
"TR 118,7,5120\nMvaa 112,136,32\nTR 120,8,0\nZvaa 104,136,24\nDP 1,20,38,2048\nMvaa 104,140,28\nTR 122,8,1024\nZvaa 96,140,20\nTR 124,8,4096\n"
"Mvaa 96,136,16\nDP 2,20,39,3072\nZvaa 88,136,8\ns_add_u32 m0,m0,0x1000\nTR 126,8,5120\nMvaa 88,140,12\nZvaa 80,140,4\nDP 1,20,40,0\nMvaa 80,136,0\n"
"WL 8\nZvaa 196,128,0\nWL 7\nRD 136,3,6144\nMvaa 196,132,4\ns_mov_b32 s44,s47\nRD 140,4,6144\nZvaa 204,132,12\ns_add_u32 s47,s47,0x8000\nMvaa 204,128,8\n"
"s_cmp_eq_u32 s47,0x28000\nZvaa 212,128,16\ns_cselect_b32 s94,s96,s95\nMvaa 212,132,20\ns_cselect_b32 s47,0,s47\nZvaa 220,132,28\nDP 2,20,41,1024\n"
"Mvaa 220,128,24\nv_add_u32 v3,s94,v3\nv_add_u32 v4,s94,v4\nZvaa 228,128,32\nMvaa 228,132,36\nDP 1,20,42,2048\nZvaa 236,132,44\nMvaa 236,128,40\n"
"v_add_u32 v5,s94,v5\nv_add_u32 v6,s94,v6\nZvaa 244,128,48\nDP 2,20,43,3072\nMvaa 244,132,52\nv_add_u32 v1,s34,v1\nv_add_u32 v2,s34,v2\nZvaa 252,132,60\n"
"v_add_u32 v7,s94,v7\nv_add_u32 v8,s94,v8\nMvaa 252,128,56\ns_waitcnt vmcnt(16) lgkmcnt(0)\ns_barrier\nZvaa 140,136,56\nRD 128,3,0\nMvaa 140,140,60\n"
"RD 132,4,0\nZvaa 132,140,52\nMvaa 132,136,48\nZvaa 124,136,40\nMvaa 124,140,44\nZvaa 116,140,36\nMvaa 116,136,32\nZvaa 108,136,24\nMvaa 108,140,28\n"
"Zvaa 100,140,20\nMvaa 100,136,16\nZvaa 92,136,8\nMvaa 92,140,12\nZvaa 84,140,4\nMvaa 84,136,0\nWL 1\nMaaa 240,128,64\nWL 0\nRD 136,3,2048\n"
"Maaa 240,132,68\ns_add_u32 m0,s48,0\nRD 140,4,2048\nMaaa 248,132,76\nTR 0,5,0\nMaaa 248,128,72\nDP 1,20,36,0\nMvaa 144,128,80\nTR 2,5,1024\n"
"Mvaa 144,132,84\nTR 4,5,4096\nMvaa 152,132,92\nTR 6,5,5120\nMvaa 152,128,88\nDP 2,20,37,1024\nMvaa 160,128,96\nTR 8,6,0\nMvaa 160,132,100\n"
"TR 10,6,1024\nMvaa 168,132,108\nDP 1,20,38,2048\nMvaa 168,128,104\nTR 12,6,4096\nMvaa 176,128,112\nTR 14,6,5120\nMvaa 176,132,116\nDP 2,20,39,3072\n"
"Mvaa 184,132,124\nTR 16,7,0\nMvaa 184,128,120\nWL 10\nTR 18,7,1024\nMvaa 72,136,120\nWL 10\ns_add_u32 m0,m0,0x1000\nRD 128,3,4096\nMvaa 72,140,124\n"
"RD 132,4,4096\nMvaa 64,140,116\nDP 1,20,40,0\nMvaa 64,136,112\nTR 20,7,4096\nMvaa 56,136,104\nTR 22,7,5120\nMvaa 56,140,108\nDP 2,20,41,1024\n"
"Mvaa 48,140,100\nTR 24,8,0\nMvaa 48,136,96\nTR 26,8,1024\nMvaa 40,136,88\nDP 1,20,42,2048\nMvaa 40,140,92\nTR 28,8,4096\nMvaa 32,140,84\nTR 30,8,5120\n"
"Mvaa 32,136,80\nDP 2,20,43,3072\nMvaa 24,136,72\ns_add_u32 m0,s48,32768\nv_add_u32 v1,s35,v1\nv_add_u32 v2,s35,v2\nMvaa 24,140,76\nMvaa 16,140,68\n"
"DP 1,20,36,0\nMvaa 16,136,64\nWL 7\nMaaa 244,128,64\nWL 6\nRD 136,3,6144\nMaaa 244,132,68\ns_mov_b32 s44,s47\nRD 140,4,6144\nMaaa 252,132,76\n"
"s_add_u32 s47,s47,0x8000\nMaaa 252,128,72\ns_cmp_eq_u32 s47,0x28000\nMvaa 148,128,80\ns_cselect_b32 s94,s96,s95\nMvaa 148,132,84\n"
"s_cselect_b32 s47,0,s47\nMvaa 156,132,92\nDP 2,20,37,1024\nMvaa 156,128,88\nv_add_u32 v3,s94,v3\nv_add_u32 v4,s94,v4\nMvaa 164,128,96\n"
"Mvaa 164,132,100\nDP 1,20,38,2048\nMvaa 172,132,108\nMvaa 172,128,104\nv_add_u32 v5,s94,v5\nv_add_u32 v6,s94,v6\nMvaa 180,128,112\nDP 2,20,39,3072\n"
"Mvaa 180,132,116\nMvaa 188,132,124\nv_add_u32 v7,s94,v7\nv_add_u32 v8,s94,v8\nMvaa 188,128,120\ns_waitcnt vmcnt(20) lgkmcnt(0)\ns_barrier\n"
"Mvaa 76,136,120\nRD 128,3,0\nMvaa 76,140,124\nRD 132,4,0\nMvaa 68,140,116\nMvaa 68,136,112\nMvaa 60,136,104\n"
"buffer_load_dword a239,off,s[24:27],0 offset:0 sc1\nMvaa 60,140,108\nMvaa 52,140,100\nMvaa 52,136,96\nMvaa 44,136,88\n"
"buffer_load_dwordx4 a[144:147],v9,s[12:15],0 offen offset:0\nMvaa 44,140,92\nMvaa 36,140,84\nMvaa 36,136,80\nMvaa 28,136,72\nMvaa 28,140,76\n"
"Mvaa 20,140,68\nMvaa 20,136,64\nWL 1\nMvaa 192,128,64\nWL 0\nRD 136,3,2048\nMvaa 192,132,68\ns_add_u32 m0,m0,0x1000\nRD 140,4,2048\nMvaa 200,132,76\n"
"TR 32,5,0\nMvaa 200,128,72\nDP 1,20,40,0\nMvaa 208,128,80\nTR 34,5,1024\nMvaa 208,132,84\nTR 36,5,4096\nMvaa 216,132,92\nTR 38,5,5120\nMvaa 216,128,88\n"
"DP 2,20,41,1024\nMvaa 224,128,96\nTR 40,6,0\nMvaa 224,132,100\nTR 42,6,1024\nMvaa 232,132,108\nDP 1,20,42,2048\nMvaa 232,128,104\nTR 44,6,4096\n"
"Mvaa 240,128,112\nTR 46,6,5120\nMvaa 240,132,116\nDP 2,20,43,3072\nMvaa 248,132,124\nv_add_u32 v1,s34,v1\nv_add_u32 v2,s34,v2\nMvaa 248,128,120\nWL 9\n"
"TR 48,7,0\nMvaa 136,136,120\nWL 9\ns_add_u32 m0,s48,65536\nRD 128,3,4096\nMvaa 136,140,124\nRD 132,4,4096\nMvaa 128,140,116\nDP 1,20,36,0\n"
"Mvaa 128,136,112\nTR 50,7,1024\nMvaa 120,136,104\nTR 52,7,4096\nMvaa 120,140,108\nDP 2,20,37,1024\nMvaa 112,140,100\nTR 54,7,5120\nMvaa 112,136,96\n"
"TR 56,8,0\nMvaa 104,136,88\nDP 1,20,38,2048\nMvaa 104,140,92\nTR 58,8,1024\nMvaa 96,140,84\nTR 60,8,4096\nglobal_load_dword a238,v13,s[8:9]\n"
"Mvaa 96,136,80\ns_add_u32 s8,s8,s18\ns_addc_u32 s9,s9,0\nDP 2,20,39,3072\nMvaa 88,136,72\ns_add_u32 m0,m0,0x1000\nTR 62,8,5120\nMvaa 88,140,76\n"
"Mvaa 80,140,68\nDP 1,20,40,0\nMvaa 80,136,64\nWL 8\nMvaa 196,128,64\nWL 7\nRD 136,3,6144\nMvaa 196,132,68\ns_mov_b32 s44,s47\nRD 140,4,6144\n"
"Mvaa 204,132,76\ns_add_u32 s47,s47,0x8000\nMvaa 204,128,72\ns_cmp_eq_u32 s47,0x28000\nMvaa 212,128,80\ns_cselect_b32 s94,s96,s95\nMvaa 212,132,84\n"
"s_cselect_b32 s47,0,s47\nMvaa 220,132,92\nDP 2,20,41,1024\nMvaa 220,128,88\nv_add_u32 v3,s94,v3\nv_add_u32 v4,s94,v4\nMvaa 228,128,96\n"
"Mvaa 228,132,100\nDP 1,20,42,2048\nMvaa 236,132,108\nMvaa 236,128,104\nv_add_u32 v5,s94,v5\nv_add_u32 v6,s94,v6\nMvaa 244,128,112\nDP 2,20,43,3072\n"
"Mvaa 244,132,116\nv_add_u32 v1,s35,v1\nv_add_u32 v2,s35,v2\nMvaa 252,132,124\nv_add_u32 v7,s94,v7\nv_add_u32 v8,s94,v8\nMvaa 252,128,120\n"
"s_waitcnt vmcnt(27) lgkmcnt(0)\ns_barrier\nMvaa 140,136,120\nRD 128,3,0\nMvaa 140,140,124\nRD 132,4,0\nMvaa 132,140,116\nMvaa 132,136,112\n"
"Mvaa 124,136,104\nMvaa 124,140,108\nMvaa 116,140,100\nMvaa 116,136,96\nMvaa 108,136,88\nbuffer_load_dwordx4 a[148:151],v9,s[12:15],0 offen offset:64\n"
"Mvaa 108,140,92\nMvaa 100,140,84\nMvaa 100,136,80\nMvaa 92,136,72\nMvaa 92,140,76\nMvaa 84,140,68\nMvaa 84,136,64\nWL 1\nMaaa 240,128,0\nWL 0\n"
"RD 136,3,2048\nMaaa 240,132,4\nRD 140,4,2048\nMaaa 248,132,12\nTR 64,5,0\nMaaa 248,128,8\nTR 66,5,1024\nMvaa 144,128,16\nTR 68,5,4096\nMvaa 144,132,20\n"
"TR 70,5,5120\nMvaa 152,132,28\nTR 72,6,0\nMvaa 152,128,24\ns_add_u32 m0,s48,s44\nTR 74,6,1024\nMvaa 160,128,32\nTR 76,6,4096\nMvaa 160,132,36\n"
"DP 1,20,36,0\nMvaa 168,132,44\nTR 78,6,5120\nMvaa 168,128,40\nTR 80,7,0\nMvaa 176,128,48\nDP 2,20,37,1024\nMvaa 176,132,52\nTR 82,7,1024\n"
"Mvaa 184,132,60\nTR 84,7,4096\nMvaa 184,128,56\nWL 12\nDP 1,20,38,2048\nMvaa 72,136,56\nWL 11\nRD 128,3,4096\nMvaa 72,140,60\nRD 132,4,4096\n"
"Mvaa 64,140,52\nTR 86,7,5120\nMvaa 64,136,48\nTR 88,8,0\nMvaa 56,136,40\nTR 90,8,1024\nMvaa 56,140,44\nTR 92,8,4096\nMvaa 48,140,36\nTR 94,8,5120\n"
"Mvaa 48,136,32\nMvaa 40,136,24\nMvaa 40,140,28\nDP 2,20,39,3072\nMvaa 32,140,20\ns_add_u32 m0,m0,0x1000\nMvaa 32,136,16\nMvaa 24,136,8\nDP 1,20,40,0\n"
"Mvaa 24,140,12\nMvaa 16,140,4\nbuffer_load_dwordx4 a[152:155],v9,s[12:15],0 offen offset:128\nMvaa 16,136,0\nWL 6\nDP 2,20,41,1024\nMaaa 244,128,0\n"
"WL 5\nRD 136,3,6144\nMaaa 244,132,4\ns_mov_b32 s44,s47\nRD 140,4,6144\nMaaa 252,132,12\ns_add_u32 s47,s47,0x8000\nMaaa 252,128,8\n"
"s_cmp_eq_u32 s47,0x28000\nMvaa 148,128,16\ns_cselect_b32 s94,s96,s95\nMvaa 148,132,20\ns_cselect_b32 s47,0,s47\nMvaa 156,132,28\nMvaa 156,128,24\n"
"v_add_u32 v3,s94,v3\nv_add_u32 v4,s94,v4\nMvaa 164,128,32\nMvaa 164,132,36\nDP 1,20,42,2048\nMvaa 172,132,44\nMvaa 172,128,40\nv_add_u32 v5,s94,v5\n"
"v_add_u32 v6,s94,v6\nMvaa 180,128,48\nDP 2,20,43,3072\nMvaa 180,132,52\nv_add_u32 v1,s34,v1\nv_add_u32 v2,s34,v2\nMvaa 188,132,60\nv_add_u32 v7,s94,v7\n"
"v_add_u32 v8,s94,v8\nMvaa 188,128,56\ns_waitcnt vmcnt(29) lgkmcnt(0)\ns_barrier\nMvaa 76,136,56\ns_mul_hi_u32 s62,s11,0xc30c30d\nRD 128,3,0\n"
"Mvaa 76,140,60\ns_mul_i32 s63,s62,21\nRD 132,4,0\nv_lshrrev_b32 v15,7,v0\nMvaa 68,140,52\ns_sub_u32 s63,s11,s63\nv_and_b32 v11,6,v15\nMvaa 68,136,48\n"
"s_mul_hi_u32 s70,s63,0x24924925\nv_lshlrev_b32 v11,4,v11\nMvaa 60,136,40\ns_mul_i32 s71,s70,7\nv_xor_b32 v11,v11,v0\nMvaa 60,140,44\n"
"s_sub_u32 s71,s63,s71\nv_and_b32 v11,0x70,v11\nMvaa 52,140,36\ns_mul_i32 s45,s71,172032\nv_and_b32 v12,4,v15\nMvaa 52,136,32\ns_mul_i32 s49,s10,43008\n"
"v_add_u32 v15,v15,v12\nMvaa 44,136,24\ns_add_u32 s45,s45,s49\nv_mul_u32_u24 v15,2688,v15\nMvaa 44,140,28\ns_mul_i32 s49,s70,896\nv_add_u32 v11,v11,v15\n"
"Mvaa 36,140,20\ns_add_u32 s45,s45,s49\nMvaa 36,136,16\ns_mul_i32 s49,s62,112\nMvaa 28,136,8\ns_add_u32 s45,s45,s49\nMvaa 28,140,12\n"
"s_mul_i32 s63,s62,172032\nMvaa 20,140,4\ns_mul_i32 s49,s71,24576\nMvaa 20,136,0\nWL 1\ns_add_u32 s63,s63,s49\nMvaa 192,128,0\nWL 0\n"
"s_mul_i32 s49,s70,8192\nRD 136,3,2048\nMvaa 192,132,4\ns_add_u32 s63,s63,s49\nRD 140,4,2048\nMvaa 200,132,12\ns_mul_i32 s49,s10,2048\nTR 96,5,0\n"
"Mvaa 200,128,8\ns_add_u32 s63,s63,s49\nTR 98,5,1024\nMvaa 208,128,16\ns_add_u32 s63,s63,0x4700000\nTR 100,5,4096\nMvaa 208,132,20\n"
"s_cmp_ge_u32 s11,168\ns_cselect_b32 s45,0x7ff00000,s45\ns_cselect_b32 s63,0x7ff00000,s63\nTR 102,5,5120\nMvaa 216,132,28\ns_add_u32 s62,s45,10752\n"
"TR 104,6,0\nMvaa 216,128,24\ns_add_u32 m0,s48,s44\nTR 106,6,1024\nMvaa 224,128,32\nTR 108,6,4096\nMvaa 224,132,36\nDP 1,20,36,0\nMvaa 232,132,44\n"
"TR 110,6,5120\nMvaa 232,128,40\nTR 112,7,0\nMvaa 240,128,48\nDP 2,20,37,1024\nMvaa 240,132,52\nTR 114,7,1024\nMvaa 248,132,60\nTR 116,7,4096\n"
"Mvaa 248,128,56\nWL 12\nDP 1,20,38,2048\nMvaa 136,136,56\nWL 11\nRD 128,3,4096\nMvaa 136,140,60\nRD 132,4,4096\nMvaa 128,140,52\nTR 118,7,5120\n"
"Mvaa 128,136,48\nTR 120,8,0\nMvaa 120,136,40\nTR 122,8,1024\nMvaa 120,140,44\nTR 124,8,4096\nMvaa 112,140,36\nTR 126,8,5120\nMvaa 112,136,32\n"
"Mvaa 104,136,24\nMvaa 104,140,28\nDP 2,20,39,3072\nMvaa 96,140,20\ns_add_u32 m0,m0,0x1000\nMvaa 96,136,16\nMvaa 88,136,8\nDP 1,20,40,0\nMvaa 88,140,12\n"
"Mvaa 80,140,4\nbuffer_load_dwordx4 a[156:159],v9,s[12:15],0 offen offset:192\nMvaa 80,136,0\nWL 6\nDP 2,20,41,1024\nMvaa 196,128,0\nWL 5\n"
"RD 136,3,6144\nMvaa 196,132,4\ns_mov_b32 s44,s47\nRD 140,4,6144\nMvaa 204,132,12\ns_add_u32 s47,s47,0x8000\nMvaa 204,128,8\ns_cmp_eq_u32 s47,0x28000\n"
"Mvaa 212,128,16\ns_cselect_b32 s94,s96,s95\nMvaa 212,132,20\ns_cselect_b32 s47,0,s47\nMvaa 220,132,28\nMvaa 220,128,24\ns_mov_b32 s12,s84\n"
"v_add_u32 v3,s94,v3\nv_add_u32 v4,s94,v4\nMvaa 228,128,32\ns_and_b32 s13,s85,0xffff\nMvaa 228,132,36\ns_mov_b32 s14,1204224\nDP 1,20,42,2048\n"
"Mvaa 236,132,44\nMvaa 236,128,40\nv_add_u32 v5,s94,v5\nv_add_u32 v6,s94,v6\nMvaa 244,128,48\nDP 2,20,43,3072\nMvaa 244,132,52\nv_add_u32 v1,s35,v1\n"
"v_add_u32 v2,s35,v2\nMvaa 252,132,60\nv_add_u32 v7,s94,v7\nv_add_u32 v8,s94,v8\nMvaa 252,128,56\ns_waitcnt vmcnt(28) lgkmcnt(0)\ns_barrier\n"
"Mvaa 140,136,56\nRD 128,3,0\nMvaa 140,140,60\nRD 132,4,0\nMvaa 132,140,52\nMvaa 132,136,48\nMvaa 124,136,40\nMvaa 124,140,44\nMvaa 116,140,36\n"
"buffer_load_dwordx4 a[160:163],v11,s[12:15],s45 offen\nMvaa 116,136,32\nMvaa 108,136,24\nMvaa 108,140,28\nMvaa 100,140,20\nMvaa 100,136,16\n"
"Mvaa 92,136,8\nMvaa 92,140,12\nMvaa 84,140,4\nMvaa 84,136,0\nWL 1\nMaaa 240,128,64\nWL 0\nRD 136,3,2048\nMaaa 240,132,68\nRD 140,4,2048\n"
"Maaa 248,132,76\nTR 0,5,0\nMaaa 248,128,72\nTR 2,5,1024\nMvaa 144,128,80\nTR 4,5,4096\nMvaa 144,132,84\nTR 6,5,5120\nMvaa 152,132,92\nTR 8,6,0\n"
"Mvaa 152,128,88\ns_add_u32 m0,s48,s44\nTR 10,6,1024\nMvaa 160,128,96\nTR 12,6,4096\nMvaa 160,132,100\nDP 1,20,36,0\nMvaa 168,132,108\nTR 14,6,5120\n"
"Mvaa 168,128,104\nTR 16,7,0\nMvaa 176,128,112\nDP 2,20,37,1024\nMvaa 176,132,116\nTR 18,7,1024\nMvaa 184,132,124\nTR 20,7,4096\nMvaa 184,128,120\n"
"WL 12\nDP 1,20,38,2048\nMvaa 72,136,120\nWL 11\nRD 128,3,4096\nMvaa 72,140,124\nRD 132,4,4096\nMvaa 64,140,116\nTR 22,7,5120\nMvaa 64,136,112\n"
"TR 24,8,0\nMvaa 56,136,104\nTR 26,8,1024\nMvaa 56,140,108\nTR 28,8,4096\nMvaa 48,140,100\nTR 30,8,5120\nMvaa 48,136,96\nMvaa 40,136,88\nMvaa 40,140,92\n"
"DP 2,20,39,3072\nMvaa 32,140,84\ns_add_u32 m0,m0,0x1000\nv_accvgpr_read_b32 v12,a239\nMvaa 32,136,80\nv_readfirstlane_b32 s65,v12\nMvaa 24,136,72\n"
"DP 1,20,40,0\nMvaa 24,140,76\nMvaa 16,140,68\nbuffer_load_dwordx4 a[164:167],v11,s[12:15],s62 offen\nMvaa 16,136,64\nWL 6\nDP 2,20,41,1024\n"
"Maaa 244,128,64\nWL 5\nRD 136,3,6144\nMaaa 244,132,68\ns_mov_b32 s44,s47\nRD 140,4,6144\nMaaa 252,132,76\ns_add_u32 s47,s47,0x8000\nMaaa 252,128,72\n"
"s_cmp_eq_u32 s47,0x28000\nMvaa 148,128,80\ns_cselect_b32 s94,s96,s95\nMvaa 148,132,84\ns_cselect_b32 s47,0,s47\nMvaa 156,132,92\nMvaa 156,128,88\n"
"v_add_u32 v3,s94,v3\nv_add_u32 v4,s94,v4\nMvaa 164,128,96\nMvaa 164,132,100\nDP 1,20,42,2048\nMvaa 172,132,108\nMvaa 172,128,104\nv_add_u32 v5,s94,v5\n"
"v_add_u32 v6,s94,v6\nMvaa 180,128,112\nDP 2,20,43,3072\nMvaa 180,132,116\nv_add_u32 v1,s34,v1\nv_add_u32 v2,s34,v2\nMvaa 188,132,124\n"
"v_add_u32 v7,s94,v7\nv_add_u32 v8,s94,v8\nMvaa 188,128,120\ns_waitcnt vmcnt(29) lgkmcnt(0)\ns_barrier\nMvaa 76,136,120\nRD 128,3,0\nMvaa 76,140,124\n"
"s_mul_i32 s12,s59,131072\nRD 132,4,0\nMvaa 68,140,116\ns_mul_i32 s49,s10,32768\nMvaa 68,136,112\ns_add_u32 s12,s12,s49\nMvaa 60,136,104\n"
"s_add_u32 s12,s12,0x4500000\nMvaa 60,140,108\ns_mov_b32 s14,0x200000\nMvaa 52,140,100\nMvaa 52,136,96\nMvaa 44,136,88\nMvaa 44,140,92\nMvaa 36,140,84\n"
"Mvaa 36,136,80\nMvaa 28,136,72\nMvaa 28,140,76\nMvaa 20,140,68\nMvaa 20,136,64\nWL 1\nMvaa 192,128,64\nWL 0\nRD 136,3,2048\nMvaa 192,132,68\n"
"RD 140,4,2048\nMvaa 200,132,76\nTR 32,5,0\nMvaa 200,128,72\nTR 34,5,1024\nMvaa 208,128,80\nTR 36,5,4096\nMvaa 208,132,84\nTR 38,5,5120\n"
"Mvaa 216,132,92\nTR 40,6,0\nMvaa 216,128,88\ns_add_u32 m0,s48,s44\nTR 42,6,1024\nMvaa 224,128,96\nTR 44,6,4096\nMvaa 224,132,100\nDP 1,20,36,0\n"
"Mvaa 232,132,108\nTR 46,6,5120\nMvaa 232,128,104\nTR 48,7,0\nMvaa 240,128,112\nDP 2,20,37,1024\nMvaa 240,132,116\nTR 50,7,1024\nMvaa 248,132,124\n"
"TR 52,7,4096\nMvaa 248,128,120\nWL 12\nDP 1,20,38,2048\nMvaa 136,136,120\nWL 11\nRD 128,3,4096\nMvaa 136,140,124\nRD 132,4,4096\nMvaa 128,140,116\n"
"TR 54,7,5120\nMvaa 128,136,112\nTR 56,8,0\nMvaa 120,136,104\nTR 58,8,1024\nMvaa 120,140,108\nTR 60,8,4096\nMvaa 112,140,100\nTR 62,8,5120\n"
"Mvaa 112,136,96\nMvaa 104,136,88\nMvaa 104,140,92\nDP 2,20,39,3072\nMvaa 96,140,84\ns_add_u32 m0,m0,0x1000\nglobal_load_dword a238,v13,s[8:9]\n"
"Mvaa 96,136,80\ns_add_u32 s8,s8,s18\ns_addc_u32 s9,s9,0\nMvaa 88,136,72\nDP 1,20,40,0\nMvaa 88,140,76\nMvaa 80,140,68\nMvaa 80,136,64\nWL 6\n"
"DP 2,20,41,1024\nMvaa 196,128,64\nWL 5\nRD 136,3,6144\nMvaa 196,132,68\ns_mov_b32 s44,s47\nRD 140,4,6144\nMvaa 204,132,76\ns_add_u32 s47,s47,0x8000\n"
"Mvaa 204,128,72\ns_cmp_eq_u32 s47,0x28000\nMvaa 212,128,80\ns_cselect_b32 s94,s96,s95\nMvaa 212,132,84\ns_cselect_b32 s47,0,s47\nMvaa 220,132,92\n"
"s_add_u32 s65,s65,1\nMvaa 220,128,88\ns_mul_i32 s50,s11,16384\nv_add_u32 v3,s94,v3\nv_add_u32 v4,s94,v4\nMvaa 228,128,96\ns_mul_i32 s49,s10,4096\n"
"Mvaa 228,132,100\ns_add_u32 s50,s50,s49\nDP 1,20,42,2048\nMvaa 236,132,108\ns_add_u32 s50,s50,0x4300000\nMvaa 236,128,104\nv_add_u32 v5,s94,v5\n"
"v_add_u32 v6,s94,v6\nMvaa 244,128,112\nDP 2,20,43,3072\nMvaa 244,132,116\nv_add_u32 v1,s35,v1\nv_add_u32 v2,s35,v2\nMvaa 252,132,124\n"
"v_add_u32 v7,s94,v7\nv_add_u32 v8,s94,v8\nMvaa 252,128,120\ns_waitcnt vmcnt(28) lgkmcnt(0)\ns_barrier\nMvaa 140,136,120\nRD 128,3,0\nMvaa 140,140,124\n"
"RD 132,4,0\nMvaa 132,140,116\nMvaa 132,136,112\nMvaa 124,136,104\nMvaa 124,140,108\nMvaa 116,140,100\nMvaa 116,136,96\nMvaa 108,136,88\n"
"buffer_store_dwordx4 a[144:147],v0,s[24:27],s50 offen offset:0 sc1\nMvaa 108,140,92\nMvaa 100,140,84\nMvaa 100,136,80\nMvaa 92,136,72\nMvaa 92,140,76\n"
"Mvaa 84,140,68\nMvaa 84,136,64\nWL 1\nMaaa 240,128,0\nWL 0\nRD 136,3,2048\nMaaa 240,132,4\nRD 140,4,2048\nMaaa 248,132,12\nTR 64,5,0\nMaaa 248,128,8\n"
"TR 66,5,1024\nMvaa 144,128,16\nTR 68,5,4096\nMvaa 144,132,20\nTR 70,5,5120\nMvaa 152,132,28\nTR 72,6,0\nMvaa 152,128,24\ns_add_u32 m0,s48,s44\n"
"TR 74,6,1024\nMvaa 160,128,32\nTR 76,6,4096\nMvaa 160,132,36\nDP 1,20,36,0\nMvaa 168,132,44\nTR 78,6,5120\nMvaa 168,128,40\nTR 80,7,0\nMvaa 176,128,48\n"
"DP 2,20,37,1024\nMvaa 176,132,52\nTR 82,7,1024\nMvaa 184,132,60\nTR 84,7,4096\nMvaa 184,128,56\nWL 12\nDP 1,20,38,2048\nMvaa 72,136,56\nWL 11\n"
"RD 128,3,4096\nMvaa 72,140,60\nRD 132,4,4096\nMvaa 64,140,52\nTR 86,7,5120\nMvaa 64,136,48\nTR 88,8,0\nMvaa 56,136,40\nTR 90,8,1024\nMvaa 56,140,44\n"
"TR 92,8,4096\nMvaa 48,140,36\nTR 94,8,5120\nMvaa 48,136,32\nMvaa 40,136,24\nMvaa 40,140,28\nDP 2,20,39,3072\nMvaa 32,140,20\ns_add_u32 m0,m0,0x1000\n"
"Mvaa 32,136,16\nMvaa 24,136,8\nDP 1,20,40,0\nMvaa 24,140,12\nMvaa 16,140,4\nbuffer_store_dwordx4 a[148:151],v0,s[24:27],s50 offen offset:1024 sc1\n"
"Mvaa 16,136,0\nWL 6\nDP 2,20,41,1024\nMaaa 244,128,0\nWL 5\nRD 136,3,6144\nMaaa 244,132,4\ns_mov_b32 s44,s47\nRD 140,4,6144\nMaaa 252,132,12\n"
"s_add_u32 s47,s47,0x8000\nMaaa 252,128,8\ns_cmp_eq_u32 s47,0x28000\nMvaa 148,128,16\ns_cselect_b32 s94,s96,s95\nMvaa 148,132,20\n"
"s_cselect_b32 s47,0,s47\nMvaa 156,132,28\nMvaa 156,128,24\nv_add_u32 v3,s94,v3\nv_add_u32 v4,s94,v4\nMvaa 164,128,32\nMvaa 164,132,36\nDP 1,20,42,2048\n"
"Mvaa 172,132,44\nMvaa 172,128,40\nv_add_u32 v5,s94,v5\nv_add_u32 v6,s94,v6\nMvaa 180,128,48\nDP 2,20,43,3072\nMvaa 180,132,52\nv_add_u32 v1,s34,v1\n"
"v_add_u32 v2,s34,v2\nMvaa 188,132,60\nv_add_u32 v7,s94,v7\nv_add_u32 v8,s94,v8\nMvaa 188,128,56\ns_waitcnt vmcnt(29) lgkmcnt(0)\ns_barrier\n"
"Mvaa 76,136,56\nRD 128,3,0\nMvaa 76,140,60\nRD 132,4,0\nMvaa 68,140,52\nMvaa 68,136,48\nMvaa 60,136,40\nMvaa 60,140,44\nMvaa 52,140,36\nMvaa 52,136,32\n"
"Mvaa 44,136,24\nbuffer_store_dwordx4 a[152:155],v0,s[24:27],s50 offen offset:2048 sc1\nMvaa 44,140,28\nMvaa 36,140,20\nMvaa 36,136,16\nMvaa 28,136,8\n"
"Mvaa 28,140,12\nMvaa 20,140,4\nMvaa 20,136,0\nWL 1\nMvaa 192,128,0\nWL 0\nRD 136,3,2048\nMvaa 192,132,4\nRD 140,4,2048\nMvaa 200,132,12\nTR 96,5,0\n"
"Mvaa 200,128,8\nTR 98,5,1024\nMvaa 208,128,16\nTR 100,5,4096\nMvaa 208,132,20\nTR 102,5,5120\nMvaa 216,132,28\nTR 104,6,0\nMvaa 216,128,24\n"
"s_add_u32 m0,s48,s44\nTR 106,6,1024\nMvaa 224,128,32\nTR 108,6,4096\nMvaa 224,132,36\nDP 1,20,36,0\nMvaa 232,132,44\nTR 110,6,5120\nMvaa 232,128,40\n"
"TR 112,7,0\nMvaa 240,128,48\nDP 2,20,37,1024\nMvaa 240,132,52\nTR 114,7,1024\nMvaa 248,132,60\nTR 116,7,4096\nMvaa 248,128,56\nWL 12\nDP 1,20,38,2048\n"
"Mvaa 136,136,56\nWL 11\nRD 128,3,4096\nMvaa 136,140,60\nRD 132,4,4096\nMvaa 128,140,52\nTR 118,7,5120\nMvaa 128,136,48\nTR 120,8,0\nMvaa 120,136,40\n"
"TR 122,8,1024\nMvaa 120,140,44\nTR 124,8,4096\nMvaa 112,140,36\nTR 126,8,5120\nMvaa 112,136,32\nMvaa 104,136,24\nMvaa 104,140,28\nDP 2,20,39,3072\n"
"Mvaa 96,140,20\ns_add_u32 m0,m0,0x1000\nMvaa 96,136,16\nMvaa 88,136,8\nDP 1,20,40,0\nMvaa 88,140,12\nMvaa 80,140,4\n"
"buffer_store_dwordx4 a[156:159],v0,s[24:27],s50 offen offset:3072 sc1\nMvaa 80,136,0\nWL 6\nDP 2,20,41,1024\nMvaa 196,128,0\nWL 5\nRD 136,3,6144\n"
"Mvaa 196,132,4\ns_mov_b32 s44,s47\nRD 140,4,6144\nMvaa 204,132,12\ns_add_u32 s47,s47,0x8000\nMvaa 204,128,8\ns_cmp_eq_u32 s47,0x28000\nMvaa 212,128,16\n"
"s_cselect_b32 s94,s96,s95\nMvaa 212,132,20\ns_cselect_b32 s47,0,s47\nMvaa 220,132,28\nMvaa 220,128,24\nv_add_u32 v3,s94,v3\nv_add_u32 v4,s94,v4\n"
"Mvaa 228,128,32\nMvaa 228,132,36\nDP 1,20,42,2048\nMvaa 236,132,44\nMvaa 236,128,40\nv_add_u32 v5,s94,v5\nv_add_u32 v6,s94,v6\nMvaa 244,128,48\n"
"DP 2,20,43,3072\nMvaa 244,132,52\nv_add_u32 v1,s35,v1\nv_add_u32 v2,s35,v2\nMvaa 252,132,60\nv_add_u32 v7,s94,v7\nv_add_u32 v8,s94,v8\nMvaa 252,128,56\n"
"s_waitcnt vmcnt(29) lgkmcnt(0)\ns_barrier\nMvaa 140,136,56\nRD 128,3,0\nMvaa 140,140,60\nRD 132,4,0\nMvaa 132,140,52\nMvaa 132,136,48\nMvaa 124,136,40\n"
"Mvaa 124,140,44\nMvaa 116,140,36\nMvaa 116,136,32\nMvaa 108,136,24\nbuffer_store_dwordx4 a[160:163],v0,s[24:27],s63 offen offset:0 sc1\n"
"Mvaa 108,140,28\nMvaa 100,140,20\nMvaa 100,136,16\nMvaa 92,136,8\nMvaa 92,140,12\nMvaa 84,140,4\nMvaa 84,136,0\nWL 1\nMaaa 240,128,64\nWL 0\n"
"RD 136,3,2048\nMaaa 240,132,68\nRD 140,4,2048\nMaaa 248,132,76\nTR 0,5,0\nMaaa 248,128,72\nTR 2,5,1024\nMvaa 144,128,80\nTR 4,5,4096\nMvaa 144,132,84\n"
"TR 6,5,5120\nMvaa 152,132,92\nTR 8,6,0\nMvaa 152,128,88\ns_add_u32 m0,s48,s44\nTR 10,6,1024\nMvaa 160,128,96\nTR 12,6,4096\nMvaa 160,132,100\n"
"DP 1,20,36,0\nMvaa 168,132,108\nTR 14,6,5120\nMvaa 168,128,104\nTR 16,7,0\nMvaa 176,128,112\nDP 2,20,37,1024\nMvaa 176,132,116\nTR 18,7,1024\n"
"Mvaa 184,132,124\nTR 20,7,4096\nMvaa 184,128,120\nWL 12\nDP 1,20,38,2048\nMvaa 72,136,120\nWL 11\nRD 128,3,4096\nMvaa 72,140,124\nRD 132,4,4096\n"
"Mvaa 64,140,116\nTR 22,7,5120\nMvaa 64,136,112\nTR 24,8,0\nMvaa 56,136,104\nTR 26,8,1024\nMvaa 56,140,108\nTR 28,8,4096\nMvaa 48,140,100\nTR 30,8,5120\n"
"Mvaa 48,136,96\nMvaa 40,136,88\nMvaa 40,140,92\nDP 2,20,39,3072\nMvaa 32,140,84\ns_add_u32 m0,m0,0x1000\nMvaa 32,136,80\nMvaa 24,136,72\nDP 1,20,40,0\n"
"Mvaa 24,140,76\nMvaa 16,140,68\nbuffer_store_dwordx4 a[164:167],v0,s[24:27],s63 offen offset:1024 sc1\nMvaa 16,136,64\nWL 6\nDP 2,20,41,1024\n"
"Maaa 244,128,64\nWL 5\nRD 136,3,6144\nMaaa 244,132,68\ns_mov_b32 s44,s47\nRD 140,4,6144\nMaaa 252,132,76\ns_add_u32 s47,s47,0x8000\nMaaa 252,128,72\n"
"s_cmp_eq_u32 s47,0x28000\nMvaa 148,128,80\ns_cselect_b32 s94,s96,s95\nMvaa 148,132,84\ns_cselect_b32 s47,0,s47\nMvaa 156,132,92\nMvaa 156,128,88\n"
"v_add_u32 v3,s94,v3\nv_add_u32 v4,s94,v4\nMvaa 164,128,96\nMvaa 164,132,100\nDP 1,20,42,2048\nMvaa 172,132,108\nMvaa 172,128,104\nv_add_u32 v5,s94,v5\n"
"v_add_u32 v6,s94,v6\nMvaa 180,128,112\nDP 2,20,43,3072\nMvaa 180,132,116\nv_add_u32 v1,s34,v1\nv_add_u32 v2,s34,v2\nMvaa 188,132,124\n"
"v_add_u32 v7,s94,v7\nv_add_u32 v8,s94,v8\nMvaa 188,128,120\ns_waitcnt vmcnt(30) lgkmcnt(0)\ns_barrier\nMvaa 76,136,120\nRD 128,3,0\nMvaa 76,140,124\n"
"RD 132,4,0\nMvaa 68,140,116\nMvaa 68,136,112\nMvaa 60,136,104\nMvaa 60,140,108\nMvaa 52,140,100\nMvaa 52,136,96\nMvaa 44,136,88\nMvaa 44,140,92\n"
"Mvaa 36,140,84\nMvaa 36,136,80\nMvaa 28,136,72\nMvaa 28,140,76\nMvaa 20,140,68\nMvaa 20,136,64\nWL 1\nMvaa 192,128,64\nWL 0\nRD 136,3,2048\n"
"Mvaa 192,132,68\nRD 140,4,2048\nMvaa 200,132,76\nTR 32,5,0\nMvaa 200,128,72\nTR 34,5,1024\nMvaa 208,128,80\nTR 36,5,4096\nMvaa 208,132,84\n"
"TR 38,5,5120\nMvaa 216,132,92\nTR 40,6,0\nMvaa 216,128,88\ns_add_u32 m0,s48,s44\nTR 42,6,1024\nMvaa 224,128,96\nTR 44,6,4096\nMvaa 224,132,100\n"
"DP 1,20,36,0\nMvaa 232,132,108\nTR 46,6,5120\nMvaa 232,128,104\nTR 48,7,0\nMvaa 240,128,112\nDP 2,20,37,1024\nMvaa 240,132,116\nTR 50,7,1024\n"
"Mvaa 248,132,124\nTR 52,7,4096\nMvaa 248,128,120\nWL 12\nDP 1,20,38,2048\nMvaa 136,136,120\nWL 11\nRD 128,3,4096\nMvaa 136,140,124\nRD 132,4,4096\n"
"Mvaa 128,140,116\nTR 54,7,5120\nMvaa 128,136,112\nTR 56,8,0\nMvaa 120,136,104\nTR 58,8,1024\nMvaa 120,140,108\nTR 60,8,4096\nMvaa 112,140,100\n"
"TR 62,8,5120\nMvaa 112,136,96\nMvaa 104,136,88\nMvaa 104,140,92\nDP 2,20,39,3072\nMvaa 96,140,84\ns_add_u32 m0,m0,0x1000\n"
"global_load_dword a238,v13,s[8:9]\nMvaa 96,136,80\ns_add_u32 s8,s8,s18\ns_addc_u32 s9,s9,0\nMvaa 88,136,72\nDP 1,20,40,0\nMvaa 88,140,76\n"
"Mvaa 80,140,68\nMvaa 80,136,64\nWL 6\nDP 2,20,41,1024\nMvaa 196,128,64\nWL 5\nRD 136,3,6144\nMvaa 196,132,68\ns_mov_b32 s44,s47\nRD 140,4,6144\n"
"Mvaa 204,132,76\ns_add_u32 s47,s47,0x8000\nMvaa 204,128,72\ns_cmp_eq_u32 s47,0x28000\nMvaa 212,128,80\ns_cselect_b32 s94,s96,s95\nMvaa 212,132,84\n"
"s_cselect_b32 s47,0,s47\nMvaa 220,132,92\nMvaa 220,128,88\nv_add_u32 v3,s94,v3\nv_add_u32 v4,s94,v4\nMvaa 228,128,96\nMvaa 228,132,100\n"
"DP 1,20,42,2048\nMvaa 236,132,108\nMvaa 236,128,104\nv_add_u32 v5,s94,v5\nv_add_u32 v6,s94,v6\nMvaa 244,128,112\nDP 2,20,43,3072\nMvaa 244,132,116\n"
"v_add_u32 v1,s35,v1\nv_add_u32 v2,s35,v2\nMvaa 252,132,124\nv_add_u32 v7,s94,v7\nv_add_u32 v8,s94,v8\nMvaa 252,128,120\ns_waitcnt vmcnt(29) lgkmcnt(0)\n"
"s_barrier\nMvaa 140,136,120\nRD 128,3,0\nMvaa 140,140,124\nRD 132,4,0\nMvaa 132,140,116\nMvaa 132,136,112\nMvaa 124,136,104\nMvaa 124,140,108\n"
"Mvaa 116,140,100\nMvaa 116,136,96\nMvaa 108,136,88\nMvaa 108,140,92\nMvaa 100,140,84\nMvaa 100,136,80\nMvaa 92,136,72\nMvaa 92,140,76\nMvaa 84,140,68\n"
"Mvaa 84,136,64\nWL 1\nMaaa 240,128,0\nWL 0\nRD 136,3,2048\nMaaa 240,132,4\nRD 140,4,2048\nMaaa 248,132,12\nTR 64,5,0\nMaaa 248,128,8\nTR 66,5,1024\n"
"Mvaa 144,128,16\nTR 68,5,4096\nMvaa 144,132,20\nTR 70,5,5120\nMvaa 152,132,28\nTR 72,6,0\nMvaa 152,128,24\ns_add_u32 m0,s48,s44\nTR 74,6,1024\n"
"Mvaa 160,128,32\nTR 76,6,4096\nMvaa 160,132,36\nDP 1,20,36,0\nMvaa 168,132,44\nTR 78,6,5120\nMvaa 168,128,40\nTR 80,7,0\nMvaa 176,128,48\n"
"DP 2,20,37,1024\nMvaa 176,132,52\nTR 82,7,1024\nMvaa 184,132,60\nTR 84,7,4096\nMvaa 184,128,56\nWL 12\nDP 1,20,38,2048\nMvaa 72,136,56\nWL 11\n"
"RD 128,3,4096\nMvaa 72,140,60\nRD 132,4,4096\nMvaa 64,140,52\nTR 86,7,5120\nMvaa 64,136,48\nTR 88,8,0\nMvaa 56,136,40\nTR 90,8,1024\nMvaa 56,140,44\n"
"TR 92,8,4096\nMvaa 48,140,36\nTR 94,8,5120\nMvaa 48,136,32\nMvaa 40,136,24\nMvaa 40,140,28\nDP 2,20,39,3072\nMvaa 32,140,20\ns_add_u32 m0,m0,0x1000\n"
"Mvaa 32,136,16\nMvaa 24,136,8\nDP 1,20,40,0\nMvaa 24,140,12\nMvaa 16,140,4\nMvaa 16,136,0\nWL 6\nDP 2,20,41,1024\nMaaa 244,128,0\nWL 5\nRD 136,3,6144\n"
"Maaa 244,132,4\ns_mov_b32 s44,s47\nRD 140,4,6144\nMaaa 252,132,12\ns_add_u32 s47,s47,0x8000\nMaaa 252,128,8\ns_cmp_eq_u32 s47,0x28000\nMvaa 148,128,16\n"
"s_cselect_b32 s94,s96,s95\nMvaa 148,132,20\ns_cselect_b32 s47,0,s47\nMvaa 156,132,28\nMvaa 156,128,24\nv_add_u32 v3,s94,v3\nv_add_u32 v4,s94,v4\n"
"Mvaa 164,128,32\nMvaa 164,132,36\nDP 1,20,42,2048\nMvaa 172,132,44\nMvaa 172,128,40\nv_add_u32 v5,s94,v5\nv_add_u32 v6,s94,v6\nMvaa 180,128,48\n"
"DP 2,20,43,3072\nMvaa 180,132,52\nv_add_u32 v1,s34,v1\nv_add_u32 v2,s34,v2\nMvaa 188,132,60\nv_add_u32 v7,s94,v7\nv_add_u32 v8,s94,v8\nMvaa 188,128,56\n"
"s_waitcnt vmcnt(27) lgkmcnt(0)\ns_barrier\nMvaa 76,136,56\nRD 128,3,0\nMvaa 76,140,60\nRD 132,4,0\nMvaa 68,140,52\nMvaa 68,136,48\nMvaa 60,136,40\n"
"Mvaa 60,140,44\nMvaa 52,140,36\nMvaa 52,136,32\nMvaa 44,136,24\nMvaa 44,140,28\nMvaa 36,140,20\nMvaa 36,136,16\nMvaa 28,136,8\nMvaa 28,140,12\n"
"Mvaa 20,140,4\nMvaa 20,136,0\nWL 1\nMvaa 192,128,0\nWL 0\nRD 136,3,2048\nMvaa 192,132,4\nRD 140,4,2048\nMvaa 200,132,12\nTR 96,5,0\nMvaa 200,128,8\n"
"TR 98,5,1024\nMvaa 208,128,16\nTR 100,5,4096\nMvaa 208,132,20\nTR 102,5,5120\nMvaa 216,132,28\nTR 104,6,0\nMvaa 216,128,24\ns_add_u32 m0,s48,s44\n"
"TR 106,6,1024\nMvaa 224,128,32\nTR 108,6,4096\nMvaa 224,132,36\nDP 1,20,36,0\nMvaa 232,132,44\nTR 110,6,5120\nMvaa 232,128,40\nTR 112,7,0\n"
"Mvaa 240,128,48\nDP 2,20,37,1024\nMvaa 240,132,52\nTR 114,7,1024\nMvaa 248,132,60\nTR 116,7,4096\nMvaa 248,128,56\nWL 12\nDP 1,20,38,2048\n"
"Mvaa 136,136,56\nWL 11\nRD 128,3,4096\nMvaa 136,140,60\nRD 132,4,4096\nMvaa 128,140,52\nTR 118,7,5120\nMvaa 128,136,48\nTR 120,8,0\nMvaa 120,136,40\n"
"TR 122,8,1024\nMvaa 120,140,44\nTR 124,8,4096\nMvaa 112,140,36\nTR 126,8,5120\nMvaa 112,136,32\nMvaa 104,136,24\nMvaa 104,140,28\nDP 2,20,39,3072\n"
"Mvaa 96,140,20\ns_add_u32 m0,m0,0x1000\nMvaa 96,136,16\nMvaa 88,136,8\nDP 1,20,40,0\nMvaa 88,140,12\nMvaa 80,140,4\nMvaa 80,136,0\nWL 6\n"
"DP 2,20,41,1024\nMvaa 196,128,0\nWL 5\nRD 136,3,6144\nMvaa 196,132,4\ns_mov_b32 s44,s47\nRD 140,4,6144\nMvaa 204,132,12\ns_add_u32 s47,s47,0x8000\n"
"Mvaa 204,128,8\ns_cmp_eq_u32 s47,0x28000\nMvaa 212,128,16\ns_cselect_b32 s94,s96,s95\nMvaa 212,132,20\ns_cselect_b32 s47,0,s47\nMvaa 220,132,28\n"
"s_mul_i32 s15,s11,512\nMvaa 220,128,24\ns_mul_i32 s49,s10,128\nv_add_u32 v3,s94,v3\nv_add_u32 v4,s94,v4\nMvaa 228,128,32\ns_add_u32 s15,s15,s49\n"
"Mvaa 228,132,36\ns_add_u32 s15,s15,0xa0000\nDP 1,20,42,2048\nMvaa 236,132,44\nMvaa 236,128,40\nv_add_u32 v5,s94,v5\nv_add_u32 v6,s94,v6\n"
"Mvaa 244,128,48\nDP 2,20,43,3072\nMvaa 244,132,52\nv_add_u32 v1,s35,v1\nv_add_u32 v2,s35,v2\nMvaa 252,132,60\nv_add_u32 v7,s94,v7\nv_add_u32 v8,s94,v8\n"
"Mvaa 252,128,56\ns_waitcnt vmcnt(25) lgkmcnt(0)\ns_barrier\nMvaa 140,136,56\nRD 128,3,0\nMvaa 140,140,60\nRD 132,4,0\nMvaa 132,140,52\n"
"v_mov_b32 v12,s65\ns_mov_b64 exec,1\nbuffer_store_dword v12,off,s[24:27],s15 sc1\ns_mov_b64 exec,-1\nMvaa 132,136,48\nMvaa 124,136,40\n"
"s_mul_i32 s15,s59,8\nMvaa 124,140,44\ns_mul_i32 s49,s10,2\nMvaa 116,140,36\ns_add_u32 s15,s15,s49\nMvaa 116,136,32\ns_add_u32 s15,s15,0x80\n"
"Mvaa 108,136,24\ns_mul_i32 s70,s56,8\nMvaa 108,140,28\ns_mul_i32 s49,s10,2\nMvaa 100,140,20\ns_add_u32 s70,s70,s49\nMvaa 100,136,16\n"
"s_add_u32 s70,s70,0xfffffffd\nMvaa 92,136,8\ns_mul_i32 s71,s57,21\nMvaa 92,140,12\ns_add_u32 s71,s71,0xfffffffb\nMvaa 84,140,4\ns_mov_b32 s62,655360\n"
"Mvaa 84,136,0\nWL 1\nMaaa 240,128,64\nWL 0\nRD 136,3,2048\nMaaa 240,132,68\nRD 140,4,2048\nMaaa 248,132,76\nTR 0,5,0\nMaaa 248,128,72\nTR 2,5,1024\n"
"Mvaa 144,128,80\nTR 4,5,4096\nMvaa 144,132,84\nTR 6,5,5120\nMvaa 152,132,92\nTR 8,6,0\nMvaa 152,128,88\ns_add_u32 m0,s48,s44\nTR 10,6,1024\n"
"Mvaa 160,128,96\nTR 12,6,4096\nMvaa 160,132,100\nDP 1,20,36,0\nMvaa 168,132,108\nTR 14,6,5120\nMvaa 168,128,104\nTR 16,7,0\nMvaa 176,128,112\n"
"DP 2,20,37,1024\nMvaa 176,132,116\nTR 18,7,1024\nMvaa 184,132,124\nTR 20,7,4096\nMvaa 184,128,120\nWL 12\nDP 1,20,38,2048\nMvaa 72,136,120\nWL 11\n"
"RD 128,3,4096\nMvaa 72,140,124\nRD 132,4,4096\nMvaa 64,140,116\nTR 22,7,5120\nMvaa 64,136,112\nTR 24,8,0\nMvaa 56,136,104\nTR 26,8,1024\n"
"Mvaa 56,140,108\nTR 28,8,4096\nMvaa 48,140,100\nTR 30,8,5120\nMvaa 48,136,96\nMvaa 40,136,88\nv_lshrrev_b32 v12,4,v0\nMvaa 40,140,92\nDP 2,20,39,3072\n"
"Mvaa 32,140,84\ns_add_u32 m0,m0,0x1000\ns_waitcnt vmcnt(25)\nglobal_load_dword a238,v13,s[8:9]\nv_min_u32 v12,25,v12\nMvaa 32,136,80\n"
"s_add_u32 s8,s8,s18\ns_addc_u32 s9,s9,0\nv_mov_b32 v11,s15\nMvaa 24,136,72\nDP 1,20,40,0\nMvaa 24,140,76\nv_mov_b32 v9,s70\nMvaa 16,140,68\n"
"v_cmp_gt_u32 vcc,2,v12\nMvaa 16,136,64\nWL 6\nDP 2,20,41,1024\nMaaa 244,128,64\nWL 5\nRD 136,3,6144\nv_mov_b32 v15,s71\nMaaa 244,132,68\n"
"s_mov_b32 s44,s47\nRD 140,4,6144\nv_cndmask_b32 v11,v9,v11,vcc\nMaaa 252,132,76\ns_add_u32 s47,s47,0x8000\nv_cmp_gt_u32 vcc,5,v12\nMaaa 252,128,72\n"
"s_cmp_eq_u32 s47,0x28000\nv_cndmask_b32 v11,v15,v11,vcc\nMvaa 148,128,80\ns_cselect_b32 s94,s96,s95\nv_add_u32 v15,v12,v11\nMvaa 148,132,84\n"
"s_cselect_b32 s47,0,s47\nv_max_i32 v15,0,v15\nMvaa 156,132,92\nv_lshlrev_b32 v15,9,v15\nMvaa 156,128,88\nv_add_u32 v3,s94,v3\nv_add_u32 v4,s94,v4\n"
"Mvaa 164,128,96\nMvaa 164,132,100\nDP 1,20,42,2048\nMvaa 172,132,108\nMvaa 172,128,104\nv_add_u32 v5,s94,v5\nv_add_u32 v6,s94,v6\nMvaa 180,128,112\n"
"DP 2,20,43,3072\nMvaa 180,132,116\nv_add_u32 v1,s34,v1\nv_add_u32 v2,s34,v2\nMvaa 188,132,124\nv_add_u32 v7,s94,v7\nv_add_u32 v8,s94,v8\n"
"Mvaa 188,128,120\ns_waitcnt vmcnt(26) lgkmcnt(0)\ns_barrier\nMvaa 76,136,120\nRD 128,3,0\nMvaa 76,140,124\nRD 132,4,0\nMvaa 68,140,116\n"
"Mvaa 68,136,112\nMvaa 60,136,104\nMvaa 60,140,108\nMvaa 52,140,100\nMvaa 52,136,96\nMvaa 44,136,88\nMvaa 44,140,92\nMvaa 36,140,84\nMvaa 36,136,80\n"
"Mvaa 28,136,72\nMvaa 28,140,76\nMvaa 20,140,68\nMvaa 20,136,64\nWL 1\nMvaa 192,128,64\nWL 0\nRD 136,3,2048\nMvaa 192,132,68\nRD 140,4,2048\n"
"Mvaa 200,132,76\nTR 32,5,0\nMvaa 200,128,72\nTR 34,5,1024\nMvaa 208,128,80\nTR 36,5,4096\nMvaa 208,132,84\nTR 38,5,5120\nMvaa 216,132,92\nTR 40,6,0\n"
"Mvaa 216,128,88\ns_add_u32 m0,s48,s44\nTR 42,6,1024\nMvaa 224,128,96\nTR 44,6,4096\nMvaa 224,132,100\nDP 1,20,36,0\nMvaa 232,132,108\nTR 46,6,5120\n"
"Mvaa 232,128,104\nTR 48,7,0\nMvaa 240,128,112\nDP 2,20,37,1024\nMvaa 240,132,116\nTR 50,7,1024\nMvaa 248,132,124\nTR 52,7,4096\nMvaa 248,128,120\n"
"WL 12\nDP 1,20,38,2048\nMvaa 136,136,120\nWL 11\nRD 128,3,4096\nMvaa 136,140,124\nRD 132,4,4096\nMvaa 128,140,116\nTR 54,7,5120\nMvaa 128,136,112\n"
"TR 56,8,0\nMvaa 120,136,104\nTR 58,8,1024\nMvaa 120,140,108\nTR 60,8,4096\nMvaa 112,140,100\nTR 62,8,5120\nMvaa 112,136,96\nMvaa 104,136,88\n"
"Mvaa 104,140,92\nDP 2,20,39,3072\nMvaa 96,140,84\ns_add_u32 m0,m0,0x1000\nMvaa 96,136,80\nMvaa 88,136,72\nDP 1,20,40,0\nMvaa 88,140,76\nMvaa 80,140,68\n"
"Mvaa 80,136,64\nWL 6\nDP 2,20,41,1024\nMvaa 196,128,64\nWL 5\nRD 136,3,6144\nMvaa 196,132,68\ns_mov_b32 s44,s47\nRD 140,4,6144\nMvaa 204,132,76\n"
"s_add_u32 s47,s47,0x8000\nMvaa 204,128,72\ns_cmp_eq_u32 s47,0x28000\nMvaa 212,128,80\ns_cselect_b32 s94,s96,s95\nMvaa 212,132,84\n"
"s_cselect_b32 s47,0,s47\nMvaa 220,132,92\nMvaa 220,128,88\nv_add_u32 v3,s94,v3\nv_add_u32 v4,s94,v4\nMvaa 228,128,96\nMvaa 228,132,100\n"
"DP 1,20,42,2048\nMvaa 236,132,108\nMvaa 236,128,104\nv_add_u32 v5,s94,v5\nv_add_u32 v6,s94,v6\nMvaa 244,128,112\nDP 2,20,43,3072\nMvaa 244,132,116\n"
"v_add_u32 v1,s35,v1\nv_add_u32 v2,s35,v2\nMvaa 252,132,124\nv_add_u32 v7,s94,v7\nv_add_u32 v8,s94,v8\nMvaa 252,128,120\ns_waitcnt vmcnt(26) lgkmcnt(0)\n"
"s_barrier\nMvaa 140,136,120\nRD 128,3,0\nMvaa 140,140,124\nRD 132,4,0\nMvaa 132,140,116\nMvaa 132,136,112\nMvaa 124,136,104\nMvaa 124,140,108\n"
"Mvaa 116,140,100\nMvaa 116,136,96\nMvaa 108,136,88\nMvaa 108,140,92\nMvaa 100,140,84\nMvaa 100,136,80\nMvaa 92,136,72\nMvaa 92,140,76\nMvaa 84,140,68\n"
"Mvaa 84,136,64\nWL 1\nMaaa 240,128,0\nWL 0\nRD 136,3,2048\nMaaa 240,132,4\nRD 140,4,2048\nMaaa 248,132,12\nTR 64,5,0\nMaaa 248,128,8\nTR 66,5,1024\n"
"Mvaa 144,128,16\nTR 68,5,4096\nMvaa 144,132,20\nTR 70,5,5120\nMvaa 152,132,28\nTR 72,6,0\nMvaa 152,128,24\ns_add_u32 m0,s48,s44\nTR 74,6,1024\n"
"Mvaa 160,128,32\nTR 76,6,4096\nMvaa 160,132,36\nDP 1,20,36,0\nMvaa 168,132,44\nTR 78,6,5120\nMvaa 168,128,40\nTR 80,7,0\nMvaa 176,128,48\n"
"DP 2,20,37,1024\nMvaa 176,132,52\nTR 82,7,1024\nMvaa 184,132,60\nTR 84,7,4096\nMvaa 184,128,56\nWL 12\nDP 1,20,38,2048\nMvaa 72,136,56\nWL 11\n"
"RD 128,3,4096\nMvaa 72,140,60\nRD 132,4,4096\nMvaa 64,140,52\nTR 86,7,5120\nMvaa 64,136,48\nTR 88,8,0\nMvaa 56,136,40\nTR 90,8,1024\nMvaa 56,140,44\n"
"TR 92,8,4096\nMvaa 48,140,36\nTR 94,8,5120\nMvaa 48,136,32\nMvaa 40,136,24\nMvaa 40,140,28\nDP 2,20,39,3072\nMvaa 32,140,20\ns_add_u32 m0,m0,0x1000\n"
"Mvaa 32,136,16\nMvaa 24,136,8\nDP 1,20,40,0\nMvaa 24,140,12\nMvaa 16,140,4\nbuffer_load_dword v14,v15,s[24:27],s62 offen sc1\nMvaa 16,136,0\nWL 6\n"
"DP 2,20,41,1024\nMaaa 244,128,0\nWL 5\nRD 136,3,6144\nMaaa 244,132,4\ns_mov_b32 s44,s47\nRD 140,4,6144\nMaaa 252,132,12\ns_add_u32 s47,s47,0x8000\n"
"Maaa 252,128,8\ns_cmp_eq_u32 s47,0x28000\nMvaa 148,128,16\ns_cselect_b32 s94,s96,s95\nMvaa 148,132,20\ns_cselect_b32 s47,0,s47\nMvaa 156,132,28\n"
"Mvaa 156,128,24\nv_add_u32 v3,s94,v3\nv_add_u32 v4,s94,v4\nMvaa 164,128,32\nMvaa 164,132,36\nDP 1,20,42,2048\nMvaa 172,132,44\nMvaa 172,128,40\n"
"v_add_u32 v5,s94,v5\nv_add_u32 v6,s94,v6\nMvaa 180,128,48\nDP 2,20,43,3072\nMvaa 180,132,52\nv_add_u32 v1,s34,v1\nv_add_u32 v2,s34,v2\nMvaa 188,132,60\n"
"v_add_u32 v7,s94,v7\nv_add_u32 v8,s94,v8\nMvaa 188,128,56\ns_waitcnt vmcnt(27) lgkmcnt(0)\ns_barrier\nMvaa 76,136,56\nRD 128,3,0\nMvaa 76,140,60\n"
"RD 132,4,0\nMvaa 68,140,52\nMvaa 68,136,48\nMvaa 60,136,40\nMvaa 60,140,44\nMvaa 52,140,36\nMvaa 52,136,32\nMvaa 44,136,24\nMvaa 44,140,28\n"
"Mvaa 36,140,20\nMvaa 36,136,16\nMvaa 28,136,8\nMvaa 28,140,12\nMvaa 20,140,4\nMvaa 20,136,0\nWL 1\nMvaa 192,128,0\nWL 0\nRD 136,3,2048\nMvaa 192,132,4\n"
"RD 140,4,2048\nMvaa 200,132,12\nTR 96,5,0\nMvaa 200,128,8\nTR 98,5,1024\nMvaa 208,128,16\nTR 100,5,4096\nMvaa 208,132,20\nTR 102,5,5120\n"
"Mvaa 216,132,28\nTR 104,6,0\nMvaa 216,128,24\ns_add_u32 m0,s48,s44\nTR 106,6,1024\nMvaa 224,128,32\nTR 108,6,4096\nMvaa 224,132,36\nDP 1,20,36,0\n"
"Mvaa 232,132,44\nTR 110,6,5120\nMvaa 232,128,40\nTR 112,7,0\nMvaa 240,128,48\nDP 2,20,37,1024\nMvaa 240,132,52\nTR 114,7,1024\nMvaa 248,132,60\n"
"TR 116,7,4096\nMvaa 248,128,56\nWL 12\nDP 1,20,38,2048\nMvaa 136,136,56\nWL 11\nRD 128,3,4096\nMvaa 136,140,60\nRD 132,4,4096\nMvaa 128,140,52\n"
"TR 118,7,5120\nMvaa 128,136,48\nTR 120,8,0\nMvaa 120,136,40\nTR 122,8,1024\nMvaa 120,140,44\nTR 124,8,4096\nMvaa 112,140,36\nTR 126,8,5120\n"
"Mvaa 112,136,32\nMvaa 104,136,24\nMvaa 104,140,28\nDP 2,20,39,3072\nMvaa 96,140,20\ns_add_u32 m0,m0,0x1000\nMvaa 96,136,16\nMvaa 88,136,8\n"
"DP 1,20,40,0\nMvaa 88,140,12\nMvaa 80,140,4\nMvaa 80,136,0\nWL 6\nDP 2,20,41,1024\nMvaa 196,128,0\nWL 5\nRD 136,3,6144\nMvaa 196,132,4\n"
"s_mov_b32 s44,s47\nRD 140,4,6144\nMvaa 204,132,12\ns_add_u32 s47,s47,0x8000\nMvaa 204,128,8\ns_cmp_eq_u32 s47,0x28000\nMvaa 212,128,16\n"
"s_cselect_b32 s94,s96,s95\nMvaa 212,132,20\ns_cselect_b32 s47,0,s47\nMvaa 220,132,28\nMvaa 220,128,24\nv_add_u32 v3,s94,v3\nv_add_u32 v4,s94,v4\n"
"Mvaa 228,128,32\nMvaa 228,132,36\nDP 1,20,42,2048\nMvaa 236,132,44\nMvaa 236,128,40\nv_add_u32 v5,s94,v5\nv_add_u32 v6,s94,v6\nMvaa 244,128,48\n"
"DP 2,20,43,3072\nMvaa 244,132,52\nv_add_u32 v1,s35,v1\nv_add_u32 v2,s35,v2\nMvaa 252,132,60\nv_add_u32 v7,s94,v7\nv_add_u32 v8,s94,v8\nMvaa 252,128,56\n"
"s_waitcnt vmcnt(25) lgkmcnt(0)\ns_barrier\nMvaa 140,136,56\nRD 128,3,0\ns_waitcnt vmcnt(11)\nv_cmp_ne_u32 vcc,s65,v14\ns_nop 0\n"
"s_cbranch_vccnz fused_op_sp5\nfused_op_spb6:\nMvaa 140,140,60\nRD 132,4,0\nMvaa 132,140,52\ns_add_u32 s15,s12,0\nMvaa 132,136,48\n"
"buffer_load_dwordx4 a[144:147],v0,s[24:27],s15 offen offset:0 sc1\nMvaa 124,136,40\nMvaa 124,140,44\nMvaa 116,140,36\nMvaa 116,136,32\nMvaa 108,136,24\n"
"s_add_u32 s15,s12,16384\nMvaa 108,140,28\nbuffer_load_dwordx4 a[148:151],v0,s[24:27],s15 offen offset:0 sc1\nMvaa 100,140,20\nMvaa 100,136,16\n"
"Mvaa 92,136,8\nMvaa 92,140,12\nMvaa 84,140,4\nMvaa 84,136,0\nWL 1\nMaaa 240,128,64\nWL 0\nRD 136,3,2048\nMaaa 240,132,68\nRD 140,4,2048\n"
"Maaa 248,132,76\nTR 0,5,0\nMaaa 248,128,72\nTR 2,5,1024\nMvaa 144,128,80\nTR 4,5,4096\nMvaa 144,132,84\nTR 6,5,5120\nMvaa 152,132,92\nTR 8,6,0\n"
"Mvaa 152,128,88\ns_add_u32 m0,s48,s44\nTR 10,6,1024\nMvaa 160,128,96\nTR 12,6,4096\nMvaa 160,132,100\nDP 1,20,36,0\nMvaa 168,132,108\nTR 14,6,5120\n"
"Mvaa 168,128,104\nTR 16,7,0\nMvaa 176,128,112\nDP 2,20,37,1024\nMvaa 176,132,116\nTR 18,7,1024\nMvaa 184,132,124\nTR 20,7,4096\nMvaa 184,128,120\n"
"WL 12\nDP 1,20,38,2048\nMvaa 72,136,120\nWL 11\nRD 128,3,4096\nMvaa 72,140,124\nRD 132,4,4096\nMvaa 64,140,116\nTR 22,7,5120\nMvaa 64,136,112\n"
"TR 24,8,0\nMvaa 56,136,104\nTR 26,8,1024\nMvaa 56,140,108\nTR 28,8,4096\nMvaa 48,140,100\nTR 30,8,5120\nMvaa 48,136,96\nMvaa 40,136,88\nMvaa 40,140,92\n"
"DP 2,20,39,3072\nMvaa 32,140,84\ns_add_u32 m0,m0,0x1000\nglobal_load_dword a238,v13,s[8:9]\nMvaa 32,136,80\ns_add_u32 s8,s8,s18\ns_addc_u32 s9,s9,0\n"
"Mvaa 24,136,72\nDP 1,20,40,0\nMvaa 24,140,76\nMvaa 16,140,68\nMvaa 16,136,64\nWL 6\nDP 2,20,41,1024\nMaaa 244,128,64\nWL 5\nRD 136,3,6144\n"
"Maaa 244,132,68\ns_mov_b32 s44,s47\nRD 140,4,6144\nMaaa 252,132,76\ns_add_u32 s47,s47,0x8000\nMaaa 252,128,72\ns_cmp_eq_u32 s47,0x28000\n"
"Mvaa 148,128,80\ns_cselect_b32 s94,s96,s95\nMvaa 148,132,84\ns_cselect_b32 s47,0,s47\nMvaa 156,132,92\nMvaa 156,128,88\nv_add_u32 v3,s94,v3\n"
"v_add_u32 v4,s94,v4\nMvaa 164,128,96\nMvaa 164,132,100\nDP 1,20,42,2048\nMvaa 172,132,108\nMvaa 172,128,104\nv_add_u32 v5,s94,v5\nv_add_u32 v6,s94,v6\n"
"Mvaa 180,128,112\nDP 2,20,43,3072\nMvaa 180,132,116\nv_add_u32 v1,s34,v1\nv_add_u32 v2,s34,v2\nMvaa 188,132,124\nv_add_u32 v7,s94,v7\n"
"v_add_u32 v8,s94,v8\nMvaa 188,128,120\ns_waitcnt vmcnt(28) lgkmcnt(0)\ns_barrier\nMvaa 76,136,120\nRD 128,3,0\nMvaa 76,140,124\nRD 132,4,0\n"
"Mvaa 68,140,116\ns_add_u32 s15,s12,0\nMvaa 68,136,112\nbuffer_load_dwordx4 a[152:155],v0,s[24:27],s15 offen offset:1024 sc1\nMvaa 60,136,104\n"
"Mvaa 60,140,108\nMvaa 52,140,100\nMvaa 52,136,96\nMvaa 44,136,88\ns_add_u32 s15,s12,16384\nMvaa 44,140,92\n"
"buffer_load_dwordx4 a[156:159],v0,s[24:27],s15 offen offset:1024 sc1\nMvaa 36,140,84\nMvaa 36,136,80\nMvaa 28,136,72\nMvaa 28,140,76\nMvaa 20,140,68\n"
"Mvaa 20,136,64\nWL 1\nMvaa 192,128,64\nWL 0\nRD 136,3,2048\nMvaa 192,132,68\nRD 140,4,2048\nMvaa 200,132,76\nTR 32,5,0\nMvaa 200,128,72\nTR 34,5,1024\n"
"Mvaa 208,128,80\nTR 36,5,4096\nMvaa 208,132,84\nTR 38,5,5120\nMvaa 216,132,92\nTR 40,6,0\nMvaa 216,128,88\ns_add_u32 m0,s48,s44\nTR 42,6,1024\n"
"Mvaa 224,128,96\nTR 44,6,4096\nMvaa 224,132,100\nDP 1,20,36,0\nMvaa 232,132,108\nTR 46,6,5120\nMvaa 232,128,104\nTR 48,7,0\nMvaa 240,128,112\n"
"DP 2,20,37,1024\nMvaa 240,132,116\nTR 50,7,1024\nMvaa 248,132,124\nTR 52,7,4096\nMvaa 248,128,120\nWL 12\nDP 1,20,38,2048\nMvaa 136,136,120\nWL 11\n"
"RD 128,3,4096\nMvaa 136,140,124\nRD 132,4,4096\nMvaa 128,140,116\nTR 54,7,5120\nMvaa 128,136,112\nTR 56,8,0\nMvaa 120,136,104\nTR 58,8,1024\n"
"Mvaa 120,140,108\nTR 60,8,4096\nMvaa 112,140,100\nTR 62,8,5120\nMvaa 112,136,96\nMvaa 104,136,88\nMvaa 104,140,92\nDP 2,20,39,3072\nMvaa 96,140,84\n"
"s_add_u32 m0,m0,0x1000\nMvaa 96,136,80\nMvaa 88,136,72\nDP 1,20,40,0\nMvaa 88,140,76\nMvaa 80,140,68\nMvaa 80,136,64\nWL 6\nDP 2,20,41,1024\n"
"Mvaa 196,128,64\nWL 5\nRD 136,3,6144\nMvaa 196,132,68\ns_mov_b32 s44,s47\nRD 140,4,6144\nMvaa 204,132,76\ns_add_u32 s47,s47,0x8000\nMvaa 204,128,72\n"
"s_cmp_eq_u32 s47,0x28000\nMvaa 212,128,80\ns_cselect_b32 s94,s96,s95\nMvaa 212,132,84\ns_cselect_b32 s47,0,s47\nMvaa 220,132,92\nMvaa 220,128,88\n"
"v_add_u32 v3,s94,v3\nv_add_u32 v4,s94,v4\nMvaa 228,128,96\nMvaa 228,132,100\nDP 1,20,42,2048\nMvaa 236,132,108\nMvaa 236,128,104\nv_add_u32 v5,s94,v5\n"
"v_add_u32 v6,s94,v6\nMvaa 244,128,112\nDP 2,20,43,3072\nMvaa 244,132,116\nv_add_u32 v1,s35,v1\nv_add_u32 v2,s35,v2\nMvaa 252,132,124\n"
"v_add_u32 v7,s94,v7\nv_add_u32 v8,s94,v8\nMvaa 252,128,120\ns_waitcnt vmcnt(29) lgkmcnt(0)\ns_barrier\nMvaa 140,136,120\nRD 128,3,0\nMvaa 140,140,124\n"
"RD 132,4,0\nMvaa 132,140,116\ns_add_u32 s15,s12,0\nMvaa 132,136,112\nbuffer_load_dwordx4 a[160:163],v0,s[24:27],s15 offen offset:2048 sc1\n"
"Mvaa 124,136,104\nMvaa 124,140,108\nMvaa 116,140,100\nMvaa 116,136,96\nMvaa 108,136,88\ns_add_u32 s15,s12,16384\nMvaa 108,140,92\n"
"buffer_load_dwordx4 a[164:167],v0,s[24:27],s15 offen offset:2048 sc1\nMvaa 100,140,84\nMvaa 100,136,80\nMvaa 92,136,72\nMvaa 92,140,76\nMvaa 84,140,68\n"
"Mvaa 84,136,64\nWL 1\nMaaa 240,128,0\nWL 0\nRD 136,3,2048\nMaaa 240,132,4\nRD 140,4,2048\nMaaa 248,132,12\nTR 64,5,0\nMaaa 248,128,8\nTR 66,5,1024\n"
"Mvaa 144,128,16\nTR 68,5,4096\nMvaa 144,132,20\nTR 70,5,5120\nMvaa 152,132,28\nTR 72,6,0\nMvaa 152,128,24\ns_add_u32 m0,s48,s44\nTR 74,6,1024\n"
"Mvaa 160,128,32\nTR 76,6,4096\nMvaa 160,132,36\nDP 1,20,36,0\nMvaa 168,132,44\nTR 78,6,5120\nMvaa 168,128,40\nTR 80,7,0\nMvaa 176,128,48\n"
"DP 2,20,37,1024\nMvaa 176,132,52\nTR 82,7,1024\nMvaa 184,132,60\nTR 84,7,4096\nMvaa 184,128,56\nWL 12\nDP 1,20,38,2048\nMvaa 72,136,56\nWL 11\n"
"RD 128,3,4096\nMvaa 72,140,60\nRD 132,4,4096\nMvaa 64,140,52\nTR 86,7,5120\nMvaa 64,136,48\nTR 88,8,0\nMvaa 56,136,40\nTR 90,8,1024\nMvaa 56,140,44\n"
"TR 92,8,4096\nMvaa 48,140,36\nTR 94,8,5120\nMvaa 48,136,32\nMvaa 40,136,24\nMvaa 40,140,28\nDP 2,20,39,3072\nMvaa 32,140,20\ns_add_u32 m0,m0,0x1000\n"
"Mvaa 32,136,16\nMvaa 24,136,8\nDP 1,20,40,0\nMvaa 24,140,12\nMvaa 16,140,4\nMvaa 16,136,0\nWL 6\nDP 2,20,41,1024\nMaaa 244,128,0\nWL 5\nRD 136,3,6144\n"
"Maaa 244,132,4\ns_mov_b32 s44,s47\nRD 140,4,6144\nMaaa 252,132,12\ns_add_u32 s47,s47,0x8000\nMaaa 252,128,8\ns_cmp_eq_u32 s47,0x28000\nMvaa 148,128,16\n"
"s_cselect_b32 s94,s96,s95\nMvaa 148,132,20\ns_cselect_b32 s47,0,s47\nMvaa 156,132,28\nMvaa 156,128,24\nv_add_u32 v3,s94,v3\nv_add_u32 v4,s94,v4\n"
"Mvaa 164,128,32\nMvaa 164,132,36\nDP 1,20,42,2048\nMvaa 172,132,44\nMvaa 172,128,40\nv_add_u32 v5,s94,v5\nv_add_u32 v6,s94,v6\nMvaa 180,128,48\n"
"DP 2,20,43,3072\nMvaa 180,132,52\nv_add_u32 v1,s34,v1\nv_add_u32 v2,s34,v2\nMvaa 188,132,60\nv_add_u32 v7,s94,v7\nv_add_u32 v8,s94,v8\nMvaa 188,128,56\n"
"s_waitcnt vmcnt(31) lgkmcnt(0)\ns_barrier\nMvaa 76,136,56\nRD 128,3,0\nMvaa 76,140,60\nRD 132,4,0\nMvaa 68,140,52\ns_add_u32 s15,s12,0\nMvaa 68,136,48\n"
"buffer_load_dwordx4 a[168:171],v0,s[24:27],s15 offen offset:3072 sc1\nMvaa 60,136,40\nMvaa 60,140,44\nMvaa 52,140,36\nMvaa 52,136,32\nMvaa 44,136,24\n"
"s_add_u32 s15,s12,16384\nMvaa 44,140,28\nbuffer_load_dwordx4 a[172:175],v0,s[24:27],s15 offen offset:3072 sc1\nMvaa 36,140,20\nMvaa 36,136,16\n"
"Mvaa 28,136,8\nMvaa 28,140,12\nMvaa 20,140,4\nMvaa 20,136,0\nWL 1\nMvaa 192,128,0\nWL 0\nRD 136,3,2048\nMvaa 192,132,4\nRD 140,4,2048\nMvaa 200,132,12\n"
"TR 96,5,0\nMvaa 200,128,8\nTR 98,5,1024\nMvaa 208,128,16\nTR 100,5,4096\nMvaa 208,132,20\nTR 102,5,5120\nMvaa 216,132,28\nTR 104,6,0\nMvaa 216,128,24\n"
"s_add_u32 m0,s48,s44\nTR 106,6,1024\nMvaa 224,128,32\nTR 108,6,4096\nMvaa 224,132,36\nDP 1,20,36,0\nMvaa 232,132,44\nTR 110,6,5120\nMvaa 232,128,40\n"
"TR 112,7,0\nMvaa 240,128,48\nDP 2,20,37,1024\nMvaa 240,132,52\nTR 114,7,1024\nMvaa 248,132,60\nTR 116,7,4096\nMvaa 248,128,56\nWL 12\nDP 1,20,38,2048\n"
"Mvaa 136,136,56\nWL 11\nRD 128,3,4096\nMvaa 136,140,60\nRD 132,4,4096\nMvaa 128,140,52\nTR 118,7,5120\nMvaa 128,136,48\nTR 120,8,0\nMvaa 120,136,40\n"
"TR 122,8,1024\nMvaa 120,140,44\nTR 124,8,4096\nMvaa 112,140,36\nTR 126,8,5120\nMvaa 112,136,32\nMvaa 104,136,24\nMvaa 104,140,28\nDP 2,20,39,3072\n"
"Mvaa 96,140,20\ns_add_u32 m0,m0,0x1000\nMvaa 96,136,16\nMvaa 88,136,8\nDP 1,20,40,0\nMvaa 88,140,12\nMvaa 80,140,4\nMvaa 80,136,0\nWL 6\n"
"DP 2,20,41,1024\nMvaa 196,128,0\nWL 5\nRD 136,3,6144\nMvaa 196,132,4\ns_mov_b32 s44,s47\nRD 140,4,6144\nMvaa 204,132,12\ns_add_u32 s47,s47,0x8000\n"
"Mvaa 204,128,8\ns_cmp_eq_u32 s47,0x28000\nMvaa 212,128,16\ns_cselect_b32 s94,s96,s95\nMvaa 212,132,20\ns_cselect_b32 s47,0,s47\nMvaa 220,132,28\n"
"Mvaa 220,128,24\nv_add_u32 v3,s94,v3\nv_add_u32 v4,s94,v4\nMvaa 228,128,32\nMvaa 228,132,36\nDP 1,20,42,2048\nMvaa 236,132,44\nMvaa 236,128,40\n"
"v_add_u32 v5,s94,v5\nv_add_u32 v6,s94,v6\nMvaa 244,128,48\nDP 2,20,43,3072\nMvaa 244,132,52\nv_add_u32 v1,s35,v1\nv_add_u32 v2,s35,v2\nMvaa 252,132,60\n"
"v_add_u32 v7,s94,v7\nv_add_u32 v8,s94,v8\nMvaa 252,128,56\ns_waitcnt vmcnt(30) lgkmcnt(0)\ns_barrier\nMvaa 140,136,56\nRD 128,3,0\nMvaa 140,140,60\n"
"RD 132,4,0\nMvaa 132,140,52\ns_add_u32 s15,s12,4096\nMvaa 132,136,48\nbuffer_load_dwordx4 a[176:179],v0,s[24:27],s15 offen offset:0 sc1\n"
"Mvaa 124,136,40\nMvaa 124,140,44\nMvaa 116,140,36\nMvaa 116,136,32\nMvaa 108,136,24\ns_add_u32 s15,s12,20480\nMvaa 108,140,28\n"
"buffer_load_dwordx4 a[180:183],v0,s[24:27],s15 offen offset:0 sc1\nMvaa 100,140,20\nMvaa 100,136,16\nMvaa 92,136,8\nMvaa 92,140,12\nMvaa 84,140,4\n"
"Mvaa 84,136,0\nWL 1\nMaaa 240,128,64\nWL 0\nRD 136,3,2048\nMaaa 240,132,68\nRD 140,4,2048\nMaaa 248,132,76\nTR 0,5,0\nMaaa 248,128,72\nTR 2,5,1024\n"
"Mvaa 144,128,80\nTR 4,5,4096\nMvaa 144,132,84\nTR 6,5,5120\nMvaa 152,132,92\nTR 8,6,0\nMvaa 152,128,88\ns_add_u32 m0,s48,s44\nTR 10,6,1024\n"
"Mvaa 160,128,96\nTR 12,6,4096\nMvaa 160,132,100\nDP 1,20,36,0\nMvaa 168,132,108\nTR 14,6,5120\nMvaa 168,128,104\nTR 16,7,0\nMvaa 176,128,112\n"
"DP 2,20,37,1024\nMvaa 176,132,116\nTR 18,7,1024\nMvaa 184,132,124\nTR 20,7,4096\nMvaa 184,128,120\nWL 12\nDP 1,20,38,2048\nMvaa 72,136,120\nWL 11\n"
"RD 128,3,4096\nMvaa 72,140,124\nRD 132,4,4096\nMvaa 64,140,116\nTR 22,7,5120\nMvaa 64,136,112\nTR 24,8,0\nMvaa 56,136,104\nTR 26,8,1024\n"
"Mvaa 56,140,108\nTR 28,8,4096\nMvaa 48,140,100\nTR 30,8,5120\nMvaa 48,136,96\nMvaa 40,136,88\nMvaa 40,140,92\nDP 2,20,39,3072\nMvaa 32,140,84\n"
"s_add_u32 m0,m0,0x1000\nglobal_load_dword a238,v13,s[8:9]\nMvaa 32,136,80\ns_add_u32 s8,s8,s18\ns_addc_u32 s9,s9,0\nMvaa 24,136,72\nDP 1,20,40,0\n"
"Mvaa 24,140,76\nMvaa 16,140,68\nMvaa 16,136,64\nWL 6\nDP 2,20,41,1024\nMaaa 244,128,64\nWL 5\nRD 136,3,6144\nMaaa 244,132,68\ns_mov_b32 s44,s47\n"
"RD 140,4,6144\nMaaa 252,132,76\ns_add_u32 s47,s47,0x8000\nMaaa 252,128,72\ns_cmp_eq_u32 s47,0x28000\nMvaa 148,128,80\ns_cselect_b32 s94,s96,s95\n"
"Mvaa 148,132,84\ns_cselect_b32 s47,0,s47\nMvaa 156,132,92\nMvaa 156,128,88\nv_add_u32 v3,s94,v3\nv_add_u32 v4,s94,v4\nMvaa 164,128,96\n"
"Mvaa 164,132,100\nDP 1,20,42,2048\nMvaa 172,132,108\nMvaa 172,128,104\nv_add_u32 v5,s94,v5\nv_add_u32 v6,s94,v6\nMvaa 180,128,112\nDP 2,20,43,3072\n"
"Mvaa 180,132,116\nv_add_u32 v1,s34,v1\nv_add_u32 v2,s34,v2\nMvaa 188,132,124\nv_add_u32 v7,s94,v7\nv_add_u32 v8,s94,v8\nMvaa 188,128,120\n"
"s_waitcnt vmcnt(31) lgkmcnt(0)\ns_barrier\nMvaa 76,136,120\nRD 128,3,0\nMvaa 76,140,124\nRD 132,4,0\nMvaa 68,140,116\ns_add_u32 s15,s12,4096\n"
"Mvaa 68,136,112\nbuffer_load_dwordx4 a[184:187],v0,s[24:27],s15 offen offset:1024 sc1\nMvaa 60,136,104\nMvaa 60,140,108\nMvaa 52,140,100\n"
"Mvaa 52,136,96\nMvaa 44,136,88\ns_add_u32 s15,s12,20480\nMvaa 44,140,92\nbuffer_load_dwordx4 a[188:191],v0,s[24:27],s15 offen offset:1024 sc1\n"
"Mvaa 36,140,84\nMvaa 36,136,80\nMvaa 28,136,72\nMvaa 28,140,76\nMvaa 20,140,68\nMvaa 20,136,64\nWL 1\nMvaa 192,128,64\nWL 0\nRD 136,3,2048\n"
"Mvaa 192,132,68\nRD 140,4,2048\nMvaa 200,132,76\nTR 32,5,0\nMvaa 200,128,72\nTR 34,5,1024\nMvaa 208,128,80\nTR 36,5,4096\nMvaa 208,132,84\n"
"TR 38,5,5120\nMvaa 216,132,92\nTR 40,6,0\nMvaa 216,128,88\ns_add_u32 m0,s48,s44\nTR 42,6,1024\nMvaa 224,128,96\nTR 44,6,4096\nMvaa 224,132,100\n"
"DP 1,20,36,0\nMvaa 232,132,108\nTR 46,6,5120\nMvaa 232,128,104\nTR 48,7,0\nMvaa 240,128,112\nDP 2,20,37,1024\nMvaa 240,132,116\nTR 50,7,1024\n"
"Mvaa 248,132,124\nTR 52,7,4096\nMvaa 248,128,120\nWL 12\nDP 1,20,38,2048\nMvaa 136,136,120\nWL 11\nRD 128,3,4096\nMvaa 136,140,124\nRD 132,4,4096\n"
"Mvaa 128,140,116\nTR 54,7,5120\nMvaa 128,136,112\nTR 56,8,0\nMvaa 120,136,104\nTR 58,8,1024\nMvaa 120,140,108\nTR 60,8,4096\nMvaa 112,140,100\n"
"TR 62,8,5120\nMvaa 112,136,96\nMvaa 104,136,88\nMvaa 104,140,92\nDP 2,20,39,3072\nMvaa 96,140,84\ns_add_u32 m0,m0,0x1000\nMvaa 96,136,80\n"
"Mvaa 88,136,72\nDP 1,20,40,0\nMvaa 88,140,76\nMvaa 80,140,68\nMvaa 80,136,64\nWL 6\nDP 2,20,41,1024\nMvaa 196,128,64\nWL 5\nRD 136,3,6144\n"
"Mvaa 196,132,68\ns_mov_b32 s44,s47\nRD 140,4,6144\nMvaa 204,132,76\ns_add_u32 s47,s47,0x8000\nMvaa 204,128,72\ns_cmp_eq_u32 s47,0x28000\n"
"Mvaa 212,128,80\ns_cselect_b32 s94,s96,s95\nMvaa 212,132,84\ns_cselect_b32 s47,0,s47\nMvaa 220,132,92\nMvaa 220,128,88\nv_add_u32 v3,s94,v3\n"
"v_add_u32 v4,s94,v4\nMvaa 228,128,96\nMvaa 228,132,100\nDP 1,20,42,2048\nMvaa 236,132,108\nMvaa 236,128,104\nv_add_u32 v5,s94,v5\nv_add_u32 v6,s94,v6\n"
"Mvaa 244,128,112\nDP 2,20,43,3072\nMvaa 244,132,116\nv_add_u32 v1,s35,v1\nv_add_u32 v2,s35,v2\nMvaa 252,132,124\nv_add_u32 v7,s94,v7\n"
"v_add_u32 v8,s94,v8\nMvaa 252,128,120\ns_waitcnt vmcnt(31) lgkmcnt(0)\ns_barrier\nMvaa 140,136,120\nRD 128,3,0\nMvaa 140,140,124\nRD 132,4,0\n"
"Mvaa 132,140,116\ns_add_u32 s15,s12,4096\nMvaa 132,136,112\nbuffer_load_dwordx4 a[192:195],v0,s[24:27],s15 offen offset:2048 sc1\nMvaa 124,136,104\n"
"Mvaa 124,140,108\nMvaa 116,140,100\nMvaa 116,136,96\nMvaa 108,136,88\ns_add_u32 s15,s12,20480\nMvaa 108,140,92\n"
"buffer_load_dwordx4 a[196:199],v0,s[24:27],s15 offen offset:2048 sc1\nMvaa 100,140,84\nMvaa 100,136,80\nMvaa 92,136,72\nMvaa 92,140,76\nMvaa 84,140,68\n"
"Mvaa 84,136,64\nWL 1\nMaaa 240,128,0\nWL 0\nRD 136,3,2048\nMaaa 240,132,4\nRD 140,4,2048\nMaaa 248,132,12\nTR 64,5,0\nMaaa 248,128,8\nTR 66,5,1024\n"
"Mvaa 144,128,16\nTR 68,5,4096\nMvaa 144,132,20\nTR 70,5,5120\nMvaa 152,132,28\nTR 72,6,0\nMvaa 152,128,24\ns_add_u32 m0,s48,s44\nTR 74,6,1024\n"
"Mvaa 160,128,32\nTR 76,6,4096\nMvaa 160,132,36\nDP 1,20,36,0\nMvaa 168,132,44\nTR 78,6,5120\nMvaa 168,128,40\nTR 80,7,0\nMvaa 176,128,48\n"
"DP 2,20,37,1024\nMvaa 176,132,52\nTR 82,7,1024\nMvaa 184,132,60\nTR 84,7,4096\nMvaa 184,128,56\nWL 12\nDP 1,20,38,2048\nMvaa 72,136,56\nWL 11\n"
"RD 128,3,4096\nMvaa 72,140,60\nRD 132,4,4096\nMvaa 64,140,52\nTR 86,7,5120\nMvaa 64,136,48\nTR 88,8,0\nMvaa 56,136,40\nTR 90,8,1024\nMvaa 56,140,44\n"
"TR 92,8,4096\nMvaa 48,140,36\nTR 94,8,5120\nMvaa 48,136,32\nMvaa 40,136,24\nMvaa 40,140,28\nDP 2,20,39,3072\nMvaa 32,140,20\ns_add_u32 m0,m0,0x1000\n"
"Mvaa 32,136,16\nMvaa 24,136,8\nDP 1,20,40,0\nMvaa 24,140,12\nMvaa 16,140,4\nMvaa 16,136,0\nWL 6\nDP 2,20,41,1024\nMaaa 244,128,0\nWL 5\nRD 136,3,6144\n"
"Maaa 244,132,4\ns_mov_b32 s44,s47\nRD 140,4,6144\nMaaa 252,132,12\ns_add_u32 s47,s47,0x8000\nMaaa 252,128,8\ns_cmp_eq_u32 s47,0x28000\nMvaa 148,128,16\n"
"s_cselect_b32 s94,s96,s95\nMvaa 148,132,20\ns_cselect_b32 s47,0,s47\nMvaa 156,132,28\nMvaa 156,128,24\nv_add_u32 v3,s94,v3\nv_add_u32 v4,s94,v4\n"
"Mvaa 164,128,32\nMvaa 164,132,36\nDP 1,20,42,2048\nMvaa 172,132,44\nMvaa 172,128,40\nv_add_u32 v5,s94,v5\nv_add_u32 v6,s94,v6\nMvaa 180,128,48\n"
"DP 2,20,43,3072\nMvaa 180,132,52\nv_add_u32 v1,s34,v1\nv_add_u32 v2,s34,v2\nMvaa 188,132,60\nv_add_u32 v7,s94,v7\nv_add_u32 v8,s94,v8\nMvaa 188,128,56\n"
"s_waitcnt vmcnt(31) lgkmcnt(0)\ns_barrier\nMvaa 76,136,56\nRD 128,3,0\nMvaa 76,140,60\nRD 132,4,0\nMvaa 68,140,52\ns_add_u32 s15,s12,4096\n"
"Mvaa 68,136,48\nbuffer_load_dwordx4 a[200:203],v0,s[24:27],s15 offen offset:3072 sc1\nMvaa 60,136,40\nMvaa 60,140,44\nMvaa 52,140,36\nMvaa 52,136,32\n"
"Mvaa 44,136,24\ns_add_u32 s15,s12,20480\nMvaa 44,140,28\nbuffer_load_dwordx4 a[204:207],v0,s[24:27],s15 offen offset:3072 sc1\nMvaa 36,140,20\n"
"Mvaa 36,136,16\nMvaa 28,136,8\nMvaa 28,140,12\nMvaa 20,140,4\nMvaa 20,136,0\nWL 1\nMvaa 192,128,0\nWL 0\nRD 136,3,2048\nMvaa 192,132,4\nRD 140,4,2048\n"
"Mvaa 200,132,12\nTR 96,5,0\nMvaa 200,128,8\nTR 98,5,1024\nMvaa 208,128,16\nTR 100,5,4096\nMvaa 208,132,20\nTR 102,5,5120\nMvaa 216,132,28\nTR 104,6,0\n"
"Mvaa 216,128,24\ns_add_u32 m0,s48,s44\nTR 106,6,1024\nMvaa 224,128,32\nTR 108,6,4096\nMvaa 224,132,36\nDP 1,20,36,0\nMvaa 232,132,44\nTR 110,6,5120\n"
"Mvaa 232,128,40\nTR 112,7,0\nMvaa 240,128,48\nDP 2,20,37,1024\nMvaa 240,132,52\nTR 114,7,1024\nMvaa 248,132,60\nTR 116,7,4096\nMvaa 248,128,56\nWL 12\n"
"DP 1,20,38,2048\nMvaa 136,136,56\nWL 11\nRD 128,3,4096\nMvaa 136,140,60\nRD 132,4,4096\nMvaa 128,140,52\nTR 118,7,5120\nMvaa 128,136,48\nTR 120,8,0\n"
"Mvaa 120,136,40\nTR 122,8,1024\nMvaa 120,140,44\nTR 124,8,4096\nMvaa 112,140,36\nTR 126,8,5120\nMvaa 112,136,32\nMvaa 104,136,24\nMvaa 104,140,28\n"
"DP 2,20,39,3072\nMvaa 96,140,20\ns_add_u32 m0,m0,0x1000\nMvaa 96,136,16\nMvaa 88,136,8\nDP 1,20,40,0\nMvaa 88,140,12\nMvaa 80,140,4\nMvaa 80,136,0\n"
"WL 6\nDP 2,20,41,1024\nMvaa 196,128,0\nWL 5\nRD 136,3,6144\nMvaa 196,132,4\ns_mov_b32 s44,s47\nRD 140,4,6144\nMvaa 204,132,12\n"
"s_add_u32 s47,s47,0x8000\nMvaa 204,128,8\ns_cmp_eq_u32 s47,0x28000\nMvaa 212,128,16\ns_cselect_b32 s94,s96,s95\nMvaa 212,132,20\n"
"s_cselect_b32 s47,0,s47\nMvaa 220,132,28\nMvaa 220,128,24\nv_add_u32 v3,s94,v3\nv_add_u32 v4,s94,v4\nMvaa 228,128,32\nMvaa 228,132,36\nDP 1,20,42,2048\n"
"Mvaa 236,132,44\nMvaa 236,128,40\nv_add_u32 v5,s94,v5\nv_add_u32 v6,s94,v6\nMvaa 244,128,48\nDP 2,20,43,3072\nMvaa 244,132,52\nv_add_u32 v1,s35,v1\n"
"v_add_u32 v2,s35,v2\nMvaa 252,132,60\nv_add_u32 v7,s94,v7\nv_add_u32 v8,s94,v8\nMvaa 252,128,56\ns_waitcnt vmcnt(30) lgkmcnt(0)\ns_barrier\n"
"Mvaa 140,136,56\nRD 128,3,0\nMvaa 140,140,60\nRD 132,4,0\nMvaa 132,140,52\ns_add_u32 s15,s12,8192\ns_lshr_b32 s70,s11,3\ns_mul_i32 s45,s70,4\n"
"s_mul_i32 s49,s10,1\ns_add_u32 s45,s45,s49\ns_lshl_b32 s45,s45,13\ns_cmp_lt_u32 s45,fused_op_end-fused_op_qproj-8191\ns_cbranch_scc0 fused_op_cw7\n"
"s_add_u32 s45,s45,fused_op_qproj-fused_op_pc\nv_lshlrev_b32 v12,3,v0\nv_add_u32 v12,s45,v12\nglobal_load_dword v11,v12,s[54:55]\nfused_op_cw7:\n"
"Mvaa 132,136,48\nbuffer_load_dwordx4 a[208:211],v0,s[24:27],s15 offen offset:0 sc1\nMvaa 124,136,40\nMvaa 124,140,44\nMvaa 116,140,36\nMvaa 116,136,32\n"
"Mvaa 108,136,24\ns_add_u32 s15,s12,24576\nMvaa 108,140,28\nbuffer_load_dwordx4 a[212:215],v0,s[24:27],s15 offen offset:0 sc1\nMvaa 100,140,20\n"
"Mvaa 100,136,16\nMvaa 92,136,8\nMvaa 92,140,12\nMvaa 84,140,4\nMvaa 84,136,0\nWL 1\nMaaa 240,128,64\nWL 0\nRD 136,3,2048\nMaaa 240,132,68\n"
"RD 140,4,2048\nMaaa 248,132,76\nTR 0,5,0\nMaaa 248,128,72\nTR 2,5,1024\nMvaa 144,128,80\nTR 4,5,4096\nMvaa 144,132,84\nTR 6,5,5120\nMvaa 152,132,92\n"
"TR 8,6,0\nMvaa 152,128,88\ns_add_u32 m0,s48,s44\nTR 10,6,1024\nMvaa 160,128,96\nTR 12,6,4096\nMvaa 160,132,100\nDP 1,20,36,0\nMvaa 168,132,108\n"
"TR 14,6,5120\nMvaa 168,128,104\nTR 16,7,0\nMvaa 176,128,112\nDP 2,20,37,1024\nMvaa 176,132,116\nTR 18,7,1024\nMvaa 184,132,124\nTR 20,7,4096\n"
"Mvaa 184,128,120\nWL 12\nDP 1,20,38,2048\nMvaa 72,136,120\nWL 11\nRD 128,3,4096\nMvaa 72,140,124\nRD 132,4,4096\nMvaa 64,140,116\nTR 22,7,5120\n"
"Mvaa 64,136,112\nTR 24,8,0\nMvaa 56,136,104\nTR 26,8,1024\nMvaa 56,140,108\nTR 28,8,4096\nMvaa 48,140,100\nTR 30,8,5120\nMvaa 48,136,96\n"
"Mvaa 40,136,88\nMvaa 40,140,92\nDP 2,20,39,3072\nMvaa 32,140,84\ns_add_u32 m0,m0,0x1000\nglobal_load_dword v9,v13,s[8:9]\nMvaa 32,136,80\n"
"s_add_u32 s8,s8,s18\ns_addc_u32 s9,s9,0\nMvaa 24,136,72\nDP 1,20,40,0\nMvaa 24,140,76\nMvaa 16,140,68\nMvaa 16,136,64\nWL 6\nDP 2,20,41,1024\n"
"Maaa 244,128,64\nWL 5\nRD 136,3,6144\nMaaa 244,132,68\ns_mov_b32 s44,s47\nRD 140,4,6144\nMaaa 252,132,76\ns_add_u32 s47,s47,0x8000\nMaaa 252,128,72\n"
"s_cmp_eq_u32 s47,0x28000\nMvaa 148,128,80\ns_cselect_b32 s94,s96,s95\nMvaa 148,132,84\ns_cselect_b32 s47,0,s47\nMvaa 156,132,92\nMvaa 156,128,88\n"
"v_add_u32 v3,s94,v3\nv_add_u32 v4,s94,v4\nMvaa 164,128,96\nMvaa 164,132,100\nDP 1,20,42,2048\nMvaa 172,132,108\nMvaa 172,128,104\nv_add_u32 v5,s94,v5\n"
"v_add_u32 v6,s94,v6\nMvaa 180,128,112\nDP 2,20,43,3072\nMvaa 180,132,116\nv_add_u32 v1,s34,v1\nv_add_u32 v2,s34,v2\nMvaa 188,132,124\n"
"v_add_u32 v7,s94,v7\nv_add_u32 v8,s94,v8\nMvaa 188,128,120\ns_waitcnt vmcnt(31) lgkmcnt(0)\ns_barrier\nMvaa 76,136,120\nRD 128,3,0\nMvaa 76,140,124\n"
"RD 132,4,0\nMvaa 68,140,116\ns_add_u32 s15,s12,8192\nMvaa 68,136,112\nbuffer_load_dwordx4 a[216:219],v0,s[24:27],s15 offen offset:1024 sc1\n"
"Mvaa 60,136,104\nMvaa 60,140,108\nMvaa 52,140,100\nMvaa 52,136,96\nMvaa 44,136,88\ns_add_u32 s15,s12,24576\nMvaa 44,140,92\n"
"buffer_load_dwordx4 a[220:223],v0,s[24:27],s15 offen offset:1024 sc1\nMvaa 36,140,84\nMvaa 36,136,80\nMvaa 28,136,72\nMvaa 28,140,76\nMvaa 20,140,68\n"
"Mvaa 20,136,64\nWL 1\nMvaa 192,128,64\nWL 0\nRD 136,3,2048\nMvaa 192,132,68\nRD 140,4,2048\nMvaa 200,132,76\nTR 32,5,0\nMvaa 200,128,72\nTR 34,5,1024\n"
"Mvaa 208,128,80\nTR 36,5,4096\nMvaa 208,132,84\nTR 38,5,5120\nMvaa 216,132,92\nTR 40,6,0\nMvaa 216,128,88\ns_add_u32 m0,s48,s44\nTR 42,6,1024\n"
"Mvaa 224,128,96\nTR 44,6,4096\nMvaa 224,132,100\nDP 1,20,36,0\nMvaa 232,132,108\nTR 46,6,5120\nMvaa 232,128,104\nTR 48,7,0\nMvaa 240,128,112\n"
"DP 2,20,37,1024\nMvaa 240,132,116\nTR 50,7,1024\nMvaa 248,132,124\nTR 52,7,4096\nMvaa 248,128,120\nWL 12\nDP 1,20,38,2048\nMvaa 136,136,120\nWL 11\n"
"RD 128,3,4096\nMvaa 136,140,124\nRD 132,4,4096\nMvaa 128,140,116\nTR 54,7,5120\nMvaa 128,136,112\nTR 56,8,0\nMvaa 120,136,104\nTR 58,8,1024\n"
"Mvaa 120,140,108\nTR 60,8,4096\nMvaa 112,140,100\nTR 62,8,5120\nMvaa 112,136,96\nMvaa 104,136,88\nMvaa 104,140,92\nDP 2,20,39,3072\nMvaa 96,140,84\n"
"s_add_u32 m0,m0,0x1000\nMvaa 96,136,80\nMvaa 88,136,72\nDP 1,20,40,0\nMvaa 88,140,76\nMvaa 80,140,68\nMvaa 80,136,64\nWL 6\nDP 2,20,41,1024\n"
"Mvaa 196,128,64\nWL 5\nRD 136,3,6144\nMvaa 196,132,68\ns_mov_b32 s44,s47\nRD 140,4,6144\nMvaa 204,132,76\ns_add_u32 s47,s47,0x8000\nMvaa 204,128,72\n"
"s_cmp_eq_u32 s47,0x28000\nMvaa 212,128,80\ns_cselect_b32 s94,s96,s95\nMvaa 212,132,84\ns_cselect_b32 s47,0,s47\nMvaa 220,132,92\nMvaa 220,128,88\n"
"v_add_u32 v3,s94,v3\nv_add_u32 v4,s94,v4\nMvaa 228,128,96\nMvaa 228,132,100\nDP 1,20,42,2048\nMvaa 236,132,108\nMvaa 236,128,104\nv_add_u32 v5,s94,v5\n"
"v_add_u32 v6,s94,v6\nMvaa 244,128,112\nDP 2,20,43,3072\nMvaa 244,132,116\nv_add_u32 v1,s35,v1\nv_add_u32 v2,s35,v2\nMvaa 252,132,124\n"
"v_add_u32 v7,s94,v7\nv_add_u32 v8,s94,v8\nMvaa 252,128,120\ns_waitcnt vmcnt(31) lgkmcnt(0)\ns_barrier\nMvaa 140,136,120\nRD 128,3,0\nMvaa 140,140,124\n"
"RD 132,4,0\nMvaa 132,140,116\ns_add_u32 s15,s12,8192\nMvaa 132,136,112\nbuffer_load_dwordx4 a[224:227],v0,s[24:27],s15 offen offset:2048 sc1\n"
"Mvaa 124,136,104\nMvaa 124,140,108\nMvaa 116,140,100\nMvaa 116,136,96\nMvaa 108,136,88\ns_add_u32 s15,s12,24576\nMvaa 108,140,92\n"
"buffer_load_dwordx4 a[228:231],v0,s[24:27],s15 offen offset:2048 sc1\nMvaa 100,140,84\nMvaa 100,136,80\nMvaa 92,136,72\nMvaa 92,140,76\nMvaa 84,140,68\n"
"Mvaa 84,136,64\nWL 1\nMaaa 240,128,0\nWL 0\ns_mul_i32 s45,s56,512\nRD 136,3,2048\nv_lshrrev_b32 v13,7,v0\nMaaa 240,132,4\ns_mul_i32 s49,s10,128\n"
"RD 140,4,2048\nv_and_b32 v1,6,v13\nMaaa 248,132,12\ns_add_u32 s45,s45,s49\nTR 64,5,0\nv_lshlrev_b32 v1,4,v1\nMaaa 248,128,8\ns_add_u32 s20,s80,s45\n"
"s_addc_u32 s21,s81,0\nTR 66,5,1024\nMvaa 144,128,16\ns_and_b32 s21,s21,0xffff\nTR 68,5,4096\nv_xor_b32 v1,v1,v0\nMvaa 144,132,20\n"
"s_sub_u32 s22,3670016,s45\nTR 70,5,5120\nv_and_b32 v1,0x70,v1\nMvaa 152,132,28\ns_mov_b32 s23,0x20000\nTR 72,6,0\nv_and_b32 v12,4,v13\nMvaa 152,128,24\n"
"s_mov_b32 s36,0\nTR 74,6,1024\nv_add_u32 v13,v13,v12\nMvaa 160,128,32\ns_mov_b32 s37,31744\nTR 76,6,4096\nv_mul_u32_u24 v13,8192,v13\nMvaa 160,132,36\n"
"s_mov_b32 s38,129024\nTR 78,6,5120\nv_add_u32 v1,v1,v13\nMvaa 168,132,44\ns_mov_b32 s39,160768\nTR 80,7,0\nMvaa 168,128,40\ns_mov_b32 s40,262144\n"
"TR 82,7,1024\nMvaa 176,128,48\ns_mov_b32 s41,293888\nTR 84,7,4096\nMvaa 176,132,52\ns_mov_b32 s42,391168\nTR 86,7,5120\nMvaa 184,132,60\n"
"s_mov_b32 s43,422912\nTR 88,8,0\nMvaa 184,128,56\nWL 14\nTR 90,8,1024\nMvaa 72,136,56\nWL 14\nRD 128,3,4096\nMvaa 72,140,60\nRD 132,4,4096\n"
"Mvaa 64,140,52\nTR 92,8,4096\nMvaa 64,136,48\nTR 94,8,5120\nMvaa 56,136,40\nMvaa 56,140,44\nMvaa 48,140,36\nMvaa 48,136,32\ns_add_u32 m0,s48,s44\n"
"Mvaa 40,136,24\nMvaa 40,140,28\nDP 1,20,36,0\nMvaa 32,140,20\nMvaa 32,136,16\nDP 1,20,37,1024\nMvaa 24,136,8\nMvaa 24,140,12\nDP 1,20,38,2048\n"
"Mvaa 16,140,4\nMvaa 16,136,0\nWL 3\nDP 1,20,39,3072\nMaaa 244,128,0\nWL 2\ns_add_u32 m0,m0,0x1000\nRD 136,3,6144\nMaaa 244,132,4\ns_mov_b32 s44,s47\n"
"RD 140,4,6144\nMaaa 252,132,12\ns_add_u32 s47,s47,0x8000\nMaaa 252,128,8\ns_cmp_eq_u32 s47,0x28000\nMvaa 148,128,16\ns_cselect_b32 s94,s96,s95\n"
"Mvaa 148,132,20\ns_cselect_b32 s47,0,s47\nMvaa 156,132,28\nDP 1,20,40,0\nMvaa 156,128,24\nv_add_u32 v3,s94,v3\nv_add_u32 v4,s94,v4\nMvaa 164,128,32\n"
"DP 1,20,41,1024\nMvaa 164,132,36\nMvaa 172,132,44\nDP 1,20,42,2048\nMvaa 172,128,40\nv_add_u32 v5,s94,v5\nv_add_u32 v6,s94,v6\nMvaa 180,128,48\n"
"DP 1,20,43,3072\nMvaa 180,132,52\nv_add_u32 v1,0x80000,v1\nMvaa 188,132,60\nv_add_u32 v7,s94,v7\nv_add_u32 v8,s94,v8\nMvaa 188,128,56\n"
"s_waitcnt vmcnt(31) lgkmcnt(0)\ns_barrier\nMvaa 76,136,56\nRD 128,3,0\nMvaa 76,140,60\nRD 132,4,0\nMvaa 68,140,52\ns_add_u32 s15,s12,8192\n"
"Mvaa 68,136,48\nbuffer_load_dwordx4 a[232:235],v0,s[24:27],s15 offen offset:3072 sc1\nMvaa 60,136,40\nMvaa 60,140,44\nMvaa 52,140,36\nMvaa 52,136,32\n"
"Mvaa 44,136,24\ns_add_u32 s15,s12,24576\nMvaa 44,140,28\nbuffer_load_dwordx4 a[236:239],v0,s[24:27],s15 offen offset:3072 sc1\nMvaa 36,140,20\n"
"Mvaa 36,136,16\nMvaa 28,136,8\nMvaa 28,140,12\nMvaa 20,140,4\nMvaa 20,136,0\nWL 1\nMvaa 192,128,0\nWL 0\nRD 136,3,2048\nMvaa 192,132,4\nRD 140,4,2048\n"
"Mvaa 200,132,12\nTR 96,5,0\nMvaa 200,128,8\nTR 98,5,1024\nMvaa 208,128,16\nTR 100,5,4096\nMvaa 208,132,20\nTR 102,5,5120\nMvaa 216,132,28\nTR 104,6,0\n"
"Mvaa 216,128,24\ns_add_u32 m0,s48,s44\nTR 106,6,1024\nMvaa 224,128,32\nTR 108,6,4096\nMvaa 224,132,36\nDP 1,20,36,0\nMvaa 232,132,44\nTR 110,6,5120\n"
"Mvaa 232,128,40\nTR 112,7,0\nMvaa 240,128,48\nDP 1,20,37,1024\nMvaa 240,132,52\nTR 114,7,1024\nMvaa 248,132,60\nTR 116,7,4096\nMvaa 248,128,56\nWL 12\n"
"DP 1,20,38,2048\nMvaa 136,136,56\nWL 11\nRD 128,3,4096\nMvaa 136,140,60\nRD 132,4,4096\nMvaa 128,140,52\nTR 118,7,5120\nMvaa 128,136,48\nTR 120,8,0\n"
"Mvaa 120,136,40\nTR 122,8,1024\nMvaa 120,140,44\nTR 124,8,4096\nMvaa 112,140,36\nTR 126,8,5120\nMvaa 112,136,32\nMvaa 104,136,24\nMvaa 104,140,28\n"
"DP 1,20,39,3072\nMvaa 96,140,20\ns_add_u32 m0,m0,0x1000\nMvaa 96,136,16\nMvaa 88,136,8\nDP 1,20,40,0\nMvaa 88,140,12\nMvaa 80,140,4\nMvaa 80,136,0\n"
"WL 6\nDP 1,20,41,1024\nMvaa 196,128,0\nWL 5\nRD 136,3,6144\nMvaa 196,132,4\ns_mov_b32 s44,s47\nRD 140,4,6144\nMvaa 204,132,12\n"
"s_add_u32 s47,s47,0x8000\nMvaa 204,128,8\ns_cmp_eq_u32 s47,0x28000\nMvaa 212,128,16\ns_cselect_b32 s94,s96,s95\nMvaa 212,132,20\n"
"s_cselect_b32 s47,0,s47\nMvaa 220,132,28\nMvaa 220,128,24\nv_add_u32 v3,s94,v3\nv_add_u32 v4,s94,v4\nMvaa 228,128,32\nMvaa 228,132,36\nDP 1,20,42,2048\n"
"Mvaa 236,132,44\nMvaa 236,128,40\nv_add_u32 v5,s94,v5\nv_add_u32 v6,s94,v6\nMvaa 244,128,48\nDP 1,20,43,3072\nMvaa 244,132,52\nv_add_u32 v1,0x80000,v1\n"
"Mvaa 252,132,60\nv_add_u32 v7,s94,v7\nv_add_u32 v8,s94,v8\nMvaa 252,128,56\ns_waitcnt vmcnt(30) lgkmcnt(0)\ns_barrier\nMvaa 140,136,56\nRD 128,3,0\n"
"Mvaa 140,140,60\nRD 132,4,0\nMvaa 132,140,52\nMvaa 132,136,48\nMvaa 124,136,40\nMvaa 124,140,44\nMvaa 116,140,36\nMvaa 116,136,32\nMvaa 108,136,24\n"
"Mvaa 108,140,28\nMvaa 100,140,20\nMvaa 100,136,16\nMvaa 92,136,8\nMvaa 92,140,12\nMvaa 84,140,4\nMvaa 84,136,0\nWL 1\nMaaa 240,128,64\nWL 0\n"
"RD 136,3,2048\nMaaa 240,132,68\nRD 140,4,2048\nMaaa 248,132,76\nMaaa 248,128,72\nMvaa 144,128,80\nMvaa 144,132,84\nMvaa 152,132,92\nMvaa 152,128,88\n"
"s_add_u32 m0,s48,s44\nMvaa 160,128,96\nMvaa 160,132,100\nDP 1,20,36,0\nMvaa 168,132,108\nMvaa 168,128,104\nMvaa 176,128,112\nDP 1,20,37,1024\n"
"Mvaa 176,132,116\nMvaa 184,132,124\nMvaa 184,128,120\nWL 1\nDP 1,20,38,2048\nMvaa 72,136,120\nWL 0\nRD 128,3,4096\nMvaa 72,140,124\nRD 132,4,4096\n"
"Mvaa 64,140,116\nMvaa 64,136,112\nMvaa 56,136,104\nMvaa 56,140,108\nMvaa 48,140,100\nMvaa 48,136,96\nMvaa 40,136,88\nMvaa 40,140,92\nDP 1,20,39,3072\n"
"Mvaa 32,140,84\ns_add_u32 m0,m0,0x1000\nCV 184,184,185\nCV 185,186,187\nMvaa 32,136,80\nCV 186,72,73\nCV 187,74,75\nMvaa 24,136,72\nDP 1,20,40,0\n"
"Mvaa 24,140,76\nCV 176,176,177\nCV 177,178,179\nMvaa 16,140,68\nCV 178,64,65\nCV 179,66,67\nMvaa 16,136,64\nWL 1\nDP 1,20,41,1024\nMaaa 244,128,64\n"
"WL 0\nRD 136,3,6144\nCV 168,168,169\nMaaa 244,132,68\ns_mov_b32 s44,s47\nRD 140,4,6144\nCV 169,170,171\nMaaa 252,132,76\ns_add_u32 s47,s47,0x8000\n"
"CV 152,152,153\nCV 153,154,155\nMaaa 252,128,72\ns_cmp_eq_u32 s47,0x28000\nCV 154,40,41\nCV 155,42,43\nMvaa 148,128,80\ns_cselect_b32 s94,s96,s95\n"
"CV 144,144,145\nCV 145,146,147\nMvaa 148,132,84\ns_cselect_b32 s47,0,s47\nCV 146,32,33\nCV 147,34,35\nMvaa 156,132,92\nCV 170,56,57\nCV 171,58,59\n"
"Mvaa 156,128,88\nv_add_u32 v3,s94,v3\nv_add_u32 v4,s94,v4\nMvaa 164,128,96\nCV 160,160,161\nCV 161,162,163\nMvaa 164,132,100\nDP 1,20,42,2048\n"
"Mvaa 172,132,108\nCV 162,48,49\nCV 163,50,51\nMvaa 172,128,104\nv_add_u32 v5,s94,v5\nv_add_u32 v6,s94,v6\nMvaa 180,128,112\nDP 1,20,43,3072\n"
"Mvaa 180,132,116\nv_add_u32 v1,0x80000,v1\nMvaa 188,132,124\nv_add_u32 v7,s94,v7\nv_add_u32 v8,s94,v8\nMvaa 188,128,120\n"
"s_waitcnt vmcnt(28) lgkmcnt(0)\ns_barrier\nMvaa 76,136,120\nRD 128,3,0\nMvaa 76,140,124\nRD 132,4,0\nMvaa 68,140,116\nMvaa 68,136,112\nMvaa 60,136,104\n"
"Mvaa 60,140,108\nMvaa 52,140,100\nMvaa 52,136,96\nMvaa 44,136,88\nMvaa 44,140,92\nMvaa 36,140,84\nCV 188,188,189\nCV 189,190,191\nMvaa 36,136,80\n"
"CV 190,76,77\nCV 191,78,79\nMvaa 28,136,72\nCV 180,180,181\nCV 181,182,183\nMvaa 28,140,76\nCV 182,68,69\nCV 183,70,71\nMvaa 20,140,68\nCV 172,172,173\n"
"CV 173,174,175\nMvaa 20,136,64\nWL 1\nCV 174,60,61\nCV 175,62,63\nMvaa 192,128,64\nWL 0\nRD 136,3,2048\nCV 164,164,165\nMvaa 192,132,68\nRD 140,4,2048\n"
"CV 165,166,167\nMvaa 200,132,76\nCV 156,156,157\nCV 157,158,159\nMvaa 200,128,72\nCV 158,44,45\nCV 159,46,47\nMvaa 208,128,80\nCV 148,148,149\n"
"CV 149,150,151\nMvaa 208,132,84\nCV 150,36,37\nCV 151,38,39\nMvaa 216,132,92\nCV 166,52,53\nCV 167,54,55\nMvaa 216,128,88\ns_add_u32 m0,s48,s44\n"
"Mvaa 224,128,96\nv_accvgpr_read_b32 v12,a240\nv_accvgpr_read_b32 v13,a241\nMvaa 224,132,100\nDP 1,20,36,0\nMvaa 232,132,108\n"
"v_accvgpr_read_b32 v14,a242\nv_accvgpr_read_b32 v15,a243\nMvaa 232,128,104\nCV 12,12,13\nCV 13,14,15\nMvaa 240,128,112\nDP 1,20,37,1024\n"
"Mvaa 240,132,116\nCV 14,16,17\nCV 15,18,19\nMvaa 248,132,124\nbuffer_store_dwordx4 v[12:15],v0,s[24:27],s30 offen offset:0 sc1\nMvaa 248,128,120\nWL 1\n"
"DP 1,20,38,2048\nMvaa 136,136,120\nWL 0\nRD 128,3,4096\nv_accvgpr_read_b32 v12,a244\nMvaa 136,140,124\nRD 132,4,4096\nv_accvgpr_read_b32 v13,a245\n"
"Mvaa 128,140,116\nv_accvgpr_read_b32 v14,a246\nv_accvgpr_read_b32 v15,a247\nMvaa 128,136,112\nCV 12,12,13\nCV 13,14,15\nMvaa 120,136,104\nCV 14,20,21\n"
"CV 15,22,23\nMvaa 120,140,108\nbuffer_store_dwordx4 v[12:15],v0,s[24:27],s30 offen offset:1024 sc1\nMvaa 112,140,100\ns_nop 0\n"
"v_accvgpr_read_b32 v12,a248\nv_accvgpr_read_b32 v13,a249\nMvaa 112,136,96\nv_accvgpr_read_b32 v14,a250\nv_accvgpr_read_b32 v15,a251\nMvaa 104,136,88\n"
"CV 12,12,13\nCV 13,14,15\nMvaa 104,140,92\nDP 1,20,39,3072\nMvaa 96,140,84\ns_add_u32 m0,m0,0x1000\nCV 14,24,25\nCV 15,26,27\nMvaa 96,136,80\n"
"buffer_store_dwordx4 v[12:15],v0,s[24:27],s30 offen offset:2048 sc1\nMvaa 88,136,72\nDP 1,20,40,0\nMvaa 88,140,76\nv_accvgpr_read_b32 v12,a252\n"
"v_accvgpr_read_b32 v13,a253\nMvaa 80,140,68\nv_accvgpr_read_b32 v14,a254\nv_accvgpr_read_b32 v15,a255\nMvaa 80,136,64\nWL 1\nDP 1,20,41,1024\n"
"Mvaa 196,128,64\nWL 0\nRD 136,3,6144\nCV 12,12,13\nMvaa 196,132,68\ns_mov_b32 s44,s47\nRD 140,4,6144\nCV 13,14,15\nMvaa 204,132,76\n"
"s_add_u32 s47,s47,0x8000\nCV 14,28,29\nCV 15,30,31\nMvaa 204,128,72\ns_cmp_eq_u32 s47,0x28000\n"
"buffer_store_dwordx4 v[12:15],v0,s[24:27],s30 offen offset:3072 sc1\nMvaa 212,128,80\ns_cselect_b32 s47,0,s47\nCV 248,248,249\nCV 249,250,251\n"
"Mvaa 212,132,84\ns_add_u32 s30,s30,s28\nCV 250,136,137\nCV 251,138,139\nMvaa 220,132,92\n"
"buffer_store_dwordx4 v[144:147],v0,s[24:27],s30 offen offset:0 sc1\nMvaa 220,128,88\nv_add_u32 v5,s47,v10\nv_xor_b32 v6,32,v5\nMvaa 228,128,96\n"
"CV 240,240,241\nCV 241,242,243\nMvaa 228,132,100\nDP 1,20,42,2048\nMvaa 236,132,108\n"
"buffer_store_dwordx4 v[148:151],v0,s[24:27],s30 offen offset:1024 sc1\nMvaa 236,128,104\nv_xor_b32 v7,64,v5\nv_xor_b32 v8,96,v5\nMvaa 244,128,112\n"
"DP 1,20,43,3072\nMvaa 244,132,116\nbuffer_store_dwordx4 v[152:155],v0,s[24:27],s30 offen offset:2048 sc1\nv_add_u32 v1,0x80000,v1\nMvaa 252,132,124\n"
"CV 242,128,129\nCV 243,130,131\nMvaa 252,128,120\ns_waitcnt vmcnt(33) lgkmcnt(0)\ns_barrier\nCV 232,232,233\nMvaa 140,136,120\nTR 0,5,0\n"
"buffer_store_dwordx4 v[156:159],v0,s[24:27],s30 offen offset:3072 sc1\nMvaa 140,140,124\ns_add_u32 s30,s30,s28\nTR 2,5,1024\nCV 233,234,235\n"
"Mvaa 132,140,116\nTR 4,5,4096\nCV 234,120,121\nMvaa 132,136,112\nTR 6,5,5120\nCV 235,122,123\nMvaa 124,136,104\nTR 8,6,0\nCV 224,224,225\n"
"Mvaa 124,140,108\nTR 10,6,1024\nCV 225,226,227\nMvaa 116,140,100\nTR 12,6,4096\nCV 226,112,113\nMvaa 116,136,96\nTR 14,6,5120\nCV 227,114,115\n"
"Mvaa 108,136,88\nTR 16,7,0\nCV 216,216,217\nMvaa 108,140,92\nTR 18,7,1024\nCV 217,218,219\nMvaa 100,140,84\nTR 20,7,4096\nCV 218,104,105\n"
"Mvaa 100,136,80\nTR 22,7,5120\nCV 219,106,107\nMvaa 92,136,72\nTR 24,8,0\nCV 208,208,209\nMvaa 92,140,76\nTR 26,8,1024\nCV 209,210,211\nMvaa 84,140,68\n"
"TR 28,8,4096\nCV 210,96,97\nMvaa 84,136,64\nTR 30,8,5120\nCV 211,98,99\nCV 200,200,201\nCV 201,202,203\nCV 202,88,89\nCV 203,90,91\nCV 192,192,193\n"
"CV 193,194,195\nCV 194,80,81\nCV 195,82,83\nCV 252,252,253\nCV 253,254,255\nCV 254,140,141\nCV 255,142,143\nCV 244,244,245\nCV 245,246,247\n"
"CV 246,132,133\nCV 247,134,135\nCV 236,236,237\nCV 237,238,239\nCV 238,124,125\nCV 239,126,127\nCV 228,228,229\nCV 229,230,231\nCV 230,116,117\n"
"CV 231,118,119\nCV 220,220,221\nCV 221,222,223\nCV 222,108,109\nCV 223,110,111\nCV 212,212,213\nCV 213,214,215\nCV 214,100,101\nCV 215,102,103\n"
"CV 204,204,205\nCV 205,206,207\nCV 206,92,93\nCV 207,94,95\nCV 196,196,197\nCV 197,198,199\nCV 198,84,85\nCV 199,86,87\nfused_op_qproj:\n"
"s_cmp_lg_u32 s58,0\ns_cbranch_scc1 fused_op_qb\ns_nop 1\nWL 14\nZvaa 16,0,144\nWL 12\nTR 32,5,8192\nTR 34,5,9216\nMvaa 16,4,152\nTR 36,5,12288\n"
"TR 38,5,13312\nZvaa 20,4,156\nTR 40,6,8192\nTR 42,6,9216\nMvaa 20,0,148\nWL 14\nTR 44,6,12288\nZvaa 24,8,144\nTR 46,6,13312\nMvaa 24,12,152\n"
"TR 48,7,8192\nZvaa 28,12,156\nTR 50,7,9216\nMvaa 28,8,148\nWL 14\ns_add_u32 m0,s48,s44\nTR 52,7,12288\nZvaa 32,16,144\nTR 54,7,13312\nMvaa 32,20,152\n"
"s_add_u32 s15,s99,0\nDP 1,20,36,0\nZvaa 36,20,156\nTR 56,8,8192\nbuffer_load_dwordx4 a[64:67],v0,s[24:27],s15 offen offset:0 sc1\nMvaa 36,16,148\n"
"WL 14\nTR 58,8,9216\nZvaa 40,24,144\nWL 14\nDP 1,20,37,1024\nMvaa 40,28,152\nTR 60,8,12288\nZvaa 44,28,156\nTR 62,8,13312\nMvaa 44,24,148\nWL 14\n"
"DP 1,20,38,2048\nZvaa 48,32,144\nWL 12\nTR 0,5,16384\nTR 2,5,17408\nMvaa 48,36,152\nTR 4,5,20480\nTR 6,5,21504\nZvaa 52,36,156\nTR 8,6,16384\n"
"TR 10,6,17408\nMvaa 52,32,148\nWL 14\nTR 12,6,20480\nZvaa 56,40,144\nTR 14,6,21504\nMvaa 56,44,152\nTR 16,7,16384\nZvaa 60,44,156\nTR 18,7,17408\n"
"Mvaa 60,40,148\nWL 14\nTR 20,7,20480\nZvaa 64,48,144\nTR 22,7,21504\nMvaa 64,52,152\ns_add_u32 s15,s99,0\nDP 1,20,39,3072\nZvaa 68,52,156\n"
"s_add_u32 m0,m0,0x1000\nTR 24,8,16384\nbuffer_load_dwordx4 a[68:71],v0,s[24:27],s15 offen offset:1024 sc1\nMvaa 68,48,148\nWL 14\nTR 26,8,17408\n"
"Zvaa 72,56,144\nWL 14\nDP 1,20,40,0\nMvaa 72,60,152\nTR 28,8,20480\nZvaa 76,60,156\nTR 30,8,21504\nMvaa 76,56,148\nWL 14\nDP 1,20,41,1024\n"
"Zvaa 80,0,144\nWL 12\nTR 32,5,24576\nTR 34,5,25600\nMvaa 80,4,152\ns_mov_b32 s44,s47\nTR 36,5,28672\nTR 38,5,29696\nZvaa 84,4,156\n"
"s_add_u32 s47,s47,0x8000\nTR 40,6,24576\nTR 42,6,25600\nMvaa 84,0,148\nWL 13\ns_cmp_eq_u32 s47,0x28000\nTR 44,6,28672\nTR 46,6,29696\nZvaa 88,8,144\n"
"s_cselect_b32 s47,0,s47\nTR 48,7,24576\nTR 50,7,25600\nMvaa 88,12,152\nTR 52,7,28672\nTR 54,7,29696\nZvaa 92,12,156\nTR 56,8,24576\nTR 58,8,25600\n"
"Mvaa 92,8,148\nWL 14\nTR 60,8,28672\nZvaa 96,16,144\nTR 62,8,29696\nMvaa 96,20,152\nDP 1,20,42,2048\nZvaa 100,20,156\ns_add_u32 s15,s98,0\n"
"v_add_u32 v5,s47,v10\nv_xor_b32 v6,32,v5\nMvaa 100,16,148\nbuffer_load_dwordx4 a[72:75],v0,s[24:27],s15 offen offset:0 sc1\nv_xor_b32 v7,64,v5\n"
"v_xor_b32 v8,96,v5\nZvaa 104,24,144\nDP 1,20,43,3072\nMvaa 104,28,152\nv_add_u32 v1,0x80000,v1\nZvaa 108,28,156\n"
"buffer_store_dwordx4 v[160:163],v0,s[24:27],s30 offen offset:0 sc1\nMvaa 108,24,148\ns_waitcnt vmcnt(28) lgkmcnt(0)\ns_barrier\nZvaa 112,32,144\n"
"TR 0,5,0\nMvaa 112,36,152\nTR 2,5,1024\nZvaa 116,36,156\nTR 4,5,4096\nMvaa 116,32,148\nTR 6,5,5120\nZvaa 120,40,144\nTR 8,6,0\nMvaa 120,44,152\n"
"TR 10,6,1024\nZvaa 124,44,156\nTR 12,6,4096\nMvaa 124,40,148\nTR 14,6,5120\nZvaa 128,48,144\nTR 16,7,0\nMvaa 128,52,152\nTR 18,7,1024\nZvaa 132,52,156\n"
"TR 20,7,4096\nMvaa 132,48,148\nTR 22,7,5120\nZvaa 136,56,144\nTR 24,8,0\nMvaa 136,60,152\nTR 26,8,1024\nZvaa 140,60,156\nTR 28,8,4096\nMvaa 140,56,148\n"
"WL 13\nTR 30,8,5120\nMvaa 16,0,160\nWL 12\nTR 32,5,8192\nTR 34,5,9216\nMvaa 16,4,168\nTR 36,5,12288\nTR 38,5,13312\nMvaa 20,4,172\nTR 40,6,8192\n"
"TR 42,6,9216\nMvaa 20,0,164\nWL 14\nTR 44,6,12288\nMvaa 24,8,160\nTR 46,6,13312\nMvaa 24,12,168\nTR 48,7,8192\nMvaa 28,12,172\nTR 50,7,9216\n"
"Mvaa 28,8,164\nWL 14\ns_add_u32 m0,s48,s44\nTR 52,7,12288\nMvaa 32,16,160\nTR 54,7,13312\nMvaa 32,20,168\ns_add_u32 s15,s12,12288\nDP 1,20,36,0\n"
"Mvaa 36,20,172\nTR 56,8,8192\nbuffer_load_dwordx4 a[128:131],v0,s[24:27],s15 offen offset:0 sc1\nMvaa 36,16,164\nWL 14\nTR 58,8,9216\nMvaa 40,24,160\n"
"WL 14\nDP 1,20,37,1024\nMvaa 40,28,168\nTR 60,8,12288\nMvaa 44,28,172\nTR 62,8,13312\nMvaa 44,24,164\nWL 14\nDP 1,20,38,2048\nMvaa 48,32,160\nWL 12\n"
"TR 0,5,16384\nTR 2,5,17408\nMvaa 48,36,168\nTR 4,5,20480\nTR 6,5,21504\nMvaa 52,36,172\ns_add_u32 s15,s12,12288\nTR 8,6,16384\nTR 10,6,17408\n"
"Mvaa 52,32,164\nWL 14\nTR 12,6,20480\nbuffer_load_dwordx4 a[136:139],v0,s[24:27],s15 offen offset:1024 sc1\nMvaa 56,40,160\nTR 14,6,21504\n"
"Mvaa 56,44,168\nTR 16,7,16384\nMvaa 60,44,172\nTR 18,7,17408\nMvaa 60,40,164\nWL 14\nTR 20,7,20480\nMvaa 64,48,160\nTR 22,7,21504\nMvaa 64,52,168\n"
"DP 1,20,39,3072\nMvaa 68,52,172\ns_add_u32 m0,m0,0x1000\nTR 24,8,16384\nMvaa 68,48,164\nWL 14\nTR 26,8,17408\nMvaa 72,56,160\nWL 14\n"
"s_add_u32 s15,s12,28672\nDP 1,20,40,0\nMvaa 72,60,168\nTR 28,8,20480\nbuffer_load_dwordx4 a[132:135],v0,s[24:27],s15 offen offset:0 sc1\n"
"Mvaa 76,60,172\nTR 30,8,21504\nMvaa 76,56,164\nWL 14\nDP 1,20,41,1024\nMvaa 80,0,160\nWL 12\nTR 32,5,24576\nTR 34,5,25600\nMvaa 80,4,168\n"
"s_mov_b32 s44,s47\nTR 36,5,28672\nTR 38,5,29696\nMvaa 84,4,172\ns_add_u32 s47,s47,0x8000\nTR 40,6,24576\nTR 42,6,25600\nMvaa 84,0,164\nWL 13\n"
"s_cmp_eq_u32 s47,0x28000\nTR 44,6,28672\nTR 46,6,29696\nMvaa 88,8,160\ns_cselect_b32 s47,0,s47\nTR 48,7,24576\nTR 50,7,25600\nMvaa 88,12,168\n"
"TR 52,7,28672\nTR 54,7,29696\nMvaa 92,12,172\ns_add_u32 s15,s12,28672\nTR 56,8,24576\nTR 58,8,25600\nMvaa 92,8,164\nWL 14\nTR 60,8,28672\n"
"buffer_load_dwordx4 a[140:143],v0,s[24:27],s15 offen offset:1024 sc1\nMvaa 96,16,160\nTR 62,8,29696\nMvaa 96,20,168\nDP 1,20,42,2048\nMvaa 100,20,172\n"
"v_add_u32 v5,s47,v10\nv_xor_b32 v6,32,v5\nMvaa 100,16,164\nv_xor_b32 v7,64,v5\nv_xor_b32 v8,96,v5\nMvaa 104,24,160\nDP 1,20,43,3072\nMvaa 104,28,168\n"
"v_add_u32 v1,0x80000,v1\nMvaa 108,28,172\nbuffer_store_dwordx4 v[164:167],v0,s[24:27],s30 offen offset:1024 sc1\nMvaa 108,24,164\n"
"s_waitcnt vmcnt(33) lgkmcnt(0)\ns_barrier\nMvaa 112,32,160\ns_add_u32 s15,s98,0\nTR 0,5,0\nMvaa 112,36,168\nTR 2,5,1024\n"
"buffer_load_dwordx4 a[76:79],v0,s[24:27],s15 offen offset:1024 sc1\nMvaa 116,36,172\nTR 4,5,4096\nMvaa 116,32,164\nTR 6,5,5120\nMvaa 120,40,160\n"
"TR 8,6,0\nMvaa 120,44,168\nTR 10,6,1024\nMvaa 124,44,172\nTR 12,6,4096\nMvaa 124,40,164\nTR 14,6,5120\nMvaa 128,48,160\nTR 16,7,0\nMvaa 128,52,168\n"
"TR 18,7,1024\nMvaa 132,52,172\nTR 20,7,4096\nMvaa 132,48,164\nTR 22,7,5120\nMvaa 136,56,160\nTR 24,8,0\nMvaa 136,60,168\nTR 26,8,1024\nMvaa 140,60,172\n"
"TR 28,8,4096\nMvaa 140,56,164\nWL 13\nTR 30,8,5120\nMvaa 16,0,176\nWL 12\nTR 32,5,8192\nTR 34,5,9216\nMvaa 16,4,184\nTR 36,5,12288\nTR 38,5,13312\n"
"Mvaa 20,4,188\nTR 40,6,8192\nTR 42,6,9216\nMvaa 20,0,180\nWL 14\nTR 44,6,12288\nMvaa 24,8,176\nTR 46,6,13312\nMvaa 24,12,184\nTR 48,7,8192\n"
"Mvaa 28,12,188\nTR 50,7,9216\nMvaa 28,8,180\nWL 14\ns_add_u32 m0,s48,s44\nTR 52,7,12288\nMvaa 32,16,176\nTR 54,7,13312\nMvaa 32,20,184\n"
"s_add_u32 s15,s98,16384\nDP 1,20,36,0\nMvaa 36,20,188\nTR 56,8,8192\nbuffer_load_dwordx4 a[80:83],v0,s[24:27],s15 offen offset:0 sc1\nMvaa 36,16,180\n"
"WL 14\nTR 58,8,9216\nMvaa 40,24,176\nWL 14\nDP 1,20,37,1024\nMvaa 40,28,184\nTR 60,8,12288\nMvaa 44,28,188\nTR 62,8,13312\nMvaa 44,24,180\nWL 14\n"
"DP 1,20,38,2048\nMvaa 48,32,176\nWL 12\nTR 0,5,16384\nTR 2,5,17408\nMvaa 48,36,184\ns_add_u32 s15,s98,16384\nTR 4,5,20480\nTR 6,5,21504\n"
"Mvaa 52,36,188\nTR 8,6,16384\nTR 10,6,17408\nbuffer_load_dwordx4 a[84:87],v0,s[24:27],s15 offen offset:1024 sc1\nMvaa 52,32,180\nWL 14\nTR 12,6,20480\n"
"Mvaa 56,40,176\nTR 14,6,21504\nMvaa 56,44,184\nTR 16,7,16384\nMvaa 60,44,188\nTR 18,7,17408\nMvaa 60,40,180\nWL 14\nTR 20,7,20480\nMvaa 64,48,176\n"
"TR 22,7,21504\nMvaa 64,52,184\nDP 1,20,39,3072\nMvaa 68,52,188\ns_add_u32 m0,m0,0x1000\nTR 24,8,16384\nMvaa 68,48,180\nWL 14\nTR 26,8,17408\n"
"Mvaa 72,56,176\nWL 14\ns_add_u32 s15,s99,0\nDP 1,20,40,0\nMvaa 72,60,184\nTR 28,8,20480\n"
"buffer_load_dwordx4 a[88:91],v0,s[24:27],s15 offen offset:2048 sc1\nMvaa 76,60,188\nTR 30,8,21504\nMvaa 76,56,180\nWL 14\nDP 1,20,41,1024\n"
"Mvaa 80,0,176\nWL 12\nTR 32,5,24576\nTR 34,5,25600\nMvaa 80,4,184\ns_mov_b32 s44,s47\nTR 36,5,28672\nTR 38,5,29696\nMvaa 84,4,188\n"
"s_add_u32 s47,s47,0x8000\nTR 40,6,24576\nTR 42,6,25600\nMvaa 84,0,180\nWL 13\ns_cmp_eq_u32 s47,0x28000\nTR 44,6,28672\nTR 46,6,29696\nMvaa 88,8,176\n"
"s_cselect_b32 s47,0,s47\nTR 48,7,24576\nTR 50,7,25600\nMvaa 88,12,184\ns_add_u32 s15,s99,0\nTR 52,7,28672\nTR 54,7,29696\nMvaa 92,12,188\n"
"TR 56,8,24576\nTR 58,8,25600\nbuffer_load_dwordx4 a[92:95],v0,s[24:27],s15 offen offset:3072 sc1\nMvaa 92,8,180\nWL 14\nTR 60,8,28672\nMvaa 96,16,176\n"
"TR 62,8,29696\nMvaa 96,20,184\nDP 1,20,42,2048\nMvaa 100,20,188\nv_add_u32 v5,s47,v10\nv_xor_b32 v6,32,v5\nMvaa 100,16,180\nv_xor_b32 v7,64,v5\n"
"v_xor_b32 v8,96,v5\nMvaa 104,24,176\nDP 1,20,43,3072\nMvaa 104,28,184\nbuffer_store_dwordx4 v[168:171],v0,s[24:27],s30 offen offset:2048 sc1\n"
"Mvaa 108,28,188\ns_mul_i32 s45,s57,172032\nMvaa 108,24,180\ns_waitcnt vmcnt(39) lgkmcnt(0)\ns_barrier\nMvaa 112,32,176\ns_mul_i32 s49,s10,8192\n"
"TR 0,5,0\nMvaa 112,36,184\ns_add_u32 s45,s45,s49\nTR 2,5,1024\nMvaa 116,36,188\ns_add_u32 s45,s45,0x4700000\nTR 4,5,4096\nMvaa 116,32,180\n"
"s_cmp_eq_u32 s10,3\ns_cselect_b32 s45,0x7ff00000,s45\nTR 6,5,5120\nMvaa 120,40,176\ns_mov_b64 s[20:21],s[24:25]\nTR 8,6,0\nMvaa 120,44,184\n"
"s_mov_b64 s[22:23],s[26:27]\nTR 10,6,1024\nMvaa 124,44,188\ns_mov_b32 s36,0\nTR 12,6,4096\nMvaa 124,40,180\ns_mov_b32 s37,0\nTR 14,6,5120\n"
"Mvaa 128,48,176\ns_mov_b32 s38,0\nTR 16,7,0\nMvaa 128,52,184\ns_mov_b32 s39,0\nTR 18,7,1024\nMvaa 132,52,188\ns_mov_b32 s40,4096\nTR 20,7,4096\n"
"Mvaa 132,48,180\ns_mov_b32 s41,4096\nTR 22,7,5120\nMvaa 136,56,176\ns_mov_b32 s42,4096\nTR 24,8,0\nMvaa 136,60,184\ns_mov_b32 s43,4096\nTR 26,8,1024\n"
"v_add_u32 v1,s45,v0\nMvaa 140,60,188\nTR 28,8,4096\nMvaa 140,56,180\nWL 13\nTR 30,8,5120\nMvaa 16,0,192\nWL 12\nTR 32,5,8192\nTR 34,5,9216\n"
"Mvaa 16,4,200\nTR 36,5,12288\nTR 38,5,13312\nMvaa 20,4,204\nTR 40,6,8192\nTR 42,6,9216\nMvaa 20,0,196\nWL 14\nTR 44,6,12288\nMvaa 24,8,192\n"
"TR 46,6,13312\nMvaa 24,12,200\nTR 48,7,8192\nMvaa 28,12,204\nTR 50,7,9216\nMvaa 28,8,196\nWL 14\ns_add_u32 m0,s48,s44\nTR 52,7,12288\nMvaa 32,16,192\n"
"TR 54,7,13312\nMvaa 32,20,200\ns_add_u32 s15,s98,0\nDS 1,20,36,0\nMvaa 36,20,204\nTR 56,8,8192\n"
"buffer_load_dwordx4 a[96:99],v0,s[24:27],s15 offen offset:2048 sc1\nMvaa 36,16,196\nWL 14\nTR 58,8,9216\nMvaa 40,24,192\nWL 14\nDS 1,20,37,1024\n"
"Mvaa 40,28,200\nTR 60,8,12288\nMvaa 44,28,204\nTR 62,8,13312\nMvaa 44,24,196\nWL 14\nDS 1,20,38,2048\nMvaa 48,32,192\nWL 12\nTR 0,5,16384\n"
"TR 2,5,17408\nMvaa 48,36,200\nTR 4,5,20480\nTR 6,5,21504\nMvaa 52,36,204\nTR 8,6,16384\nTR 10,6,17408\nMvaa 52,32,196\nWL 14\nTR 12,6,20480\n"
"Mvaa 56,40,192\nTR 14,6,21504\nMvaa 56,44,200\ns_add_u32 s15,s98,0\nTR 16,7,16384\nMvaa 60,44,204\nTR 18,7,17408\n"
"buffer_load_dwordx4 a[100:103],v0,s[24:27],s15 offen offset:3072 sc1\nMvaa 60,40,196\nWL 14\nTR 20,7,20480\nMvaa 64,48,192\nTR 22,7,21504\n"
"Mvaa 64,52,200\nDS 1,20,39,3072\nMvaa 68,52,204\ns_add_u32 m0,m0,0x1000\nTR 24,8,16384\nMvaa 68,48,196\nWL 14\nTR 26,8,17408\nMvaa 72,56,192\nWL 14\n"
"DS 1,20,40,0\nMvaa 72,60,200\nTR 28,8,20480\nMvaa 76,60,204\nTR 30,8,21504\nMvaa 76,56,196\nWL 14\nDS 1,20,41,1024\nMvaa 80,0,192\nWL 12\n"
"TR 32,5,24576\nTR 34,5,25600\nMvaa 80,4,200\ns_mov_b32 s44,s47\nTR 36,5,28672\nTR 38,5,29696\nMvaa 84,4,204\ns_add_u32 s47,s47,0x8000\nTR 40,6,24576\n"
"TR 42,6,25600\nMvaa 84,0,196\nWL 13\ns_cmp_eq_u32 s47,0x28000\nTR 44,6,28672\nTR 46,6,29696\nMvaa 88,8,192\ns_cselect_b32 s47,0,s47\nTR 48,7,24576\n"
"TR 50,7,25600\nMvaa 88,12,200\ns_add_u32 s15,s98,16384\nTR 52,7,28672\nTR 54,7,29696\nMvaa 92,12,204\nTR 56,8,24576\nTR 58,8,25600\n"
"buffer_load_dwordx4 a[104:107],v0,s[24:27],s15 offen offset:2048 sc1\nMvaa 92,8,196\nWL 14\nTR 60,8,28672\nMvaa 96,16,192\nTR 62,8,29696\n"
"Mvaa 96,20,200\nDS 1,20,42,2048\nMvaa 100,20,204\nv_add_u32 v5,s47,v10\nv_xor_b32 v6,32,v5\nMvaa 100,16,196\nv_xor_b32 v7,64,v5\nv_xor_b32 v8,96,v5\n"
"Mvaa 104,24,192\nDS 1,20,43,3072\nMvaa 104,28,200\nv_add_u32 v1,0x6000,v1\nMvaa 108,28,204\n"
"buffer_store_dwordx4 v[172:175],v0,s[24:27],s30 offen offset:3072 sc1\nMvaa 108,24,196\ns_waitcnt vmcnt(40) lgkmcnt(0)\ns_barrier\nMvaa 112,32,192\n"
"s_add_u32 s15,s98,16384\nTR 0,5,0\nMvaa 112,36,200\ns_add_u32 s30,s30,s28\nTR 2,5,1024\n"
"buffer_load_dwordx4 a[108:111],v0,s[24:27],s15 offen offset:3072 sc1\nMvaa 116,36,204\nTR 4,5,4096\nMvaa 116,32,196\nTR 6,5,5120\nMvaa 120,40,192\n"
"TR 8,6,0\nMvaa 120,44,200\nTR 10,6,1024\nMvaa 124,44,204\nTR 12,6,4096\nMvaa 124,40,196\nTR 14,6,5120\nMvaa 128,48,192\nTR 16,7,0\nMvaa 128,52,200\n"
"TR 18,7,1024\nMvaa 132,52,204\nTR 20,7,4096\nMvaa 132,48,196\nTR 22,7,5120\nMvaa 136,56,192\nTR 24,8,0\nMvaa 136,60,200\nTR 26,8,1024\nMvaa 140,60,204\n"
"TR 28,8,4096\nMvaa 140,56,196\nWL 13\nTR 30,8,5120\nMvaa 16,0,208\nWL 12\nTR 32,5,8192\nTR 34,5,9216\nMvaa 16,4,216\nTR 36,5,12288\nTR 38,5,13312\n"
"Mvaa 20,4,220\nTR 40,6,8192\nTR 42,6,9216\nMvaa 20,0,212\nWL 14\nTR 44,6,12288\nMvaa 24,8,208\nTR 46,6,13312\nMvaa 24,12,216\nTR 48,7,8192\n"
"Mvaa 28,12,220\nTR 50,7,9216\nMvaa 28,8,212\nWL 14\ns_add_u32 m0,s48,s44\nTR 52,7,12288\nMvaa 32,16,208\nTR 54,7,13312\nMvaa 32,20,216\n"
"s_add_u32 s15,s99,4096\nDS 1,20,36,0\nMvaa 36,20,220\nTR 56,8,8192\nbuffer_load_dwordx4 a[112:115],v0,s[24:27],s15 offen offset:0 sc1\nMvaa 36,16,212\n"
"WL 14\nTR 58,8,9216\nMvaa 40,24,208\nWL 14\nDS 1,20,37,1024\nMvaa 40,28,216\nTR 60,8,12288\nMvaa 44,28,220\nTR 62,8,13312\nMvaa 44,24,212\nWL 14\n"
"DS 1,20,38,2048\nMvaa 48,32,208\nWL 12\nTR 0,5,16384\nTR 2,5,17408\nMvaa 48,36,216\nTR 4,5,20480\nTR 6,5,21504\nMvaa 52,36,220\nTR 8,6,16384\n"
"TR 10,6,17408\nMvaa 52,32,212\nWL 14\nTR 12,6,20480\nMvaa 56,40,208\nTR 14,6,21504\nMvaa 56,44,216\ns_add_u32 s15,s99,4096\nTR 16,7,16384\n"
"Mvaa 60,44,220\nTR 18,7,17408\nbuffer_load_dwordx4 a[116:119],v0,s[24:27],s15 offen offset:1024 sc1\nMvaa 60,40,212\nWL 14\nTR 20,7,20480\n"
"Mvaa 64,48,208\nTR 22,7,21504\nMvaa 64,52,216\nDS 1,20,39,3072\nMvaa 68,52,220\ns_add_u32 m0,m0,0x1000\nTR 24,8,16384\nMvaa 68,48,212\nWL 14\n"
"TR 26,8,17408\nMvaa 72,56,208\nWL 14\nDS 1,20,40,0\nMvaa 72,60,216\nTR 28,8,20480\nMvaa 76,60,220\nTR 30,8,21504\nMvaa 76,56,212\nWL 14\n"
"DS 1,20,41,1024\nMvaa 80,0,208\nWL 12\nTR 32,5,24576\nTR 34,5,25600\nMvaa 80,4,216\ns_mov_b32 s44,s47\nTR 36,5,28672\nTR 38,5,29696\nMvaa 84,4,220\n"
"s_add_u32 s47,s47,0x8000\nTR 40,6,24576\nTR 42,6,25600\nMvaa 84,0,212\nWL 13\ns_cmp_eq_u32 s47,0x28000\nTR 44,6,28672\nTR 46,6,29696\nMvaa 88,8,208\n"
"s_cselect_b32 s47,0,s47\nTR 48,7,24576\nTR 50,7,25600\nMvaa 88,12,216\ns_add_u32 s15,s98,4096\nTR 52,7,28672\nTR 54,7,29696\nMvaa 92,12,220\n"
"TR 56,8,24576\nTR 58,8,25600\nbuffer_load_dwordx4 a[120:123],v0,s[24:27],s15 offen offset:0 sc1\nMvaa 92,8,212\nWL 14\nTR 60,8,28672\nMvaa 96,16,208\n"
"TR 62,8,29696\nMvaa 96,20,216\nDS 1,20,42,2048\nMvaa 100,20,220\nv_add_u32 v5,s47,v10\nv_xor_b32 v6,32,v5\nMvaa 100,16,212\nv_xor_b32 v7,64,v5\n"
"v_xor_b32 v8,96,v5\nMvaa 104,24,208\nDS 1,20,43,3072\nMvaa 104,28,216\nv_add_u32 v1,0x6000,v1\nMvaa 108,28,220\n"
"buffer_store_dwordx4 v[176:179],v0,s[24:27],s30 offen offset:0 sc1\nMvaa 108,24,212\ns_waitcnt vmcnt(40) lgkmcnt(0)\ns_barrier\nMvaa 112,32,208\n"
"s_add_u32 s15,s98,4096\nTR 0,5,0\nMvaa 112,36,216\nTR 2,5,1024\nbuffer_load_dwordx4 a[124:127],v0,s[24:27],s15 offen offset:1024 sc1\nMvaa 116,36,220\n"
"TR 4,5,4096\nMvaa 116,32,212\nTR 6,5,5120\nMvaa 120,40,208\nTR 8,6,0\nMvaa 120,44,216\nTR 10,6,1024\nMvaa 124,44,220\nTR 12,6,4096\nMvaa 124,40,212\n"
"TR 14,6,5120\nMvaa 128,48,208\nTR 16,7,0\nMvaa 128,52,216\nTR 18,7,1024\nMvaa 132,52,220\nTR 20,7,4096\nMvaa 132,48,212\nTR 22,7,5120\nMvaa 136,56,208\n"
"TR 24,8,0\nMvaa 136,60,216\nTR 26,8,1024\nMvaa 140,60,220\nTR 28,8,4096\nMvaa 140,56,212\nWL 13\nTR 30,8,5120\nMvaa 16,0,224\nWL 12\nTR 32,5,8192\n"
"TR 34,5,9216\nMvaa 16,4,232\nTR 36,5,12288\nTR 38,5,13312\nMvaa 20,4,236\nTR 40,6,8192\nTR 42,6,9216\nMvaa 20,0,228\nWL 14\nTR 44,6,12288\n"
"Mvaa 24,8,224\nTR 46,6,13312\nMvaa 24,12,232\nTR 48,7,8192\nMvaa 28,12,236\nTR 50,7,9216\nMvaa 28,8,228\nWL 14\ns_add_u32 m0,s48,s44\nTR 52,7,12288\n"
"Mvaa 32,16,224\nTR 54,7,13312\nMvaa 32,20,232\ns_add_u32 s15,s98,20480\nDS 1,20,36,0\nMvaa 36,20,236\nTR 56,8,8192\n"
"buffer_load_dwordx4 a[240:243],v0,s[24:27],s15 offen offset:0 sc1\nMvaa 36,16,228\nWL 14\nTR 58,8,9216\nMvaa 40,24,224\nWL 14\nDS 1,20,37,1024\n"
"Mvaa 40,28,232\nTR 60,8,12288\nMvaa 44,28,236\nTR 62,8,13312\nMvaa 44,24,228\nWL 14\nDS 1,20,38,2048\nMvaa 48,32,224\nWL 12\nTR 0,5,16384\n"
"TR 2,5,17408\nMvaa 48,36,232\nTR 4,5,20480\nTR 6,5,21504\nMvaa 52,36,236\nTR 8,6,16384\nTR 10,6,17408\nMvaa 52,32,228\nWL 14\nTR 12,6,20480\n"
"Mvaa 56,40,224\nTR 14,6,21504\nMvaa 56,44,232\ns_add_u32 s15,s98,20480\nTR 16,7,16384\nMvaa 60,44,236\nTR 18,7,17408\n"
"buffer_load_dwordx4 a[244:247],v0,s[24:27],s15 offen offset:1024 sc1\nMvaa 60,40,228\nWL 14\nTR 20,7,20480\nMvaa 64,48,224\nTR 22,7,21504\n"
"Mvaa 64,52,232\nDS 1,20,39,3072\nMvaa 68,52,236\ns_add_u32 m0,m0,0x1000\nTR 24,8,16384\nMvaa 68,48,228\nWL 14\nTR 26,8,17408\nMvaa 72,56,224\nWL 14\n"
"DS 1,20,40,0\nMvaa 72,60,232\nTR 28,8,20480\nMvaa 76,60,236\nTR 30,8,21504\nMvaa 76,56,228\nWL 14\nDS 1,20,41,1024\nMvaa 80,0,224\nWL 12\n"
"TR 32,5,24576\nTR 34,5,25600\nMvaa 80,4,232\ns_mov_b32 s44,s47\nTR 36,5,28672\nTR 38,5,29696\nMvaa 84,4,236\ns_add_u32 s47,s47,0x8000\nTR 40,6,24576\n"
"TR 42,6,25600\nMvaa 84,0,228\nWL 13\ns_cmp_eq_u32 s47,0x28000\nTR 44,6,28672\nTR 46,6,29696\nMvaa 88,8,224\ns_cselect_b32 s47,0,s47\nTR 48,7,24576\n"
"TR 50,7,25600\nMvaa 88,12,232\ns_add_u32 s15,s99,4096\nTR 52,7,28672\nTR 54,7,29696\nMvaa 92,12,236\nTR 56,8,24576\nTR 58,8,25600\n"
"buffer_load_dwordx4 a[192:195],v0,s[24:27],s15 offen offset:2048 sc1\nMvaa 92,8,228\nWL 14\nTR 60,8,28672\nMvaa 96,16,224\nTR 62,8,29696\n"
"Mvaa 96,20,232\nDS 1,20,42,2048\nMvaa 100,20,236\nv_add_u32 v5,s47,v10\nv_xor_b32 v6,32,v5\nMvaa 100,16,228\nv_xor_b32 v7,64,v5\nv_xor_b32 v8,96,v5\n"
"Mvaa 104,24,224\nDS 1,20,43,3072\nMvaa 104,28,232\nv_add_u32 v1,0x6000,v1\nMvaa 108,28,236\n"
"buffer_store_dwordx4 v[180:183],v0,s[24:27],s30 offen offset:1024 sc1\nMvaa 108,24,228\ns_waitcnt vmcnt(39) lgkmcnt(0)\ns_barrier\nMvaa 112,32,224\n"
"s_add_u32 s15,s99,4096\nTR 0,5,0\nMvaa 112,36,232\nTR 2,5,1024\nbuffer_load_dwordx4 a[196:199],v0,s[24:27],s15 offen offset:3072 sc1\nMvaa 116,36,236\n"
"TR 4,5,4096\nMvaa 116,32,228\nTR 6,5,5120\nMvaa 120,40,224\nTR 8,6,0\nMvaa 120,44,232\nTR 10,6,1024\nMvaa 124,44,236\nTR 12,6,4096\nMvaa 124,40,228\n"
"TR 14,6,5120\nMvaa 128,48,224\nTR 16,7,0\nMvaa 128,52,232\nTR 18,7,1024\nMvaa 132,52,236\nTR 20,7,4096\nMvaa 132,48,228\nTR 22,7,5120\nMvaa 136,56,224\n"
"TR 24,8,0\nMvaa 136,60,232\nTR 26,8,1024\nMvaa 140,60,236\nTR 28,8,4096\nMvaa 140,56,228\nWL 13\nTR 30,8,5120\nMvaa 16,0,128\nWL 12\nTR 32,5,8192\n"
"TR 34,5,9216\nMvaa 16,4,136\nTR 36,5,12288\nTR 38,5,13312\nMvaa 20,4,140\nTR 40,6,8192\nTR 42,6,9216\nMvaa 20,0,132\nWL 14\nTR 44,6,12288\n"
"Mvaa 24,8,128\nTR 46,6,13312\nMvaa 24,12,136\nTR 48,7,8192\nMvaa 28,12,140\nTR 50,7,9216\nMvaa 28,8,132\nWL 14\ns_add_u32 m0,s48,s44\nTR 52,7,12288\n"
"Mvaa 32,16,128\nTR 54,7,13312\nMvaa 32,20,136\ns_add_u32 s15,s98,4096\nDS 1,20,36,0\nMvaa 36,20,140\nTR 56,8,8192\n"
"buffer_load_dwordx4 a[200:203],v0,s[24:27],s15 offen offset:2048 sc1\nMvaa 36,16,132\nWL 14\nTR 58,8,9216\nMvaa 40,24,128\nWL 14\nDS 1,20,37,1024\n"
"Mvaa 40,28,136\nTR 60,8,12288\nMvaa 44,28,140\nTR 62,8,13312\nMvaa 44,24,132\nWL 14\nDS 1,20,38,2048\nMvaa 48,32,128\nWL 12\nTR 0,5,16384\n"
"TR 2,5,17408\nMvaa 48,36,136\nTR 4,5,20480\nTR 6,5,21504\nMvaa 52,36,140\nTR 8,6,16384\nTR 10,6,17408\nMvaa 52,32,132\nWL 14\nTR 12,6,20480\n"
"Mvaa 56,40,128\nTR 14,6,21504\nMvaa 56,44,136\ns_add_u32 s15,s98,4096\nTR 16,7,16384\nMvaa 60,44,140\nTR 18,7,17408\n"
"buffer_load_dwordx4 a[204:207],v0,s[24:27],s15 offen offset:3072 sc1\nMvaa 60,40,132\nWL 14\nTR 20,7,20480\nMvaa 64,48,128\nTR 22,7,21504\n"
"Mvaa 64,52,136\nDS 1,20,39,3072\nMvaa 68,52,140\ns_add_u32 m0,m0,0x1000\nTR 24,8,16384\nMvaa 68,48,132\nWL 14\nTR 26,8,17408\nMvaa 72,56,128\nWL 14\n"
"DS 1,20,40,0\nMvaa 72,60,136\nTR 28,8,20480\nMvaa 76,60,140\nTR 30,8,21504\nMvaa 76,56,132\nWL 14\nDS 1,20,41,1024\nMvaa 80,0,128\nWL 12\n"
"TR 32,5,24576\nTR 34,5,25600\nMvaa 80,4,136\ns_mov_b32 s44,s47\nTR 36,5,28672\nTR 38,5,29696\nMvaa 84,4,140\ns_add_u32 s47,s47,0x8000\nTR 40,6,24576\n"
"TR 42,6,25600\nMvaa 84,0,132\nWL 13\ns_cmp_eq_u32 s47,0x28000\nTR 44,6,28672\nTR 46,6,29696\nMvaa 88,8,128\ns_cselect_b32 s47,0,s47\nTR 48,7,24576\n"
"TR 50,7,25600\nMvaa 88,12,136\ns_add_u32 s15,s98,20480\nTR 52,7,28672\nTR 54,7,29696\nMvaa 92,12,140\nTR 56,8,24576\nTR 58,8,25600\n"
"buffer_load_dwordx4 a[208:211],v0,s[24:27],s15 offen offset:2048 sc1\nMvaa 92,8,132\nWL 14\nTR 60,8,28672\nMvaa 96,16,128\nTR 62,8,29696\n"
"Mvaa 96,20,136\nDS 1,20,42,2048\nMvaa 100,20,140\nbuffer_store_dwordx4 v[184:187],v0,s[24:27],s30 offen offset:2048 sc1\nMvaa 100,16,132\n"
"Mvaa 104,24,128\nDS 1,20,43,3072\nMvaa 104,28,136\nv_add_u32 v1,0x6000,v1\nMvaa 108,28,140\nMvaa 108,24,132\ns_waitcnt vmcnt(40) lgkmcnt(0)\ns_barrier\n"
"Mvaa 112,32,128\ns_add_u32 s15,s98,20480\nMvaa 112,36,136\nbuffer_load_dwordx4 a[212:215],v0,s[24:27],s15 offen offset:3072 sc1\nMvaa 116,36,140\n"
"Mvaa 116,32,132\nMvaa 120,40,128\nMvaa 120,44,136\nMvaa 124,44,140\nMvaa 124,40,132\nMvaa 128,48,128\nMvaa 128,52,136\nMvaa 132,52,140\n"
"Mvaa 132,48,132\nMvaa 136,56,128\nMvaa 136,60,136\nMvaa 140,60,140\nMvaa 140,56,132\nv_lshrrev_b32 v12,4,v0\nv_and_b32 v13,3,v12\n"
"v_bfe_u32 v12,v12,3,2\nv_add_u32 v14,7,v13\nv_lshrrev_b32 v15,2,v14\nv_xor_b32 v15,v15,v12\nv_and_b32 v14,3,v14\nv_lshl_add_u32 v14,v15,2,v14\n"
"v_and_b32 v15,0xffffff87,v10\nv_lshl_add_u32 v2,v14,3,v15\nv_add_u32 v14,11,v13\nv_lshrrev_b32 v15,2,v14\nv_xor_b32 v15,v15,v12\nv_and_b32 v14,3,v14\n"
"v_lshl_add_u32 v14,v15,2,v14\nv_and_b32 v15,0xffffff87,v10\nv_lshl_add_u32 v4,v14,3,v15\nv_add_u32 v5,s47,v10\nv_add_u32 v7,s47,v2\nv_xor_b32 v6,32,v5\n"
"v_add_u32 v8,s47,v4\nTR 32,5,0\nTR 34,5,1024\nTR 36,5,4096\nTR 38,5,5120\nTR 40,6,0\nTR 42,6,1024\nTR 44,6,4096\nTR 46,6,5120\nTR 48,7,0\nTR 50,7,1024\n"
"TR 52,7,4096\nTR 54,7,5120\nTR 56,8,0\nTR 58,8,1024\nTR 60,8,4096\nTR 62,8,5120\nv_mov_b32 v12,0x3db8aa3b\nv_mov_b32 v13,v12\ns_nop 7\n"
"s_mul_i32 s70,s68,131072\ns_mul_i32 s49,s10,32768\ns_add_u32 s70,s70,s49\ns_add_u32 s70,s70,0x2100000\ns_cmp_eq_u32 s60,0\n"
"s_cselect_b32 s70,0x7ff00000,s70\nv_pk_mul_f32 v[16:17],v[16:17],v[12:13]\nv_pk_mul_f32 v[32:33],v[32:33],v[12:13]\n"
"v_pk_mul_f32 v[18:19],v[18:19],v[12:13]\nv_pk_mul_f32 v[34:35],v[34:35],v[12:13]\nv_pk_mul_f32 v[24:25],v[24:25],v[12:13]\n"
"v_pk_mul_f32 v[40:41],v[40:41],v[12:13]\nv_pk_mul_f32 v[26:27],v[26:27],v[12:13]\nv_pk_mul_f32 v[42:43],v[42:43],v[12:13]\nCV 16,16,17\nCV 32,32,33\n"
"CV 17,18,19\nCV 33,34,35\nCV 18,24,25\nCV 34,40,41\nCV 19,26,27\nCV 35,42,43\nv_accvgpr_write_b32 a128,v16\nv_accvgpr_write_b32 a132,v32\n"
"v_accvgpr_write_b32 a129,v17\nv_accvgpr_write_b32 a133,v33\nv_accvgpr_write_b32 a130,v18\nv_accvgpr_write_b32 a134,v34\nv_accvgpr_write_b32 a131,v19\n"
"v_accvgpr_write_b32 a135,v35\ns_add_u32 s15,s99,8192\nbuffer_load_dwordx4 a[216:219],v0,s[24:27],s15 offen offset:0 sc1\n"
"v_pk_mul_f32 v[48:49],v[48:49],v[12:13]\nv_pk_mul_f32 v[64:65],v[64:65],v[12:13]\nv_pk_mul_f32 v[50:51],v[50:51],v[12:13]\n"
"v_pk_mul_f32 v[66:67],v[66:67],v[12:13]\nv_pk_mul_f32 v[56:57],v[56:57],v[12:13]\nv_pk_mul_f32 v[72:73],v[72:73],v[12:13]\n"
"v_pk_mul_f32 v[58:59],v[58:59],v[12:13]\nv_pk_mul_f32 v[74:75],v[74:75],v[12:13]\nCV 48,48,49\nCV 64,64,65\nCV 49,50,51\nCV 65,66,67\nCV 50,56,57\n"
"CV 66,72,73\nCV 51,58,59\nCV 67,74,75\nv_accvgpr_write_b32 a136,v48\nv_accvgpr_write_b32 a140,v64\nv_accvgpr_write_b32 a137,v49\n"
"v_accvgpr_write_b32 a141,v65\nv_accvgpr_write_b32 a138,v50\nv_accvgpr_write_b32 a142,v66\nv_accvgpr_write_b32 a139,v51\nv_accvgpr_write_b32 a143,v67\n"
"s_add_u32 s15,s99,8192\nbuffer_load_dwordx4 a[220:223],v0,s[24:27],s15 offen offset:1024 sc1\n"
"buffer_store_dwordx4 a[128:131],v0,s[24:27],s70 offen offset:0 sc1\nv_pk_mul_f32 v[80:81],v[80:81],v[12:13]\nv_pk_mul_f32 v[96:97],v[96:97],v[12:13]\n"
"v_pk_mul_f32 v[82:83],v[82:83],v[12:13]\nv_pk_mul_f32 v[98:99],v[98:99],v[12:13]\nv_pk_mul_f32 v[88:89],v[88:89],v[12:13]\n"
"v_pk_mul_f32 v[104:105],v[104:105],v[12:13]\nv_pk_mul_f32 v[90:91],v[90:91],v[12:13]\nv_pk_mul_f32 v[106:107],v[106:107],v[12:13]\nCV 80,80,81\n"
"CV 96,96,97\nCV 81,82,83\nCV 97,98,99\nCV 82,88,89\nCV 98,104,105\nCV 83,90,91\nCV 99,106,107\nv_accvgpr_write_b32 a144,v80\n"
"v_accvgpr_write_b32 a148,v96\nv_accvgpr_write_b32 a145,v81\nv_accvgpr_write_b32 a149,v97\nv_accvgpr_write_b32 a146,v82\nv_accvgpr_write_b32 a150,v98\n"
"v_accvgpr_write_b32 a147,v83\nv_accvgpr_write_b32 a151,v99\ns_add_u32 s15,s98,8192\nbuffer_load_dwordx4 a[224:227],v0,s[24:27],s15 offen offset:0 sc1\n"
"v_pk_mul_f32 v[112:113],v[112:113],v[12:13]\nv_pk_mul_f32 v[128:129],v[128:129],v[12:13]\nv_pk_mul_f32 v[114:115],v[114:115],v[12:13]\n"
"v_pk_mul_f32 v[130:131],v[130:131],v[12:13]\nv_pk_mul_f32 v[120:121],v[120:121],v[12:13]\nv_pk_mul_f32 v[136:137],v[136:137],v[12:13]\n"
"v_pk_mul_f32 v[122:123],v[122:123],v[12:13]\nv_pk_mul_f32 v[138:139],v[138:139],v[12:13]\nCV 112,112,113\nCV 128,128,129\nCV 113,114,115\n"
"CV 129,130,131\nCV 114,120,121\nCV 130,136,137\nCV 115,122,123\nCV 131,138,139\nv_accvgpr_write_b32 a152,v112\nv_accvgpr_write_b32 a156,v128\n"
"v_accvgpr_write_b32 a153,v113\nv_accvgpr_write_b32 a157,v129\nv_accvgpr_write_b32 a154,v114\nv_accvgpr_write_b32 a158,v130\n"
"v_accvgpr_write_b32 a155,v115\nv_accvgpr_write_b32 a159,v131\ns_add_u32 s15,s98,8192\n"
"buffer_load_dwordx4 a[228:231],v0,s[24:27],s15 offen offset:1024 sc1\nbuffer_store_dwordx4 a[132:135],v0,s[24:27],s70 offen offset:1024 sc1\n"
"v_pk_mul_f32 v[20:21],v[20:21],v[12:13]\nv_pk_mul_f32 v[36:37],v[36:37],v[12:13]\nv_pk_mul_f32 v[22:23],v[22:23],v[12:13]\n"
"v_pk_mul_f32 v[38:39],v[38:39],v[12:13]\nv_pk_mul_f32 v[28:29],v[28:29],v[12:13]\nv_pk_mul_f32 v[44:45],v[44:45],v[12:13]\n"
"v_pk_mul_f32 v[30:31],v[30:31],v[12:13]\nv_pk_mul_f32 v[46:47],v[46:47],v[12:13]\nCV 20,20,21\nCV 36,36,37\nCV 21,22,23\nCV 37,38,39\nCV 22,28,29\n"
"CV 38,44,45\nCV 23,30,31\nCV 39,46,47\nv_accvgpr_write_b32 a160,v20\nv_accvgpr_write_b32 a164,v36\nv_accvgpr_write_b32 a161,v21\n"
"v_accvgpr_write_b32 a165,v37\nv_accvgpr_write_b32 a162,v22\nv_accvgpr_write_b32 a166,v38\nv_accvgpr_write_b32 a163,v23\nv_accvgpr_write_b32 a167,v39\n"
"buffer_store_dwordx4 a[136:139],v0,s[24:27],s70 offen offset:2048 sc1\nv_pk_mul_f32 v[52:53],v[52:53],v[12:13]\n"
"v_pk_mul_f32 v[68:69],v[68:69],v[12:13]\nv_pk_mul_f32 v[54:55],v[54:55],v[12:13]\nv_pk_mul_f32 v[70:71],v[70:71],v[12:13]\n"
"v_pk_mul_f32 v[60:61],v[60:61],v[12:13]\nv_pk_mul_f32 v[76:77],v[76:77],v[12:13]\nv_pk_mul_f32 v[62:63],v[62:63],v[12:13]\n"
"v_pk_mul_f32 v[78:79],v[78:79],v[12:13]\nCV 52,52,53\nCV 68,68,69\nCV 53,54,55\nCV 69,70,71\nCV 54,60,61\nCV 70,76,77\nCV 55,62,63\nCV 71,78,79\n"
"v_accvgpr_write_b32 a168,v52\nv_accvgpr_write_b32 a172,v68\nv_accvgpr_write_b32 a169,v53\nv_accvgpr_write_b32 a173,v69\nv_accvgpr_write_b32 a170,v54\n"
"v_accvgpr_write_b32 a174,v70\nv_accvgpr_write_b32 a171,v55\nv_accvgpr_write_b32 a175,v71\nv_pk_mul_f32 v[84:85],v[84:85],v[12:13]\n"
"v_pk_mul_f32 v[100:101],v[100:101],v[12:13]\nv_pk_mul_f32 v[86:87],v[86:87],v[12:13]\nv_pk_mul_f32 v[102:103],v[102:103],v[12:13]\n"
"v_pk_mul_f32 v[92:93],v[92:93],v[12:13]\nv_pk_mul_f32 v[108:109],v[108:109],v[12:13]\nv_pk_mul_f32 v[94:95],v[94:95],v[12:13]\n"
"v_pk_mul_f32 v[110:111],v[110:111],v[12:13]\nCV 84,84,85\nCV 100,100,101\nCV 85,86,87\nCV 101,102,103\nCV 86,92,93\nCV 102,108,109\nCV 87,94,95\n"
"CV 103,110,111\nv_accvgpr_write_b32 a176,v84\nv_accvgpr_write_b32 a180,v100\nv_accvgpr_write_b32 a177,v85\nv_accvgpr_write_b32 a181,v101\n"
"v_accvgpr_write_b32 a178,v86\nv_accvgpr_write_b32 a182,v102\nv_accvgpr_write_b32 a179,v87\nv_accvgpr_write_b32 a183,v103\n"
"buffer_store_dwordx4 a[140:143],v0,s[24:27],s70 offen offset:3072 sc1\ns_add_u32 s70,s70,4096\nv_pk_mul_f32 v[116:117],v[116:117],v[12:13]\n"
"v_pk_mul_f32 v[132:133],v[132:133],v[12:13]\nv_pk_mul_f32 v[118:119],v[118:119],v[12:13]\nv_pk_mul_f32 v[134:135],v[134:135],v[12:13]\n"
"v_pk_mul_f32 v[124:125],v[124:125],v[12:13]\nv_pk_mul_f32 v[140:141],v[140:141],v[12:13]\nv_pk_mul_f32 v[126:127],v[126:127],v[12:13]\n"
"v_pk_mul_f32 v[142:143],v[142:143],v[12:13]\nCV 116,116,117\nCV 132,132,133\nCV 117,118,119\nCV 133,134,135\nCV 118,124,125\nCV 134,140,141\n"
"CV 119,126,127\nCV 135,142,143\nv_accvgpr_write_b32 a184,v116\nv_accvgpr_write_b32 a188,v132\nv_accvgpr_write_b32 a185,v117\n"
"v_accvgpr_write_b32 a189,v133\nv_accvgpr_write_b32 a186,v118\nv_accvgpr_write_b32 a190,v134\nv_accvgpr_write_b32 a187,v119\n"
"v_accvgpr_write_b32 a191,v135\nbuffer_store_dwordx4 a[144:147],v0,s[24:27],s70 offen offset:0 sc1\nWL 14\nZvaa 16,32,72\nWL 12\nTR 0,5,8192\n"
"TR 2,5,9216\nMvaa 16,36,76\nTR 4,5,12288\nTR 6,5,13312\nZvaa 20,36,84\nTR 8,6,8192\nMvaa 20,32,80\nWL 14\nTR 10,6,9216\nZvaa 24,40,72\nWL 14\n"
"s_add_u32 m0,s48,s44\nTR 12,6,12288\nbuffer_store_dwordx4 a[148:151],v0,s[24:27],s70 offen offset:1024 sc1\nMvaa 24,44,76\nTR 14,6,13312\n"
"Zvaa 28,44,84\nDS 1,20,36,0\nMvaa 28,40,80\nWL 14\ns_add_u32 s15,s98,24576\nTR 16,7,8192\nZvaa 32,48,72\nWL 13\nTR 18,7,9216\n"
"buffer_load_dwordx4 a[232:235],v0,s[24:27],s15 offen offset:0 sc1\nMvaa 32,52,76\nTR 20,7,12288\nZvaa 36,52,84\nTR 22,7,13312\nMvaa 36,48,80\nWL 14\n"
"DS 1,20,37,1024\nZvaa 40,56,72\nWL 12\nTR 24,8,8192\nMvaa 40,60,76\nTR 26,8,9216\nZvaa 44,60,84\nTR 28,8,12288\nMvaa 44,56,80\nWL 13\nTR 30,8,13312\n"
"Zvaa 48,0,64\nWL 12\nTR 32,5,16384\nMvaa 48,4,68\nDS 1,20,38,2048\nZvaa 52,4,76\nTR 34,5,17408\n"
"buffer_store_dwordx4 a[152:155],v0,s[24:27],s70 offen offset:2048 sc1\nMvaa 52,0,72\nTR 36,5,20480\nZvaa 56,0,80\nTR 38,5,21504\nMvaa 56,4,84\nWL 14\n"
"DS 1,20,39,3072\nZvaa 60,8,64\nWL 12\ns_add_u32 m0,m0,0x1000\nTR 40,6,16384\nMvaa 60,12,68\ns_add_u32 s15,s98,24576\nTR 42,6,17408\nZvaa 64,12,76\n"
"TR 44,6,20480\nbuffer_load_dwordx4 a[236:239],v0,s[24:27],s15 offen offset:1024 sc1\nMvaa 64,8,72\nDS 1,20,40,0\nZvaa 68,8,80\nTR 46,6,21504\n"
"Mvaa 68,12,84\nWL 14\nTR 48,7,16384\nZvaa 72,16,64\nWL 13\nTR 50,7,17408\nMvaa 72,20,68\nDS 1,20,41,1024\nZvaa 76,20,76\ns_mov_b32 s44,s47\n"
"TR 52,7,20480\nMvaa 76,16,72\ns_add_u32 s47,s47,0x8000\nTR 54,7,21504\nZvaa 80,16,80\ns_cmp_eq_u32 s47,0x28000\nTR 56,8,16384\n"
"buffer_store_dwordx4 a[156:159],v0,s[24:27],s70 offen offset:3072 sc1\nMvaa 80,20,84\nWL 15\ns_cselect_b32 s47,0,s47\nDS 1,20,42,2048\nZvaa 84,24,64\n"
"WL 13\ns_add_u32 s70,s70,4096\nTR 58,8,17408\nMvaa 84,28,68\nTR 60,8,20480\nZvaa 88,28,76\nTR 62,8,21504\nMvaa 88,24,72\nDS 1,20,43,3072\n"
"Zvaa 92,24,80\nv_add_u32 v1,0x6000,v1\nMvaa 92,28,84\nWL 14\nv_add_u32 v5,s47,v10\nv_add_u32 v7,s47,v2\nZvaa 96,64,32\nWL 12\nv_xor_b32 v6,32,v5\n"
"v_add_u32 v8,s47,v4\nMvaa 96,68,36\ns_waitcnt vmcnt(50) lgkmcnt(0)\ns_barrier\nZvaa 100,76,36\nTR 0,5,0\nMvaa 100,72,32\nTR 2,5,1024\nZvaa 104,80,32\n"
"TR 4,5,4096\nMvaa 104,84,36\nTR 6,5,5120\nZvaa 108,64,40\nTR 8,6,0\nMvaa 108,68,44\nTR 10,6,1024\nZvaa 112,76,44\nTR 12,6,4096\nMvaa 112,72,40\n"
"TR 14,6,5120\nZvaa 116,80,40\nTR 16,7,0\nMvaa 116,84,44\nTR 18,7,1024\nZvaa 120,64,48\nTR 20,7,4096\nMvaa 120,68,52\nTR 22,7,5120\nZvaa 124,76,52\n"
"TR 24,8,0\nMvaa 124,72,48\nTR 26,8,1024\nZvaa 128,80,48\nTR 28,8,4096\nMvaa 128,84,52\nTR 30,8,5120\nZvaa 132,64,56\n"
"buffer_store_dwordx4 a[160:163],v0,s[24:27],s70 offen offset:0 sc1\nMvaa 132,68,60\nZvaa 136,76,60\nMvaa 136,72,56\nZvaa 140,80,56\nMvaa 140,84,60\n"
"WL 14\nMvaa 16,0,96\nWL 12\nTR 32,5,8192\nTR 34,5,9216\nMvaa 16,4,100\nTR 36,5,12288\nTR 38,5,13312\nMvaa 20,4,108\nTR 40,6,8192\nMvaa 20,0,104\nWL 14\n"
"TR 42,6,9216\nMvaa 24,8,96\nWL 14\ns_add_u32 m0,s48,s44\nTR 44,6,12288\nbuffer_store_dwordx4 a[164:167],v0,s[24:27],s70 offen offset:1024 sc1\n"
"Mvaa 24,12,100\nTR 46,6,13312\nMvaa 28,12,108\nDS 1,20,36,0\nMvaa 28,8,104\nWL 14\ns_add_u32 s15,s99,8192\nTR 48,7,8192\nMvaa 32,16,96\nWL 13\n"
"TR 50,7,9216\nbuffer_load_dwordx4 a[64:67],v0,s[24:27],s15 offen offset:2048 sc1\nMvaa 32,20,100\nTR 52,7,12288\nMvaa 36,20,108\nTR 54,7,13312\n"
"Mvaa 36,16,104\nWL 14\nDS 1,20,37,1024\nMvaa 40,24,96\nWL 12\nTR 56,8,8192\nMvaa 40,28,100\nTR 58,8,9216\n"
"buffer_store_dwordx4 a[168:171],v0,s[24:27],s70 offen offset:2048 sc1\nMvaa 44,28,108\nTR 60,8,12288\nMvaa 44,24,104\nWL 13\ns_add_u32 s15,s99,8192\n"
"TR 62,8,13312\nMvaa 48,32,88\nWL 12\nTR 0,5,16384\nbuffer_load_dwordx4 a[68:71],v0,s[24:27],s15 offen offset:3072 sc1\nMvaa 48,36,92\nDS 1,20,38,2048\n"
"Mvaa 52,36,100\nTR 2,5,17408\nMvaa 52,32,96\nTR 4,5,20480\nMvaa 56,32,104\nTR 6,5,21504\nMvaa 56,36,108\nWL 14\nDS 1,20,39,3072\nMvaa 60,40,88\nWL 12\n"
"s_add_u32 m0,m0,0x1000\nTR 8,6,16384\nbuffer_store_dwordx4 a[172:175],v0,s[24:27],s70 offen offset:3072 sc1\nMvaa 60,44,92\ns_add_u32 s15,s98,8192\n"
"TR 10,6,17408\nMvaa 64,44,100\ns_add_u32 s70,s70,4096\nTR 12,6,20480\nbuffer_load_dwordx4 a[72:75],v0,s[24:27],s15 offen offset:2048 sc1\n"
"Mvaa 64,40,96\nDS 1,20,40,0\nMvaa 68,40,104\nTR 14,6,21504\nMvaa 68,44,108\nWL 14\nTR 16,7,16384\nMvaa 72,48,88\nWL 13\nTR 18,7,17408\nMvaa 72,52,92\n"
"DS 1,20,41,1024\nMvaa 76,52,100\ns_mov_b32 s44,s47\nTR 20,7,20480\nMvaa 76,48,96\ns_add_u32 s47,s47,0x8000\nTR 22,7,21504\n"
"buffer_store_dwordx4 a[176:179],v0,s[24:27],s70 offen offset:0 sc1\nMvaa 80,48,104\ns_cmp_eq_u32 s47,0x28000\nTR 24,8,16384\nMvaa 80,52,108\nWL 15\n"
"s_cselect_b32 s47,0,s47\nDS 1,20,42,2048\nMvaa 84,56,88\nWL 13\ns_add_u32 s15,s98,8192\nTR 26,8,17408\nMvaa 84,60,92\nTR 28,8,20480\n"
"buffer_load_dwordx4 a[76:79],v0,s[24:27],s15 offen offset:3072 sc1\nMvaa 88,60,100\nTR 30,8,21504\nMvaa 88,56,96\nDS 1,20,43,3072\nMvaa 92,56,104\n"
"v_add_u32 v1,0x6000,v1\nMvaa 92,60,108\nWL 14\nv_add_u32 v5,s47,v10\nv_add_u32 v7,s47,v2\nMvaa 96,88,0\nWL 12\nv_xor_b32 v6,32,v5\nv_add_u32 v8,s47,v4\n"
"Mvaa 96,92,4\ns_waitcnt vmcnt(54) lgkmcnt(0)\ns_barrier\nMvaa 100,100,4\nTR 32,5,0\nMvaa 100,96,0\ns_mul_i32 s8,s56,8192\nTR 34,5,1024\nMvaa 104,104,0\n"
"s_add_u32 s8,s8,0x10000\nTR 36,5,4096\nv_lshrrev_b32 v15,4,v0\nMvaa 104,108,4\nTR 38,5,5120\nv_min_u32 v15,31,v15\nMvaa 108,88,8\nTR 40,6,0\n"
"v_lshlrev_b32 v15,7,v15\nMvaa 108,92,12\nTR 42,6,1024\nMvaa 112,100,12\nTR 44,6,4096\nMvaa 112,96,8\nTR 46,6,5120\nMvaa 116,104,8\nTR 48,7,0\n"
"Mvaa 116,108,12\nTR 50,7,1024\nMvaa 120,88,16\nTR 52,7,4096\nMvaa 120,92,20\nTR 54,7,5120\nMvaa 124,100,20\nTR 56,8,0\nMvaa 124,96,16\nTR 58,8,1024\n"
"Mvaa 128,104,16\nTR 60,8,4096\nMvaa 128,108,20\nTR 62,8,5120\nMvaa 132,88,24\nMvaa 132,92,28\nMvaa 136,100,28\nMvaa 136,96,24\nMvaa 140,104,24\n"
"Mvaa 140,108,28\nWL 14\nMvaa 16,32,120\nWL 12\nTR 0,5,8192\nTR 2,5,9216\nMvaa 16,36,124\nTR 4,5,12288\nTR 6,5,13312\nMvaa 20,36,244\nTR 8,6,8192\n"
"Mvaa 20,32,240\nWL 14\nTR 10,6,9216\nMvaa 24,40,120\nWL 14\ns_add_u32 m0,s48,s44\nTR 12,6,12288\n"
"buffer_store_dwordx4 a[180:183],v0,s[24:27],s70 offen offset:1024 sc1\nMvaa 24,44,124\nTR 14,6,13312\nMvaa 28,44,244\nDS 1,20,36,0\nMvaa 28,40,240\n"
"WL 14\nTR 16,7,8192\nMvaa 32,48,120\nWL 13\ns_add_u32 s15,s98,24576\nTR 18,7,9216\nbuffer_load_dword v14,v15,s[24:27],s8 offen sc1\nMvaa 32,52,124\n"
"TR 20,7,12288\nbuffer_load_dwordx4 a[80:83],v0,s[24:27],s15 offen offset:2048 sc1\nMvaa 36,52,244\nTR 22,7,13312\nMvaa 36,48,240\nWL 14\n"
"DS 1,20,37,1024\nMvaa 40,56,120\nWL 12\nTR 24,8,8192\nMvaa 40,60,124\nTR 26,8,9216\n"
"buffer_store_dwordx4 a[184:187],v0,s[24:27],s70 offen offset:2048 sc1\nMvaa 44,60,244\nTR 28,8,12288\nMvaa 44,56,240\nWL 13\ns_add_u32 s15,s98,24576\n"
"TR 30,8,13312\nMvaa 48,0,112\nWL 12\nTR 32,5,16384\nbuffer_load_dwordx4 a[84:87],v0,s[24:27],s15 offen offset:3072 sc1\nMvaa 48,4,116\nDS 1,20,38,2048\n"
"Mvaa 52,4,124\nTR 34,5,17408\nMvaa 52,0,120\nTR 36,5,20480\nMvaa 56,0,240\nTR 38,5,21504\nMvaa 56,4,244\nWL 14\nDS 1,20,39,3072\nMvaa 60,8,112\nWL 12\n"
"s_add_u32 m0,m0,0x1000\nTR 40,6,16384\nbuffer_store_dwordx4 a[188:191],v0,s[24:27],s70 offen offset:3072 sc1\nMvaa 60,12,116\ns_add_u32 s15,s99,12288\n"
"TR 42,6,17408\nMvaa 64,12,124\ns_add_u32 s70,s70,4096\nTR 44,6,20480\nbuffer_load_dwordx4 a[88:91],v0,s[24:27],s15 offen offset:0 sc1\nMvaa 64,8,120\n"
"DS 1,20,40,0\nMvaa 68,8,240\nTR 46,6,21504\nMvaa 68,12,244\nWL 14\nTR 48,7,16384\nMvaa 72,16,112\nWL 13\nTR 50,7,17408\nMvaa 72,20,116\n"
"DS 1,20,41,1024\nMvaa 76,20,124\ns_mov_b32 s44,s47\nTR 52,7,20480\nMvaa 76,16,120\ns_add_u32 s47,s47,0x8000\nTR 54,7,21504\nMvaa 80,16,240\n"
"s_cmp_eq_u32 s47,0x28000\nTR 56,8,16384\nMvaa 80,20,244\nWL 15\ns_cselect_b32 s47,0,s47\nDS 1,20,42,2048\nMvaa 84,24,112\nWL 13\n"
"s_add_u32 s15,s99,12288\nTR 58,8,17408\nMvaa 84,28,116\nTR 60,8,20480\nbuffer_load_dwordx4 a[92:95],v0,s[24:27],s15 offen offset:1024 sc1\n"
"Mvaa 88,28,124\nTR 62,8,21504\nMvaa 88,24,120\nDS 1,20,43,3072\nMvaa 92,24,240\nbuffer_store_dwordx4 v[188:191],v0,s[24:27],s30 offen offset:3072 sc1\n"
"Mvaa 92,28,244\nWL 14\ns_add_u32 s30,s30,s29\nv_add_u32 v5,s47,v10\nv_add_u32 v7,s47,v2\nMvaa 96,112,32\nWL 12\nv_xor_b32 v6,32,v5\n"
"v_add_u32 v8,s47,v4\nMvaa 96,116,36\ns_waitcnt vmcnt(57) lgkmcnt(0)\ns_barrier\nMvaa 100,124,36\nTR 0,5,0\nv_add_u32 v1,s66,v0\nMvaa 100,120,32\n"
"TR 2,5,1024\nMvaa 104,240,32\nTR 4,5,4096\nMvaa 104,244,36\nTR 6,5,5120\nMvaa 108,112,40\nTR 8,6,0\nMvaa 108,116,44\nTR 10,6,1024\nMvaa 112,124,44\n"
"TR 12,6,4096\nMvaa 112,120,40\nTR 14,6,5120\nMvaa 116,240,40\nTR 16,7,0\nMvaa 116,244,44\nTR 18,7,1024\nMvaa 120,112,48\nTR 20,7,4096\nMvaa 120,116,52\n"
"TR 22,7,5120\nMvaa 124,124,52\nTR 24,8,0\nMvaa 124,120,48\nTR 26,8,1024\nMvaa 128,240,48\nTR 28,8,4096\nMvaa 128,244,52\nTR 30,8,5120\nMvaa 132,112,56\n"
"Mvaa 132,116,60\nMvaa 136,124,60\nMvaa 136,120,56\nMvaa 140,240,56\nMvaa 140,244,60\ns_waitcnt vmcnt(14)\nv_cmp_ne_u32 vcc,s65,v14\ns_nop 0\n"
"s_cbranch_vccnz fused_op_sp8\nfused_op_spb9:\nWL 14\nMvaa 16,0,200\nWL 12\nTR 32,5,8192\nTR 34,5,9216\nMvaa 16,4,204\nTR 36,5,12288\nTR 38,5,13312\n"
"Mvaa 20,4,212\nTR 40,6,8192\nMvaa 20,0,208\nWL 14\nTR 42,6,9216\nMvaa 24,8,200\nWL 14\ns_add_u32 m0,s48,s44\nTR 44,6,12288\nMvaa 24,12,204\n"
"TR 46,6,13312\nMvaa 28,12,212\nDS 1,20,36,0\nMvaa 28,8,208\nWL 14\ns_add_u32 s15,s98,12288\nTR 48,7,8192\nMvaa 32,16,200\nWL 13\nTR 50,7,9216\n"
"buffer_load_dwordx4 a[96:99],v0,s[24:27],s15 offen offset:0 sc1\nMvaa 32,20,204\nTR 52,7,12288\nMvaa 36,20,212\nTR 54,7,13312\nMvaa 36,16,208\nWL 14\n"
"DS 1,20,37,1024\nMvaa 40,24,200\nWL 12\nTR 56,8,8192\nMvaa 40,28,204\nTR 58,8,9216\nMvaa 44,28,212\nTR 60,8,12288\nMvaa 44,24,208\nWL 13\n"
"s_add_u32 s15,s98,12288\nTR 62,8,13312\nMvaa 48,32,192\nWL 12\nTR 0,5,16384\nbuffer_load_dwordx4 a[100:103],v0,s[24:27],s15 offen offset:1024 sc1\n"
"Mvaa 48,36,196\nDS 1,20,38,2048\nMvaa 52,36,204\nTR 2,5,17408\nMvaa 52,32,200\nTR 4,5,20480\nMvaa 56,32,208\nTR 6,5,21504\nMvaa 56,36,212\nWL 14\n"
"DS 1,20,39,3072\nMvaa 60,40,192\nWL 12\ns_add_u32 m0,m0,0x1000\nTR 8,6,16384\nMvaa 60,44,196\ns_add_u32 s15,s98,28672\nTR 10,6,17408\nMvaa 64,44,204\n"
"TR 12,6,20480\nbuffer_load_dwordx4 a[104:107],v0,s[24:27],s15 offen offset:0 sc1\nMvaa 64,40,200\nDS 1,20,40,0\nMvaa 68,40,208\nTR 14,6,21504\n"
"Mvaa 68,44,212\nWL 14\nTR 16,7,16384\nMvaa 72,48,192\nWL 13\nTR 18,7,17408\nMvaa 72,52,196\nDS 1,20,41,1024\nMvaa 76,52,204\ns_mov_b32 s44,s47\n"
"TR 20,7,20480\nMvaa 76,48,200\ns_add_u32 s47,s47,0x8000\nTR 22,7,21504\nMvaa 80,48,208\ns_cmp_eq_u32 s47,0x28000\nTR 24,8,16384\nMvaa 80,52,212\nWL 15\n"
"s_cselect_b32 s47,0,s47\nDS 1,20,42,2048\nMvaa 84,56,192\nWL 13\ns_add_u32 s15,s98,28672\nTR 26,8,17408\nMvaa 84,60,196\nTR 28,8,20480\n"
"buffer_load_dwordx4 a[108:111],v0,s[24:27],s15 offen offset:1024 sc1\nMvaa 88,60,204\nTR 30,8,21504\nMvaa 88,56,200\nDS 1,20,43,3072\nMvaa 92,56,208\n"
"v_add_u32 v1,0x10000,v1\nMvaa 92,60,212\nWL 14\nv_add_u32 v5,s47,v10\nv_add_u32 v7,s47,v2\nMvaa 96,192,0\nWL 12\nv_xor_b32 v6,32,v5\n"
"v_add_u32 v8,s47,v4\nMvaa 96,196,4\ns_waitcnt vmcnt(46) lgkmcnt(0)\ns_barrier\nMvaa 100,204,4\nTR 32,5,0\nMvaa 100,200,0\nTR 34,5,1024\nMvaa 104,208,0\n"
"TR 36,5,4096\nMvaa 104,212,4\nTR 38,5,5120\nMvaa 108,192,8\nTR 40,6,0\nMvaa 108,196,12\nTR 42,6,1024\nMvaa 112,204,12\nTR 44,6,4096\nMvaa 112,200,8\n"
"TR 46,6,5120\nMvaa 116,208,8\nTR 48,7,0\nMvaa 116,212,12\nTR 50,7,1024\nMvaa 120,192,16\nTR 52,7,4096\nMvaa 120,196,20\nTR 54,7,5120\nMvaa 124,204,20\n"
"TR 56,8,0\nMvaa 124,200,16\nTR 58,8,1024\nMvaa 128,208,16\nTR 60,8,4096\nMvaa 128,212,20\nTR 62,8,5120\nMvaa 132,192,24\n"
"buffer_store_dwordx4 v[192:195],v0,s[24:27],s30 offen offset:0 sc1\nMvaa 132,196,28\nMvaa 136,204,28\nMvaa 136,200,24\nMvaa 140,208,24\n"
"Mvaa 140,212,28\ns_mul_i32 s45,s68,512\ns_mul_i32 s49,s10,128\ns_add_u32 s45,s45,s49\ns_add_u32 s45,s45,0x30000\ns_waitcnt vmcnt(20)\n"
"v_mov_b32 v13,s65\ns_mov_b64 exec,1\nbuffer_store_dword v13,off,s[24:27],s45 sc1\ns_mov_b64 exec,-1\nWL 14\nMvaa 16,32,224\nWL 12\nTR 0,5,8192\n"
"TR 2,5,9216\nMvaa 16,36,228\nTR 4,5,12288\nTR 6,5,13312\nMvaa 20,36,236\nTR 8,6,8192\nMvaa 20,32,232\nWL 14\nTR 10,6,9216\nMvaa 24,40,224\nWL 14\n"
"s_add_u32 m0,s48,s44\nTR 12,6,12288\nbuffer_store_dwordx4 v[196:199],v0,s[24:27],s30 offen offset:1024 sc1\nMvaa 24,44,228\nTR 14,6,13312\n"
"Mvaa 28,44,236\nDS 1,20,36,0\nMvaa 28,40,232\nWL 14\nTR 16,7,8192\nMvaa 32,48,224\nWL 13\nTR 18,7,9216\nMvaa 32,52,228\nTR 20,7,12288\nMvaa 36,52,236\n"
"TR 22,7,13312\nMvaa 36,48,232\nWL 14\nDS 1,20,37,1024\nMvaa 40,56,224\nWL 12\nTR 24,8,8192\nMvaa 40,60,228\nTR 26,8,9216\nMvaa 44,60,236\n"
"TR 28,8,12288\nMvaa 44,56,232\nWL 13\nTR 30,8,13312\nMvaa 48,0,216\nWL 12\nTR 32,5,16384\nMvaa 48,4,220\nDS 1,20,38,2048\nMvaa 52,4,228\nTR 34,5,17408\n"
"buffer_store_dwordx4 v[200:203],v0,s[24:27],s30 offen offset:2048 sc1\nMvaa 52,0,224\nTR 36,5,20480\nMvaa 56,0,232\nTR 38,5,21504\nMvaa 56,4,236\n"
"WL 14\nDS 1,20,39,3072\nMvaa 60,8,216\nWL 12\ns_add_u32 m0,m0,0x1000\nTR 40,6,16384\nMvaa 60,12,220\nTR 42,6,17408\nMvaa 64,12,228\nTR 44,6,20480\n"
"Mvaa 64,8,224\nDS 1,20,40,0\nMvaa 68,8,232\nTR 46,6,21504\nMvaa 68,12,236\nWL 14\nTR 48,7,16384\nMvaa 72,16,216\nWL 13\nTR 50,7,17408\nMvaa 72,20,220\n"
"DS 1,20,41,1024\nMvaa 76,20,228\ns_mov_b32 s44,s47\nTR 52,7,20480\nMvaa 76,16,224\ns_add_u32 s47,s47,0x8000\nTR 54,7,21504\nMvaa 80,16,232\n"
"s_cmp_eq_u32 s47,0x28000\nTR 56,8,16384\nbuffer_store_dwordx4 v[204:207],v0,s[24:27],s30 offen offset:3072 sc1\nMvaa 80,20,236\nWL 15\n"
"s_cselect_b32 s47,0,s47\nDS 1,20,42,2048\nMvaa 84,24,216\nWL 13\ns_add_u32 s30,s30,s28\nTR 58,8,17408\nMvaa 84,28,220\nTR 60,8,20480\nMvaa 88,28,228\n"
"TR 62,8,21504\nMvaa 88,24,224\nDS 1,20,43,3072\nMvaa 92,24,232\nv_add_u32 v1,0xffff8000,v1\nMvaa 92,28,236\nWL 14\nv_add_u32 v5,s47,v10\n"
"v_add_u32 v7,s47,v2\nMvaa 96,216,32\nWL 12\nv_xor_b32 v6,32,v5\nv_add_u32 v8,s47,v4\nMvaa 96,220,36\ns_waitcnt vmcnt(42) lgkmcnt(0)\ns_barrier\n"
"Mvaa 100,228,36\nTR 0,5,0\nMvaa 100,224,32\nTR 2,5,1024\nMvaa 104,232,32\nTR 4,5,4096\nMvaa 104,236,36\nTR 6,5,5120\nMvaa 108,216,40\nTR 8,6,0\n"
"Mvaa 108,220,44\nTR 10,6,1024\nMvaa 112,228,44\nTR 12,6,4096\nMvaa 112,224,40\nTR 14,6,5120\nMvaa 116,232,40\nTR 16,7,0\nMvaa 116,236,44\nTR 18,7,1024\n"
"Mvaa 120,216,48\nTR 20,7,4096\nMvaa 120,220,52\nTR 22,7,5120\nMvaa 124,228,52\nTR 24,8,0\nMvaa 124,224,48\nTR 26,8,1024\nMvaa 128,232,48\nTR 28,8,4096\n"
"Mvaa 128,236,52\nTR 30,8,5120\nMvaa 132,216,56\nbuffer_store_dwordx4 v[208:211],v0,s[24:27],s30 offen offset:0 sc1\nMvaa 132,220,60\nMvaa 136,228,60\n"
"Mvaa 136,224,56\nMvaa 140,232,56\nMvaa 140,236,60\nWL 14\nMvaa 16,0,72\nWL 12\nTR 32,5,8192\nTR 34,5,9216\nMvaa 16,4,76\nTR 36,5,12288\nTR 38,5,13312\n"
"Mvaa 20,4,84\nTR 40,6,8192\nMvaa 20,0,80\nWL 14\nTR 42,6,9216\nMvaa 24,8,72\nWL 14\ns_add_u32 m0,s48,s44\nTR 44,6,12288\n"
"buffer_store_dwordx4 v[212:215],v0,s[24:27],s30 offen offset:1024 sc1\nMvaa 24,12,76\nTR 46,6,13312\nMvaa 28,12,84\nDS 1,20,36,0\nMvaa 28,8,80\nWL 14\n"
"TR 48,7,8192\nMvaa 32,16,72\nWL 13\nTR 50,7,9216\nMvaa 32,20,76\nTR 52,7,12288\nMvaa 36,20,84\nTR 54,7,13312\nMvaa 36,16,80\nWL 14\nDS 1,20,37,1024\n"
"Mvaa 40,24,72\nWL 12\nTR 56,8,8192\nMvaa 40,28,76\nTR 58,8,9216\nMvaa 44,28,84\nTR 60,8,12288\nMvaa 44,24,80\nWL 13\nTR 62,8,13312\nMvaa 48,32,64\n"
"WL 12\nTR 0,5,16384\nMvaa 48,36,68\nDS 1,20,38,2048\nMvaa 52,36,76\nTR 2,5,17408\n"
"buffer_store_dwordx4 v[216:219],v0,s[24:27],s30 offen offset:2048 sc1\nMvaa 52,32,72\nTR 4,5,20480\nMvaa 56,32,80\nTR 6,5,21504\nMvaa 56,36,84\nWL 14\n"
"DS 1,20,39,3072\nMvaa 60,40,64\nWL 12\ns_add_u32 m0,m0,0x1000\nTR 8,6,16384\nMvaa 60,44,68\nTR 10,6,17408\nMvaa 64,44,76\nTR 12,6,20480\nMvaa 64,40,72\n"
"DS 1,20,40,0\nMvaa 68,40,80\nTR 14,6,21504\nMvaa 68,44,84\nWL 14\nTR 16,7,16384\nMvaa 72,48,64\nWL 13\nTR 18,7,17408\nMvaa 72,52,68\nDS 1,20,41,1024\n"
"Mvaa 76,52,76\ns_mov_b32 s44,s47\nTR 20,7,20480\nMvaa 76,48,72\ns_add_u32 s47,s47,0x8000\nTR 22,7,21504\nMvaa 80,48,80\ns_cmp_eq_u32 s47,0x28000\n"
"TR 24,8,16384\nbuffer_store_dwordx4 v[220:223],v0,s[24:27],s30 offen offset:3072 sc1\nMvaa 80,52,84\nWL 15\ns_cselect_b32 s47,0,s47\nDS 1,20,42,2048\n"
"Mvaa 84,56,64\nWL 13\ns_add_u32 s30,s30,s28\nTR 26,8,17408\nMvaa 84,60,68\nTR 28,8,20480\nMvaa 88,60,76\nTR 30,8,21504\nMvaa 88,56,72\nDS 1,20,43,3072\n"
"Mvaa 92,56,80\nv_add_u32 v1,0x18000,v1\nMvaa 92,60,84\nWL 14\nv_add_u32 v5,s47,v10\nv_add_u32 v7,s47,v2\nMvaa 96,64,0\nWL 12\nv_xor_b32 v6,32,v5\n"
"v_add_u32 v8,s47,v4\nMvaa 96,68,4\ns_waitcnt vmcnt(38) lgkmcnt(0)\ns_barrier\nMvaa 100,76,4\nTR 32,5,0\nMvaa 100,72,0\nTR 34,5,1024\nMvaa 104,80,0\n"
"TR 36,5,4096\nMvaa 104,84,4\nTR 38,5,5120\nMvaa 108,64,8\nTR 40,6,0\nMvaa 108,68,12\nTR 42,6,1024\nMvaa 112,76,12\nTR 44,6,4096\nMvaa 112,72,8\n"
"TR 46,6,5120\nMvaa 116,80,8\nTR 48,7,0\nMvaa 116,84,12\nTR 50,7,1024\nMvaa 120,64,16\nTR 52,7,4096\nMvaa 120,68,20\nTR 54,7,5120\nMvaa 124,76,20\n"
"TR 56,8,0\nMvaa 124,72,16\nTR 58,8,1024\nMvaa 128,80,16\nTR 60,8,4096\nMvaa 128,84,20\nTR 62,8,5120\nMvaa 132,64,24\n"
"buffer_store_dwordx4 v[224:227],v0,s[24:27],s30 offen offset:0 sc1\nMvaa 132,68,28\nMvaa 136,76,28\nMvaa 136,72,24\nMvaa 140,80,24\nMvaa 140,84,28\n"
"s_waitcnt vmcnt(36) lgkmcnt(14)\nMvaa 16,32,96\nTR 0,5,8192\nTR 2,5,9216\ns_waitcnt vmcnt(34) lgkmcnt(14)\nMvaa 16,36,100\nTR 4,5,12288\nTR 6,5,13312\n"
"s_waitcnt vmcnt(27)\nMvaa 20,36,108\nTR 8,6,8192\nMvaa 20,32,104\nWL 14\nTR 10,6,9216\nMvaa 24,40,96\nWL 14\ns_add_u32 m0,s48,s44\nTR 12,6,12288\n"
"buffer_store_dwordx4 v[228:231],v0,s[24:27],s30 offen offset:1024 sc1\nMvaa 24,44,100\nTR 14,6,13312\nMvaa 28,44,108\nDS 1,20,36,0\nMvaa 28,40,104\n"
"WL 14\nTR 16,7,8192\nMvaa 32,48,96\nWL 13\nTR 18,7,9216\nMvaa 32,52,100\nTR 20,7,12288\nMvaa 36,52,108\nTR 22,7,13312\nMvaa 36,48,104\nWL 14\n"
"DS 1,20,37,1024\nMvaa 40,56,96\nWL 12\nTR 24,8,8192\nMvaa 40,60,100\nTR 26,8,9216\nMvaa 44,60,108\nTR 28,8,12288\nMvaa 44,56,104\nWL 13\nTR 30,8,13312\n"
"Mvaa 48,0,88\nWL 12\nTR 32,5,16384\nMvaa 48,4,92\nDS 1,20,38,2048\nMvaa 52,4,100\nTR 34,5,17408\nMvaa 52,0,96\nTR 36,5,20480\nMvaa 56,0,104\n"
"TR 38,5,21504\nMvaa 56,4,108\nWL 14\nDS 1,20,39,3072\nMvaa 60,8,88\nWL 12\ns_add_u32 m0,m0,0x1000\nTR 40,6,16384\nMvaa 60,12,92\nTR 42,6,17408\n"
"Mvaa 64,12,100\nTR 44,6,20480\nMvaa 64,8,96\nDS 1,20,40,0\nMvaa 68,8,104\nTR 46,6,21504\nMvaa 68,12,108\nWL 14\nTR 48,7,16384\nMvaa 72,16,88\nWL 13\n"
"TR 50,7,17408\nMvaa 72,20,92\nDS 1,20,41,1024\nMvaa 76,20,100\ns_mov_b32 s44,s47\nTR 52,7,20480\nMvaa 76,16,96\ns_add_u32 s47,s47,0x8000\n"
"TR 54,7,21504\nMvaa 80,16,104\ns_cmp_eq_u32 s47,0x28000\nTR 56,8,16384\nMvaa 80,20,108\nWL 15\ns_cselect_b32 s47,0,s47\nDS 1,20,42,2048\nMvaa 84,24,88\n"
"WL 13\nTR 58,8,17408\nMvaa 84,28,92\nTR 60,8,20480\nMvaa 88,28,100\nTR 62,8,21504\nMvaa 88,24,96\nDS 1,20,43,3072\nMvaa 92,24,104\n"
"v_add_u32 v1,0xffff8000,v1\nMvaa 92,28,108\nWL 14\nbuffer_store_dwordx4 v[232:235],v0,s[24:27],s30 offen offset:2048 sc1\nMvaa 96,88,32\nWL 12\n"
"v_add_u32 v3,s47,v0\nMvaa 96,92,36\ns_waitcnt vmcnt(36) lgkmcnt(0)\ns_barrier\nMvaa 100,100,36\nRD 0,3,0\nMvaa 100,96,32\nRD 4,3,1024\nMvaa 104,104,32\n"
"RD 8,3,2048\nMvaa 104,108,36\nRD 12,3,3072\nMvaa 108,88,40\nRD 16,3,4096\nMvaa 108,92,44\nRD 20,3,5120\nMvaa 112,100,44\nRD 24,3,6144\nMvaa 112,96,40\n"
"RD 28,3,7168\nMvaa 116,104,40\nbuffer_store_dwordx4 v[236:239],v0,s[24:27],s30 offen offset:3072 sc1\nMvaa 116,108,44\ns_add_u32 s30,s30,s28\n"
"Mvaa 120,88,48\nMvaa 120,92,52\nMvaa 124,100,52\nMvaa 124,96,48\nMvaa 128,104,48\nMvaa 128,108,52\nMvaa 132,88,56\nMvaa 132,92,60\nMvaa 136,100,60\n"
"Mvaa 136,96,56\nMvaa 140,104,56\nMvaa 140,108,60\ns_nop 7\nbuffer_store_dwordx4 v[240:243],v0,s[24:27],s30 offen offset:0 sc1\nv_lshrrev_b32 v11,4,v0\n"
"v_and_b32 v9,15,v11\nv_lshrrev_b32 v12,4,v11\nv_lshlrev_b32 v11,2,v12\nv_sub_u32 v11,v9,v11\nv_and_b32 v9,1,v9\nv_cmp_eq_u32 vcc,0,v9\n"
"v_cmp_le_i32 s[72:73],0,v11\nv_cmp_ge_i32 s[80:81],-2,v11\ns_nop 0\ns_and_b64 s[72:73],s[72:73],vcc\ns_and_b64 s[80:81],s[80:81],vcc\n"
"v_cmp_eq_u32 vcc,1,v9\nv_cmp_le_i32 s[74:75],1,v11\nv_cmp_ge_i32 s[82:83],-1,v11\ns_nop 0\ns_and_b64 s[74:75],s[74:75],vcc\n"
"s_and_b64 s[82:83],s[82:83],vcc\nv_cmp_eq_u32 vcc,0,v9\nv_cmp_le_i32 s[76:77],2,v11\nv_cmp_ge_i32 s[84:85],0,v11\ns_nop 0\n"
"s_and_b64 s[76:77],s[76:77],vcc\ns_and_b64 s[84:85],s[84:85],vcc\nv_cmp_eq_u32 vcc,1,v9\nv_cmp_le_i32 s[78:79],3,v11\nv_cmp_ge_i32 s[86:87],1,v11\n"
"s_nop 0\ns_and_b64 s[78:79],s[78:79],vcc\ns_and_b64 s[86:87],s[86:87],vcc\ns_cmp_eq_u32 s98,70254592\ns_mov_b32 s99,0x7f000000\n"
"s_cselect_b32 s98,0xf1000000,s99\nv_cmp_ne_u32 vcc,3,v12\nv_mov_b32 v9,0xf1000000\ns_mov_b32 s45,0x3e41848f\ns_mov_b32 s49,0x3fb8aa3b\nCV 16,16,17\n"
"CV 20,20,21\nCV 32,32,33\nCV 36,36,37\nCV 17,18,19\nCV 21,22,23\nCV 33,34,35\nCV 37,38,39\nCV 18,24,25\nCV 22,28,29\nCV 34,40,41\nCV 38,44,45\n"
"CV 19,26,27\nCV 23,30,31\nCV 35,42,43\nCV 39,46,47\nv_cndmask_b32 v18,0,v18,vcc\nv_cndmask_b32 v22,0,v22,vcc\nv_cndmask_b32 v34,0,v34,vcc\n"
"v_cndmask_b32 v38,0,v38,vcc\nv_cndmask_b32 v19,0,v19,vcc\nv_cndmask_b32 v23,0,v23,vcc\nv_cndmask_b32 v35,0,v35,vcc\nv_cndmask_b32 v39,0,v39,vcc\n"
"CV 48,48,49\nCV 52,52,53\nCV 56,56,57\nCV 72,72,73\nCV 76,76,77\nCV 80,80,81\nCV 49,50,51\nCV 53,54,55\nCV 57,58,59\nCV 73,74,75\nCV 77,78,79\n"
"buffer_store_dwordx4 v[244:247],v0,s[24:27],s30 offen offset:1024 sc1\nCV 81,82,83\nCV 50,60,61\nCV 54,64,65\nCV 58,68,69\nCV 74,84,85\nCV 78,88,89\n"
"CV 82,92,93\nCV 51,62,63\nCV 55,66,67\nCV 59,70,71\nCV 75,86,87\nCV 79,90,91\nCV 83,94,95\nZvvv 24,48,16\nZvvv 28,52,16\nZvvv 40,52,20\nZvvv 44,56,20\n"
"Zvvv 60,72,32\nZvvv 64,76,32\nZvvv 68,76,36\nZvvv 84,80,36\nCV 96,96,97\nCV 108,108,109\nCV 120,120,121\nCV 132,132,133\nCV 97,98,99\nCV 109,110,111\n"
"CV 121,122,123\nCV 133,134,135\nCV 98,100,101\nCV 110,112,113\nCV 122,124,125\nCV 134,136,137\nCV 99,102,103\nCV 111,114,115\nCV 123,126,127\n"
"CV 135,138,139\nCV 100,104,105\nCV 112,116,117\nCV 124,128,129\nCV 136,140,141\nCV 101,106,107\nCV 113,118,119\nCV 125,130,131\nCV 137,142,143\n"
"CV 104,24,25\nCV 116,40,41\nCV 128,60,61\nCV 140,68,69\nCV 105,26,27\nCV 117,42,43\nCV 129,62,63\nCV 141,70,71\nCV 106,28,29\nCV 118,44,45\n"
"CV 130,64,65\nCV 142,84,85\nCV 107,30,31\nCV 119,46,47\nCV 131,66,67\nCV 143,86,87\nv_lshlrev_b32 v24,16,v104\nv_lshlrev_b32 v40,16,v116\n"
"v_lshlrev_b32 v60,16,v128\nv_lshlrev_b32 v68,16,v140\nv_and_b32 v25,0xffff0000,v104\nv_and_b32 v41,0xffff0000,v116\nv_and_b32 v61,0xffff0000,v128\n"
"v_and_b32 v69,0xffff0000,v140\nv_lshlrev_b32 v26,16,v105\nv_lshlrev_b32 v42,16,v117\nv_lshlrev_b32 v62,16,v129\n"
"buffer_store_dwordx4 v[248:251],v0,s[24:27],s30 offen offset:2048 sc1\nv_lshlrev_b32 v70,16,v141\nv_and_b32 v27,0xffff0000,v105\n"
"v_and_b32 v43,0xffff0000,v117\nv_and_b32 v63,0xffff0000,v129\nv_and_b32 v71,0xffff0000,v141\nv_lshlrev_b32 v28,16,v106\nv_lshlrev_b32 v44,16,v118\n"
"v_lshlrev_b32 v64,16,v130\nv_lshlrev_b32 v84,16,v142\nv_and_b32 v29,0xffff0000,v106\nv_and_b32 v45,0xffff0000,v118\nv_and_b32 v65,0xffff0000,v130\n"
"v_and_b32 v85,0xffff0000,v142\nv_lshlrev_b32 v30,16,v107\nv_lshlrev_b32 v46,16,v119\nv_lshlrev_b32 v66,16,v131\nv_lshlrev_b32 v86,16,v143\n"
"v_and_b32 v31,0xffff0000,v107\nv_and_b32 v47,0xffff0000,v119\nv_and_b32 v67,0xffff0000,v131\nv_and_b32 v87,0xffff0000,v143\nv_mul_f32 v24,s45,v24\n"
"v_mul_f32 v40,s45,v40\nv_mul_f32 v60,s45,v60\nv_mul_f32 v68,s45,v68\nv_mul_f32 v25,s45,v25\nv_mul_f32 v41,s45,v41\nv_mul_f32 v61,s45,v61\n"
"v_mul_f32 v69,s45,v69\nv_mul_f32 v26,s45,v26\nv_mul_f32 v42,s45,v42\nv_mul_f32 v62,s45,v62\nv_mul_f32 v70,s45,v70\nv_mul_f32 v27,s45,v27\n"
"v_mul_f32 v43,s45,v43\nv_mul_f32 v63,s45,v63\nv_mul_f32 v71,s45,v71\nv_mul_f32 v28,s45,v28\nv_mul_f32 v44,s45,v44\nv_mul_f32 v64,s45,v64\n"
"v_mul_f32 v84,s45,v84\nv_mul_f32 v29,s45,v29\nv_mul_f32 v45,s45,v45\nv_mul_f32 v65,s45,v65\nv_mul_f32 v85,s45,v85\nv_mul_f32 v30,s45,v30\n"
"v_mul_f32 v46,s45,v46\nv_mul_f32 v66,s45,v66\nv_mul_f32 v86,s45,v86\nv_mul_f32 v31,s45,v31\nv_mul_f32 v47,s45,v47\nv_mul_f32 v67,s45,v67\n"
"v_mul_f32 v87,s45,v87\nCV 104,24,25\nCV 116,40,41\nCV 128,60,61\nCV 140,68,69\nCV 105,26,27\nCV 117,42,43\nCV 129,62,63\nCV 141,70,71\nCV 106,28,29\n"
"CV 118,44,45\nCV 130,64,65\nCV 142,84,85\nCV 107,30,31\nCV 119,46,47\nCV 131,66,67\nCV 143,86,87\nv_lshlrev_b32 v24,16,v104\nv_lshlrev_b32 v40,16,v116\n"
"v_lshlrev_b32 v60,16,v128\nbuffer_store_dwordx4 v[252:255],v0,s[24:27],s30 offen offset:3072 sc1\ns_add_u32 s30,s30,s28\nv_lshlrev_b32 v68,16,v140\n"
"v_and_b32 v25,0xffff0000,v104\nv_and_b32 v41,0xffff0000,v116\nv_and_b32 v61,0xffff0000,v128\nv_and_b32 v69,0xffff0000,v140\nv_lshlrev_b32 v26,16,v105\n"
"v_lshlrev_b32 v42,16,v117\nv_lshlrev_b32 v62,16,v129\nv_lshlrev_b32 v70,16,v141\nv_and_b32 v27,0xffff0000,v105\nv_and_b32 v43,0xffff0000,v117\n"
"v_and_b32 v63,0xffff0000,v129\nv_and_b32 v71,0xffff0000,v141\nv_lshlrev_b32 v28,16,v106\nv_lshlrev_b32 v44,16,v118\nv_lshlrev_b32 v64,16,v130\n"
"v_lshlrev_b32 v84,16,v142\nv_and_b32 v29,0xffff0000,v106\nv_and_b32 v45,0xffff0000,v118\nv_and_b32 v65,0xffff0000,v130\nv_and_b32 v85,0xffff0000,v142\n"
"v_lshlrev_b32 v30,16,v107\nv_lshlrev_b32 v46,16,v119\nv_lshlrev_b32 v66,16,v131\nv_lshlrev_b32 v86,16,v143\nv_and_b32 v31,0xffff0000,v107\n"
"v_and_b32 v47,0xffff0000,v119\nv_and_b32 v67,0xffff0000,v131\nv_and_b32 v87,0xffff0000,v143\nv_cndmask_b32 v24,v9,v24,s[80:81]\n"
"v_cndmask_b32 v40,v9,v40,s[80:81]\nv_cndmask_b32 v60,v9,v60,s[80:81]\nv_cndmask_b32 v68,v9,v68,s[80:81]\nv_cndmask_b32 v28,v9,v28,s[72:73]\n"
"v_cndmask_b32 v44,v9,v44,s[72:73]\nv_cndmask_b32 v64,v9,v64,s[72:73]\nv_cndmask_b32 v84,v9,v84,s[72:73]\nv_cndmask_b32 v25,v9,v25,s[82:83]\n"
"v_cndmask_b32 v41,v9,v41,s[82:83]\nv_cndmask_b32 v61,v9,v61,s[82:83]\nv_cndmask_b32 v69,v9,v69,s[82:83]\nv_cndmask_b32 v29,v9,v29,s[74:75]\n"
"v_cndmask_b32 v45,v9,v45,s[74:75]\nv_cndmask_b32 v65,v9,v65,s[74:75]\nv_cndmask_b32 v85,v9,v85,s[74:75]\nv_cndmask_b32 v26,v9,v26,s[84:85]\n"
"v_cndmask_b32 v42,v9,v42,s[84:85]\nv_cndmask_b32 v62,v9,v62,s[84:85]\nv_cndmask_b32 v70,v9,v70,s[84:85]\nv_cndmask_b32 v30,v9,v30,s[76:77]\n"
"v_cndmask_b32 v46,v9,v46,s[76:77]\nv_cndmask_b32 v66,v9,v66,s[76:77]\nv_cndmask_b32 v86,v9,v86,s[76:77]\nv_cndmask_b32 v27,v9,v27,s[86:87]\n"
"v_cndmask_b32 v43,v9,v43,s[86:87]\nv_cndmask_b32 v63,v9,v63,s[86:87]\nv_cndmask_b32 v71,v9,v71,s[86:87]\nv_cndmask_b32 v31,v9,v31,s[78:79]\n"
"v_cndmask_b32 v47,v9,v47,s[78:79]\nv_cndmask_b32 v67,v9,v67,s[78:79]\nv_cndmask_b32 v87,v9,v87,s[78:79]\nv_min_f32 v24,s98,v24\n"
"v_max3_f32 v90,v40,v41,v42\nv_min_f32 v60,s98,v60\nv_max3_f32 v94,v68,v69,v70\nv_min_f32 v25,s98,v25\nv_max3_f32 v90,v90,v43,v44\n"
"v_min_f32 v61,s98,v61\nv_max3_f32 v94,v94,v71,v84\nv_min_f32 v26,s98,v26\nv_max3_f32 v90,v90,v45,v46\nv_min_f32 v62,s98,v62\n"
"v_max3_f32 v94,v94,v85,v86\nv_min_f32 v27,s98,v27\nv_max_f32 v90,v90,v47\nv_min_f32 v63,s98,v63\nv_max_f32 v94,v94,v87\nv_max3_f32 v88,v24,v25,v26\n"
"v_mov_b32 v91,v90\nv_max3_f32 v92,v60,v61,v62\nv_mov_b32 v95,v94\nv_max3_f32 v88,v88,v27,v28\nv_permlane32_swap_b32 v90,v91\n"
"v_max3_f32 v92,v92,v63,v64\nv_permlane32_swap_b32 v94,v95\nv_max3_f32 v88,v88,v29,v30\nv_max_f32 v90,v90,v91\nv_max3_f32 v92,v92,v65,v66\n"
"v_max_f32 v94,v94,v95\nv_max_f32 v88,v88,v31\nv_mov_b32 v91,v90\nv_max_f32 v92,v92,v67\nv_mov_b32 v95,v94\nv_mov_b32 v89,v88\n"
"v_permlane16_swap_b32 v90,v91\nv_mov_b32 v93,v92\nv_permlane16_swap_b32 v94,v95\nv_permlane32_swap_b32 v88,v89\nv_max_f32 v90,v90,v91\n"
"v_permlane32_swap_b32 v92,v93\nv_max_f32 v94,v94,v95\nv_max_f32 v88,v88,v89\nv_sub_f32 v40,v40,v90\nv_max_f32 v92,v92,v93\nv_sub_f32 v68,v68,v94\n"
"v_mov_b32 v89,v88\nv_sub_f32 v41,v41,v90\nv_mov_b32 v93,v92\nv_sub_f32 v69,v69,v94\nv_permlane16_swap_b32 v88,v89\nv_sub_f32 v42,v42,v90\n"
"v_permlane16_swap_b32 v92,v93\nv_sub_f32 v70,v70,v94\nv_max_f32 v88,v88,v89\nv_sub_f32 v43,v43,v90\nv_max_f32 v92,v92,v93\nv_sub_f32 v71,v71,v94\n"
"v_sub_f32 v24,v24,v88\nv_sub_f32 v44,v44,v90\nv_sub_f32 v60,v60,v92\nv_sub_f32 v84,v84,v94\nv_sub_f32 v25,v25,v88\nv_sub_f32 v45,v45,v90\n"
"v_sub_f32 v61,v61,v92\nv_sub_f32 v85,v85,v94\nv_sub_f32 v26,v26,v88\nv_sub_f32 v46,v46,v90\nv_sub_f32 v62,v62,v92\nv_sub_f32 v86,v86,v94\n"
"v_sub_f32 v27,v27,v88\nv_sub_f32 v47,v47,v90\nv_sub_f32 v63,v63,v92\nv_sub_f32 v87,v87,v94\nv_sub_f32 v28,v28,v88\nv_mul_f32 v40,s49,v40\n"
"v_sub_f32 v64,v64,v92\nv_mul_f32 v68,s49,v68\nv_sub_f32 v29,v29,v88\nv_mul_f32 v41,s49,v41\nv_sub_f32 v65,v65,v92\nv_mul_f32 v69,s49,v69\n"
"v_sub_f32 v30,v30,v88\nv_mul_f32 v42,s49,v42\nv_sub_f32 v66,v66,v92\nv_mul_f32 v70,s49,v70\nv_sub_f32 v31,v31,v88\nv_mul_f32 v43,s49,v43\n"
"v_sub_f32 v67,v67,v92\nv_mul_f32 v71,s49,v71\nv_mul_f32 v24,s49,v24\nv_mul_f32 v44,s49,v44\nv_mul_f32 v60,s49,v60\nv_mul_f32 v84,s49,v84\n"
"v_mul_f32 v25,s49,v25\nv_mul_f32 v45,s49,v45\nv_mul_f32 v61,s49,v61\nv_mul_f32 v85,s49,v85\nv_mul_f32 v26,s49,v26\nv_mul_f32 v46,s49,v46\n"
"v_mul_f32 v62,s49,v62\nv_mul_f32 v86,s49,v86\nv_mul_f32 v27,s49,v27\nv_mul_f32 v47,s49,v47\nv_mul_f32 v63,s49,v63\nv_mul_f32 v87,s49,v87\n"
"v_mul_f32 v28,s49,v28\nv_exp_f32 v40,v40\nv_mul_f32 v64,s49,v64\nv_exp_f32 v68,v68\nv_mul_f32 v29,s49,v29\nv_exp_f32 v41,v41\nv_mul_f32 v65,s49,v65\n"
"v_exp_f32 v69,v69\nv_mul_f32 v30,s49,v30\nv_exp_f32 v42,v42\nv_mul_f32 v66,s49,v66\nv_exp_f32 v70,v70\nv_mul_f32 v31,s49,v31\nv_exp_f32 v43,v43\n"
"v_mul_f32 v67,s49,v67\nv_exp_f32 v71,v71\nv_exp_f32 v24,v24\nv_exp_f32 v44,v44\nv_exp_f32 v60,v60\nv_exp_f32 v84,v84\nv_exp_f32 v25,v25\n"
"v_exp_f32 v45,v45\nv_exp_f32 v61,v61\nv_exp_f32 v85,v85\nv_exp_f32 v26,v26\nv_exp_f32 v46,v46\nv_exp_f32 v62,v62\nv_exp_f32 v86,v86\nv_exp_f32 v27,v27\n"
"v_exp_f32 v47,v47\nv_exp_f32 v63,v63\nv_exp_f32 v87,v87\nv_exp_f32 v28,v28\nv_add_f32 v90,v40,v41\nv_exp_f32 v64,v64\nv_add_f32 v94,v68,v69\n"
"v_exp_f32 v29,v29\nv_add_f32 v90,v90,v42\nv_exp_f32 v65,v65\nv_add_f32 v94,v94,v70\nv_exp_f32 v30,v30\nv_add_f32 v90,v90,v43\nv_exp_f32 v66,v66\n"
"v_add_f32 v94,v94,v71\nv_exp_f32 v31,v31\nv_add_f32 v90,v90,v44\nv_exp_f32 v67,v67\nv_add_f32 v94,v94,v84\nv_add_f32 v88,v24,v25\n"
"v_add_f32 v90,v90,v45\nv_add_f32 v92,v60,v61\nv_add_f32 v94,v94,v85\nv_add_f32 v88,v88,v26\nv_add_f32 v90,v90,v46\nv_add_f32 v92,v92,v62\n"
"v_add_f32 v94,v94,v86\nv_add_f32 v88,v88,v27\nv_add_f32 v90,v90,v47\nv_add_f32 v92,v92,v63\nv_add_f32 v94,v94,v87\nv_add_f32 v88,v88,v28\n"
"v_mov_b32 v91,v90\nv_add_f32 v92,v92,v64\nv_mov_b32 v95,v94\nv_add_f32 v88,v88,v29\nv_permlane32_swap_b32 v90,v91\nv_add_f32 v92,v92,v65\n"
"v_permlane32_swap_b32 v94,v95\nv_add_f32 v88,v88,v30\nv_add_f32 v90,v90,v91\nv_add_f32 v92,v92,v66\nv_add_f32 v94,v94,v95\nv_add_f32 v88,v88,v31\n"
"v_mov_b32 v91,v90\nv_add_f32 v92,v92,v67\nv_mov_b32 v95,v94\nv_mov_b32 v89,v88\nv_permlane16_swap_b32 v90,v91\nv_mov_b32 v93,v92\n"
"v_permlane16_swap_b32 v94,v95\nv_permlane32_swap_b32 v88,v89\nv_add_f32 v90,v90,v91\nv_permlane32_swap_b32 v92,v93\nv_add_f32 v94,v94,v95\n"
"v_add_f32 v88,v88,v89\nv_rcp_f32 v90,v90\nv_add_f32 v92,v92,v93\nv_rcp_f32 v94,v94\nv_mov_b32 v89,v88\nv_mul_f32 v40,v90,v40\nv_mov_b32 v93,v92\n"
"v_mul_f32 v68,v94,v68\nv_permlane16_swap_b32 v88,v89\nv_mul_f32 v41,v90,v41\nv_permlane16_swap_b32 v92,v93\nv_mul_f32 v69,v94,v69\n"
"v_add_f32 v88,v88,v89\nv_mul_f32 v42,v90,v42\nv_add_f32 v92,v92,v93\nv_mul_f32 v70,v94,v70\nv_rcp_f32 v88,v88\nv_mul_f32 v43,v90,v43\n"
"v_rcp_f32 v92,v92\nv_mul_f32 v71,v94,v71\nv_mul_f32 v24,v88,v24\nv_mul_f32 v44,v90,v44\nv_mul_f32 v60,v92,v60\nv_mul_f32 v84,v94,v84\n"
"v_mul_f32 v25,v88,v25\nv_mul_f32 v45,v90,v45\nv_mul_f32 v61,v92,v61\nv_mul_f32 v85,v94,v85\nv_mul_f32 v26,v88,v26\nv_mul_f32 v46,v90,v46\n"
"v_mul_f32 v62,v92,v62\nv_mul_f32 v86,v94,v86\nv_mul_f32 v27,v88,v27\nv_mul_f32 v47,v90,v47\nv_mul_f32 v63,v92,v63\nv_mul_f32 v87,v94,v87\n"
"v_mul_f32 v28,v88,v28\nCV 116,40,41\nv_mul_f32 v64,v92,v64\nCV 140,68,69\nv_mul_f32 v29,v88,v29\nCV 117,42,43\nv_mul_f32 v65,v92,v65\nCV 141,70,71\n"
"v_mul_f32 v30,v88,v30\nCV 118,44,45\nv_mul_f32 v66,v92,v66\nCV 142,84,85\nv_mul_f32 v31,v88,v31\nCV 119,46,47\nv_mul_f32 v67,v92,v67\nCV 143,86,87\n"
"CV 104,24,25\nCV 128,60,61\nCV 105,26,27\nCV 129,62,63\nCV 106,28,29\nCV 130,64,65\nCV 107,30,31\nCV 131,66,67\ns_nop 0\nZvvv 24,96,104\n"
"Zvvv 28,108,104\nZvvv 40,98,116\nZvvv 44,110,116\nZvvv 60,120,128\nZvvv 64,132,128\nZvvv 68,122,140\nZvvv 84,134,140\ns_waitcnt vmcnt(0)\n"
"v_mov_b32 v13,s65\ns_mov_b64 exec,1\nbuffer_store_dword v13,off,s[24:27],s7 sc1\ns_mov_b64 exec,-1\ns_mul_i32 s70,s56,131072\ns_mul_i32 s49,s10,32768\n"
"s_add_u32 s70,s70,s49\ns_mul_i32 s49,s57,2048\ns_add_u32 s70,s70,s49\ns_add_u32 s70,s70,0x4100000\ns_add_u32 s71,s70,16384\nCV 24,24,25\nCV 40,40,41\n"
"CV 60,60,61\nCV 68,68,69\nCV 25,26,27\nCV 41,42,43\nCV 61,62,63\nCV 69,70,71\nCV 26,28,29\nCV 42,44,45\nCV 62,64,65\nCV 70,84,85\nCV 27,30,31\n"
"CV 43,46,47\nCV 63,66,67\nCV 71,86,87\nv_cndmask_b32 v26,0,v26,vcc\nv_cndmask_b32 v42,0,v42,vcc\nv_cndmask_b32 v62,0,v62,vcc\n"
"v_cndmask_b32 v70,0,v70,vcc\nv_cndmask_b32 v27,0,v27,vcc\nv_cndmask_b32 v43,0,v43,vcc\nv_cndmask_b32 v63,0,v63,vcc\nv_cndmask_b32 v71,0,v71,vcc\n"
"buffer_store_dwordx4 v[24:27],v0,s[24:27],s70 offen offset:0 sc1\nbuffer_store_dwordx4 v[40:43],v0,s[24:27],s71 offen offset:0 sc1\n"
"buffer_store_dwordx4 v[60:63],v0,s[24:27],s70 offen offset:1024 sc1\nbuffer_store_dwordx4 v[68:71],v0,s[24:27],s71 offen offset:1024 sc1\n"
"s_mul_i32 s8,s68,131072\ns_mul_i32 s49,s10,32768\ns_add_u32 s8,s8,s49\ns_add_u32 s8,s8,0x3100000\ns_mul_i32 s9,s68,512\ns_mul_i32 s49,s10,128\n"
"s_add_u32 s9,s9,s49\ns_add_u32 s9,s9,0x40000\ns_add_u32 s50,s54,fused_op_ta-fused_op_pc\ns_addc_u32 s51,s55,0\ns_cmp_lg_u32 s60,0\n"
"s_cbranch_scc1 fused_op_enter\ns_add_u32 s50,s54,fused_op_tw-fused_op_pc\ns_addc_u32 s51,s55,0\ns_branch fused_op_enter\nfused_op_qb:\ns_nop 1\n"
"s_cmp_eq_u32 s61,2\ns_mov_b32 s95,0x18000\ns_cselect_b32 s95,0x10000,s95\ns_cmp_eq_u32 s60,2\ns_mov_b32 s96,0x18000\ns_cselect_b32 s96,0x10000,s96\n"
"WL 14\nZvaa 16,0,144\nWL 12\nTR 32,5,8192\nTR 34,5,9216\nMvaa 16,4,152\nTR 36,5,12288\nTR 38,5,13312\nZvaa 20,4,156\nTR 40,6,8192\nTR 42,6,9216\n"
"Mvaa 20,0,148\nWL 14\nTR 44,6,12288\nbuffer_store_dwordx4 v[160:163],v0,s[24:27],s30 offen offset:0 sc1\nZvaa 24,8,144\nTR 46,6,13312\nMvaa 24,12,152\n"
"TR 48,7,8192\nZvaa 28,12,156\nTR 50,7,9216\nMvaa 28,8,148\nWL 14\ns_add_u32 m0,s48,s44\nTR 52,7,12288\nZvaa 32,16,144\nTR 54,7,13312\nMvaa 32,20,152\n"
"DP 1,20,36,0\nZvaa 36,20,156\nTR 56,8,8192\nMvaa 36,16,148\nWL 14\nTR 58,8,9216\nZvaa 40,24,144\nWL 14\nDP 1,20,37,1024\nMvaa 40,28,152\nTR 60,8,12288\n"
"Zvaa 44,28,156\nTR 62,8,13312\nMvaa 44,24,148\nWL 14\nDP 1,20,38,2048\nZvaa 48,32,144\nWL 12\nTR 0,5,16384\nTR 2,5,17408\nMvaa 48,36,152\nTR 4,5,20480\n"
"TR 6,5,21504\nZvaa 52,36,156\nTR 8,6,16384\nTR 10,6,17408\nMvaa 52,32,148\nWL 14\nTR 12,6,20480\nZvaa 56,40,144\nTR 14,6,21504\nMvaa 56,44,152\n"
"TR 16,7,16384\nbuffer_store_dwordx4 v[164:167],v0,s[24:27],s30 offen offset:1024 sc1\nZvaa 60,44,156\nTR 18,7,17408\nMvaa 60,40,148\nWL 14\n"
"TR 20,7,20480\nZvaa 64,48,144\nTR 22,7,21504\nMvaa 64,52,152\nDP 1,20,39,3072\nZvaa 68,52,156\ns_add_u32 m0,m0,0x1000\nTR 24,8,16384\nMvaa 68,48,148\n"
"WL 14\nTR 26,8,17408\nZvaa 72,56,144\nWL 14\nDP 1,20,40,0\nMvaa 72,60,152\nTR 28,8,20480\nZvaa 76,60,156\nTR 30,8,21504\nMvaa 76,56,148\nWL 14\n"
"DP 1,20,41,1024\nZvaa 80,0,144\nWL 12\nTR 32,5,24576\nTR 34,5,25600\nMvaa 80,4,152\ns_mov_b32 s44,s47\nTR 36,5,28672\nTR 38,5,29696\nZvaa 84,4,156\n"
"s_add_u32 s47,s47,0x8000\nTR 40,6,24576\nTR 42,6,25600\nMvaa 84,0,148\nWL 13\ns_cmp_eq_u32 s47,0x28000\nTR 44,6,28672\nTR 46,6,29696\nZvaa 88,8,144\n"
"s_cselect_b32 s47,0,s47\nTR 48,7,24576\nTR 50,7,25600\nMvaa 88,12,152\nTR 52,7,28672\nTR 54,7,29696\nZvaa 92,12,156\nTR 56,8,24576\nTR 58,8,25600\n"
"Mvaa 92,8,148\nWL 14\nTR 60,8,28672\nZvaa 96,16,144\nTR 62,8,29696\nMvaa 96,20,152\nDP 1,20,42,2048\nZvaa 100,20,156\nv_add_u32 v5,s47,v10\n"
"v_xor_b32 v6,32,v5\nMvaa 100,16,148\nv_xor_b32 v7,64,v5\nv_xor_b32 v8,96,v5\nZvaa 104,24,144\nDP 1,20,43,3072\nMvaa 104,28,152\n"
"v_add_u32 v1,0x80000,v1\nZvaa 108,28,156\nbuffer_store_dwordx4 v[168:171],v0,s[24:27],s30 offen offset:2048 sc1\nMvaa 108,24,148\n"
"s_waitcnt vmcnt(27) lgkmcnt(0)\ns_barrier\nZvaa 112,32,144\nTR 0,5,0\nMvaa 112,36,152\nTR 2,5,1024\nZvaa 116,36,156\nTR 4,5,4096\nMvaa 116,32,148\n"
"TR 6,5,5120\nZvaa 120,40,144\nTR 8,6,0\nMvaa 120,44,152\nTR 10,6,1024\nZvaa 124,44,156\nTR 12,6,4096\nMvaa 124,40,148\nTR 14,6,5120\nZvaa 128,48,144\n"
"TR 16,7,0\nMvaa 128,52,152\nTR 18,7,1024\nZvaa 132,52,156\nTR 20,7,4096\nMvaa 132,48,148\nTR 22,7,5120\nZvaa 136,56,144\nTR 24,8,0\nMvaa 136,60,152\n"
"TR 26,8,1024\nZvaa 140,60,156\nTR 28,8,4096\nMvaa 140,56,148\nWL 13\nTR 30,8,5120\nMvaa 16,0,160\nWL 12\nTR 32,5,8192\nTR 34,5,9216\nMvaa 16,4,168\n"
"TR 36,5,12288\nTR 38,5,13312\nMvaa 20,4,172\nTR 40,6,8192\nTR 42,6,9216\nMvaa 20,0,164\nWL 14\nTR 44,6,12288\n"
"buffer_store_dwordx4 v[172:175],v0,s[24:27],s30 offen offset:3072 sc1\nMvaa 24,8,160\ns_add_u32 s30,s30,s28\nTR 46,6,13312\nMvaa 24,12,168\n"
"TR 48,7,8192\nMvaa 28,12,172\nTR 50,7,9216\nMvaa 28,8,164\nWL 14\ns_add_u32 m0,s48,s44\nTR 52,7,12288\nMvaa 32,16,160\nTR 54,7,13312\nMvaa 32,20,168\n"
"s_add_u32 s15,s12,12288\nDP 1,20,36,0\nMvaa 36,20,172\nTR 56,8,8192\nbuffer_load_dwordx4 a[128:131],v0,s[24:27],s15 offen offset:0 sc1\nMvaa 36,16,164\n"
"WL 14\nTR 58,8,9216\nMvaa 40,24,160\nWL 14\nDP 1,20,37,1024\nMvaa 40,28,168\nTR 60,8,12288\nMvaa 44,28,172\nTR 62,8,13312\nMvaa 44,24,164\nWL 14\n"
"DP 1,20,38,2048\nMvaa 48,32,160\nWL 12\nTR 0,5,16384\nTR 2,5,17408\nMvaa 48,36,168\nTR 4,5,20480\nTR 6,5,21504\nMvaa 52,36,172\nTR 8,6,16384\n"
"TR 10,6,17408\nMvaa 52,32,164\nWL 14\nTR 12,6,20480\nMvaa 56,40,160\nTR 14,6,21504\nMvaa 56,44,168\ns_add_u32 s15,s12,12288\nTR 16,7,16384\n"
"buffer_store_dwordx4 v[176:179],v0,s[24:27],s30 offen offset:0 sc1\nMvaa 60,44,172\nTR 18,7,17408\n"
"buffer_load_dwordx4 a[136:139],v0,s[24:27],s15 offen offset:1024 sc1\nMvaa 60,40,164\nWL 14\nTR 20,7,20480\nMvaa 64,48,160\nTR 22,7,21504\n"
"Mvaa 64,52,168\nDP 1,20,39,3072\nMvaa 68,52,172\ns_add_u32 m0,m0,0x1000\nTR 24,8,16384\nMvaa 68,48,164\nWL 14\nTR 26,8,17408\nMvaa 72,56,160\nWL 14\n"
"DP 1,20,40,0\nMvaa 72,60,168\nTR 28,8,20480\nMvaa 76,60,172\nTR 30,8,21504\nMvaa 76,56,164\nWL 14\nDP 1,20,41,1024\nMvaa 80,0,160\nWL 12\n"
"TR 32,5,24576\nTR 34,5,25600\nMvaa 80,4,168\ns_mov_b32 s44,s47\nTR 36,5,28672\nTR 38,5,29696\nMvaa 84,4,172\ns_add_u32 s47,s47,0x8000\nTR 40,6,24576\n"
"TR 42,6,25600\nMvaa 84,0,164\nWL 13\ns_cmp_eq_u32 s47,0x28000\nTR 44,6,28672\nTR 46,6,29696\nMvaa 88,8,160\ns_cselect_b32 s47,0,s47\nTR 48,7,24576\n"
"TR 50,7,25600\nMvaa 88,12,168\ns_add_u32 s15,s12,28672\nTR 52,7,28672\nTR 54,7,29696\nMvaa 92,12,172\nTR 56,8,24576\nTR 58,8,25600\n"
"buffer_load_dwordx4 a[132:135],v0,s[24:27],s15 offen offset:0 sc1\nMvaa 92,8,164\nWL 14\nTR 60,8,28672\nMvaa 96,16,160\nTR 62,8,29696\nMvaa 96,20,168\n"
"DP 1,20,42,2048\nMvaa 100,20,172\nv_add_u32 v5,s47,v10\nv_xor_b32 v6,32,v5\nMvaa 100,16,164\nv_xor_b32 v7,64,v5\nv_xor_b32 v8,96,v5\nMvaa 104,24,160\n"
"DP 1,20,43,3072\nMvaa 104,28,168\nv_add_u32 v1,0x80000,v1\nMvaa 108,28,172\nbuffer_store_dwordx4 v[180:183],v0,s[24:27],s30 offen offset:1024 sc1\n"
"Mvaa 108,24,164\ns_waitcnt vmcnt(33) lgkmcnt(0)\ns_barrier\nMvaa 112,32,160\ns_add_u32 s15,s12,28672\nTR 0,5,0\nMvaa 112,36,168\nTR 2,5,1024\n"
"buffer_load_dwordx4 a[140:143],v0,s[24:27],s15 offen offset:1024 sc1\nMvaa 116,36,172\nTR 4,5,4096\nMvaa 116,32,164\nTR 6,5,5120\nMvaa 120,40,160\n"
"TR 8,6,0\nMvaa 120,44,168\nTR 10,6,1024\nMvaa 124,44,172\nTR 12,6,4096\nMvaa 124,40,164\nTR 14,6,5120\nMvaa 128,48,160\nTR 16,7,0\nMvaa 128,52,168\n"
"TR 18,7,1024\nMvaa 132,52,172\nTR 20,7,4096\nMvaa 132,48,164\nTR 22,7,5120\nMvaa 136,56,160\nTR 24,8,0\nMvaa 136,60,168\nTR 26,8,1024\nMvaa 140,60,172\n"
"TR 28,8,4096\nMvaa 140,56,164\nWL 13\nTR 30,8,5120\nMvaa 16,0,176\nWL 12\nTR 32,5,8192\nTR 34,5,9216\nMvaa 16,4,184\nTR 36,5,12288\nTR 38,5,13312\n"
"Mvaa 20,4,188\nTR 40,6,8192\nTR 42,6,9216\nMvaa 20,0,180\nWL 14\nTR 44,6,12288\nbuffer_store_dwordx4 v[184:187],v0,s[24:27],s30 offen offset:2048 sc1\n"
"Mvaa 24,8,176\nTR 46,6,13312\nMvaa 24,12,184\nTR 48,7,8192\nMvaa 28,12,188\nTR 50,7,9216\nMvaa 28,8,180\nWL 14\ns_add_u32 m0,s48,s44\nTR 52,7,12288\n"
"Mvaa 32,16,176\nTR 54,7,13312\nMvaa 32,20,184\nDP 1,20,36,0\nMvaa 36,20,188\nTR 56,8,8192\nMvaa 36,16,180\nWL 14\nTR 58,8,9216\nMvaa 40,24,176\nWL 14\n"
"DP 1,20,37,1024\nMvaa 40,28,184\nTR 60,8,12288\nMvaa 44,28,188\nTR 62,8,13312\nMvaa 44,24,180\nWL 14\nDP 1,20,38,2048\nMvaa 48,32,176\nWL 12\n"
"TR 0,5,16384\nTR 2,5,17408\nMvaa 48,36,184\nTR 4,5,20480\nTR 6,5,21504\nMvaa 52,36,188\nTR 8,6,16384\nTR 10,6,17408\nMvaa 52,32,180\nWL 14\n"
"TR 12,6,20480\nMvaa 56,40,176\nTR 14,6,21504\nMvaa 56,44,184\nTR 16,7,16384\nbuffer_store_dwordx4 v[188:191],v0,s[24:27],s30 offen offset:3072 sc1\n"
"Mvaa 60,44,188\ns_add_u32 s30,s30,s29\nTR 18,7,17408\nMvaa 60,40,180\nWL 14\nTR 20,7,20480\nMvaa 64,48,176\nTR 22,7,21504\nMvaa 64,52,184\n"
"DP 1,20,39,3072\nMvaa 68,52,188\ns_add_u32 m0,m0,0x1000\nTR 24,8,16384\nMvaa 68,48,180\nWL 14\nTR 26,8,17408\nMvaa 72,56,176\nWL 14\nDP 1,20,40,0\n"
"Mvaa 72,60,184\nTR 28,8,20480\nMvaa 76,60,188\nTR 30,8,21504\nMvaa 76,56,180\nWL 14\nDP 1,20,41,1024\nMvaa 80,0,176\nWL 12\nTR 32,5,24576\n"
"TR 34,5,25600\nMvaa 80,4,184\ns_mov_b32 s44,s47\nTR 36,5,28672\nTR 38,5,29696\nMvaa 84,4,188\ns_add_u32 s47,s47,0x8000\nTR 40,6,24576\nTR 42,6,25600\n"
"Mvaa 84,0,180\nWL 13\ns_cmp_eq_u32 s47,0x28000\nTR 44,6,28672\nTR 46,6,29696\nMvaa 88,8,176\ns_cselect_b32 s47,0,s47\nTR 48,7,24576\nTR 50,7,25600\n"
"Mvaa 88,12,184\nTR 52,7,28672\nTR 54,7,29696\nMvaa 92,12,188\nTR 56,8,24576\nTR 58,8,25600\nMvaa 92,8,180\nWL 14\nTR 60,8,28672\nMvaa 96,16,176\n"
"TR 62,8,29696\nMvaa 96,20,184\nDP 1,20,42,2048\nMvaa 100,20,188\nv_add_u32 v5,s47,v10\nv_xor_b32 v6,32,v5\nMvaa 100,16,180\nv_xor_b32 v7,64,v5\n"
"v_xor_b32 v8,96,v5\nMvaa 104,24,176\nDP 1,20,43,3072\nMvaa 104,28,184\nv_add_u32 v1,0x80000,v1\nMvaa 108,28,188\n"
"buffer_store_dwordx4 v[192:195],v0,s[24:27],s30 offen offset:0 sc1\nMvaa 108,24,180\ns_waitcnt vmcnt(37) lgkmcnt(0)\ns_barrier\nMvaa 112,32,176\n"
"TR 0,5,0\nMvaa 112,36,184\nTR 2,5,1024\nMvaa 116,36,188\nTR 4,5,4096\nMvaa 116,32,180\nTR 6,5,5120\nMvaa 120,40,176\nTR 8,6,0\nMvaa 120,44,184\n"
"TR 10,6,1024\nMvaa 124,44,188\nTR 12,6,4096\nMvaa 124,40,180\nTR 14,6,5120\nMvaa 128,48,176\nTR 16,7,0\nMvaa 128,52,184\nTR 18,7,1024\nMvaa 132,52,188\n"
"TR 20,7,4096\nMvaa 132,48,180\nTR 22,7,5120\nMvaa 136,56,176\nTR 24,8,0\nMvaa 136,60,184\nTR 26,8,1024\nMvaa 140,60,188\nTR 28,8,4096\nMvaa 140,56,180\n"
"WL 13\nTR 30,8,5120\nMvaa 16,0,192\nWL 12\nTR 32,5,8192\nMvaa 16,4,200\nTR 34,5,9216\nMvaa 20,4,204\nTR 36,5,12288\n"
"buffer_store_dwordx4 v[196:199],v0,s[24:27],s30 offen offset:1024 sc1\nMvaa 20,0,196\nWL 13\nTR 38,5,13312\nMvaa 24,8,192\nWL 12\n"
"s_mov_b64 s[20:21],s[24:25]\nTR 40,6,8192\nv_add_u32 v1,s66,v0\nMvaa 24,12,200\ns_mov_b64 s[22:23],s[26:27]\nTR 42,6,9216\nMvaa 28,12,204\n"
"s_mov_b32 s36,0\nTR 44,6,12288\nMvaa 28,8,196\nWL 13\ns_mov_b32 s37,0\nTR 46,6,13312\nMvaa 32,16,192\nWL 12\ns_mov_b32 s38,0\nTR 48,7,8192\n"
"Mvaa 32,20,200\ns_mov_b32 s39,0\nTR 50,7,9216\nMvaa 36,20,204\ns_mov_b32 s40,4096\nTR 52,7,12288\nMvaa 36,16,196\nWL 13\ns_mov_b32 s41,4096\n"
"TR 54,7,13312\nMvaa 40,24,192\nWL 12\ns_mov_b32 s42,4096\nTR 56,8,8192\nMvaa 40,28,200\ns_mov_b32 s43,4096\nTR 58,8,9216\n"
"buffer_store_dwordx4 v[200:203],v0,s[24:27],s30 offen offset:2048 sc1\nMvaa 44,28,204\nTR 60,8,12288\nMvaa 44,24,196\nWL 13\nTR 62,8,13312\n"
"Mvaa 48,32,192\nWL 12\nTR 0,5,16384\nMvaa 48,36,200\nTR 2,5,17408\nMvaa 52,36,204\nTR 4,5,20480\nMvaa 52,32,196\nWL 13\nTR 6,5,21504\nMvaa 56,40,192\n"
"WL 12\nTR 8,6,16384\nMvaa 56,44,200\nTR 10,6,17408\nMvaa 60,44,204\nTR 12,6,20480\nMvaa 60,40,196\nWL 13\nTR 14,6,21504\nMvaa 64,48,192\nWL 12\n"
"TR 16,7,16384\nbuffer_store_dwordx4 v[204:207],v0,s[24:27],s30 offen offset:3072 sc1\nMvaa 64,52,200\ns_add_u32 s30,s30,s28\nTR 18,7,17408\n"
"Mvaa 68,52,204\nTR 20,7,20480\nMvaa 68,48,196\nWL 13\nTR 22,7,21504\nMvaa 72,56,192\nWL 12\nTR 24,8,16384\nMvaa 72,60,200\nTR 26,8,17408\n"
"Mvaa 76,60,204\nTR 28,8,20480\nMvaa 76,56,196\nWL 13\nTR 30,8,21504\nMvaa 80,0,192\nWL 12\nTR 32,5,24576\nTR 34,5,25600\nMvaa 80,4,200\n"
"s_mov_b32 s44,s47\nTR 36,5,28672\nTR 38,5,29696\nMvaa 84,4,204\ns_add_u32 s47,s47,0x8000\nTR 40,6,24576\nMvaa 84,0,196\nWL 14\n"
"s_cmp_eq_u32 s47,0x28000\nTR 42,6,25600\nbuffer_store_dwordx4 v[208:211],v0,s[24:27],s30 offen offset:0 sc1\nMvaa 88,8,192\nWL 14\n"
"s_cselect_b32 s47,0,s47\nTR 44,6,28672\nMvaa 88,12,200\nTR 46,6,29696\nMvaa 92,12,204\nTR 48,7,24576\nMvaa 92,8,196\nWL 14\nTR 50,7,25600\n"
"Mvaa 96,16,192\nWL 14\nTR 52,7,28672\nMvaa 96,20,200\nTR 54,7,29696\nMvaa 100,20,204\nTR 56,8,24576\nMvaa 100,16,196\nWL 14\nTR 58,8,25600\n"
"Mvaa 104,24,192\nWL 14\nTR 60,8,28672\nMvaa 104,28,200\nTR 62,8,29696\nMvaa 108,28,204\ns_waitcnt vmcnt(5)\nv_add_u32 v5,s47,v10\nv_xor_b32 v6,32,v5\n"
"Mvaa 108,24,196\ns_barrier\nv_xor_b32 v7,64,v5\nv_xor_b32 v8,96,v5\nWL 14\nMvaa 112,32,192\nWL 12\nTR 0,5,0\n"
"buffer_store_dwordx4 v[212:215],v0,s[24:27],s30 offen offset:1024 sc1\nMvaa 112,36,200\nTR 2,5,1024\nMvaa 116,36,204\nTR 4,5,4096\nMvaa 116,32,196\n"
"WL 13\nTR 6,5,5120\nMvaa 120,40,192\nWL 12\nTR 8,6,0\nMvaa 120,44,200\nTR 10,6,1024\nMvaa 124,44,204\nTR 12,6,4096\nMvaa 124,40,196\nWL 13\n"
"TR 14,6,5120\nMvaa 128,48,192\nWL 12\nTR 16,7,0\nMvaa 128,52,200\nTR 18,7,1024\nMvaa 132,52,204\nTR 20,7,4096\nMvaa 132,48,196\nWL 13\nTR 22,7,5120\n"
"Mvaa 136,56,192\nWL 12\nTR 24,8,0\nMvaa 136,60,200\nTR 26,8,1024\nMvaa 140,60,204\nTR 28,8,4096\nMvaa 140,56,196\nWL 13\nTR 30,8,5120\nMvaa 16,0,208\n"
"WL 12\nTR 32,5,8192\nMvaa 16,4,216\nTR 34,5,9216\nMvaa 20,4,220\nTR 36,5,12288\nbuffer_store_dwordx4 v[216:219],v0,s[24:27],s30 offen offset:2048 sc1\n"
"Mvaa 20,0,212\nWL 13\nTR 38,5,13312\nMvaa 24,8,208\nWL 12\nTR 40,6,8192\nMvaa 24,12,216\nTR 42,6,9216\nMvaa 28,12,220\nTR 44,6,12288\nMvaa 28,8,212\n"
"WL 13\nTR 46,6,13312\nMvaa 32,16,208\nWL 12\nTR 48,7,8192\nMvaa 32,20,216\nTR 50,7,9216\nMvaa 36,20,220\nTR 52,7,12288\nMvaa 36,16,212\nWL 13\n"
"TR 54,7,13312\nMvaa 40,24,208\nWL 12\nTR 56,8,8192\nMvaa 40,28,216\nTR 58,8,9216\n"
"buffer_store_dwordx4 v[220:223],v0,s[24:27],s30 offen offset:3072 sc1\nMvaa 44,28,220\ns_add_u32 s30,s30,s28\nTR 60,8,12288\nMvaa 44,24,212\nWL 13\n"
"TR 62,8,13312\nMvaa 48,32,208\nWL 12\nTR 0,5,16384\nMvaa 48,36,216\nTR 2,5,17408\nMvaa 52,36,220\nTR 4,5,20480\nMvaa 52,32,212\nWL 13\nTR 6,5,21504\n"
"Mvaa 56,40,208\nWL 12\nTR 8,6,16384\nMvaa 56,44,216\nTR 10,6,17408\nMvaa 60,44,220\nTR 12,6,20480\nMvaa 60,40,212\nWL 13\nTR 14,6,21504\n"
"Mvaa 64,48,208\nWL 12\nTR 16,7,16384\nbuffer_store_dwordx4 v[224:227],v0,s[24:27],s30 offen offset:0 sc1\nMvaa 64,52,216\nTR 18,7,17408\n"
"Mvaa 68,52,220\nTR 20,7,20480\nMvaa 68,48,212\nWL 13\nTR 22,7,21504\nMvaa 72,56,208\nWL 12\nTR 24,8,16384\nMvaa 72,60,216\nTR 26,8,17408\n"
"Mvaa 76,60,220\nTR 28,8,20480\nMvaa 76,56,212\nWL 13\nTR 30,8,21504\nMvaa 80,0,208\nWL 12\nTR 32,5,24576\nTR 34,5,25600\nMvaa 80,4,216\n"
"s_mov_b32 s44,s47\nTR 36,5,28672\nTR 38,5,29696\nMvaa 84,4,220\ns_add_u32 s47,s47,0x8000\nTR 40,6,24576\nMvaa 84,0,212\nWL 14\n"
"s_cmp_eq_u32 s47,0x28000\nTR 42,6,25600\nbuffer_store_dwordx4 v[228:231],v0,s[24:27],s30 offen offset:1024 sc1\nMvaa 88,8,208\nWL 14\n"
"s_cselect_b32 s47,0,s47\nTR 44,6,28672\nMvaa 88,12,216\nTR 46,6,29696\nMvaa 92,12,220\nTR 48,7,24576\nMvaa 92,8,212\nWL 14\nTR 50,7,25600\n"
"Mvaa 96,16,208\nWL 14\nTR 52,7,28672\nMvaa 96,20,216\nTR 54,7,29696\nMvaa 100,20,220\nTR 56,8,24576\nMvaa 100,16,212\nWL 14\nTR 58,8,25600\n"
"Mvaa 104,24,208\nWL 14\nTR 60,8,28672\nMvaa 104,28,216\nTR 62,8,29696\nMvaa 108,28,220\nv_add_u32 v5,s47,v10\nv_xor_b32 v6,32,v5\nMvaa 108,24,212\n"
"WL 14\nv_xor_b32 v7,64,v5\nv_xor_b32 v8,96,v5\nMvaa 112,32,208\nWL 12\nTR 0,5,0\nbuffer_store_dwordx4 v[232:235],v0,s[24:27],s30 offen offset:2048 sc1\n"
"Mvaa 112,36,216\nTR 2,5,1024\nMvaa 116,36,220\nTR 4,5,4096\nMvaa 116,32,212\nWL 13\nTR 6,5,5120\nMvaa 120,40,208\nWL 12\nTR 8,6,0\nMvaa 120,44,216\n"
"TR 10,6,1024\nMvaa 124,44,220\nTR 12,6,4096\nMvaa 124,40,212\nWL 13\nTR 14,6,5120\nMvaa 128,48,208\nWL 12\nTR 16,7,0\nMvaa 128,52,216\nTR 18,7,1024\n"
"Mvaa 132,52,220\nTR 20,7,4096\nMvaa 132,48,212\nWL 13\nTR 22,7,5120\nMvaa 136,56,208\nWL 12\nTR 24,8,0\nMvaa 136,60,216\nTR 26,8,1024\nMvaa 140,60,220\n"
"TR 28,8,4096\nMvaa 140,56,212\nWL 13\nTR 30,8,5120\nMvaa 16,0,224\nWL 12\nTR 32,5,8192\nMvaa 16,4,232\nTR 34,5,9216\nMvaa 20,4,236\nTR 36,5,12288\n"
"buffer_store_dwordx4 v[236:239],v0,s[24:27],s30 offen offset:3072 sc1\nMvaa 20,0,228\nWL 13\ns_add_u32 s30,s30,s28\nTR 38,5,13312\nMvaa 24,8,224\n"
"WL 12\nTR 40,6,8192\nMvaa 24,12,232\ns_mul_i32 s8,s56,8192\nTR 42,6,9216\nMvaa 28,12,236\ns_add_u32 s8,s8,0x10000\nTR 44,6,12288\n"
"v_lshrrev_b32 v15,4,v0\nMvaa 28,8,228\nWL 13\nTR 46,6,13312\nv_min_u32 v15,31,v15\nMvaa 32,16,224\nWL 12\nTR 48,7,8192\nv_lshlrev_b32 v15,7,v15\n"
"Mvaa 32,20,232\nTR 50,7,9216\nMvaa 36,20,236\nTR 52,7,12288\nMvaa 36,16,228\nWL 13\nTR 54,7,13312\nMvaa 40,24,224\nWL 12\nTR 56,8,8192\nMvaa 40,28,232\n"
"TR 58,8,9216\nbuffer_store_dwordx4 v[240:243],v0,s[24:27],s30 offen offset:0 sc1\nMvaa 44,28,236\nTR 60,8,12288\nMvaa 44,24,228\nWL 13\nTR 62,8,13312\n"
"Mvaa 48,32,224\nWL 12\nTR 0,5,16384\nMvaa 48,36,232\nTR 2,5,17408\nMvaa 52,36,236\nTR 4,5,20480\nMvaa 52,32,228\nWL 13\nTR 6,5,21504\nMvaa 56,40,224\n"
"WL 12\nTR 8,6,16384\nMvaa 56,44,232\nTR 10,6,17408\nMvaa 60,44,236\nTR 12,6,20480\nMvaa 60,40,228\nWL 13\nTR 14,6,21504\nMvaa 64,48,224\nWL 12\n"
"TR 16,7,16384\nbuffer_store_dwordx4 v[244:247],v0,s[24:27],s30 offen offset:1024 sc1\nMvaa 64,52,232\nTR 18,7,17408\nMvaa 68,52,236\nTR 20,7,20480\n"
"Mvaa 68,48,228\nWL 13\nTR 22,7,21504\nMvaa 72,56,224\nWL 12\nTR 24,8,16384\nMvaa 72,60,232\nTR 26,8,17408\nMvaa 76,60,236\nTR 28,8,20480\n"
"Mvaa 76,56,228\nWL 13\nTR 30,8,21504\nMvaa 80,0,224\nWL 12\nTR 32,5,24576\nTR 34,5,25600\nMvaa 80,4,232\ns_mov_b32 s44,s47\nTR 36,5,28672\n"
"TR 38,5,29696\nMvaa 84,4,236\ns_add_u32 s47,s47,0x8000\nTR 40,6,24576\nMvaa 84,0,228\nWL 14\ns_cmp_eq_u32 s47,0x28000\nTR 42,6,25600\n"
"buffer_store_dwordx4 v[248:251],v0,s[24:27],s30 offen offset:2048 sc1\nMvaa 88,8,224\nWL 14\ns_cselect_b32 s47,0,s47\nTR 44,6,28672\nMvaa 88,12,232\n"
"TR 46,6,29696\nMvaa 92,12,236\nTR 48,7,24576\nMvaa 92,8,228\nWL 14\nTR 50,7,25600\nMvaa 96,16,224\nWL 14\nTR 52,7,28672\nMvaa 96,20,232\nTR 54,7,29696\n"
"Mvaa 100,20,236\nTR 56,8,24576\nMvaa 100,16,228\nWL 14\nTR 58,8,25600\nMvaa 104,24,224\nWL 14\nTR 60,8,28672\nMvaa 104,28,232\nTR 62,8,29696\n"
"Mvaa 108,28,236\nv_add_u32 v5,s47,v10\nv_xor_b32 v6,32,v5\nMvaa 108,24,228\nWL 14\nv_xor_b32 v7,64,v5\nv_xor_b32 v8,96,v5\nMvaa 112,32,224\nWL 12\n"
"TR 0,5,0\nbuffer_store_dwordx4 v[252:255],v0,s[24:27],s30 offen offset:3072 sc1\nMvaa 112,36,232\ns_add_u32 s30,s30,s28\nTR 2,5,1024\nMvaa 116,36,236\n"
"TR 4,5,4096\nMvaa 116,32,228\nWL 13\nTR 6,5,5120\nMvaa 120,40,224\nWL 12\nTR 8,6,0\nMvaa 120,44,232\nTR 10,6,1024\nMvaa 124,44,236\nTR 12,6,4096\n"
"Mvaa 124,40,228\nWL 13\nTR 14,6,5120\nMvaa 128,48,224\nWL 12\nTR 16,7,0\nMvaa 128,52,232\nTR 18,7,1024\nMvaa 132,52,236\nTR 20,7,4096\nMvaa 132,48,228\n"
"WL 13\nTR 22,7,5120\nMvaa 136,56,224\nWL 12\nTR 24,8,0\nMvaa 136,60,232\nTR 26,8,1024\nMvaa 140,60,236\nTR 28,8,4096\nMvaa 140,56,228\nWL 13\n"
"TR 30,8,5120\nMvaa 16,0,128\nWL 12\nTR 32,5,8192\nMvaa 16,4,136\nTR 34,5,9216\nMvaa 20,4,140\nTR 36,5,12288\nMvaa 20,0,132\nWL 13\nTR 38,5,13312\n"
"Mvaa 24,8,128\nWL 12\nTR 40,6,8192\nMvaa 24,12,136\nTR 42,6,9216\nMvaa 28,12,140\nTR 44,6,12288\nMvaa 28,8,132\nWL 13\nTR 46,6,13312\nMvaa 32,16,128\n"
"WL 12\nTR 48,7,8192\nMvaa 32,20,136\nTR 50,7,9216\nMvaa 36,20,140\nTR 52,7,12288\nMvaa 36,16,132\nWL 13\nTR 54,7,13312\nMvaa 40,24,128\nWL 12\n"
"TR 56,8,8192\nMvaa 40,28,136\nTR 58,8,9216\nMvaa 44,28,140\nTR 60,8,12288\nMvaa 44,24,132\nWL 13\nTR 62,8,13312\nMvaa 48,32,128\nWL 12\nTR 0,5,16384\n"
"Mvaa 48,36,136\nTR 2,5,17408\nMvaa 52,36,140\nTR 4,5,20480\nMvaa 52,32,132\nWL 13\nTR 6,5,21504\nMvaa 56,40,128\nWL 12\nTR 8,6,16384\nMvaa 56,44,136\n"
"TR 10,6,17408\nMvaa 60,44,140\nTR 12,6,20480\nMvaa 60,40,132\nWL 13\nTR 14,6,21504\nMvaa 64,48,128\nWL 12\nTR 16,7,16384\nMvaa 64,52,136\n"
"TR 18,7,17408\nMvaa 68,52,140\nTR 20,7,20480\nMvaa 68,48,132\nWL 13\nTR 22,7,21504\nMvaa 72,56,128\nWL 12\nTR 24,8,16384\nMvaa 72,60,136\n"
"TR 26,8,17408\nMvaa 76,60,140\nTR 28,8,20480\nMvaa 76,56,132\nWL 13\nTR 30,8,21504\nMvaa 80,0,128\nWL 12\nTR 32,5,24576\nTR 34,5,25600\nMvaa 80,4,136\n"
"TR 36,5,28672\nTR 38,5,29696\nMvaa 84,4,140\nTR 40,6,24576\nMvaa 84,0,132\nWL 14\nTR 42,6,25600\nMvaa 88,8,128\ns_waitcnt vmcnt(0)\nTR 44,6,28672\n"
"v_mov_b32 v13,s65\ns_mov_b64 exec,1\nbuffer_store_dword v13,off,s[24:27],s7 sc1\ns_mov_b64 exec,-1\nWL 15\nMvaa 88,12,136\nTR 46,6,29696\n"
"Mvaa 92,12,140\nTR 48,7,24576\nMvaa 92,8,132\nWL 14\nTR 50,7,25600\nMvaa 96,16,128\nWL 14\nTR 52,7,28672\nMvaa 96,20,136\nTR 54,7,29696\n"
"Mvaa 100,20,140\nTR 56,8,24576\nMvaa 100,16,132\nWL 14\nTR 58,8,25600\nMvaa 104,24,128\nWL 14\nTR 60,8,28672\nMvaa 104,28,136\nTR 62,8,29696\n"
"Mvaa 108,28,140\nMvaa 108,24,132\nWL 14\nMvaa 112,32,128\nWL 12\nMvaa 112,36,136\nMvaa 116,36,140\nMvaa 116,32,132\nWL 10\nMvaa 120,40,128\nWL 8\n"
"Mvaa 120,44,136\nMvaa 124,44,140\nMvaa 124,40,132\nWL 6\nMvaa 128,48,128\nWL 4\nMvaa 128,52,136\nMvaa 132,52,140\nMvaa 132,48,132\nWL 2\n"
"Mvaa 136,56,128\nWL 0\nMvaa 136,60,136\nMvaa 140,60,140\nMvaa 140,56,132\nv_mov_b32 v12,0x3db8aa3b\nv_mov_b32 v13,v12\ns_nop 7\n"
"v_pk_mul_f32 v[16:17],v[16:17],v[12:13]\nv_pk_mul_f32 v[32:33],v[32:33],v[12:13]\nv_pk_mul_f32 v[18:19],v[18:19],v[12:13]\n"
"v_pk_mul_f32 v[34:35],v[34:35],v[12:13]\nv_pk_mul_f32 v[24:25],v[24:25],v[12:13]\nv_pk_mul_f32 v[40:41],v[40:41],v[12:13]\n"
"v_pk_mul_f32 v[26:27],v[26:27],v[12:13]\nv_pk_mul_f32 v[42:43],v[42:43],v[12:13]\nCV 16,16,17\nCV 32,32,33\nCV 17,18,19\nCV 33,34,35\nCV 18,24,25\n"
"CV 34,40,41\nCV 19,26,27\nCV 35,42,43\nv_accvgpr_write_b32 a128,v16\nv_accvgpr_write_b32 a132,v32\nv_accvgpr_write_b32 a129,v17\n"
"v_accvgpr_write_b32 a133,v33\nv_accvgpr_write_b32 a130,v18\nv_accvgpr_write_b32 a134,v34\nv_accvgpr_write_b32 a131,v19\nv_accvgpr_write_b32 a135,v35\n"
"v_pk_mul_f32 v[48:49],v[48:49],v[12:13]\nv_pk_mul_f32 v[64:65],v[64:65],v[12:13]\nv_pk_mul_f32 v[50:51],v[50:51],v[12:13]\n"
"v_pk_mul_f32 v[66:67],v[66:67],v[12:13]\nv_pk_mul_f32 v[56:57],v[56:57],v[12:13]\nv_pk_mul_f32 v[72:73],v[72:73],v[12:13]\n"
"v_pk_mul_f32 v[58:59],v[58:59],v[12:13]\nv_pk_mul_f32 v[74:75],v[74:75],v[12:13]\nCV 48,48,49\nCV 64,64,65\nCV 49,50,51\nCV 65,66,67\nCV 50,56,57\n"
"CV 66,72,73\nCV 51,58,59\nCV 67,74,75\nv_accvgpr_write_b32 a136,v48\nv_accvgpr_write_b32 a140,v64\nv_accvgpr_write_b32 a137,v49\n"
"v_accvgpr_write_b32 a141,v65\nv_accvgpr_write_b32 a138,v50\nv_accvgpr_write_b32 a142,v66\nv_accvgpr_write_b32 a139,v51\nv_accvgpr_write_b32 a143,v67\n"
"buffer_load_dword v14,v15,s[24:27],s8 offen sc1\nv_pk_mul_f32 v[80:81],v[80:81],v[12:13]\nv_pk_mul_f32 v[96:97],v[96:97],v[12:13]\n"
"v_pk_mul_f32 v[82:83],v[82:83],v[12:13]\nv_pk_mul_f32 v[98:99],v[98:99],v[12:13]\nv_pk_mul_f32 v[88:89],v[88:89],v[12:13]\n"
"v_pk_mul_f32 v[104:105],v[104:105],v[12:13]\nv_pk_mul_f32 v[90:91],v[90:91],v[12:13]\nv_pk_mul_f32 v[106:107],v[106:107],v[12:13]\nCV 80,80,81\n"
"CV 96,96,97\nCV 81,82,83\nCV 97,98,99\nCV 82,88,89\nCV 98,104,105\nCV 83,90,91\nCV 99,106,107\nv_accvgpr_write_b32 a144,v80\n"
"v_accvgpr_write_b32 a148,v96\nv_accvgpr_write_b32 a145,v81\nv_accvgpr_write_b32 a149,v97\nv_accvgpr_write_b32 a146,v82\nv_accvgpr_write_b32 a150,v98\n"
"v_accvgpr_write_b32 a147,v83\nv_accvgpr_write_b32 a151,v99\nv_pk_mul_f32 v[112:113],v[112:113],v[12:13]\nv_pk_mul_f32 v[128:129],v[128:129],v[12:13]\n"
"v_pk_mul_f32 v[114:115],v[114:115],v[12:13]\nv_pk_mul_f32 v[130:131],v[130:131],v[12:13]\nv_pk_mul_f32 v[120:121],v[120:121],v[12:13]\n"
"v_pk_mul_f32 v[136:137],v[136:137],v[12:13]\nv_pk_mul_f32 v[122:123],v[122:123],v[12:13]\nv_pk_mul_f32 v[138:139],v[138:139],v[12:13]\nCV 112,112,113\n"
"CV 128,128,129\nCV 113,114,115\nCV 129,130,131\nCV 114,120,121\nCV 130,136,137\nCV 115,122,123\nCV 131,138,139\nv_accvgpr_write_b32 a152,v112\n"
"v_accvgpr_write_b32 a156,v128\nv_accvgpr_write_b32 a153,v113\nv_accvgpr_write_b32 a157,v129\nv_accvgpr_write_b32 a154,v114\n"
"v_accvgpr_write_b32 a158,v130\nv_accvgpr_write_b32 a155,v115\nv_accvgpr_write_b32 a159,v131\ns_waitcnt vmcnt(0)\nv_cmp_ne_u32 vcc,s65,v14\ns_nop 0\n"
"s_cbranch_vccnz fused_op_sp10\nfused_op_spb11:\ns_barrier\ns_add_u32 m0,s48,131072\ns_nop 0\nDS 1,20,36,0\nDS 1,20,37,1024\nDS 1,20,38,2048\n"
"DS 1,20,39,3072\ns_add_u32 m0,m0,0x1000\ns_nop 0\nDS 1,20,40,0\nDS 1,20,41,1024\nDS 1,20,42,2048\nDS 1,20,43,3072\nv_add_u32 v1,0x10000,v1\n"
"v_pk_mul_f32 v[20:21],v[20:21],v[12:13]\nv_pk_mul_f32 v[36:37],v[36:37],v[12:13]\nv_pk_mul_f32 v[22:23],v[22:23],v[12:13]\n"
"v_pk_mul_f32 v[38:39],v[38:39],v[12:13]\nv_pk_mul_f32 v[28:29],v[28:29],v[12:13]\nv_pk_mul_f32 v[44:45],v[44:45],v[12:13]\n"
"v_pk_mul_f32 v[30:31],v[30:31],v[12:13]\nv_pk_mul_f32 v[46:47],v[46:47],v[12:13]\nCV 20,20,21\nCV 36,36,37\nCV 21,22,23\nCV 37,38,39\nCV 22,28,29\n"
"CV 38,44,45\nCV 23,30,31\nCV 39,46,47\nv_accvgpr_write_b32 a160,v20\nv_accvgpr_write_b32 a164,v36\nv_accvgpr_write_b32 a161,v21\n"
"v_accvgpr_write_b32 a165,v37\nv_accvgpr_write_b32 a162,v22\nv_accvgpr_write_b32 a166,v38\nv_accvgpr_write_b32 a163,v23\nv_accvgpr_write_b32 a167,v39\n"
"v_pk_mul_f32 v[52:53],v[52:53],v[12:13]\nv_pk_mul_f32 v[68:69],v[68:69],v[12:13]\nv_pk_mul_f32 v[54:55],v[54:55],v[12:13]\n"
"v_pk_mul_f32 v[70:71],v[70:71],v[12:13]\nv_pk_mul_f32 v[60:61],v[60:61],v[12:13]\nv_pk_mul_f32 v[76:77],v[76:77],v[12:13]\n"
"v_pk_mul_f32 v[62:63],v[62:63],v[12:13]\nv_pk_mul_f32 v[78:79],v[78:79],v[12:13]\nCV 52,52,53\nCV 68,68,69\nCV 53,54,55\nCV 69,70,71\nCV 54,60,61\n"
"CV 70,76,77\nCV 55,62,63\nCV 71,78,79\nv_accvgpr_write_b32 a168,v52\nv_accvgpr_write_b32 a172,v68\nv_accvgpr_write_b32 a169,v53\n"
"v_accvgpr_write_b32 a173,v69\nv_accvgpr_write_b32 a170,v54\nv_accvgpr_write_b32 a174,v70\nv_accvgpr_write_b32 a171,v55\nv_accvgpr_write_b32 a175,v71\n"
"s_add_u32 m0,s48,0\ns_nop 0\nDS 1,20,36,0\nDS 1,20,37,1024\nDS 1,20,38,2048\nDS 1,20,39,3072\ns_add_u32 m0,m0,0x1000\ns_nop 0\nDS 1,20,40,0\n"
"DS 1,20,41,1024\nDS 1,20,42,2048\nDS 1,20,43,3072\nv_add_u32 v1,0xffff8000,v1\nv_pk_mul_f32 v[84:85],v[84:85],v[12:13]\n"
"v_pk_mul_f32 v[100:101],v[100:101],v[12:13]\nv_pk_mul_f32 v[86:87],v[86:87],v[12:13]\nv_pk_mul_f32 v[102:103],v[102:103],v[12:13]\n"
"v_pk_mul_f32 v[92:93],v[92:93],v[12:13]\nv_pk_mul_f32 v[108:109],v[108:109],v[12:13]\nv_pk_mul_f32 v[94:95],v[94:95],v[12:13]\n"
"v_pk_mul_f32 v[110:111],v[110:111],v[12:13]\nCV 84,84,85\nCV 100,100,101\nCV 85,86,87\nCV 101,102,103\nCV 86,92,93\nCV 102,108,109\nCV 87,94,95\n"
"CV 103,110,111\nv_accvgpr_write_b32 a176,v84\nv_accvgpr_write_b32 a180,v100\nv_accvgpr_write_b32 a177,v85\nv_accvgpr_write_b32 a181,v101\n"
"v_accvgpr_write_b32 a178,v86\nv_accvgpr_write_b32 a182,v102\nv_accvgpr_write_b32 a179,v87\nv_accvgpr_write_b32 a183,v103\n"
"v_pk_mul_f32 v[116:117],v[116:117],v[12:13]\nv_pk_mul_f32 v[132:133],v[132:133],v[12:13]\nv_pk_mul_f32 v[118:119],v[118:119],v[12:13]\n"
"v_pk_mul_f32 v[134:135],v[134:135],v[12:13]\nv_pk_mul_f32 v[124:125],v[124:125],v[12:13]\nv_pk_mul_f32 v[140:141],v[140:141],v[12:13]\n"
"v_pk_mul_f32 v[126:127],v[126:127],v[12:13]\nv_pk_mul_f32 v[142:143],v[142:143],v[12:13]\nCV 116,116,117\nCV 132,132,133\nCV 117,118,119\n"
"CV 133,134,135\nCV 118,124,125\nCV 134,140,141\nCV 119,126,127\nCV 135,142,143\nv_accvgpr_write_b32 a184,v116\nv_accvgpr_write_b32 a188,v132\n"
"v_accvgpr_write_b32 a185,v117\nv_accvgpr_write_b32 a189,v133\nv_accvgpr_write_b32 a186,v118\nv_accvgpr_write_b32 a190,v134\n"
"v_accvgpr_write_b32 a187,v119\nv_accvgpr_write_b32 a191,v135\ns_mov_b32 s44,s47\ns_add_u32 s47,s47,0x8000\ns_cmp_eq_u32 s47,0x28000\n"
"s_cselect_b32 s47,0,s47\nv_add_u32 v3,s47,v0\ns_waitcnt vmcnt(8)\ns_barrier\nRD 0,3,0\nRD 4,3,1024\nRD 8,3,2048\nRD 12,3,3072\nRD 16,3,4096\n"
"RD 20,3,5120\nRD 24,3,6144\nRD 28,3,7168\ns_mul_i32 s8,s68,131072\ns_mul_i32 s49,s10,32768\ns_add_u32 s8,s8,s49\ns_add_u32 s8,s8,0x2100000\n"
"s_mul_i32 s9,s68,512\ns_mul_i32 s49,s10,128\ns_add_u32 s9,s9,s49\ns_add_u32 s9,s9,0x30000\ns_add_u32 s50,s54,fused_op_tw-fused_op_pc\n"
"s_addc_u32 s51,s55,0\ns_cmp_eq_u32 s60,0\ns_cbranch_scc1 fused_op_first0b\ns_add_u32 s50,s54,fused_op_tk-fused_op_pc\ns_addc_u32 s51,s55,0\n"
"s_cmp_eq_u32 s61,2\ns_cbranch_scc1 fused_op_first2\ns_branch fused_op_first0b\nfused_op_enter:\ns_bitcmp1_b32 s61,0\ns_cbranch_scc1 fused_op_first1\n"
"fused_op_first0:\nv_lshrrev_b32 v238,4,v0\nv_lshrrev_b32 v239,4,v238\nv_and_b32 v238,15,v238\nv_lshlrev_b32 v239,2,v239\nv_sub_u32 v238,v238,v239\n"
"s_lshl_b32 s45,s10,5\nv_add_u32 v238,s45,v238\nv_cvt_f32_i32 v238,v238\nv_mul_f32 v246,0x71800000,v238\nWL 0\nv_mov_b32 v93,0\nv_mov_b32 v94,0\n"
"v_mov_b32 v95,0\nv_mov_b32 v96,0\nv_mov_b32 v97,0\nv_mov_b32 v98,0\nv_mov_b32 v99,0\nv_mov_b32 v100,0\nv_mov_b32 v101,0\nv_mov_b32 v102,0\n"
"v_mov_b32 v103,0\nv_mov_b32 v104,0\nv_mov_b32 v105,0\nv_mov_b32 v106,0\nv_mov_b32 v107,0\nv_mov_b32 v108,0\nv_mov_b32 v109,0\nv_mov_b32 v110,0\n"
"v_mov_b32 v111,0\nv_mov_b32 v112,0\nv_mov_b32 v113,0\nv_mov_b32 v114,0\nv_mov_b32 v115,0\nv_mov_b32 v116,0\nv_mov_b32 v117,0\nv_mov_b32 v118,0\n"
"v_mov_b32 v119,0\nv_mov_b32 v120,0\nv_mov_b32 v121,0\nv_mov_b32 v122,0\nv_mov_b32 v123,0\nv_mov_b32 v124,0\nv_mov_b32 v125,0\nv_mov_b32 v126,0\n"
"v_mov_b32 v127,0\nv_mov_b32 v128,0\nv_mov_b32 v129,0\nv_mov_b32 v130,0\nv_mov_b32 v131,0\nv_mov_b32 v132,0\nv_mov_b32 v133,0\nv_mov_b32 v134,0\n"
"v_mov_b32 v135,0\nv_mov_b32 v136,0\nv_mov_b32 v137,0\nv_mov_b32 v138,0\nv_mov_b32 v139,0\nv_mov_b32 v140,0\nv_mov_b32 v141,0\nv_mov_b32 v142,0\n"
"v_mov_b32 v143,0\nv_mov_b32 v234,0\nv_mov_b32 v235,0\nZvaa 144,0,128\nRD 32,3,8192\nv_mov_b32 v16,0\nMvaa 144,4,132\nRD 36,3,9216\nv_mov_b32 v17,0\n"
"Mvaa 144,8,136\nRD 40,3,10240\nv_mov_b32 v18,0\nMvaa 144,12,140\nRD 44,3,11264\nv_mov_b32 v19,0\nMvaa 144,16,144\nRD 48,3,12288\nv_mov_b32 v20,0\n"
"Mvaa 144,20,148\nRD 52,3,13312\nv_mov_b32 v21,0\nMvaa 144,24,152\nRD 56,3,14336\nv_mov_b32 v22,0\nMvaa 144,28,156\ns_add_u32 m0,s48,s44\nRD 60,3,15360\n"
"v_mov_b32 v23,0\nZvaa 148,28,188\nv_mov_b32 v24,0\nv_mov_b32 v25,0\nMvaa 148,24,184\nDS 1,20,36,0\nMvaa 148,20,180\nv_mov_b32 v26,0\nv_mov_b32 v27,0\n"
"Mvaa 148,16,176\nv_mov_b32 v28,0\nv_mov_b32 v29,0\nMvaa 148,12,172\nDS 1,20,37,1024\nMvaa 148,8,168\nv_mov_b32 v30,0\nv_mov_b32 v31,0\nMvaa 148,4,164\n"
"v_mov_b32 v32,0\nv_mov_b32 v33,0\nMvaa 148,0,160\nWL 0\nDS 1,20,38,2048\nZvaa 152,32,128\nRD 0,3,16384\nv_mov_b32 v34,0\nMvaa 152,36,132\nRD 4,3,17408\n"
"v_mov_b32 v35,0\nMvaa 152,40,136\nRD 8,3,18432\nv_mov_b32 v36,0\nMvaa 152,44,140\nRD 12,3,19456\nv_mov_b32 v37,0\nMvaa 152,48,144\nRD 16,3,20480\n"
"v_mov_b32 v38,0\nMvaa 152,52,148\nRD 20,3,21504\nv_mov_b32 v39,0\nMvaa 152,56,152\nRD 24,3,22528\nv_mov_b32 v40,0\nMvaa 152,60,156\nRD 28,3,23552\n"
"v_mov_b32 v41,0\nZvaa 156,60,188\nv_mov_b32 v42,0\nv_mov_b32 v43,0\nMvaa 156,56,184\nDS 1,20,39,3072\nMvaa 156,52,180\ns_add_u32 m0,m0,0x1000\n"
"v_mov_b32 v44,0\nv_mov_b32 v45,0\nMvaa 156,48,176\nv_mov_b32 v46,0\nv_mov_b32 v47,0\nMvaa 156,44,172\nDS 1,20,40,0\nMvaa 156,40,168\nv_mov_b32 v48,0\n"
"v_mov_b32 v49,0\nMvaa 156,36,164\nv_mov_b32 v50,0\nv_mov_b32 v51,0\nMvaa 156,32,160\nWL 0\nDS 1,20,41,1024\nZvaa 160,0,128\nRD 32,3,24576\n"
"v_mov_b32 v52,0\nMvaa 160,4,132\ns_mov_b32 s44,s47\nRD 36,3,25600\nv_mov_b32 v53,0\nMvaa 160,8,136\ns_add_u32 s47,s47,0x8000\nRD 40,3,26624\n"
"v_mov_b32 v54,0\nMvaa 160,12,140\ns_cmp_eq_u32 s47,0x28000\nRD 44,3,27648\nv_mov_b32 v55,0\nMvaa 160,16,144\ns_cselect_b32 s47,0,s47\nRD 48,3,28672\n"
"v_mov_b32 v56,0\nMvaa 160,20,148\nRD 52,3,29696\nv_mov_b32 v57,0\nMvaa 160,24,152\nRD 56,3,30720\nv_mov_b32 v58,0\nMvaa 160,28,156\nRD 60,3,31744\n"
"v_mov_b32 v59,0\nZvaa 164,28,188\nv_mov_b32 v60,0\nv_mov_b32 v61,0\nMvaa 164,24,184\nDS 1,20,42,2048\nMvaa 164,20,180\nv_mov_b32 v62,0\n"
"v_mov_b32 v63,0\nMvaa 164,16,176\nv_mov_b32 v64,0\nv_mov_b32 v65,0\nMvaa 164,12,172\nDS 1,20,43,3072\nMvaa 164,8,168\nv_add_u32 v1,0x18000,v1\n"
"v_mov_b32 v66,0\nMvaa 164,4,164\nv_add_u32 v3,s47,v0\nv_mov_b32 v67,0\nMvaa 164,0,160\ns_waitcnt vmcnt(24) lgkmcnt(0)\ns_barrier\nv_mov_b32 v68,0\n"
"Zvaa 168,32,128\nRD 0,3,0\nv_mov_b32 v69,0\nMvaa 168,36,132\nRD 4,3,1024\nv_mov_b32 v70,0\nMvaa 168,40,136\nRD 8,3,2048\nv_mov_b32 v71,0\n"
"Mvaa 168,44,140\nRD 12,3,3072\nv_mov_b32 v72,0\nMvaa 168,48,144\nRD 16,3,4096\nv_mov_b32 v73,0\nMvaa 168,52,148\nRD 20,3,5120\nv_mov_b32 v74,0\n"
"Mvaa 168,56,152\nRD 24,3,6144\nv_mov_b32 v75,0\nMvaa 168,60,156\nRD 28,3,7168\nv_mov_b32 v76,0\nZvaa 172,60,188\nv_mov_b32 v77,0\nv_mov_b32 v78,0\n"
"Mvaa 172,56,184\nv_mov_b32 v79,0\nv_mov_b32 v80,0\nMvaa 172,52,180\nv_mov_b32 v81,0\nv_mov_b32 v82,0\nMvaa 172,48,176\nv_mov_b32 v83,0\n"
"v_mov_b32 v84,0\nMvaa 172,44,172\nv_mov_b32 v85,0\nv_mov_b32 v86,0\nMvaa 172,40,168\nv_mov_b32 v87,0\nv_mov_b32 v88,0\nMvaa 172,36,164\nWL 0\n"
"v_mov_b32 v89,0\nv_mov_b32 v90,0\nMvaa 172,32,160\nv_mov_b32 v91,0\nv_mov_b32 v92,0\ns_waitcnt vmcnt(8)\nv_mov_b32 v13,s65\ns_mov_b64 exec,1\n"
"buffer_store_dword v13,off,s[24:27],s5 sc1\ns_mov_b64 exec,-1\ns_nop 3\nv_max3_f32 v238,v144,v145,v146\nv_max3_f32 v240,v148,v149,v150\n"
"v_max3_f32 v238,v238,v147,v152\nv_max3_f32 v240,v240,v151,v156\nv_max3_f32 v238,v238,v153,v154\nv_max3_f32 v240,v240,v157,v158\n"
"v_max3_f32 v238,v238,v155,v160\nv_max3_f32 v240,v240,v159,v164\nv_max3_f32 v238,v238,v161,v162\nv_max3_f32 v240,v240,v165,v166\n"
"v_max3_f32 v238,v238,v163,v168\nv_max3_f32 v240,v240,v167,v172\nv_max3_f32 v238,v238,v169,v170\nv_max3_f32 v240,v240,v173,v174\n"
"v_max_f32 v238,v238,v171\nv_max_f32 v240,v240,v175\nv_mov_b32 v239,v238\nv_mov_b32 v241,v240\ns_nop 0\nv_permlane32_swap_b32 v238,v239\n"
"v_permlane32_swap_b32 v240,v241\nv_max_f32 v238,v238,v239\nv_max_f32 v240,v240,v241\nv_mov_b32 v239,v238\nv_mov_b32 v241,v240\ns_nop 0\n"
"v_permlane16_swap_b32 v238,v239\nv_permlane16_swap_b32 v240,v241\nv_max_f32 v238,v238,v239\nv_max_f32 v240,v240,v241\nv_mov_b32 v232,v238\n"
"v_mov_b32 v233,v240\nv_sub_f32 v224,0,v232\nv_sub_f32 v228,0,v233\nv_sub_f32 v225,0,v232\nv_sub_f32 v229,0,v233\nv_sub_f32 v226,0,v232\n"
"v_sub_f32 v230,0,v233\nv_sub_f32 v227,0,v232\nv_sub_f32 v231,0,v233\nv_sub_f32 v144,v144,v238\nv_sub_f32 v148,v148,v240\nv_sub_f32 v145,v145,v238\n"
"v_sub_f32 v149,v149,v240\nv_sub_f32 v146,v146,v238\nv_sub_f32 v150,v150,v240\nv_sub_f32 v147,v147,v238\nv_sub_f32 v151,v151,v240\n"
"v_sub_f32 v152,v152,v238\nv_sub_f32 v156,v156,v240\nv_sub_f32 v153,v153,v238\nv_sub_f32 v157,v157,v240\nv_sub_f32 v154,v154,v238\n"
"v_sub_f32 v158,v158,v240\nv_sub_f32 v155,v155,v238\nv_sub_f32 v159,v159,v240\nv_sub_f32 v160,v160,v238\nv_sub_f32 v164,v164,v240\n"
"v_sub_f32 v161,v161,v238\nv_sub_f32 v165,v165,v240\nv_sub_f32 v162,v162,v238\nv_sub_f32 v166,v166,v240\nv_sub_f32 v163,v163,v238\n"
"v_sub_f32 v167,v167,v240\nv_sub_f32 v168,v168,v238\nv_sub_f32 v172,v172,v240\nv_sub_f32 v169,v169,v238\nv_sub_f32 v173,v173,v240\n"
"v_sub_f32 v170,v170,v238\nv_sub_f32 v174,v174,v240\nv_sub_f32 v171,v171,v238\nv_sub_f32 v175,v175,v240\nv_exp_f32 v144,v144\nv_exp_f32 v145,v145\n"
"v_add_f32 v234,v144,v234\nv_exp_f32 v146,v146\nv_add_f32 v234,v145,v234\nv_exp_f32 v147,v147\nv_add_f32 v234,v146,v234\nv_exp_f32 v148,v148\n"
"v_add_f32 v234,v147,v234\nv_exp_f32 v149,v149\nv_add_f32 v235,v148,v235\nv_exp_f32 v150,v150\nv_add_f32 v235,v149,v235\nv_exp_f32 v151,v151\n"
"v_add_f32 v235,v150,v235\nv_exp_f32 v152,v152\nv_add_f32 v235,v151,v235\nv_exp_f32 v153,v153\nv_exp_f32 v154,v154\nv_exp_f32 v155,v155\n"
"v_exp_f32 v156,v156\nv_exp_f32 v157,v157\nv_exp_f32 v158,v158\nv_exp_f32 v159,v159\ns_sub_u32 s62,s61,3\ns_branch fused_op_h1\nfused_op_first2:\n"
"v_lshrrev_b32 v238,4,v0\nv_lshrrev_b32 v239,4,v238\nv_and_b32 v238,15,v238\nv_lshlrev_b32 v239,2,v239\nv_sub_u32 v238,v238,v239\ns_lshl_b32 s45,s10,5\n"
"v_add_u32 v238,s45,v238\nv_cvt_f32_i32 v238,v238\nv_mul_f32 v246,0x71800000,v238\nWL 0\ns_mov_b32 s19,s44\nv_mov_b32 v64,0\nv_mov_b32 v65,0\n"
"v_mov_b32 v66,0\nv_mov_b32 v67,0\nv_mov_b32 v68,0\nv_mov_b32 v69,0\nv_mov_b32 v70,0\nv_mov_b32 v71,0\nv_mov_b32 v72,0\nv_mov_b32 v73,0\n"
"v_mov_b32 v74,0\nv_mov_b32 v75,0\nv_mov_b32 v76,0\nv_mov_b32 v77,0\nv_mov_b32 v78,0\nv_mov_b32 v79,0\nv_mov_b32 v80,0\nv_mov_b32 v81,0\n"
"v_mov_b32 v82,0\nv_mov_b32 v83,0\nv_mov_b32 v84,0\nv_mov_b32 v85,0\nv_mov_b32 v86,0\nv_mov_b32 v87,0\nv_mov_b32 v88,0\nv_mov_b32 v89,0\n"
"v_mov_b32 v90,0\nv_mov_b32 v91,0\nv_mov_b32 v92,0\nv_mov_b32 v93,0\nv_mov_b32 v94,0\nv_mov_b32 v95,0\nv_mov_b32 v96,0\nv_mov_b32 v97,0\n"
"v_mov_b32 v98,0\nv_mov_b32 v99,0\nv_mov_b32 v100,0\nv_mov_b32 v101,0\nv_mov_b32 v102,0\nv_mov_b32 v103,0\nv_mov_b32 v104,0\nv_mov_b32 v105,0\n"
"v_mov_b32 v106,0\nv_mov_b32 v107,0\nv_mov_b32 v108,0\nv_mov_b32 v109,0\nv_mov_b32 v110,0\nv_mov_b32 v111,0\nv_mov_b32 v112,0\nv_mov_b32 v113,0\n"
"v_mov_b32 v114,0\nv_mov_b32 v115,0\nv_mov_b32 v116,0\nv_mov_b32 v117,0\nv_mov_b32 v118,0\nv_mov_b32 v119,0\nv_mov_b32 v120,0\nv_mov_b32 v121,0\n"
"v_mov_b32 v122,0\nv_mov_b32 v123,0\nv_mov_b32 v124,0\nv_mov_b32 v125,0\nv_mov_b32 v126,0\nv_mov_b32 v127,0\nv_mov_b32 v128,0\nv_mov_b32 v129,0\n"
"v_mov_b32 v130,0\nv_mov_b32 v131,0\nv_mov_b32 v132,0\nv_mov_b32 v133,0\nv_mov_b32 v134,0\nv_mov_b32 v135,0\nv_mov_b32 v136,0\nv_mov_b32 v137,0\n"
"v_mov_b32 v138,0\nv_mov_b32 v139,0\nv_mov_b32 v140,0\nv_mov_b32 v141,0\nv_mov_b32 v142,0\nv_mov_b32 v143,0\nv_mov_b32 v234,0\nv_mov_b32 v235,0\n"
"Zvaa 144,0,128\nRD 32,3,8192\nv_mov_b32 v16,0\nMvaa 144,4,132\ns_add_u32 m0,s48,32768\nRD 36,3,9216\nDS 1,20,36,0\nMvaa 144,8,136\nRD 40,3,10240\n"
"v_mov_b32 v17,0\nMvaa 144,12,140\nRD 44,3,11264\nDS 1,20,37,1024\nMvaa 144,16,144\nRD 48,3,12288\nv_mov_b32 v18,0\nMvaa 144,20,148\nRD 52,3,13312\n"
"v_mov_b32 v19,0\nMvaa 144,24,152\nRD 56,3,14336\nv_mov_b32 v20,0\nMvaa 144,28,156\nRD 60,3,15360\nv_mov_b32 v21,0\nZvaa 148,28,188\nDS 1,20,38,2048\n"
"Mvaa 148,24,184\nDS 1,20,39,3072\nMvaa 148,20,180\ns_add_u32 m0,m0,0x1000\ns_nop 0\nDS 1,20,40,0\nMvaa 148,16,176\nDS 1,20,41,1024\nMvaa 148,12,172\n"
"DS 1,20,42,2048\nMvaa 148,8,168\nDS 1,20,43,3072\nv_add_u32 v1,s95,v1\nMvaa 148,4,164\ns_add_u32 m0,s48,65536\ns_nop 0\nDS 1,20,36,0\nMvaa 148,0,160\n"
"WL 0\nDS 1,20,37,1024\nZvaa 152,32,128\nRD 0,3,16384\nv_mov_b32 v22,0\nMvaa 152,36,132\nRD 4,3,17408\nv_mov_b32 v23,0\nMvaa 152,40,136\nRD 8,3,18432\n"
"v_mov_b32 v24,0\nMvaa 152,44,140\nRD 12,3,19456\nv_mov_b32 v25,0\nMvaa 152,48,144\nRD 16,3,20480\nv_mov_b32 v26,0\nMvaa 152,52,148\nRD 20,3,21504\n"
"v_mov_b32 v27,0\nMvaa 152,56,152\nRD 24,3,22528\nv_mov_b32 v28,0\nMvaa 152,60,156\nRD 28,3,23552\nv_mov_b32 v29,0\nZvaa 156,60,188\nDS 1,20,38,2048\n"
"Mvaa 156,56,184\nDS 1,20,39,3072\nMvaa 156,52,180\ns_add_u32 m0,m0,0x1000\ns_nop 0\nDS 1,20,40,0\nMvaa 156,48,176\nDS 1,20,41,1024\nMvaa 156,44,172\n"
"DS 1,20,42,2048\nMvaa 156,40,168\nDS 1,20,43,3072\nv_add_u32 v1,s6,v0\nMvaa 156,36,164\ns_add_u32 m0,s48,s19\ns_nop 0\nDS 1,20,36,0\nMvaa 156,32,160\n"
"WL 0\nDS 1,20,37,1024\nZvaa 160,0,128\nRD 32,3,24576\nv_mov_b32 v30,0\nMvaa 160,4,132\ns_mov_b32 s44,s47\nRD 36,3,25600\nv_mov_b32 v31,0\n"
"Mvaa 160,8,136\ns_add_u32 s47,s47,0x8000\nRD 40,3,26624\nv_mov_b32 v32,0\nMvaa 160,12,140\ns_cmp_eq_u32 s47,0x28000\nRD 44,3,27648\nv_mov_b32 v33,0\n"
"Mvaa 160,16,144\ns_cselect_b32 s47,0,s47\nRD 48,3,28672\nv_mov_b32 v34,0\nMvaa 160,20,148\nRD 52,3,29696\nv_mov_b32 v35,0\nMvaa 160,24,152\n"
"RD 56,3,30720\nv_mov_b32 v36,0\nMvaa 160,28,156\nRD 60,3,31744\nv_mov_b32 v37,0\nZvaa 164,28,188\nDS 1,20,38,2048\nMvaa 164,24,184\nDS 1,20,39,3072\n"
"Mvaa 164,20,180\ns_add_u32 m0,m0,0x1000\ns_nop 0\nDS 1,20,40,0\nMvaa 164,16,176\nDS 1,20,41,1024\nMvaa 164,12,172\nDS 1,20,42,2048\nMvaa 164,8,168\n"
"DS 1,20,43,3072\nv_add_u32 v1,0x10000,v1\nMvaa 164,4,164\nv_add_u32 v3,s47,v0\nv_mov_b32 v38,0\nMvaa 164,0,160\ns_waitcnt vmcnt(24) lgkmcnt(0)\n"
"s_barrier\nv_mov_b32 v39,0\nZvaa 168,32,128\nRD 0,3,0\nv_mov_b32 v40,0\nMvaa 168,36,132\nRD 4,3,1024\nv_mov_b32 v41,0\nMvaa 168,40,136\nRD 8,3,2048\n"
"v_mov_b32 v42,0\nMvaa 168,44,140\nRD 12,3,3072\nv_mov_b32 v43,0\nMvaa 168,48,144\nRD 16,3,4096\nv_mov_b32 v44,0\nMvaa 168,52,148\nRD 20,3,5120\n"
"v_mov_b32 v45,0\nMvaa 168,56,152\nRD 24,3,6144\nv_mov_b32 v46,0\nMvaa 168,60,156\nRD 28,3,7168\nv_mov_b32 v47,0\nZvaa 172,60,188\nv_mov_b32 v48,0\n"
"v_mov_b32 v49,0\nMvaa 172,56,184\nv_mov_b32 v50,0\nv_mov_b32 v51,0\nMvaa 172,52,180\nv_mov_b32 v52,0\nv_mov_b32 v53,0\nMvaa 172,48,176\n"
"v_mov_b32 v54,0\nv_mov_b32 v55,0\nMvaa 172,44,172\nv_mov_b32 v56,0\nv_mov_b32 v57,0\nMvaa 172,40,168\nv_mov_b32 v58,0\nv_mov_b32 v59,0\n"
"Mvaa 172,36,164\nWL 0\nv_mov_b32 v60,0\nv_mov_b32 v61,0\nMvaa 172,32,160\nv_mov_b32 v62,0\nv_mov_b32 v63,0\ns_waitcnt vmcnt(8)\nv_mov_b32 v13,s65\n"
"s_mov_b64 exec,1\nbuffer_store_dword v13,off,s[24:27],s5 sc1\ns_mov_b64 exec,-1\ns_nop 3\nv_add_f32 v240,0x73840000,v246\nv_min_f32 v148,v240,v148\n"
"v_add_f32 v241,0x73780000,v246\nv_min_f32 v149,v241,v149\nv_add_f32 v242,0x73680000,v246\nv_min_f32 v150,v242,v150\nv_add_f32 v243,0x73580000,v246\n"
"v_min_f32 v151,v243,v151\nv_add_f32 v240,0x71000000,v246\nv_min_f32 v144,v240,v144\nv_min_f32 v156,v240,v156\nv_add_f32 v241,0xf1000000,v246\n"
"v_min_f32 v145,v241,v145\nv_min_f32 v157,v241,v157\nv_add_f32 v242,0xf1c00000,v246\nv_min_f32 v146,v242,v146\nv_min_f32 v158,v242,v158\n"
"v_add_f32 v243,0xf2200000,v246\nv_min_f32 v147,v243,v147\nv_min_f32 v159,v243,v159\nv_add_f32 v240,0xf3780000,v246\nv_min_f32 v152,v240,v152\n"
"v_min_f32 v164,v240,v164\nv_add_f32 v241,0xf3840000,v246\nv_min_f32 v153,v241,v153\nv_min_f32 v165,v241,v165\nv_add_f32 v242,0xf38c0000,v246\n"
"v_min_f32 v154,v242,v154\nv_min_f32 v166,v242,v166\nv_add_f32 v243,0xf3940000,v246\nv_min_f32 v155,v243,v155\nv_min_f32 v167,v243,v167\n"
"v_add_f32 v240,0xf3fc0000,v246\nv_min_f32 v160,v240,v160\nv_min_f32 v172,v240,v172\nv_add_f32 v241,0xf4020000,v246\nv_min_f32 v161,v241,v161\n"
"v_min_f32 v173,v241,v173\nv_add_f32 v242,0xf4060000,v246\nv_min_f32 v162,v242,v162\nv_min_f32 v174,v242,v174\nv_add_f32 v243,0xf40a0000,v246\n"
"v_min_f32 v163,v243,v163\nv_min_f32 v175,v243,v175\nv_add_f32 v240,0xf43e0000,v246\nv_min_f32 v168,v240,v168\nv_add_f32 v241,0xf4420000,v246\n"
"v_min_f32 v169,v241,v169\nv_add_f32 v242,0xf4460000,v246\nv_min_f32 v170,v242,v170\nv_add_f32 v243,0xf44a0000,v246\nv_min_f32 v171,v243,v171\n"
"v_add_f32 v246,0xf4800000,v246\nv_max3_f32 v238,v144,v145,v146\nv_max3_f32 v240,v148,v149,v150\nv_max3_f32 v238,v238,v147,v152\n"
"v_max3_f32 v240,v240,v151,v156\nv_max3_f32 v238,v238,v153,v154\nv_max3_f32 v240,v240,v157,v158\nv_max3_f32 v238,v238,v155,v160\n"
"v_max3_f32 v240,v240,v159,v164\nv_max3_f32 v238,v238,v161,v162\nv_max3_f32 v240,v240,v165,v166\nv_max3_f32 v238,v238,v163,v168\n"
"v_max3_f32 v240,v240,v167,v172\nv_max3_f32 v238,v238,v169,v170\nv_max3_f32 v240,v240,v173,v174\nv_max_f32 v238,v238,v171\nv_max_f32 v240,v240,v175\n"
"v_mov_b32 v239,v238\nv_mov_b32 v241,v240\ns_nop 0\nv_permlane32_swap_b32 v238,v239\nv_permlane32_swap_b32 v240,v241\nv_max_f32 v238,v238,v239\n"
"v_max_f32 v240,v240,v241\nv_mov_b32 v239,v238\nv_mov_b32 v241,v240\ns_nop 0\nv_permlane16_swap_b32 v238,v239\nv_permlane16_swap_b32 v240,v241\n"
"v_max_f32 v238,v238,v239\nv_max_f32 v240,v240,v241\nv_mov_b32 v232,v238\nv_mov_b32 v233,v240\nv_sub_f32 v224,0,v232\nv_sub_f32 v228,0,v233\n"
"v_sub_f32 v225,0,v232\nv_sub_f32 v229,0,v233\nv_sub_f32 v226,0,v232\nv_sub_f32 v230,0,v233\nv_sub_f32 v227,0,v232\nv_sub_f32 v231,0,v233\n"
"v_sub_f32 v144,v144,v238\nv_sub_f32 v148,v148,v240\nv_sub_f32 v145,v145,v238\nv_sub_f32 v149,v149,v240\nv_sub_f32 v146,v146,v238\n"
"v_sub_f32 v150,v150,v240\nv_sub_f32 v147,v147,v238\nv_sub_f32 v151,v151,v240\nv_sub_f32 v152,v152,v238\nv_sub_f32 v156,v156,v240\n"
"v_sub_f32 v153,v153,v238\nv_sub_f32 v157,v157,v240\nv_sub_f32 v154,v154,v238\nv_sub_f32 v158,v158,v240\nv_sub_f32 v155,v155,v238\n"
"v_sub_f32 v159,v159,v240\nv_sub_f32 v160,v160,v238\nv_sub_f32 v164,v164,v240\nv_sub_f32 v161,v161,v238\nv_sub_f32 v165,v165,v240\n"
"v_sub_f32 v162,v162,v238\nv_sub_f32 v166,v166,v240\nv_sub_f32 v163,v163,v238\nv_sub_f32 v167,v167,v240\nv_sub_f32 v168,v168,v238\n"
"v_sub_f32 v172,v172,v240\nv_sub_f32 v169,v169,v238\nv_sub_f32 v173,v173,v240\nv_sub_f32 v170,v170,v238\nv_sub_f32 v174,v174,v240\n"
"v_sub_f32 v171,v171,v238\nv_sub_f32 v175,v175,v240\nv_exp_f32 v144,v144\nv_exp_f32 v145,v145\nv_add_f32 v234,v144,v234\nv_exp_f32 v146,v146\n"
"v_add_f32 v234,v145,v234\nv_exp_f32 v147,v147\nv_add_f32 v234,v146,v234\nv_exp_f32 v148,v148\nv_exp_f32 v149,v149\nv_exp_f32 v150,v150\n"
"v_exp_f32 v151,v151\nv_exp_f32 v152,v152\nv_exp_f32 v153,v153\nv_exp_f32 v154,v154\ns_branch fused_op_t2k\nfused_op_first2w:\nv_lshrrev_b32 v238,4,v0\n"
"v_lshrrev_b32 v239,4,v238\nv_and_b32 v238,15,v238\nv_lshlrev_b32 v239,2,v239\nv_sub_u32 v238,v238,v239\ns_lshl_b32 s45,s10,5\nv_add_u32 v238,s45,v238\n"
"v_cvt_f32_i32 v238,v238\nv_mul_f32 v246,0x71800000,v238\ns_mul_i32 s45,s56,229376\ns_mul_i32 s49,s10,57344\ns_add_u32 s45,s45,s49\n"
"s_add_u32 s20,s88,s45\ns_addc_u32 s21,s89,0\ns_and_b32 s21,s21,0xffff\ns_sub_u32 s22,3670016,s45\ns_mov_b32 s23,0x20000\ns_mov_b32 s36,0\n"
"s_mov_b32 s37,6144\ns_mov_b32 s38,12288\ns_mov_b32 s39,18432\ns_mov_b32 s40,28672\ns_mov_b32 s41,34816\ns_mov_b32 s42,40960\ns_mov_b32 s43,47104\n"
"v_lshrrev_b32 v13,7,v0\nv_and_b32 v1,6,v13\nv_lshlrev_b32 v1,4,v1\nv_xor_b32 v1,v1,v0\nv_and_b32 v1,0x70,v1\nv_mul_u32_u24 v13,896,v13\n"
"v_add_u32 v1,v1,v13\nWL 0\nv_mov_b32 v93,0\nv_mov_b32 v94,0\nv_mov_b32 v95,0\nv_mov_b32 v96,0\nv_mov_b32 v97,0\nv_mov_b32 v98,0\nv_mov_b32 v99,0\n"
"v_mov_b32 v100,0\nv_mov_b32 v101,0\nv_mov_b32 v102,0\nv_mov_b32 v103,0\nv_mov_b32 v104,0\nv_mov_b32 v105,0\nv_mov_b32 v106,0\nv_mov_b32 v107,0\n"
"v_mov_b32 v108,0\nv_mov_b32 v109,0\nv_mov_b32 v110,0\nv_mov_b32 v111,0\nv_mov_b32 v112,0\nv_mov_b32 v113,0\nv_mov_b32 v114,0\nv_mov_b32 v115,0\n"
"v_mov_b32 v116,0\nv_mov_b32 v117,0\nv_mov_b32 v118,0\nv_mov_b32 v119,0\nv_mov_b32 v120,0\nv_mov_b32 v121,0\nv_mov_b32 v122,0\nv_mov_b32 v123,0\n"
"v_mov_b32 v124,0\nv_mov_b32 v125,0\nv_mov_b32 v126,0\nv_mov_b32 v127,0\nv_mov_b32 v128,0\nv_mov_b32 v129,0\nv_mov_b32 v130,0\nv_mov_b32 v131,0\n"
"v_mov_b32 v132,0\nv_mov_b32 v133,0\nv_mov_b32 v134,0\nv_mov_b32 v135,0\nv_mov_b32 v136,0\nv_mov_b32 v137,0\nv_mov_b32 v138,0\nv_mov_b32 v139,0\n"
"v_mov_b32 v140,0\nv_mov_b32 v141,0\nv_mov_b32 v142,0\nv_mov_b32 v143,0\nv_mov_b32 v234,0\nv_mov_b32 v235,0\nZvaa 144,0,128\nRD 32,3,8192\n"
"v_mov_b32 v16,0\nMvaa 144,4,132\nRD 36,3,9216\nv_mov_b32 v17,0\nMvaa 144,8,136\nRD 40,3,10240\nv_mov_b32 v18,0\nMvaa 144,12,140\nRD 44,3,11264\n"
"v_mov_b32 v19,0\nMvaa 144,16,144\nRD 48,3,12288\nv_mov_b32 v20,0\nMvaa 144,20,148\nRD 52,3,13312\nv_mov_b32 v21,0\nMvaa 144,24,152\nRD 56,3,14336\n"
"v_mov_b32 v22,0\nMvaa 144,28,156\ns_add_u32 m0,s48,s44\nRD 60,3,15360\nv_mov_b32 v23,0\nZvaa 148,28,188\nv_mov_b32 v24,0\nv_mov_b32 v25,0\n"
"Mvaa 148,24,184\nDS 1,20,36,0\nMvaa 148,20,180\nv_mov_b32 v26,0\nv_mov_b32 v27,0\nMvaa 148,16,176\nv_mov_b32 v28,0\nv_mov_b32 v29,0\nMvaa 148,12,172\n"
"DS 1,20,37,1024\nMvaa 148,8,168\nv_mov_b32 v30,0\nv_mov_b32 v31,0\nMvaa 148,4,164\nv_mov_b32 v32,0\nv_mov_b32 v33,0\nMvaa 148,0,160\nWL 0\n"
"DS 1,20,38,2048\nZvaa 152,32,128\nRD 0,3,16384\nv_mov_b32 v34,0\nMvaa 152,36,132\nRD 4,3,17408\nv_mov_b32 v35,0\nMvaa 152,40,136\nRD 8,3,18432\n"
"v_mov_b32 v36,0\nMvaa 152,44,140\nRD 12,3,19456\nv_mov_b32 v37,0\nMvaa 152,48,144\nRD 16,3,20480\nv_mov_b32 v38,0\nMvaa 152,52,148\nRD 20,3,21504\n"
"v_mov_b32 v39,0\nMvaa 152,56,152\nRD 24,3,22528\nv_mov_b32 v40,0\nMvaa 152,60,156\nRD 28,3,23552\nv_mov_b32 v41,0\nZvaa 156,60,188\nv_mov_b32 v42,0\n"
"v_mov_b32 v43,0\nMvaa 156,56,184\nDS 1,20,39,3072\nMvaa 156,52,180\ns_add_u32 m0,m0,0x1000\nv_mov_b32 v44,0\nv_mov_b32 v45,0\nMvaa 156,48,176\n"
"v_mov_b32 v46,0\nv_mov_b32 v47,0\nMvaa 156,44,172\nDS 1,20,40,0\nMvaa 156,40,168\nv_mov_b32 v48,0\nv_mov_b32 v49,0\nMvaa 156,36,164\nv_mov_b32 v50,0\n"
"v_mov_b32 v51,0\nMvaa 156,32,160\nWL 0\nDS 1,20,41,1024\nZvaa 160,0,128\nRD 32,3,24576\nv_mov_b32 v52,0\nMvaa 160,4,132\ns_mov_b32 s44,s47\n"
"RD 36,3,25600\nv_mov_b32 v53,0\nMvaa 160,8,136\ns_add_u32 s47,s47,0x8000\nRD 40,3,26624\nv_mov_b32 v54,0\nMvaa 160,12,140\ns_cmp_eq_u32 s47,0x28000\n"
"RD 44,3,27648\nv_mov_b32 v55,0\nMvaa 160,16,144\ns_cselect_b32 s47,0,s47\nRD 48,3,28672\nv_mov_b32 v56,0\nMvaa 160,20,148\nRD 52,3,29696\n"
"v_mov_b32 v57,0\nMvaa 160,24,152\nRD 56,3,30720\nv_mov_b32 v58,0\nMvaa 160,28,156\nRD 60,3,31744\nv_mov_b32 v59,0\nZvaa 164,28,188\nv_mov_b32 v60,0\n"
"v_mov_b32 v61,0\nMvaa 164,24,184\nDS 1,20,42,2048\nMvaa 164,20,180\nv_mov_b32 v62,0\nv_mov_b32 v63,0\nMvaa 164,16,176\nv_mov_b32 v64,0\n"
"v_mov_b32 v65,0\nMvaa 164,12,172\nDS 1,20,43,3072\nMvaa 164,8,168\nv_add_u32 v1,0x80,v1\nv_mov_b32 v66,0\nMvaa 164,4,164\nv_add_u32 v3,s47,v0\n"
"v_mov_b32 v67,0\nMvaa 164,0,160\ns_waitcnt vmcnt(24) lgkmcnt(0)\ns_barrier\nv_mov_b32 v68,0\nZvaa 168,32,128\nRD 0,3,0\nv_mov_b32 v69,0\n"
"Mvaa 168,36,132\nRD 4,3,1024\nv_mov_b32 v70,0\nMvaa 168,40,136\nRD 8,3,2048\nv_mov_b32 v71,0\nMvaa 168,44,140\nRD 12,3,3072\nv_mov_b32 v72,0\n"
"Mvaa 168,48,144\nRD 16,3,4096\nv_mov_b32 v73,0\nMvaa 168,52,148\nRD 20,3,5120\nv_mov_b32 v74,0\nMvaa 168,56,152\nRD 24,3,6144\nv_mov_b32 v75,0\n"
"Mvaa 168,60,156\nRD 28,3,7168\nv_mov_b32 v76,0\nZvaa 172,60,188\nv_mov_b32 v77,0\nv_mov_b32 v78,0\nMvaa 172,56,184\nv_mov_b32 v79,0\nv_mov_b32 v80,0\n"
"Mvaa 172,52,180\nv_mov_b32 v81,0\nv_mov_b32 v82,0\nMvaa 172,48,176\nv_mov_b32 v83,0\nv_mov_b32 v84,0\nMvaa 172,44,172\nv_mov_b32 v85,0\n"
"v_mov_b32 v86,0\nMvaa 172,40,168\nv_mov_b32 v87,0\nv_mov_b32 v88,0\nMvaa 172,36,164\nWL 0\nv_mov_b32 v89,0\nv_mov_b32 v90,0\nMvaa 172,32,160\n"
"v_mov_b32 v91,0\nv_mov_b32 v92,0\ns_waitcnt vmcnt(8)\nv_mov_b32 v13,s65\ns_mov_b64 exec,1\nbuffer_store_dword v13,off,s[24:27],s5 sc1\n"
"s_mov_b64 exec,-1\ns_nop 3\nv_add_f32 v240,0x73840000,v246\nv_min_f32 v148,v240,v148\nv_add_f32 v241,0x73780000,v246\nv_min_f32 v149,v241,v149\n"
"v_add_f32 v242,0x73680000,v246\nv_min_f32 v150,v242,v150\nv_add_f32 v243,0x73580000,v246\nv_min_f32 v151,v243,v151\nv_add_f32 v240,0x71000000,v246\n"
"v_min_f32 v144,v240,v144\nv_min_f32 v156,v240,v156\nv_add_f32 v241,0xf1000000,v246\nv_min_f32 v145,v241,v145\nv_min_f32 v157,v241,v157\n"
"v_add_f32 v242,0xf1c00000,v246\nv_min_f32 v146,v242,v146\nv_min_f32 v158,v242,v158\nv_add_f32 v243,0xf2200000,v246\nv_min_f32 v147,v243,v147\n"
"v_min_f32 v159,v243,v159\nv_add_f32 v240,0xf3780000,v246\nv_min_f32 v152,v240,v152\nv_min_f32 v164,v240,v164\nv_add_f32 v241,0xf3840000,v246\n"
"v_min_f32 v153,v241,v153\nv_min_f32 v165,v241,v165\nv_add_f32 v242,0xf38c0000,v246\nv_min_f32 v154,v242,v154\nv_min_f32 v166,v242,v166\n"
"v_add_f32 v243,0xf3940000,v246\nv_min_f32 v155,v243,v155\nv_min_f32 v167,v243,v167\nv_add_f32 v240,0xf3fc0000,v246\nv_min_f32 v160,v240,v160\n"
"v_min_f32 v172,v240,v172\nv_add_f32 v241,0xf4020000,v246\nv_min_f32 v161,v241,v161\nv_min_f32 v173,v241,v173\nv_add_f32 v242,0xf4060000,v246\n"
"v_min_f32 v162,v242,v162\nv_min_f32 v174,v242,v174\nv_add_f32 v243,0xf40a0000,v246\nv_min_f32 v163,v243,v163\nv_min_f32 v175,v243,v175\n"
"v_add_f32 v240,0xf43e0000,v246\nv_min_f32 v168,v240,v168\nv_add_f32 v241,0xf4420000,v246\nv_min_f32 v169,v241,v169\nv_add_f32 v242,0xf4460000,v246\n"
"v_min_f32 v170,v242,v170\nv_add_f32 v243,0xf44a0000,v246\nv_min_f32 v171,v243,v171\nv_add_f32 v246,0xf4800000,v246\nv_max3_f32 v238,v144,v145,v146\n"
"v_max3_f32 v240,v148,v149,v150\nv_max3_f32 v238,v238,v147,v152\nv_max3_f32 v240,v240,v151,v156\nv_max3_f32 v238,v238,v153,v154\n"
"v_max3_f32 v240,v240,v157,v158\nv_max3_f32 v238,v238,v155,v160\nv_max3_f32 v240,v240,v159,v164\nv_max3_f32 v238,v238,v161,v162\n"
"v_max3_f32 v240,v240,v165,v166\nv_max3_f32 v238,v238,v163,v168\nv_max3_f32 v240,v240,v167,v172\nv_max3_f32 v238,v238,v169,v170\n"
"v_max3_f32 v240,v240,v173,v174\nv_max_f32 v238,v238,v171\nv_max_f32 v240,v240,v175\nv_mov_b32 v239,v238\nv_mov_b32 v241,v240\ns_nop 0\n"
"v_permlane32_swap_b32 v238,v239\nv_permlane32_swap_b32 v240,v241\nv_max_f32 v238,v238,v239\nv_max_f32 v240,v240,v241\nv_mov_b32 v239,v238\n"
"v_mov_b32 v241,v240\ns_nop 0\nv_permlane16_swap_b32 v238,v239\nv_permlane16_swap_b32 v240,v241\nv_max_f32 v238,v238,v239\nv_max_f32 v240,v240,v241\n"
"v_mov_b32 v232,v238\nv_mov_b32 v233,v240\nv_sub_f32 v224,0,v232\nv_sub_f32 v228,0,v233\nv_sub_f32 v225,0,v232\nv_sub_f32 v229,0,v233\n"
"v_sub_f32 v226,0,v232\nv_sub_f32 v230,0,v233\nv_sub_f32 v227,0,v232\nv_sub_f32 v231,0,v233\nv_sub_f32 v144,v144,v238\nv_sub_f32 v148,v148,v240\n"
"v_sub_f32 v145,v145,v238\nv_sub_f32 v149,v149,v240\nv_sub_f32 v146,v146,v238\nv_sub_f32 v150,v150,v240\nv_sub_f32 v147,v147,v238\n"
"v_sub_f32 v151,v151,v240\nv_sub_f32 v152,v152,v238\nv_sub_f32 v156,v156,v240\nv_sub_f32 v153,v153,v238\nv_sub_f32 v157,v157,v240\n"
"v_sub_f32 v154,v154,v238\nv_sub_f32 v158,v158,v240\nv_sub_f32 v155,v155,v238\nv_sub_f32 v159,v159,v240\nv_sub_f32 v160,v160,v238\n"
"v_sub_f32 v164,v164,v240\nv_sub_f32 v161,v161,v238\nv_sub_f32 v165,v165,v240\nv_sub_f32 v162,v162,v238\nv_sub_f32 v166,v166,v240\n"
"v_sub_f32 v163,v163,v238\nv_sub_f32 v167,v167,v240\nv_sub_f32 v168,v168,v238\nv_sub_f32 v172,v172,v240\nv_sub_f32 v169,v169,v238\n"
"v_sub_f32 v173,v173,v240\nv_sub_f32 v170,v170,v238\nv_sub_f32 v174,v174,v240\nv_sub_f32 v171,v171,v238\nv_sub_f32 v175,v175,v240\nv_exp_f32 v144,v144\n"
"v_exp_f32 v145,v145\nv_add_f32 v234,v144,v234\nv_exp_f32 v146,v146\nv_add_f32 v234,v145,v234\nv_exp_f32 v147,v147\nv_add_f32 v234,v146,v234\n"
"v_exp_f32 v148,v148\nv_exp_f32 v149,v149\nv_exp_f32 v150,v150\nv_exp_f32 v151,v151\nv_exp_f32 v152,v152\nv_exp_f32 v153,v153\nv_exp_f32 v154,v154\n"
"s_branch fused_op_t2w\nfused_op_first0b:\nv_lshrrev_b32 v238,4,v0\nv_lshrrev_b32 v239,4,v238\nv_and_b32 v238,15,v238\nv_lshlrev_b32 v239,2,v239\n"
"v_sub_u32 v238,v238,v239\ns_lshl_b32 s45,s10,5\nv_add_u32 v238,s45,v238\nv_cvt_f32_i32 v238,v238\nv_mul_f32 v246,0x71800000,v238\nWL 0\n"
"s_mov_b32 s19,s44\nv_mov_b32 v64,0\nv_mov_b32 v65,0\nv_mov_b32 v66,0\nv_mov_b32 v67,0\nv_mov_b32 v68,0\nv_mov_b32 v69,0\nv_mov_b32 v70,0\n"
"v_mov_b32 v71,0\nv_mov_b32 v72,0\nv_mov_b32 v73,0\nv_mov_b32 v74,0\nv_mov_b32 v75,0\nv_mov_b32 v76,0\nv_mov_b32 v77,0\nv_mov_b32 v78,0\n"
"v_mov_b32 v79,0\nv_mov_b32 v80,0\nv_mov_b32 v81,0\nv_mov_b32 v82,0\nv_mov_b32 v83,0\nv_mov_b32 v84,0\nv_mov_b32 v85,0\nv_mov_b32 v86,0\n"
"v_mov_b32 v87,0\nv_mov_b32 v88,0\nv_mov_b32 v89,0\nv_mov_b32 v90,0\nv_mov_b32 v91,0\nv_mov_b32 v92,0\nv_mov_b32 v93,0\nv_mov_b32 v94,0\n"
"v_mov_b32 v95,0\nv_mov_b32 v96,0\nv_mov_b32 v97,0\nv_mov_b32 v98,0\nv_mov_b32 v99,0\nv_mov_b32 v100,0\nv_mov_b32 v101,0\nv_mov_b32 v102,0\n"
"v_mov_b32 v103,0\nv_mov_b32 v104,0\nv_mov_b32 v105,0\nv_mov_b32 v106,0\nv_mov_b32 v107,0\nv_mov_b32 v108,0\nv_mov_b32 v109,0\nv_mov_b32 v110,0\n"
"v_mov_b32 v111,0\nv_mov_b32 v112,0\nv_mov_b32 v113,0\nv_mov_b32 v114,0\nv_mov_b32 v115,0\nv_mov_b32 v116,0\nv_mov_b32 v117,0\nv_mov_b32 v118,0\n"
"v_mov_b32 v119,0\nv_mov_b32 v120,0\nv_mov_b32 v121,0\nv_mov_b32 v122,0\nv_mov_b32 v123,0\nv_mov_b32 v124,0\nv_mov_b32 v125,0\nv_mov_b32 v126,0\n"
"v_mov_b32 v127,0\nv_mov_b32 v128,0\nv_mov_b32 v129,0\nv_mov_b32 v130,0\nv_mov_b32 v131,0\nv_mov_b32 v132,0\nv_mov_b32 v133,0\nv_mov_b32 v134,0\n"
"v_mov_b32 v135,0\nv_mov_b32 v136,0\nv_mov_b32 v137,0\nv_mov_b32 v138,0\nv_mov_b32 v139,0\nv_mov_b32 v140,0\nv_mov_b32 v141,0\nv_mov_b32 v142,0\n"
"v_mov_b32 v143,0\nv_mov_b32 v234,0\nv_mov_b32 v235,0\nZvaa 144,0,128\nRD 32,3,8192\nv_mov_b32 v16,0\nMvaa 144,4,132\ns_add_u32 m0,s48,32768\n"
"RD 36,3,9216\nDS 1,20,36,0\nMvaa 144,8,136\nRD 40,3,10240\nv_mov_b32 v17,0\nMvaa 144,12,140\nRD 44,3,11264\nDS 1,20,37,1024\nMvaa 144,16,144\n"
"RD 48,3,12288\nv_mov_b32 v18,0\nMvaa 144,20,148\nRD 52,3,13312\nv_mov_b32 v19,0\nMvaa 144,24,152\nRD 56,3,14336\nv_mov_b32 v20,0\nMvaa 144,28,156\n"
"RD 60,3,15360\nv_mov_b32 v21,0\nZvaa 148,28,188\nDS 1,20,38,2048\nMvaa 148,24,184\nDS 1,20,39,3072\nMvaa 148,20,180\ns_add_u32 m0,m0,0x1000\ns_nop 0\n"
"DS 1,20,40,0\nMvaa 148,16,176\nDS 1,20,41,1024\nMvaa 148,12,172\nDS 1,20,42,2048\nMvaa 148,8,168\nDS 1,20,43,3072\nv_add_u32 v1,s95,v1\nMvaa 148,4,164\n"
"s_add_u32 m0,s48,65536\ns_nop 0\nDS 1,20,36,0\nMvaa 148,0,160\nWL 0\nDS 1,20,37,1024\nZvaa 152,32,128\nRD 0,3,16384\nv_mov_b32 v22,0\nMvaa 152,36,132\n"
"RD 4,3,17408\nv_mov_b32 v23,0\nMvaa 152,40,136\nRD 8,3,18432\nv_mov_b32 v24,0\nMvaa 152,44,140\nRD 12,3,19456\nv_mov_b32 v25,0\nMvaa 152,48,144\n"
"RD 16,3,20480\nv_mov_b32 v26,0\nMvaa 152,52,148\nRD 20,3,21504\nv_mov_b32 v27,0\nMvaa 152,56,152\nRD 24,3,22528\nv_mov_b32 v28,0\nMvaa 152,60,156\n"
"RD 28,3,23552\nv_mov_b32 v29,0\nZvaa 156,60,188\nDS 1,20,38,2048\nMvaa 156,56,184\nDS 1,20,39,3072\nMvaa 156,52,180\ns_add_u32 m0,m0,0x1000\ns_nop 0\n"
"DS 1,20,40,0\nMvaa 156,48,176\nDS 1,20,41,1024\nMvaa 156,44,172\nDS 1,20,42,2048\nMvaa 156,40,168\nDS 1,20,43,3072\nv_add_u32 v1,0xffff8000,v1\n"
"Mvaa 156,36,164\ns_add_u32 m0,s48,s19\ns_nop 0\nDS 1,20,36,0\nMvaa 156,32,160\nWL 0\nDS 1,20,37,1024\nZvaa 160,0,128\nRD 32,3,24576\nv_mov_b32 v30,0\n"
"Mvaa 160,4,132\ns_mov_b32 s44,s47\nRD 36,3,25600\nv_mov_b32 v31,0\nMvaa 160,8,136\ns_add_u32 s47,s47,0x8000\nRD 40,3,26624\nv_mov_b32 v32,0\n"
"Mvaa 160,12,140\ns_cmp_eq_u32 s47,0x28000\nRD 44,3,27648\nv_mov_b32 v33,0\nMvaa 160,16,144\ns_cselect_b32 s47,0,s47\nRD 48,3,28672\nv_mov_b32 v34,0\n"
"Mvaa 160,20,148\nRD 52,3,29696\nv_mov_b32 v35,0\nMvaa 160,24,152\nRD 56,3,30720\nv_mov_b32 v36,0\nMvaa 160,28,156\nRD 60,3,31744\nv_mov_b32 v37,0\n"
"Zvaa 164,28,188\nDS 1,20,38,2048\nMvaa 164,24,184\nDS 1,20,39,3072\nMvaa 164,20,180\ns_add_u32 m0,m0,0x1000\ns_nop 0\nDS 1,20,40,0\nMvaa 164,16,176\n"
"DS 1,20,41,1024\nMvaa 164,12,172\nDS 1,20,42,2048\nMvaa 164,8,168\nDS 1,20,43,3072\nv_add_u32 v1,0x18000,v1\nMvaa 164,4,164\nv_add_u32 v3,s47,v0\n"
"v_mov_b32 v38,0\nMvaa 164,0,160\ns_waitcnt vmcnt(24) lgkmcnt(0)\ns_barrier\nv_mov_b32 v39,0\nZvaa 168,32,128\nRD 0,3,0\nv_mov_b32 v40,0\n"
"Mvaa 168,36,132\nRD 4,3,1024\nv_mov_b32 v41,0\nMvaa 168,40,136\nRD 8,3,2048\nv_mov_b32 v42,0\nMvaa 168,44,140\nRD 12,3,3072\nv_mov_b32 v43,0\n"
"Mvaa 168,48,144\nRD 16,3,4096\nv_mov_b32 v44,0\nMvaa 168,52,148\nRD 20,3,5120\nv_mov_b32 v45,0\nMvaa 168,56,152\nRD 24,3,6144\nv_mov_b32 v46,0\n"
"Mvaa 168,60,156\nRD 28,3,7168\nv_mov_b32 v47,0\nZvaa 172,60,188\nv_mov_b32 v48,0\nv_mov_b32 v49,0\nMvaa 172,56,184\nv_mov_b32 v50,0\nv_mov_b32 v51,0\n"
"Mvaa 172,52,180\nv_mov_b32 v52,0\nv_mov_b32 v53,0\nMvaa 172,48,176\nv_mov_b32 v54,0\nv_mov_b32 v55,0\nMvaa 172,44,172\nv_mov_b32 v56,0\n"
"v_mov_b32 v57,0\nMvaa 172,40,168\nv_mov_b32 v58,0\nv_mov_b32 v59,0\nMvaa 172,36,164\nWL 0\nv_mov_b32 v60,0\nv_mov_b32 v61,0\nMvaa 172,32,160\n"
"v_mov_b32 v62,0\nv_mov_b32 v63,0\ns_waitcnt vmcnt(8)\nv_mov_b32 v13,s65\ns_mov_b64 exec,1\nbuffer_store_dword v13,off,s[24:27],s5 sc1\n"
"s_mov_b64 exec,-1\ns_nop 3\nv_max3_f32 v238,v144,v145,v146\nv_max3_f32 v240,v148,v149,v150\nv_max3_f32 v238,v238,v147,v152\n"
"v_max3_f32 v240,v240,v151,v156\nv_max3_f32 v238,v238,v153,v154\nv_max3_f32 v240,v240,v157,v158\nv_max3_f32 v238,v238,v155,v160\n"
"v_max3_f32 v240,v240,v159,v164\nv_max3_f32 v238,v238,v161,v162\nv_max3_f32 v240,v240,v165,v166\nv_max3_f32 v238,v238,v163,v168\n"
"v_max3_f32 v240,v240,v167,v172\nv_max3_f32 v238,v238,v169,v170\nv_max3_f32 v240,v240,v173,v174\nv_max_f32 v238,v238,v171\nv_max_f32 v240,v240,v175\n"
"v_mov_b32 v239,v238\nv_mov_b32 v241,v240\ns_nop 0\nv_permlane32_swap_b32 v238,v239\nv_permlane32_swap_b32 v240,v241\nv_max_f32 v238,v238,v239\n"
"v_max_f32 v240,v240,v241\nv_mov_b32 v239,v238\nv_mov_b32 v241,v240\ns_nop 0\nv_permlane16_swap_b32 v238,v239\nv_permlane16_swap_b32 v240,v241\n"
"v_max_f32 v238,v238,v239\nv_max_f32 v240,v240,v241\nv_mov_b32 v232,v238\nv_mov_b32 v233,v240\nv_sub_f32 v224,0,v232\nv_sub_f32 v228,0,v233\n"
"v_sub_f32 v225,0,v232\nv_sub_f32 v229,0,v233\nv_sub_f32 v226,0,v232\nv_sub_f32 v230,0,v233\nv_sub_f32 v227,0,v232\nv_sub_f32 v231,0,v233\n"
"v_sub_f32 v144,v144,v238\nv_sub_f32 v148,v148,v240\nv_sub_f32 v145,v145,v238\nv_sub_f32 v149,v149,v240\nv_sub_f32 v146,v146,v238\n"
"v_sub_f32 v150,v150,v240\nv_sub_f32 v147,v147,v238\nv_sub_f32 v151,v151,v240\nv_sub_f32 v152,v152,v238\nv_sub_f32 v156,v156,v240\n"
"v_sub_f32 v153,v153,v238\nv_sub_f32 v157,v157,v240\nv_sub_f32 v154,v154,v238\nv_sub_f32 v158,v158,v240\nv_sub_f32 v155,v155,v238\n"
"v_sub_f32 v159,v159,v240\nv_sub_f32 v160,v160,v238\nv_sub_f32 v164,v164,v240\nv_sub_f32 v161,v161,v238\nv_sub_f32 v165,v165,v240\n"
"v_sub_f32 v162,v162,v238\nv_sub_f32 v166,v166,v240\nv_sub_f32 v163,v163,v238\nv_sub_f32 v167,v167,v240\nv_sub_f32 v168,v168,v238\n"
"v_sub_f32 v172,v172,v240\nv_sub_f32 v169,v169,v238\nv_sub_f32 v173,v173,v240\nv_sub_f32 v170,v170,v238\nv_sub_f32 v174,v174,v240\n"
"v_sub_f32 v171,v171,v238\nv_sub_f32 v175,v175,v240\nv_exp_f32 v144,v144\nv_exp_f32 v145,v145\nv_add_f32 v234,v144,v234\nv_exp_f32 v146,v146\n"
"v_add_f32 v234,v145,v234\nv_exp_f32 v147,v147\nv_add_f32 v234,v146,v234\nv_exp_f32 v148,v148\nv_add_f32 v234,v147,v234\nv_exp_f32 v149,v149\n"
"v_add_f32 v235,v148,v235\nv_exp_f32 v150,v150\nv_add_f32 v235,v149,v235\nv_exp_f32 v151,v151\nv_add_f32 v235,v150,v235\nv_exp_f32 v152,v152\n"
"v_add_f32 v235,v151,v235\nv_exp_f32 v153,v153\nv_exp_f32 v154,v154\nv_exp_f32 v155,v155\nv_exp_f32 v156,v156\nv_exp_f32 v157,v157\nv_exp_f32 v158,v158\n"
"v_exp_f32 v159,v159\ns_sub_u32 s62,s61,3\ns_branch fused_op_h1\nfused_op_first1:\nv_lshrrev_b32 v238,4,v0\nv_lshrrev_b32 v239,4,v238\n"
"v_and_b32 v238,15,v238\nv_lshlrev_b32 v239,2,v239\nv_sub_u32 v238,v238,v239\ns_lshl_b32 s45,s10,5\nv_add_u32 v238,s45,v238\nv_cvt_f32_i32 v238,v238\n"
"v_mul_f32 v246,0x71800000,v238\nWL 0\nv_mov_b32 v93,0\nv_mov_b32 v94,0\nv_mov_b32 v95,0\nv_mov_b32 v96,0\nv_mov_b32 v97,0\nv_mov_b32 v98,0\n"
"v_mov_b32 v99,0\nv_mov_b32 v100,0\nv_mov_b32 v101,0\nv_mov_b32 v102,0\nv_mov_b32 v103,0\nv_mov_b32 v104,0\nv_mov_b32 v105,0\nv_mov_b32 v106,0\n"
"v_mov_b32 v107,0\nv_mov_b32 v108,0\nv_mov_b32 v109,0\nv_mov_b32 v110,0\nv_mov_b32 v111,0\nv_mov_b32 v112,0\nv_mov_b32 v113,0\nv_mov_b32 v114,0\n"
"v_mov_b32 v115,0\nv_mov_b32 v116,0\nv_mov_b32 v117,0\nv_mov_b32 v118,0\nv_mov_b32 v119,0\nv_mov_b32 v120,0\nv_mov_b32 v121,0\nv_mov_b32 v122,0\n"
"v_mov_b32 v123,0\nv_mov_b32 v124,0\nv_mov_b32 v125,0\nv_mov_b32 v126,0\nv_mov_b32 v127,0\nv_mov_b32 v128,0\nv_mov_b32 v129,0\nv_mov_b32 v130,0\n"
"v_mov_b32 v131,0\nv_mov_b32 v132,0\nv_mov_b32 v133,0\nv_mov_b32 v134,0\nv_mov_b32 v135,0\nv_mov_b32 v136,0\nv_mov_b32 v137,0\nv_mov_b32 v138,0\n"
"v_mov_b32 v139,0\nv_mov_b32 v140,0\nv_mov_b32 v141,0\nv_mov_b32 v142,0\nv_mov_b32 v143,0\nv_mov_b32 v234,0\nv_mov_b32 v235,0\nZvaa 176,0,128\n"
"RD 32,3,8192\nv_mov_b32 v16,0\nMvaa 176,4,132\nRD 36,3,9216\nv_mov_b32 v17,0\nMvaa 176,8,136\nRD 40,3,10240\nv_mov_b32 v18,0\nMvaa 176,12,140\n"
"RD 44,3,11264\nv_mov_b32 v19,0\nMvaa 176,16,144\nRD 48,3,12288\nv_mov_b32 v20,0\nMvaa 176,20,148\nRD 52,3,13312\nv_mov_b32 v21,0\nMvaa 176,24,152\n"
"RD 56,3,14336\nv_mov_b32 v22,0\nMvaa 176,28,156\ns_add_u32 m0,s48,s44\nRD 60,3,15360\nv_mov_b32 v23,0\nZvaa 180,28,188\nv_mov_b32 v24,0\n"
"v_mov_b32 v25,0\nMvaa 180,24,184\nDS 1,20,36,0\nMvaa 180,20,180\nv_mov_b32 v26,0\nv_mov_b32 v27,0\nMvaa 180,16,176\nv_mov_b32 v28,0\nv_mov_b32 v29,0\n"
"Mvaa 180,12,172\nDS 1,20,37,1024\nMvaa 180,8,168\nv_mov_b32 v30,0\nv_mov_b32 v31,0\nMvaa 180,4,164\nv_mov_b32 v32,0\nv_mov_b32 v33,0\nMvaa 180,0,160\n"
"WL 0\nDS 1,20,38,2048\nZvaa 184,32,128\nRD 0,3,16384\nv_mov_b32 v34,0\nMvaa 184,36,132\nRD 4,3,17408\nv_mov_b32 v35,0\nMvaa 184,40,136\nRD 8,3,18432\n"
"v_mov_b32 v36,0\nMvaa 184,44,140\nRD 12,3,19456\nv_mov_b32 v37,0\nMvaa 184,48,144\nRD 16,3,20480\nv_mov_b32 v38,0\nMvaa 184,52,148\nRD 20,3,21504\n"
"v_mov_b32 v39,0\nMvaa 184,56,152\nRD 24,3,22528\nv_mov_b32 v40,0\nMvaa 184,60,156\nRD 28,3,23552\nv_mov_b32 v41,0\nZvaa 188,60,188\nv_mov_b32 v42,0\n"
"v_mov_b32 v43,0\nMvaa 188,56,184\nDS 1,20,39,3072\nMvaa 188,52,180\ns_add_u32 m0,m0,0x1000\nv_mov_b32 v44,0\nv_mov_b32 v45,0\nMvaa 188,48,176\n"
"v_mov_b32 v46,0\nv_mov_b32 v47,0\nMvaa 188,44,172\nDS 1,20,40,0\nMvaa 188,40,168\nv_mov_b32 v48,0\nv_mov_b32 v49,0\nMvaa 188,36,164\nv_mov_b32 v50,0\n"
"v_mov_b32 v51,0\nMvaa 188,32,160\nWL 0\nDS 1,20,41,1024\nZvaa 192,0,128\nRD 32,3,24576\nv_mov_b32 v52,0\nMvaa 192,4,132\ns_mov_b32 s44,s47\n"
"RD 36,3,25600\nv_mov_b32 v53,0\nMvaa 192,8,136\ns_add_u32 s47,s47,0x8000\nRD 40,3,26624\nv_mov_b32 v54,0\nMvaa 192,12,140\ns_cmp_eq_u32 s47,0x28000\n"
"RD 44,3,27648\nv_mov_b32 v55,0\nMvaa 192,16,144\ns_cselect_b32 s47,0,s47\nRD 48,3,28672\nv_mov_b32 v56,0\nMvaa 192,20,148\nRD 52,3,29696\n"
"v_mov_b32 v57,0\nMvaa 192,24,152\nRD 56,3,30720\nv_mov_b32 v58,0\nMvaa 192,28,156\nRD 60,3,31744\nv_mov_b32 v59,0\nZvaa 196,28,188\nv_mov_b32 v60,0\n"
"v_mov_b32 v61,0\nMvaa 196,24,184\nDS 1,20,42,2048\nMvaa 196,20,180\nv_mov_b32 v62,0\nv_mov_b32 v63,0\nMvaa 196,16,176\nv_mov_b32 v64,0\n"
"v_mov_b32 v65,0\nMvaa 196,12,172\nDS 1,20,43,3072\nMvaa 196,8,168\nv_add_u32 v1,0x18000,v1\nv_mov_b32 v66,0\nMvaa 196,4,164\nv_add_u32 v3,s47,v0\n"
"v_mov_b32 v67,0\nMvaa 196,0,160\ns_waitcnt vmcnt(24) lgkmcnt(0)\ns_barrier\nv_mov_b32 v68,0\nZvaa 200,32,128\nRD 0,3,0\nv_mov_b32 v69,0\n"
"Mvaa 200,36,132\nRD 4,3,1024\nv_mov_b32 v70,0\nMvaa 200,40,136\nRD 8,3,2048\nv_mov_b32 v71,0\nMvaa 200,44,140\nRD 12,3,3072\nv_mov_b32 v72,0\n"
"Mvaa 200,48,144\nRD 16,3,4096\nv_mov_b32 v73,0\nMvaa 200,52,148\nRD 20,3,5120\nv_mov_b32 v74,0\nMvaa 200,56,152\nRD 24,3,6144\nv_mov_b32 v75,0\n"
"Mvaa 200,60,156\nRD 28,3,7168\nv_mov_b32 v76,0\nZvaa 204,60,188\nv_mov_b32 v77,0\nv_mov_b32 v78,0\nMvaa 204,56,184\nv_mov_b32 v79,0\nv_mov_b32 v80,0\n"
"Mvaa 204,52,180\nv_mov_b32 v81,0\nv_mov_b32 v82,0\nMvaa 204,48,176\nv_mov_b32 v83,0\nv_mov_b32 v84,0\nMvaa 204,44,172\nv_mov_b32 v85,0\n"
"v_mov_b32 v86,0\nMvaa 204,40,168\nv_mov_b32 v87,0\nv_mov_b32 v88,0\nMvaa 204,36,164\nWL 0\nv_mov_b32 v89,0\nv_mov_b32 v90,0\nMvaa 204,32,160\n"
"v_mov_b32 v91,0\nv_mov_b32 v92,0\ns_waitcnt vmcnt(8)\nv_mov_b32 v13,s65\ns_mov_b64 exec,1\nbuffer_store_dword v13,off,s[24:27],s5 sc1\n"
"s_mov_b64 exec,-1\ns_nop 3\nv_max3_f32 v238,v176,v177,v178\nv_max3_f32 v240,v180,v181,v182\nv_max3_f32 v238,v238,v179,v184\n"
"v_max3_f32 v240,v240,v183,v188\nv_max3_f32 v238,v238,v185,v186\nv_max3_f32 v240,v240,v189,v190\nv_max3_f32 v238,v238,v187,v192\n"
"v_max3_f32 v240,v240,v191,v196\nv_max3_f32 v238,v238,v193,v194\nv_max3_f32 v240,v240,v197,v198\nv_max3_f32 v238,v238,v195,v200\n"
"v_max3_f32 v240,v240,v199,v204\nv_max3_f32 v238,v238,v201,v202\nv_max3_f32 v240,v240,v205,v206\nv_max_f32 v238,v238,v203\nv_max_f32 v240,v240,v207\n"
"v_mov_b32 v239,v238\nv_mov_b32 v241,v240\ns_nop 0\nv_permlane32_swap_b32 v238,v239\nv_permlane32_swap_b32 v240,v241\nv_max_f32 v238,v238,v239\n"
"v_max_f32 v240,v240,v241\nv_mov_b32 v239,v238\nv_mov_b32 v241,v240\ns_nop 0\nv_permlane16_swap_b32 v238,v239\nv_permlane16_swap_b32 v240,v241\n"
"v_max_f32 v238,v238,v239\nv_max_f32 v240,v240,v241\nv_mov_b32 v232,v238\nv_mov_b32 v233,v240\nv_sub_f32 v224,0,v232\nv_sub_f32 v228,0,v233\n"
"v_sub_f32 v225,0,v232\nv_sub_f32 v229,0,v233\nv_sub_f32 v226,0,v232\nv_sub_f32 v230,0,v233\nv_sub_f32 v227,0,v232\nv_sub_f32 v231,0,v233\n"
"v_sub_f32 v176,v176,v238\nv_sub_f32 v180,v180,v240\nv_sub_f32 v177,v177,v238\nv_sub_f32 v181,v181,v240\nv_sub_f32 v178,v178,v238\n"
"v_sub_f32 v182,v182,v240\nv_sub_f32 v179,v179,v238\nv_sub_f32 v183,v183,v240\nv_sub_f32 v184,v184,v238\nv_sub_f32 v188,v188,v240\n"
"v_sub_f32 v185,v185,v238\nv_sub_f32 v189,v189,v240\nv_sub_f32 v186,v186,v238\nv_sub_f32 v190,v190,v240\nv_sub_f32 v187,v187,v238\n"
"v_sub_f32 v191,v191,v240\nv_sub_f32 v192,v192,v238\nv_sub_f32 v196,v196,v240\nv_sub_f32 v193,v193,v238\nv_sub_f32 v197,v197,v240\n"
"v_sub_f32 v194,v194,v238\nv_sub_f32 v198,v198,v240\nv_sub_f32 v195,v195,v238\nv_sub_f32 v199,v199,v240\nv_sub_f32 v200,v200,v238\n"
"v_sub_f32 v204,v204,v240\nv_sub_f32 v201,v201,v238\nv_sub_f32 v205,v205,v240\nv_sub_f32 v202,v202,v238\nv_sub_f32 v206,v206,v240\n"
"v_sub_f32 v203,v203,v238\nv_sub_f32 v207,v207,v240\nv_exp_f32 v176,v176\nv_exp_f32 v177,v177\nv_add_f32 v234,v176,v234\nv_exp_f32 v178,v178\n"
"v_add_f32 v234,v177,v234\nv_exp_f32 v179,v179\nv_add_f32 v234,v178,v234\nv_exp_f32 v180,v180\nv_add_f32 v234,v179,v234\nv_exp_f32 v181,v181\n"
"v_add_f32 v235,v180,v235\nv_exp_f32 v182,v182\nv_add_f32 v235,v181,v235\nv_exp_f32 v183,v183\nv_add_f32 v235,v182,v235\nv_exp_f32 v184,v184\n"
"v_add_f32 v235,v183,v235\nv_exp_f32 v185,v185\nv_exp_f32 v186,v186\nv_exp_f32 v187,v187\nv_exp_f32 v188,v188\nv_exp_f32 v189,v189\nv_exp_f32 v190,v190\n"
"v_exp_f32 v191,v191\ns_sub_u32 s62,s61,3\ns_cmp_lg_u32 s62,0\ns_cbranch_scc1 fused_op_h0\ns_setpc_b64 s[50:51]\nfused_op_h0:\nNvaa 144,0,128,224\n"
"RD 32,3,8192\nCV 208,176,177\nMvaa 144,4,132\nRD 36,3,9216\nCV 209,178,179\nMvaa 144,8,136\nRD 40,3,10240\nCV 216,180,181\nMvaa 144,12,140\n"
"s_sub_u32 s46,s46,1\nRD 44,3,11264\nCV 217,182,183\nMvaa 144,16,144\ns_cbranch_scc1 fused_op_ev12\nfused_op_evb13:\nRD 48,3,12288\n"
"v_add_f32 v234,v184,v234\nMvaa 144,20,148\nRD 52,3,13312\nv_add_f32 v234,v185,v234\nMvaa 144,24,152\nRD 56,3,14336\nCV 210,184,185\nMvaa 144,28,156\n"
"s_add_u32 m0,s48,s44\nRD 60,3,15360\nv_add_f32 v234,v186,v234\nNvaa 148,28,188,228\nv_exp_f32 v192,v192\nMvaa 148,24,184\nDS 1,20,36,0\n"
"Mvaa 148,20,180\nv_exp_f32 v193,v193\nMvaa 148,16,176\nv_exp_f32 v194,v194\nMvaa 148,12,172\nDS 1,20,37,1024\nMvaa 148,8,168\nv_exp_f32 v195,v195\n"
"Mvaa 148,4,164\nv_exp_f32 v196,v196\nMvaa 148,0,160\nWL 0\nDS 1,20,38,2048\nNvaa 152,32,128,224\nRD 0,3,16384\nv_add_f32 v234,v187,v234\n"
"Mvaa 152,36,132\nRD 4,3,17408\nCV 211,186,187\nMvaa 152,40,136\nRD 8,3,18432\nv_add_f32 v235,v188,v235\nMvaa 152,44,140\nRD 12,3,19456\n"
"v_add_f32 v235,v189,v235\nMvaa 152,48,144\nRD 16,3,20480\nCV 218,188,189\nMvaa 152,52,148\nRD 20,3,21504\nv_add_f32 v235,v190,v235\nMvaa 152,56,152\n"
"RD 24,3,22528\nv_add_f32 v235,v191,v235\nMvaa 152,60,156\nRD 28,3,23552\nCV 219,190,191\nNvaa 156,60,188,228\nv_exp_f32 v197,v197\nMvaa 156,56,184\n"
"DS 1,20,39,3072\nMvaa 156,52,180\ns_add_u32 m0,m0,0x1000\nv_exp_f32 v198,v198\nMvaa 156,48,176\nv_exp_f32 v199,v199\nMvaa 156,44,172\nDS 1,20,40,0\n"
"Mvaa 156,40,168\nv_exp_f32 v200,v200\nMvaa 156,36,164\nv_exp_f32 v201,v201\nMvaa 156,32,160\nWL 0\nDS 1,20,41,1024\nNvaa 160,0,128,224\nRD 32,3,24576\n"
"v_add_f32 v234,v192,v234\nMvaa 160,4,132\ns_mov_b32 s44,s47\nRD 36,3,25600\nv_add_f32 v234,v193,v234\nMvaa 160,8,136\ns_add_u32 s47,s47,0x8000\n"
"RD 40,3,26624\nCV 212,192,193\nMvaa 160,12,140\ns_cmp_eq_u32 s47,0x28000\nRD 44,3,27648\nv_add_f32 v234,v194,v234\nMvaa 160,16,144\n"
"s_cselect_b32 s47,0,s47\nRD 48,3,28672\nv_add_f32 v234,v195,v234\nMvaa 160,20,148\nRD 52,3,29696\nCV 213,194,195\nMvaa 160,24,152\nRD 56,3,30720\n"
"v_add_f32 v235,v196,v235\nMvaa 160,28,156\nRD 60,3,31744\nv_add_f32 v235,v197,v235\nNvaa 164,28,188,228\nv_exp_f32 v202,v202\nMvaa 164,24,184\n"
"DS 1,20,42,2048\nMvaa 164,20,180\nv_exp_f32 v203,v203\nMvaa 164,16,176\nv_exp_f32 v204,v204\nMvaa 164,12,172\nDS 1,20,43,3072\nMvaa 164,8,168\n"
"v_add_u32 v1,0xffff8000,v1\nCV 220,196,197\nMvaa 164,4,164\nv_add_u32 v3,s47,v0\nv_add_f32 v235,v198,v235\nMvaa 164,0,160\n"
"s_waitcnt vmcnt(24) lgkmcnt(0)\ns_barrier\nv_add_f32 v235,v199,v235\nNvaa 168,32,128,224\nRD 0,3,0\nCV 221,198,199\nMvaa 168,36,132\nRD 4,3,1024\n"
"v_add_f32 v234,v200,v234\nMvaa 168,40,136\nRD 8,3,2048\nv_add_f32 v234,v201,v234\nMvaa 168,44,140\nRD 12,3,3072\nCV 214,200,201\nMvaa 168,48,144\n"
"RD 16,3,4096\nv_add_f32 v234,v202,v234\nMvaa 168,52,148\nRD 20,3,5120\nv_add_f32 v234,v203,v234\nMvaa 168,56,152\nRD 24,3,6144\nCV 215,202,203\n"
"Mvaa 168,60,156\nRD 28,3,7168\nv_add_f32 v235,v204,v235\nNvaa 172,60,188,228\nv_exp_f32 v205,v205\nMvaa 172,56,184\nv_exp_f32 v206,v206\n"
"Mvaa 172,52,180\nv_exp_f32 v207,v207\nMvaa 172,48,176\nv_add_f32 v235,v205,v235\nCV 222,204,205\nMvaa 172,44,172\nv_add_f32 v235,v206,v235\n"
"CV 223,206,207\nMvaa 172,40,168\nv_add_f32 v235,v207,v235\nMvaa 172,36,164\nWL 0\nMvaa 172,32,160\nMvav 16,0,208\nRD 32,3,8192\nMvav 16,4,212\n"
"RD 36,3,9216\nv_max3_f32 v238,v144,v145,v146\nMvav 20,4,220\nRD 40,3,10240\nv_max3_f32 v239,v147,v148,v149\nMvav 20,0,216\nRD 44,3,11264\n"
"v_max3_f32 v238,v238,v150,v151\nMvav 24,8,208\nRD 48,3,12288\nv_max3_f32 v239,v239,v152,v153\nMvav 24,12,212\nRD 52,3,13312\n"
"v_max3_f32 v238,v238,v154,v155\nMvav 28,12,220\nRD 56,3,14336\nv_max3_f32 v239,v239,v156,v157\nMvav 28,8,216\ns_add_u32 m0,s48,s44\nRD 60,3,15360\n"
"v_max3_f32 v238,v238,v158,v159\nMvav 32,16,208\nv_max3_f32 v239,v239,v160,v161\nv_max3_f32 v238,v238,v162,v163\nMvav 32,20,212\nDS 1,20,36,0\n"
"Mvav 36,20,220\nv_max3_f32 v239,v239,v164,v165\nv_max3_f32 v238,v238,v166,v167\nMvav 36,16,216\nv_max3_f32 v239,v239,v168,v169\n"
"v_max3_f32 v238,v238,v170,v171\nMvav 40,24,208\nDS 1,20,37,1024\nMvav 40,28,212\nv_max3_f32 v239,v239,v172,v173\nv_max3_f32 v238,v238,v174,v175\n"
"Mvav 44,28,220\nv_max_f32 v238,v238,v239\nMvav 44,24,216\nWL 0\nDS 1,20,38,2048\nMvav 48,32,208\nRD 0,3,16384\nv_cmp_lt_f32 vcc,s67,v238\n"
"Mvav 48,36,212\ns_cbranch_vccnz fused_op_rm16\nfused_op_rmb17:\nRD 4,3,17408\nMvav 52,36,220\nRD 8,3,18432\nMvav 52,32,216\nRD 12,3,19456\n"
"Mvav 56,40,208\nRD 16,3,20480\nMvav 56,44,212\nRD 20,3,21504\nMvav 60,44,220\nRD 24,3,22528\nMvav 60,40,216\nRD 28,3,23552\nMvav 64,48,208\n"
"v_exp_f32 v144,v144\nMvav 64,52,212\nDS 1,20,39,3072\nMvav 68,52,220\ns_add_u32 m0,m0,0x1000\nv_exp_f32 v145,v145\nMvav 68,48,216\nv_exp_f32 v146,v146\n"
"Mvav 72,56,208\nDS 1,20,40,0\nMvav 72,60,212\nv_exp_f32 v147,v147\nMvav 76,60,220\nv_exp_f32 v148,v148\nMvav 76,56,216\nWL 0\nDS 1,20,41,1024\n"
"Mvav 80,0,208\nRD 32,3,24576\nv_add_f32 v234,v144,v234\nMvav 80,4,212\ns_mov_b32 s44,s47\nRD 36,3,25600\nv_add_f32 v234,v145,v234\nMvav 84,4,220\n"
"s_add_u32 s47,s47,0x8000\nRD 40,3,26624\nv_add_f32 v234,v146,v234\nMvav 84,0,216\ns_cmp_eq_u32 s47,0x28000\nRD 44,3,27648\nv_add_f32 v234,v147,v234\n"
"Mvav 88,8,208\ns_cselect_b32 s47,0,s47\nRD 48,3,28672\nv_add_f32 v235,v148,v235\nMvav 88,12,212\nRD 52,3,29696\nMvav 92,12,220\nRD 56,3,30720\n"
"Mvav 92,8,216\nRD 60,3,31744\nMvav 96,16,208\nv_exp_f32 v149,v149\nMvav 96,20,212\nDS 1,20,42,2048\nMvav 100,20,220\nv_exp_f32 v150,v150\n"
"Mvav 100,16,216\nv_exp_f32 v151,v151\nMvav 104,24,208\nDS 1,20,43,3072\nMvav 104,28,212\nv_add_u32 v1,0x18000,v1\nv_add_f32 v235,v149,v235\n"
"Mvav 108,28,220\nv_add_u32 v3,s47,v0\nv_add_f32 v235,v150,v235\nMvav 108,24,216\ns_waitcnt vmcnt(24) lgkmcnt(0)\ns_barrier\nv_add_f32 v235,v151,v235\n"
"Mvav 112,32,208\nRD 0,3,0\nMvav 112,36,212\nRD 4,3,1024\nMvav 116,36,220\nRD 8,3,2048\nMvav 116,32,216\nRD 12,3,3072\nMvav 120,40,208\nRD 16,3,4096\n"
"Mvav 120,44,212\nRD 20,3,5120\nMvav 124,44,220\nRD 24,3,6144\nMvav 124,40,216\nRD 28,3,7168\nMvav 128,48,208\nv_exp_f32 v152,v152\nMvav 128,52,212\n"
"v_exp_f32 v153,v153\nMvav 132,52,220\ns_sub_u32 s62,s62,1\nv_exp_f32 v154,v154\nMvav 132,48,216\nv_exp_f32 v155,v155\nMvav 136,56,208\n"
"v_exp_f32 v156,v156\nMvav 136,60,212\ns_cmp_lg_u32 s64,0\nv_exp_f32 v157,v157\nMvav 140,60,220\nWL 0\nv_exp_f32 v158,v158\nMvav 140,56,216\n"
"s_cbranch_scc1 fused_op_rs18\nfused_op_rsb19:\nv_exp_f32 v159,v159\nfused_op_h1:\nNvaa 176,0,128,224\nRD 32,3,8192\nCV 208,144,145\nMvaa 176,4,132\n"
"RD 36,3,9216\nCV 209,146,147\nMvaa 176,8,136\nRD 40,3,10240\nCV 216,148,149\nMvaa 176,12,140\ns_sub_u32 s46,s46,1\nRD 44,3,11264\nCV 217,150,151\n"
"Mvaa 176,16,144\ns_cbranch_scc1 fused_op_ev14\nfused_op_evb15:\nRD 48,3,12288\nv_add_f32 v234,v152,v234\nMvaa 176,20,148\nRD 52,3,13312\n"
"v_add_f32 v234,v153,v234\nMvaa 176,24,152\nRD 56,3,14336\nCV 210,152,153\nMvaa 176,28,156\ns_add_u32 m0,s48,s44\nRD 60,3,15360\n"
"v_add_f32 v234,v154,v234\nNvaa 180,28,188,228\nv_exp_f32 v160,v160\nMvaa 180,24,184\nDS 1,20,36,0\nMvaa 180,20,180\nv_exp_f32 v161,v161\n"
"Mvaa 180,16,176\nv_exp_f32 v162,v162\nMvaa 180,12,172\nDS 1,20,37,1024\nMvaa 180,8,168\nv_exp_f32 v163,v163\nMvaa 180,4,164\nv_exp_f32 v164,v164\n"
"Mvaa 180,0,160\nWL 0\nDS 1,20,38,2048\nNvaa 184,32,128,224\nRD 0,3,16384\nv_add_f32 v234,v155,v234\nMvaa 184,36,132\nRD 4,3,17408\nCV 211,154,155\n"
"Mvaa 184,40,136\nRD 8,3,18432\nv_add_f32 v235,v156,v235\nMvaa 184,44,140\nRD 12,3,19456\nv_add_f32 v235,v157,v235\nMvaa 184,48,144\nRD 16,3,20480\n"
"CV 218,156,157\nMvaa 184,52,148\nRD 20,3,21504\nv_add_f32 v235,v158,v235\nMvaa 184,56,152\nRD 24,3,22528\nv_add_f32 v235,v159,v235\nMvaa 184,60,156\n"
"RD 28,3,23552\nCV 219,158,159\nNvaa 188,60,188,228\nv_exp_f32 v165,v165\nMvaa 188,56,184\nDS 1,20,39,3072\nMvaa 188,52,180\ns_add_u32 m0,m0,0x1000\n"
"v_exp_f32 v166,v166\nMvaa 188,48,176\nv_exp_f32 v167,v167\nMvaa 188,44,172\nDS 1,20,40,0\nMvaa 188,40,168\nv_exp_f32 v168,v168\nMvaa 188,36,164\n"
"v_exp_f32 v169,v169\nMvaa 188,32,160\nWL 0\nDS 1,20,41,1024\nNvaa 192,0,128,224\nRD 32,3,24576\nv_add_f32 v234,v160,v234\nMvaa 192,4,132\n"
"s_mov_b32 s44,s47\nRD 36,3,25600\nv_add_f32 v234,v161,v234\nMvaa 192,8,136\ns_add_u32 s47,s47,0x8000\nRD 40,3,26624\nCV 212,160,161\nMvaa 192,12,140\n"
"s_cmp_eq_u32 s47,0x28000\nRD 44,3,27648\nv_add_f32 v234,v162,v234\nMvaa 192,16,144\ns_cselect_b32 s47,0,s47\nRD 48,3,28672\nv_add_f32 v234,v163,v234\n"
"Mvaa 192,20,148\nRD 52,3,29696\nCV 213,162,163\nMvaa 192,24,152\nRD 56,3,30720\nv_add_f32 v235,v164,v235\nMvaa 192,28,156\nRD 60,3,31744\n"
"v_add_f32 v235,v165,v235\nNvaa 196,28,188,228\nv_exp_f32 v170,v170\nMvaa 196,24,184\nDS 1,20,42,2048\nMvaa 196,20,180\nv_exp_f32 v171,v171\n"
"Mvaa 196,16,176\nv_exp_f32 v172,v172\nMvaa 196,12,172\nDS 1,20,43,3072\nMvaa 196,8,168\nv_add_u32 v1,0xffff8000,v1\nCV 220,164,165\nMvaa 196,4,164\n"
"v_add_u32 v3,s47,v0\nv_add_f32 v235,v166,v235\nMvaa 196,0,160\ns_waitcnt vmcnt(24) lgkmcnt(0)\ns_barrier\nv_add_f32 v235,v167,v235\n"
"Nvaa 200,32,128,224\nRD 0,3,0\nCV 221,166,167\nMvaa 200,36,132\nRD 4,3,1024\nv_add_f32 v234,v168,v234\nMvaa 200,40,136\nRD 8,3,2048\n"
"v_add_f32 v234,v169,v234\nMvaa 200,44,140\nRD 12,3,3072\nCV 214,168,169\nMvaa 200,48,144\nRD 16,3,4096\nv_add_f32 v234,v170,v234\nMvaa 200,52,148\n"
"RD 20,3,5120\nv_add_f32 v234,v171,v234\nMvaa 200,56,152\nRD 24,3,6144\nCV 215,170,171\nMvaa 200,60,156\nRD 28,3,7168\nv_add_f32 v235,v172,v235\n"
"Nvaa 204,60,188,228\nv_exp_f32 v173,v173\nMvaa 204,56,184\nv_exp_f32 v174,v174\nMvaa 204,52,180\nv_exp_f32 v175,v175\nMvaa 204,48,176\n"
"v_add_f32 v235,v173,v235\nCV 222,172,173\nMvaa 204,44,172\nv_add_f32 v235,v174,v235\nCV 223,174,175\nMvaa 204,40,168\nv_add_f32 v235,v175,v235\n"
"Mvaa 204,36,164\nWL 0\nMvaa 204,32,160\nMvav 16,0,208\nRD 32,3,8192\nMvav 16,4,212\nRD 36,3,9216\nv_max3_f32 v238,v176,v177,v178\nMvav 20,4,220\n"
"RD 40,3,10240\nv_max3_f32 v239,v179,v180,v181\nMvav 20,0,216\nRD 44,3,11264\nv_max3_f32 v238,v238,v182,v183\nMvav 24,8,208\nRD 48,3,12288\n"
"v_max3_f32 v239,v239,v184,v185\nMvav 24,12,212\nRD 52,3,13312\nv_max3_f32 v238,v238,v186,v187\nMvav 28,12,220\nRD 56,3,14336\n"
"v_max3_f32 v239,v239,v188,v189\nMvav 28,8,216\ns_add_u32 m0,s48,s44\nRD 60,3,15360\nv_max3_f32 v238,v238,v190,v191\nMvav 32,16,208\n"
"v_max3_f32 v239,v239,v192,v193\nv_max3_f32 v238,v238,v194,v195\nMvav 32,20,212\nDS 1,20,36,0\nMvav 36,20,220\nv_max3_f32 v239,v239,v196,v197\n"
"v_max3_f32 v238,v238,v198,v199\nMvav 36,16,216\nv_max3_f32 v239,v239,v200,v201\nv_max3_f32 v238,v238,v202,v203\nMvav 40,24,208\nDS 1,20,37,1024\n"
"Mvav 40,28,212\nv_max3_f32 v239,v239,v204,v205\nv_max3_f32 v238,v238,v206,v207\nMvav 44,28,220\nv_max_f32 v238,v238,v239\nMvav 44,24,216\nWL 0\n"
"DS 1,20,38,2048\nMvav 48,32,208\nRD 0,3,16384\nv_cmp_lt_f32 vcc,s67,v238\nMvav 48,36,212\ns_cbranch_vccnz fused_op_rm20\nfused_op_rmb21:\nRD 4,3,17408\n"
"Mvav 52,36,220\nRD 8,3,18432\nMvav 52,32,216\nRD 12,3,19456\nMvav 56,40,208\nRD 16,3,20480\nMvav 56,44,212\nRD 20,3,21504\nMvav 60,44,220\n"
"RD 24,3,22528\nMvav 60,40,216\nRD 28,3,23552\nMvav 64,48,208\nv_exp_f32 v176,v176\nMvav 64,52,212\nDS 1,20,39,3072\nMvav 68,52,220\n"
"s_add_u32 m0,m0,0x1000\nv_exp_f32 v177,v177\nMvav 68,48,216\nv_exp_f32 v178,v178\nMvav 72,56,208\nDS 1,20,40,0\nMvav 72,60,212\nv_exp_f32 v179,v179\n"
"Mvav 76,60,220\nv_exp_f32 v180,v180\nMvav 76,56,216\nWL 0\nDS 1,20,41,1024\nMvav 80,0,208\nRD 32,3,24576\nv_add_f32 v234,v176,v234\nMvav 80,4,212\n"
"s_mov_b32 s44,s47\nRD 36,3,25600\nv_add_f32 v234,v177,v234\nMvav 84,4,220\ns_add_u32 s47,s47,0x8000\nRD 40,3,26624\nv_add_f32 v234,v178,v234\n"
"Mvav 84,0,216\ns_cmp_eq_u32 s47,0x28000\nRD 44,3,27648\nv_add_f32 v234,v179,v234\nMvav 88,8,208\ns_cselect_b32 s47,0,s47\nRD 48,3,28672\n"
"v_add_f32 v235,v180,v235\nMvav 88,12,212\nRD 52,3,29696\nMvav 92,12,220\nRD 56,3,30720\nMvav 92,8,216\nRD 60,3,31744\nMvav 96,16,208\n"
"v_exp_f32 v181,v181\nMvav 96,20,212\nDS 1,20,42,2048\nMvav 100,20,220\nv_exp_f32 v182,v182\nMvav 100,16,216\nv_exp_f32 v183,v183\nMvav 104,24,208\n"
"DS 1,20,43,3072\nMvav 104,28,212\nv_add_u32 v1,0x18000,v1\nv_add_f32 v235,v181,v235\nMvav 108,28,220\nv_add_u32 v3,s47,v0\nv_add_f32 v235,v182,v235\n"
"Mvav 108,24,216\ns_waitcnt vmcnt(24) lgkmcnt(0)\ns_barrier\nv_add_f32 v235,v183,v235\nMvav 112,32,208\nRD 0,3,0\nMvav 112,36,212\nRD 4,3,1024\n"
"Mvav 116,36,220\nRD 8,3,2048\nMvav 116,32,216\nRD 12,3,3072\nMvav 120,40,208\nRD 16,3,4096\nMvav 120,44,212\nRD 20,3,5120\nMvav 124,44,220\n"
"RD 24,3,6144\nMvav 124,40,216\nRD 28,3,7168\nMvav 128,48,208\nv_exp_f32 v184,v184\nMvav 128,52,212\nv_exp_f32 v185,v185\nMvav 132,52,220\n"
"s_sub_u32 s62,s62,1\nv_exp_f32 v186,v186\nMvav 132,48,216\nv_exp_f32 v187,v187\nMvav 136,56,208\nv_exp_f32 v188,v188\nMvav 136,60,212\n"
"s_cmp_lg_u32 s64,0\nv_exp_f32 v189,v189\nMvav 140,60,220\nWL 0\nv_exp_f32 v190,v190\nMvav 140,56,216\ns_cbranch_scc1 fused_op_rs22\nfused_op_rsb23:\n"
"v_exp_f32 v191,v191\ns_cmp_lg_u32 s62,0\ns_cbranch_scc1 fused_op_h0\ns_setpc_b64 s[50:51]\nfused_op_tk:\nNvaa 144,0,128,224\nRD 32,3,8192\n"
"CV 208,176,177\nMvaa 144,4,132\nRD 36,3,9216\nCV 209,178,179\nMvaa 144,8,136\nRD 40,3,10240\nv_add_u32 v1,0xffff8000,v1\nMvaa 144,12,140\n"
"RD 44,3,11264\nCV 216,180,181\nMvaa 144,16,144\nRD 48,3,12288\nCV 217,182,183\nMvaa 144,20,148\nRD 52,3,13312\nv_add_f32 v234,v184,v234\n"
"Mvaa 144,24,152\nRD 56,3,14336\nv_add_f32 v234,v185,v234\nMvaa 144,28,156\ns_add_u32 m0,s48,s44\nRD 60,3,15360\nCV 210,184,185\nNvaa 148,28,188,228\n"
"v_exp_f32 v192,v192\nMvaa 148,24,184\nDS 1,20,36,0\nMvaa 148,20,180\nv_exp_f32 v193,v193\nMvaa 148,16,176\nv_exp_f32 v194,v194\nMvaa 148,12,172\n"
"DS 1,20,37,1024\nMvaa 148,8,168\nv_exp_f32 v195,v195\nMvaa 148,4,164\nv_exp_f32 v196,v196\nMvaa 148,0,160\nWL 0\nDS 1,20,38,2048\nNvaa 152,32,128,224\n"
"RD 0,3,16384\nv_add_f32 v234,v186,v234\nMvaa 152,36,132\nRD 4,3,17408\nv_add_f32 v234,v187,v234\nMvaa 152,40,136\nRD 8,3,18432\nCV 211,186,187\n"
"Mvaa 152,44,140\nRD 12,3,19456\nv_add_f32 v235,v188,v235\nMvaa 152,48,144\nRD 16,3,20480\nv_add_f32 v235,v189,v235\nMvaa 152,52,148\nRD 20,3,21504\n"
"CV 218,188,189\nMvaa 152,56,152\nRD 24,3,22528\nv_add_f32 v235,v190,v235\nMvaa 152,60,156\nRD 28,3,23552\nv_add_f32 v235,v191,v235\n"
"Nvaa 156,60,188,228\nv_exp_f32 v197,v197\nMvaa 156,56,184\nDS 1,20,39,3072\nMvaa 156,52,180\ns_add_u32 m0,m0,0x1000\nv_exp_f32 v198,v198\n"
"Mvaa 156,48,176\nv_exp_f32 v199,v199\nMvaa 156,44,172\nDS 1,20,40,0\nMvaa 156,40,168\nv_exp_f32 v200,v200\nMvaa 156,36,164\nv_exp_f32 v201,v201\n"
"Mvaa 156,32,160\nWL 0\nDS 1,20,41,1024\nNvaa 160,0,128,224\nRD 32,3,24576\nCV 219,190,191\nMvaa 160,4,132\ns_mov_b32 s44,s47\nRD 36,3,25600\n"
"v_add_f32 v234,v192,v234\nMvaa 160,8,136\ns_add_u32 s47,s47,0x8000\nRD 40,3,26624\nv_add_f32 v234,v193,v234\nMvaa 160,12,140\ns_cmp_eq_u32 s47,0x28000\n"
"RD 44,3,27648\nCV 212,192,193\nMvaa 160,16,144\ns_cselect_b32 s47,0,s47\nRD 48,3,28672\nv_add_f32 v234,v194,v234\nMvaa 160,20,148\nRD 52,3,29696\n"
"v_add_f32 v234,v195,v234\nMvaa 160,24,152\nRD 56,3,30720\nCV 213,194,195\nMvaa 160,28,156\nRD 60,3,31744\nv_add_f32 v235,v196,v235\n"
"Nvaa 164,28,188,228\nv_exp_f32 v202,v202\nMvaa 164,24,184\nDS 1,20,42,2048\nMvaa 164,20,180\nv_exp_f32 v203,v203\nMvaa 164,16,176\nv_exp_f32 v204,v204\n"
"Mvaa 164,12,172\nDS 1,20,43,3072\nMvaa 164,8,168\nv_add_u32 v1,s6,v0\nv_add_f32 v235,v197,v235\nMvaa 164,4,164\nv_add_u32 v3,s47,v0\nCV 220,196,197\n"
"Mvaa 164,0,160\ns_waitcnt vmcnt(24) lgkmcnt(0)\ns_barrier\nv_add_f32 v235,v198,v235\nNvaa 168,32,128,224\nRD 0,3,0\nv_add_f32 v235,v199,v235\n"
"Mvaa 168,36,132\nRD 4,3,1024\nCV 221,198,199\nMvaa 168,40,136\nRD 8,3,2048\nv_add_f32 v234,v200,v234\nMvaa 168,44,140\nRD 12,3,3072\n"
"v_add_f32 v234,v201,v234\nMvaa 168,48,144\nRD 16,3,4096\nCV 214,200,201\nMvaa 168,52,148\nRD 20,3,5120\nv_add_f32 v234,v202,v234\nMvaa 168,56,152\n"
"RD 24,3,6144\nv_add_f32 v234,v203,v234\nMvaa 168,60,156\nRD 28,3,7168\nCV 215,202,203\nNvaa 172,60,188,228\nv_exp_f32 v205,v205\nMvaa 172,56,184\n"
"v_exp_f32 v206,v206\nMvaa 172,52,180\nv_exp_f32 v207,v207\nMvaa 172,48,176\nv_add_f32 v235,v204,v235\nCV 222,204,205\nMvaa 172,44,172\n"
"v_add_f32 v235,v205,v235\nCV 223,206,207\nMvaa 172,40,168\nv_add_f32 v235,v206,v235\nMvaa 172,36,164\nWL 0\nv_add_f32 v235,v207,v235\nMvaa 172,32,160\n"
"Mvav 16,0,208\nRD 32,3,8192\nMvav 16,4,212\nRD 36,3,9216\nv_add_f32 v240,0x73840000,v246\nv_add_f32 v241,0x73780000,v246\nMvav 20,4,220\nRD 40,3,10240\n"
"v_min_f32 v148,v240,v148\nv_min_f32 v149,v241,v149\nMvav 20,0,216\nRD 44,3,11264\nv_add_f32 v242,0x73680000,v246\nv_add_f32 v243,0x73580000,v246\n"
"Mvav 24,8,208\nRD 48,3,12288\nv_min_f32 v150,v242,v150\nv_min_f32 v151,v243,v151\nMvav 24,12,212\nRD 52,3,13312\nv_add_f32 v240,0x71000000,v246\n"
"v_add_f32 v241,0xf1000000,v246\nMvav 28,12,220\nRD 56,3,14336\nv_min_f32 v144,v240,v144\nv_min_f32 v156,v240,v156\nMvav 28,8,216\ns_add_u32 m0,s48,s44\n"
"RD 60,3,15360\nv_min_f32 v145,v241,v145\nv_min_f32 v157,v241,v157\nMvav 32,16,208\nv_add_f32 v242,0xf1c00000,v246\nv_add_f32 v243,0xf2200000,v246\n"
"v_add_f32 v240,0xf3780000,v246\nMvav 32,20,212\nDS 1,20,36,0\nv_min_f32 v146,v242,v146\nMvav 36,20,220\nv_min_f32 v158,v242,v158\n"
"v_min_f32 v147,v243,v147\nv_min_f32 v159,v243,v159\nMvav 36,16,216\nv_min_f32 v152,v240,v152\nv_min_f32 v164,v240,v164\nv_add_f32 v241,0xf3840000,v246\n"
"Mvav 40,24,208\nDS 1,20,37,1024\nv_min_f32 v153,v241,v153\nMvav 40,28,212\nv_min_f32 v165,v241,v165\nv_add_f32 v242,0xf38c0000,v246\n"
"v_add_f32 v243,0xf3940000,v246\nMvav 44,28,220\nv_min_f32 v154,v242,v154\nv_min_f32 v166,v242,v166\nv_min_f32 v155,v243,v155\nMvav 44,24,216\nWL 0\n"
"DS 1,20,38,2048\nv_min_f32 v167,v243,v167\nMvav 48,32,208\nRD 0,3,16384\nv_add_f32 v240,0xf3fc0000,v246\nv_add_f32 v241,0xf4020000,v246\n"
"Mvav 48,36,212\nRD 4,3,17408\nv_min_f32 v160,v240,v160\nv_min_f32 v172,v240,v172\nMvav 52,36,220\nRD 8,3,18432\nv_min_f32 v161,v241,v161\n"
"v_min_f32 v173,v241,v173\nMvav 52,32,216\nRD 12,3,19456\nv_add_f32 v242,0xf4060000,v246\nv_add_f32 v243,0xf40a0000,v246\nMvav 56,40,208\nRD 16,3,20480\n"
"v_min_f32 v162,v242,v162\nv_min_f32 v174,v242,v174\nMvav 56,44,212\nRD 20,3,21504\nv_min_f32 v163,v243,v163\nv_min_f32 v175,v243,v175\nMvav 60,44,220\n"
"RD 24,3,22528\nv_add_f32 v240,0xf43e0000,v246\nv_add_f32 v241,0xf4420000,v246\nMvav 60,40,216\nRD 28,3,23552\nv_min_f32 v168,v240,v168\n"
"v_min_f32 v169,v241,v169\nMvav 64,48,208\nv_add_f32 v242,0xf4460000,v246\nv_add_f32 v243,0xf44a0000,v246\nv_max3_f32 v238,v144,v145,v146\n"
"Mvav 64,52,212\nDS 1,20,39,3072\nv_min_f32 v170,v242,v170\nMvav 68,52,220\ns_add_u32 m0,m0,0x1000\nv_min_f32 v171,v243,v171\n"
"v_add_f32 v246,0xf4800000,v246\nv_max3_f32 v239,v147,v148,v149\nMvav 68,48,216\nv_max3_f32 v238,v238,v150,v151\nv_max3_f32 v239,v239,v152,v153\n"
"Mvav 72,56,208\nDS 1,20,40,0\nv_max3_f32 v238,v238,v154,v155\nMvav 72,60,212\nv_max3_f32 v239,v239,v156,v157\nv_max3_f32 v238,v238,v158,v159\n"
"Mvav 76,60,220\nv_max3_f32 v239,v239,v160,v161\nv_max3_f32 v238,v238,v162,v163\nMvav 76,56,216\nWL 0\nDS 1,20,41,1024\nv_max3_f32 v239,v239,v164,v165\n"
"Mvav 80,0,208\nRD 32,3,24576\nv_max3_f32 v238,v238,v166,v167\nv_max3_f32 v239,v239,v168,v169\nMvav 80,4,212\ns_mov_b32 s44,s47\nRD 36,3,25600\n"
"v_max3_f32 v238,v238,v170,v171\nv_max3_f32 v239,v239,v172,v173\nMvav 84,4,220\ns_add_u32 s47,s47,0x8000\nRD 40,3,26624\nv_max3_f32 v238,v238,v174,v175\n"
"Mvav 84,0,216\ns_cmp_eq_u32 s47,0x28000\nRD 44,3,27648\nv_max_f32 v238,v238,v239\nMvav 88,8,208\ns_cselect_b32 s47,0,s47\nRD 48,3,28672\n"
"v_cmp_lt_f32 vcc,s67,v238\nMvav 88,12,212\ns_cbranch_vccnz fused_op_rm42\nfused_op_rmb43:\nRD 52,3,29696\nMvav 92,12,220\nRD 56,3,30720\nMvav 92,8,216\n"
"RD 60,3,31744\nMvav 96,16,208\nv_exp_f32 v144,v144\nMvav 96,20,212\nDS 1,20,42,2048\nMvav 100,20,220\nv_exp_f32 v145,v145\nMvav 100,16,216\n"
"v_exp_f32 v146,v146\nMvav 104,24,208\nDS 1,20,43,3072\nMvav 104,28,212\nv_add_u32 v1,0x10000,v1\nv_add_f32 v234,v144,v234\nMvav 108,28,220\n"
"v_add_u32 v3,s47,v0\nv_add_f32 v234,v145,v234\nMvav 108,24,216\ns_waitcnt vmcnt(24) lgkmcnt(0)\ns_barrier\nv_add_f32 v234,v146,v234\nMvav 112,32,208\n"
"RD 0,3,0\nMvav 112,36,212\nRD 4,3,1024\nMvav 116,36,220\nRD 8,3,2048\nMvav 116,32,216\nRD 12,3,3072\nMvav 120,40,208\nRD 16,3,4096\nMvav 120,44,212\n"
"RD 20,3,5120\nMvav 124,44,220\nRD 24,3,6144\nMvav 124,40,216\nRD 28,3,7168\nMvav 128,48,208\nv_exp_f32 v147,v147\nMvav 128,52,212\nv_exp_f32 v148,v148\n"
"Mvav 132,52,220\nv_exp_f32 v149,v149\nMvav 132,48,216\nv_exp_f32 v150,v150\nMvav 136,56,208\nv_exp_f32 v151,v151\nMvav 136,60,212\ns_cmp_lg_u32 s64,0\n"
"v_exp_f32 v152,v152\nMvav 140,60,220\nWL 0\nv_exp_f32 v153,v153\nMvav 140,56,216\ns_cbranch_scc1 fused_op_rs44\nfused_op_rsb45:\nv_exp_f32 v154,v154\n"
"fused_op_t2k:\ns_cmp_lt_u32 s10,2\ns_cbranch_scc1 fused_op_tsk\nNvaa 176,0,128,224\nRD 32,3,8192\nCV 208,144,145\nMvaa 176,4,132\nRD 36,3,9216\n"
"v_add_f32 v234,v147,v234\nMvaa 176,8,136\nRD 40,3,10240\nCV 209,146,147\nMvaa 176,12,140\nRD 44,3,11264\nv_add_f32 v235,v148,v235\nMvaa 176,16,144\n"
"RD 48,3,12288\nv_add_f32 v235,v149,v235\nMvaa 176,20,148\nRD 52,3,13312\nCV 216,148,149\nMvaa 176,24,152\nRD 56,3,14336\nv_add_f32 v235,v150,v235\n"
"Mvaa 176,28,156\ns_add_u32 m0,s48,s44\nRD 60,3,15360\nv_add_f32 v235,v151,v235\nNvaa 180,28,188,228\nv_exp_f32 v155,v155\nMvaa 180,24,184\n"
"DS 1,20,36,0\nMvaa 180,20,180\nv_exp_f32 v156,v156\nMvaa 180,16,176\nv_exp_f32 v157,v157\nMvaa 180,12,172\nDS 1,20,37,1024\nMvaa 180,8,168\n"
"v_exp_f32 v158,v158\nMvaa 180,4,164\nv_exp_f32 v159,v159\nMvaa 180,0,160\nWL 0\nDS 1,20,38,2048\nNvaa 184,32,128,224\nRD 0,3,16384\nCV 217,150,151\n"
"Mvaa 184,36,132\nRD 4,3,17408\nv_add_f32 v234,v152,v234\nMvaa 184,40,136\nRD 8,3,18432\nv_add_f32 v234,v153,v234\nMvaa 184,44,140\nRD 12,3,19456\n"
"CV 210,152,153\nMvaa 184,48,144\nRD 16,3,20480\nv_add_f32 v234,v154,v234\nMvaa 184,52,148\nRD 20,3,21504\nv_add_f32 v234,v155,v234\nMvaa 184,56,152\n"
"RD 24,3,22528\nCV 211,154,155\nMvaa 184,60,156\nRD 28,3,23552\nv_add_f32 v235,v156,v235\nNvaa 188,60,188,228\nv_mov_b32 v15,0\n"
"v_add_f32 v235,v157,v235\nMvaa 188,56,184\nDS 1,20,39,3072\nMvaa 188,52,180\ns_add_u32 m0,m0,0x1000\nbuffer_load_dword v14,v15,s[24:27],s9 offen sc1\n"
"Mvaa 188,48,176\nv_exp_f32 v160,v160\nMvaa 188,44,172\nDS 1,20,40,0\nMvaa 188,40,168\nv_exp_f32 v161,v161\nMvaa 188,36,164\nv_exp_f32 v162,v162\n"
"Mvaa 188,32,160\nWL 0\nDS 1,20,41,1024\nNvaa 192,0,128,224\nRD 32,3,24576\nCV 218,156,157\nMvaa 192,4,132\ns_mov_b32 s44,s47\nRD 36,3,25600\n"
"v_add_f32 v235,v158,v235\nMvaa 192,8,136\ns_add_u32 s47,s47,0x8000\nRD 40,3,26624\nv_add_f32 v235,v159,v235\nMvaa 192,12,140\ns_cmp_eq_u32 s47,0x28000\n"
"RD 44,3,27648\nCV 219,158,159\nMvaa 192,16,144\ns_cselect_b32 s47,0,s47\nRD 48,3,28672\nv_add_f32 v234,v160,v234\nMvaa 192,20,148\nRD 52,3,29696\n"
"v_add_f32 v234,v161,v234\nMvaa 192,24,152\nRD 56,3,30720\nCV 212,160,161\nMvaa 192,28,156\nRD 60,3,31744\nv_add_f32 v234,v162,v234\n"
"Nvaa 196,28,188,228\nv_exp_f32 v163,v163\nMvaa 196,24,184\nDS 1,20,42,2048\nMvaa 196,20,180\nv_exp_f32 v164,v164\nMvaa 196,16,176\nv_exp_f32 v165,v165\n"
"Mvaa 196,12,172\nDS 1,20,43,3072\nMvaa 196,8,168\nv_add_u32 v1,0xffff8000,v1\nv_add_f32 v234,v163,v234\nMvaa 196,4,164\nv_add_u32 v3,s47,v0\n"
"CV 213,162,163\nMvaa 196,0,160\ns_waitcnt vmcnt(25) lgkmcnt(0)\ns_barrier\nv_add_f32 v235,v164,v235\nNvaa 200,32,128,224\nRD 0,3,0\n"
"v_add_f32 v235,v165,v235\nMvaa 200,36,132\nRD 4,3,1024\nCV 220,164,165\nMvaa 200,40,136\nRD 8,3,2048\nMvaa 200,44,140\nRD 12,3,3072\nMvaa 200,48,144\n"
"RD 16,3,4096\nMvaa 200,52,148\nRD 20,3,5120\nMvaa 200,56,152\nRD 24,3,6144\nMvaa 200,60,156\nRD 28,3,7168\nNvaa 204,60,188,228\nv_exp_f32 v166,v166\n"
"Mvaa 204,56,184\nv_exp_f32 v167,v167\nMvaa 204,52,180\nv_exp_f32 v168,v168\nMvaa 204,48,176\nv_exp_f32 v169,v169\nMvaa 204,44,172\nv_exp_f32 v170,v170\n"
"Mvaa 204,40,168\nv_exp_f32 v171,v171\nMvaa 204,36,164\nWL 0\nMvaa 204,32,160\nv_exp_f32 v172,v172\nv_add_f32 v235,v166,v235\nCV 221,166,167\n"
"v_exp_f32 v173,v173\nv_add_f32 v235,v167,v235\nv_add_f32 v234,v168,v234\nv_exp_f32 v174,v174\nv_add_f32 v234,v169,v234\nCV 214,168,169\n"
"v_exp_f32 v175,v175\nv_add_f32 v234,v170,v234\nCV 215,170,171\nv_add_f32 v234,v171,v234\nv_add_f32 v235,v172,v235\nv_add_f32 v235,v173,v235\n"
"CV 222,172,173\nv_add_f32 v235,v174,v235\nCV 223,174,175\nv_add_f32 v235,v175,v235\ns_waitcnt vmcnt(4)\nv_cmp_ne_u32 vcc,s65,v14\ns_nop 0\n"
"s_cbranch_vccnz fused_op_sp46\nfused_op_spb47:\ns_add_u32 s45,s8,16384\nMvav 16,0,208\nRD 32,3,8192\nMvav 16,4,212\nRD 36,3,9216\n"
"v_add_f32 v240,0x73840000,v246\nv_add_f32 v241,0x73780000,v246\nMvav 20,4,220\nRD 40,3,10240\n"
"buffer_load_dwordx4 a[128:131],v0,s[24:27],s8 offen offset:0 sc1\nMvav 20,0,216\nRD 44,3,11264\nv_min_f32 v180,v240,v180\nv_min_f32 v181,v241,v181\n"
"Mvav 24,8,208\nRD 48,3,12288\nv_add_f32 v242,0x73680000,v246\nv_add_f32 v243,0x73580000,v246\nMvav 24,12,212\nRD 52,3,13312\n"
"buffer_load_dwordx4 a[132:135],v0,s[24:27],s8 offen offset:1024 sc1\nMvav 28,12,220\nRD 56,3,14336\nv_min_f32 v182,v242,v182\nv_min_f32 v183,v243,v183\n"
"Mvav 28,8,216\ns_add_u32 m0,s48,s44\nRD 60,3,15360\nv_add_f32 v240,0x71000000,v246\nv_add_f32 v241,0xf1000000,v246\nMvav 32,16,208\n"
"buffer_load_dwordx4 a[136:139],v0,s[24:27],s8 offen offset:2048 sc1\nv_min_f32 v176,v240,v176\nMvav 32,20,212\nDS 1,20,36,0\nv_min_f32 v188,v240,v188\n"
"Mvav 36,20,220\nv_min_f32 v177,v241,v177\nv_min_f32 v189,v241,v189\nv_add_f32 v242,0xf1c00000,v246\nMvav 36,16,216\n"
"buffer_load_dwordx4 a[140:143],v0,s[24:27],s8 offen offset:3072 sc1\nv_min_f32 v178,v242,v178\nMvav 40,24,208\ns_add_u32 s8,s8,4096\nDS 1,20,37,1024\n"
"v_min_f32 v190,v242,v190\nMvav 40,28,212\nv_add_f32 v243,0xf2200000,v246\nv_add_f32 v240,0xf3780000,v246\nv_add_f32 v241,0xf3840000,v246\n"
"Mvav 44,28,220\nbuffer_load_dwordx4 a[144:147],v0,s[24:27],s8 offen offset:0 sc1\nv_min_f32 v179,v243,v179\nMvav 44,24,216\nWL 0\nDS 1,20,38,2048\n"
"v_min_f32 v191,v243,v191\nMvav 48,32,208\nRD 0,3,16384\nv_min_f32 v184,v240,v184\nv_min_f32 v196,v240,v196\nMvav 48,36,212\nRD 4,3,17408\n"
"buffer_load_dwordx4 a[148:151],v0,s[24:27],s8 offen offset:1024 sc1\nMvav 52,36,220\nRD 8,3,18432\nv_min_f32 v185,v241,v185\nv_min_f32 v197,v241,v197\n"
"Mvav 52,32,216\nRD 12,3,19456\nv_add_f32 v242,0xf38c0000,v246\nv_add_f32 v243,0xf3940000,v246\nMvav 56,40,208\nRD 16,3,20480\n"
"buffer_load_dwordx4 a[152:155],v0,s[24:27],s8 offen offset:2048 sc1\nMvav 56,44,212\nRD 20,3,21504\nv_min_f32 v186,v242,v186\nv_min_f32 v198,v242,v198\n"
"Mvav 60,44,220\nRD 24,3,22528\nv_min_f32 v187,v243,v187\nv_min_f32 v199,v243,v199\nMvav 60,40,216\nRD 28,3,23552\nv_add_f32 v240,0xf3fc0000,v246\n"
"v_add_f32 v241,0xf4020000,v246\nMvav 64,48,208\nbuffer_load_dwordx4 a[156:159],v0,s[24:27],s8 offen offset:3072 sc1\nv_min_f32 v192,v240,v192\n"
"Mvav 64,52,212\ns_add_u32 s8,s8,4096\nDS 1,20,39,3072\nv_min_f32 v204,v240,v204\nMvav 68,52,220\ns_add_u32 m0,m0,0x1000\n"
"buffer_load_dwordx4 a[160:163],v0,s[24:27],s8 offen offset:0 sc1\nv_min_f32 v193,v241,v193\nMvav 68,48,216\nv_min_f32 v205,v241,v205\n"
"v_add_f32 v242,0xf4060000,v246\nv_add_f32 v243,0xf40a0000,v246\nMvav 72,56,208\nDS 1,20,40,0\nv_min_f32 v194,v242,v194\nMvav 72,60,212\n"
"buffer_load_dwordx4 a[164:167],v0,s[24:27],s8 offen offset:1024 sc1\nv_min_f32 v206,v242,v206\nMvav 76,60,220\nv_min_f32 v195,v243,v195\n"
"v_min_f32 v207,v243,v207\nv_add_f32 v240,0xf43e0000,v246\nMvav 76,56,216\nWL 0\nDS 1,20,41,1024\nv_min_f32 v200,v240,v200\nMvav 80,0,208\n"
"RD 32,3,24576\nbuffer_load_dwordx4 a[168:171],v0,s[24:27],s8 offen offset:2048 sc1\nMvav 80,4,212\ns_mov_b32 s44,s47\nRD 36,3,25600\n"
"v_add_f32 v241,0xf4420000,v246\nv_add_f32 v242,0xf4460000,v246\nMvav 84,4,220\ns_add_u32 s47,s47,0x8000\nRD 40,3,26624\nv_min_f32 v201,v241,v201\n"
"v_min_f32 v202,v242,v202\nMvav 84,0,216\ns_cmp_eq_u32 s47,0x28000\nRD 44,3,27648\nbuffer_load_dwordx4 a[172:175],v0,s[24:27],s8 offen offset:3072 sc1\n"
"Mvav 88,8,208\ns_cselect_b32 s47,0,s47\nRD 48,3,28672\nv_add_f32 v243,0xf44a0000,v246\nv_max3_f32 v238,v176,v177,v178\nMvav 88,12,212\n"
"s_add_u32 s8,s8,4096\nRD 52,3,29696\nv_min_f32 v203,v243,v203\nv_add_f32 v246,0xf4800000,v246\nMvav 92,12,220\nRD 56,3,30720\n"
"v_max3_f32 v239,v179,v180,v181\nv_max3_f32 v238,v238,v182,v183\nMvav 92,8,216\nRD 60,3,31744\nv_max3_f32 v239,v239,v184,v185\n"
"v_max3_f32 v238,v238,v186,v187\nMvav 96,16,208\nbuffer_load_dwordx4 a[176:179],v0,s[24:27],s8 offen offset:0 sc1\nv_max3_f32 v239,v239,v188,v189\n"
"Mvav 96,20,212\nDS 1,20,42,2048\nv_max3_f32 v238,v238,v190,v191\nMvav 100,20,220\nbuffer_load_dwordx4 a[180:183],v0,s[24:27],s8 offen offset:1024 sc1\n"
"v_max3_f32 v239,v239,v192,v193\nMvav 100,16,216\nv_max3_f32 v238,v238,v194,v195\nv_max3_f32 v239,v239,v196,v197\nMvav 104,24,208\nDS 1,20,43,3072\n"
"v_max3_f32 v238,v238,v198,v199\nMvav 104,28,212\nbuffer_load_dwordx4 a[184:187],v0,s[24:27],s8 offen offset:2048 sc1\nv_add_u32 v1,s96,v1\n"
"Mvav 108,28,220\nv_add_u32 v3,s47,v0\nv_max3_f32 v239,v239,v200,v201\nv_max3_f32 v238,v238,v202,v203\nMvav 108,24,216\ns_waitcnt vmcnt(40) lgkmcnt(0)\n"
"s_barrier\nbuffer_load_dwordx4 a[188:191],v0,s[24:27],s8 offen offset:3072 sc1\nMvav 112,32,208\ns_add_u32 s8,s8,4096\nRD 0,3,0\n"
"v_max3_f32 v239,v239,v204,v205\nv_max3_f32 v238,v238,v206,v207\nMvav 112,36,212\nRD 4,3,1024\nv_max_f32 v238,v238,v239\nMvav 116,36,220\nRD 8,3,2048\n"
"v_cmp_lt_f32 vcc,s67,v238\nMvav 116,32,216\ns_cbranch_vccnz fused_op_rm48\nfused_op_rmb49:\nRD 12,3,3072\nMvav 120,40,208\nRD 16,3,4096\n"
"Mvav 120,44,212\nRD 20,3,5120\nMvav 124,44,220\nRD 24,3,6144\nMvav 124,40,216\nRD 28,3,7168\nMvav 128,48,208\nv_exp_f32 v176,v176\nMvav 128,52,212\n"
"v_exp_f32 v177,v177\nMvav 132,52,220\nv_exp_f32 v178,v178\nMvav 132,48,216\nv_exp_f32 v179,v179\nMvav 136,56,208\nv_exp_f32 v180,v180\nMvav 136,60,212\n"
"s_cmp_lg_u32 s64,0\nv_exp_f32 v181,v181\nMvav 140,60,220\nWL 0\nv_exp_f32 v182,v182\nMvav 140,56,216\ns_cbranch_scc1 fused_op_rs50\nfused_op_rsb51:\n"
"v_exp_f32 v183,v183\nv_exp_f32 v184,v184\nv_add_f32 v234,v176,v234\nCV 208,176,177\nv_exp_f32 v185,v185\nv_add_f32 v234,v177,v234\nCV 209,178,179\n"
"v_exp_f32 v186,v186\nv_add_f32 v234,v178,v234\nv_add_f32 v235,v180,v235\nv_exp_f32 v187,v187\nv_add_f32 v234,v179,v234\nv_add_f32 v235,v181,v235\n"
"v_exp_f32 v188,v188\nCV 216,180,181\nv_add_f32 v235,v182,v235\nv_exp_f32 v189,v189\nv_add_f32 v235,v183,v235\nCV 217,182,183\nv_exp_f32 v190,v190\n"
"v_add_f32 v234,v184,v234\nCV 210,184,185\nv_exp_f32 v191,v191\nv_add_f32 v234,v185,v234\nCV 211,186,187\nv_exp_f32 v192,v192\nv_add_f32 v234,v186,v234\n"
"v_add_f32 v235,v188,v235\nv_exp_f32 v193,v193\nv_add_f32 v234,v187,v234\nv_add_f32 v235,v189,v235\nv_exp_f32 v194,v194\nCV 218,188,189\n"
"v_add_f32 v235,v190,v235\nv_exp_f32 v195,v195\nv_add_f32 v235,v191,v235\nCV 219,190,191\nv_exp_f32 v196,v196\nv_add_f32 v234,v192,v234\nCV 212,192,193\n"
"v_exp_f32 v197,v197\nv_add_f32 v234,v193,v234\nCV 213,194,195\nv_exp_f32 v198,v198\nv_add_f32 v234,v194,v234\nv_add_f32 v235,v196,v235\n"
"v_exp_f32 v199,v199\nv_add_f32 v234,v195,v234\nv_add_f32 v235,v197,v235\nv_exp_f32 v200,v200\nCV 220,196,197\nv_add_f32 v235,v198,v235\n"
"v_exp_f32 v201,v201\nv_add_f32 v235,v199,v235\nCV 221,198,199\nv_exp_f32 v202,v202\nv_add_f32 v234,v200,v234\nCV 214,200,201\nv_exp_f32 v203,v203\n"
"v_add_f32 v234,v201,v234\nv_exp_f32 v204,v204\nv_add_f32 v234,v202,v234\nCV 215,202,203\nv_exp_f32 v205,v205\nv_add_f32 v234,v203,v234\n"
"v_add_f32 v235,v204,v235\nv_exp_f32 v206,v206\nv_add_f32 v235,v205,v235\nCV 222,204,205\nv_exp_f32 v207,v207\nv_add_f32 v235,v206,v235\n"
"v_add_f32 v235,v207,v235\nCV 223,206,207\ns_nop 1\nMvav 16,0,208\nRD 32,3,8192\nMvav 16,4,212\nRD 36,3,9216\nMvav 20,4,220\nRD 40,3,10240\n"
"Mvav 20,0,216\nRD 44,3,11264\nMvav 24,8,208\nRD 48,3,12288\nMvav 24,12,212\nRD 52,3,13312\nMvav 28,12,220\nRD 56,3,14336\nMvav 28,8,216\n"
"s_add_u32 m0,s48,s44\nRD 60,3,15360\nMvav 32,16,208\nMvav 32,20,212\nDS 1,20,36,0\nMvav 36,20,220\nMvav 36,16,216\nMvav 40,24,208\nDS 1,20,37,1024\n"
"Mvav 40,28,212\nMvav 44,28,220\nMvav 44,24,216\nWL 0\nDS 1,20,38,2048\nMvav 48,32,208\nRD 0,3,16384\nMvav 48,36,212\nRD 4,3,17408\nMvav 52,36,220\n"
"RD 8,3,18432\nMvav 52,32,216\nRD 12,3,19456\nMvav 56,40,208\nRD 16,3,20480\nMvav 56,44,212\nRD 20,3,21504\nMvav 60,44,220\nRD 24,3,22528\n"
"Mvav 60,40,216\nRD 28,3,23552\nMvav 64,48,208\nMvav 64,52,212\nDS 1,20,39,3072\nMvav 68,52,220\ns_add_u32 m0,m0,0x1000\nMvav 68,48,216\nMvav 72,56,208\n"
"DS 1,20,40,0\nMvav 72,60,212\nMvav 76,60,220\nMvav 76,56,216\nWL 0\nDS 1,20,41,1024\nMvav 80,0,208\nRD 32,3,24576\nMvav 80,4,212\ns_mov_b32 s44,s47\n"
"RD 36,3,25600\nMvav 84,4,220\ns_add_u32 s47,s47,0x8000\nRD 40,3,26624\nMvav 84,0,216\ns_cmp_eq_u32 s47,0x28000\nRD 44,3,27648\nMvav 88,8,208\n"
"s_cselect_b32 s47,0,s47\nRD 48,3,28672\nMvav 88,12,212\nRD 52,3,29696\nMvav 92,12,220\nRD 56,3,30720\nMvav 92,8,216\nRD 60,3,31744\nMvav 96,16,208\n"
"Mvav 96,20,212\nDS 1,20,42,2048\nMvav 100,20,220\nMvav 100,16,216\nMvav 104,24,208\nDS 1,20,43,3072\nMvav 104,28,212\nv_add_u32 v1,0xffff8000,v1\n"
"Mvav 108,28,220\nv_add_u32 v3,s47,v0\nMvav 108,24,216\ns_waitcnt vmcnt(41) lgkmcnt(0)\ns_barrier\nMvav 112,32,208\nRD 0,3,0\nMvav 112,36,212\n"
"RD 4,3,1024\nMvav 116,36,220\nRD 8,3,2048\nMvav 116,32,216\nRD 12,3,3072\nMvav 120,40,208\nRD 16,3,4096\nMvav 120,44,212\nRD 20,3,5120\n"
"Mvav 124,44,220\nRD 24,3,6144\nMvav 124,40,216\nRD 28,3,7168\nMvav 128,48,208\nMvav 128,52,212\nMvav 132,52,220\nMvav 132,48,216\nMvav 136,56,208\n"
"Mvav 136,60,212\nMvav 140,60,220\nWL 0\nMvav 140,56,216\nfused_op_tek:\nv_mov_b32 v238,v234\nv_mov_b32 v239,v235\ns_nop 0\n"
"v_permlane32_swap_b32 v234,v238\nv_permlane32_swap_b32 v235,v239\nv_add_f32 v234,v234,v238\nv_add_f32 v235,v235,v239\nv_mov_b32 v238,v234\n"
"v_mov_b32 v239,v235\ns_nop 0\nv_permlane16_swap_b32 v234,v238\nv_permlane16_swap_b32 v235,v239\nv_add_f32 v234,v234,v238\nv_add_f32 v235,v235,v239\n"
"s_nop 1\nv_rcp_f32 v236,v234\nv_rcp_f32 v237,v235\ns_nop 0\nv_pk_mul_f32 v[16:17],v[16:17],v[236:237] op_sel:[0,0] op_sel_hi:[1,0]\n"
"v_pk_mul_f32 v[20:21],v[20:21],v[236:237] op_sel:[0,1] op_sel_hi:[1,1]\nv_pk_mul_f32 v[18:19],v[18:19],v[236:237] op_sel:[0,0] op_sel_hi:[1,0]\n"
"v_pk_mul_f32 v[22:23],v[22:23],v[236:237] op_sel:[0,1] op_sel_hi:[1,1]\nv_pk_mul_f32 v[24:25],v[24:25],v[236:237] op_sel:[0,0] op_sel_hi:[1,0]\n"
"v_pk_mul_f32 v[28:29],v[28:29],v[236:237] op_sel:[0,1] op_sel_hi:[1,1]\nv_pk_mul_f32 v[26:27],v[26:27],v[236:237] op_sel:[0,0] op_sel_hi:[1,0]\n"
"v_pk_mul_f32 v[30:31],v[30:31],v[236:237] op_sel:[0,1] op_sel_hi:[1,1]\nCV 16,16,17\nCV 20,20,21\nCV 17,18,19\nCV 21,22,23\nCV 18,24,25\nCV 22,28,29\n"
"CV 19,26,27\nCV 23,30,31\nv_accvgpr_write_b32 a64,v16\nv_accvgpr_write_b32 a96,v20\nv_accvgpr_write_b32 a65,v17\nv_accvgpr_write_b32 a97,v21\n"
"v_accvgpr_write_b32 a66,v18\nv_accvgpr_write_b32 a98,v22\nv_accvgpr_write_b32 a67,v19\nv_accvgpr_write_b32 a99,v23\n"
"v_pk_mul_f32 v[32:33],v[32:33],v[236:237] op_sel:[0,0] op_sel_hi:[1,0]\nv_pk_mul_f32 v[36:37],v[36:37],v[236:237] op_sel:[0,1] op_sel_hi:[1,1]\n"
"v_pk_mul_f32 v[34:35],v[34:35],v[236:237] op_sel:[0,0] op_sel_hi:[1,0]\nv_pk_mul_f32 v[38:39],v[38:39],v[236:237] op_sel:[0,1] op_sel_hi:[1,1]\n"
"v_pk_mul_f32 v[40:41],v[40:41],v[236:237] op_sel:[0,0] op_sel_hi:[1,0]\nv_pk_mul_f32 v[44:45],v[44:45],v[236:237] op_sel:[0,1] op_sel_hi:[1,1]\n"
"v_pk_mul_f32 v[42:43],v[42:43],v[236:237] op_sel:[0,0] op_sel_hi:[1,0]\nv_pk_mul_f32 v[46:47],v[46:47],v[236:237] op_sel:[0,1] op_sel_hi:[1,1]\n"
"CV 32,32,33\nCV 36,36,37\nCV 33,34,35\nCV 37,38,39\nCV 34,40,41\nCV 38,44,45\nCV 35,42,43\nCV 39,46,47\nv_accvgpr_write_b32 a68,v32\n"
"v_accvgpr_write_b32 a100,v36\nv_accvgpr_write_b32 a69,v33\nv_accvgpr_write_b32 a101,v37\nv_accvgpr_write_b32 a70,v34\nv_accvgpr_write_b32 a102,v38\n"
"v_accvgpr_write_b32 a71,v35\nv_accvgpr_write_b32 a103,v39\nv_pk_mul_f32 v[48:49],v[48:49],v[236:237] op_sel:[0,0] op_sel_hi:[1,0]\n"
"v_pk_mul_f32 v[52:53],v[52:53],v[236:237] op_sel:[0,1] op_sel_hi:[1,1]\nv_pk_mul_f32 v[50:51],v[50:51],v[236:237] op_sel:[0,0] op_sel_hi:[1,0]\n"
"v_pk_mul_f32 v[54:55],v[54:55],v[236:237] op_sel:[0,1] op_sel_hi:[1,1]\nv_pk_mul_f32 v[56:57],v[56:57],v[236:237] op_sel:[0,0] op_sel_hi:[1,0]\n"
"v_pk_mul_f32 v[60:61],v[60:61],v[236:237] op_sel:[0,1] op_sel_hi:[1,1]\nv_pk_mul_f32 v[58:59],v[58:59],v[236:237] op_sel:[0,0] op_sel_hi:[1,0]\n"
"v_pk_mul_f32 v[62:63],v[62:63],v[236:237] op_sel:[0,1] op_sel_hi:[1,1]\nCV 48,48,49\nCV 52,52,53\nCV 49,50,51\nCV 53,54,55\nCV 50,56,57\nCV 54,60,61\n"
"CV 51,58,59\nCV 55,62,63\nv_accvgpr_write_b32 a72,v48\nv_accvgpr_write_b32 a104,v52\nv_accvgpr_write_b32 a73,v49\nv_accvgpr_write_b32 a105,v53\n"
"v_accvgpr_write_b32 a74,v50\nv_accvgpr_write_b32 a106,v54\nv_accvgpr_write_b32 a75,v51\nv_accvgpr_write_b32 a107,v55\n"
"v_pk_mul_f32 v[64:65],v[64:65],v[236:237] op_sel:[0,0] op_sel_hi:[1,0]\nv_pk_mul_f32 v[68:69],v[68:69],v[236:237] op_sel:[0,1] op_sel_hi:[1,1]\n"
"v_pk_mul_f32 v[66:67],v[66:67],v[236:237] op_sel:[0,0] op_sel_hi:[1,0]\nv_pk_mul_f32 v[70:71],v[70:71],v[236:237] op_sel:[0,1] op_sel_hi:[1,1]\n"
"v_pk_mul_f32 v[72:73],v[72:73],v[236:237] op_sel:[0,0] op_sel_hi:[1,0]\nv_pk_mul_f32 v[76:77],v[76:77],v[236:237] op_sel:[0,1] op_sel_hi:[1,1]\n"
"v_pk_mul_f32 v[74:75],v[74:75],v[236:237] op_sel:[0,0] op_sel_hi:[1,0]\nv_pk_mul_f32 v[78:79],v[78:79],v[236:237] op_sel:[0,1] op_sel_hi:[1,1]\n"
"CV 64,64,65\nCV 68,68,69\nCV 65,66,67\nCV 69,70,71\nCV 66,72,73\nCV 70,76,77\nCV 67,74,75\nCV 71,78,79\nv_accvgpr_write_b32 a76,v64\n"
"v_accvgpr_write_b32 a108,v68\nv_accvgpr_write_b32 a77,v65\nv_accvgpr_write_b32 a109,v69\nv_accvgpr_write_b32 a78,v66\nv_accvgpr_write_b32 a110,v70\n"
"v_accvgpr_write_b32 a79,v67\nv_accvgpr_write_b32 a111,v71\nv_pk_mul_f32 v[80:81],v[80:81],v[236:237] op_sel:[0,0] op_sel_hi:[1,0]\n"
"v_pk_mul_f32 v[84:85],v[84:85],v[236:237] op_sel:[0,1] op_sel_hi:[1,1]\nv_pk_mul_f32 v[82:83],v[82:83],v[236:237] op_sel:[0,0] op_sel_hi:[1,0]\n"
"v_pk_mul_f32 v[86:87],v[86:87],v[236:237] op_sel:[0,1] op_sel_hi:[1,1]\nv_pk_mul_f32 v[88:89],v[88:89],v[236:237] op_sel:[0,0] op_sel_hi:[1,0]\n"
"v_pk_mul_f32 v[92:93],v[92:93],v[236:237] op_sel:[0,1] op_sel_hi:[1,1]\nv_pk_mul_f32 v[90:91],v[90:91],v[236:237] op_sel:[0,0] op_sel_hi:[1,0]\n"
"v_pk_mul_f32 v[94:95],v[94:95],v[236:237] op_sel:[0,1] op_sel_hi:[1,1]\nCV 80,80,81\nCV 84,84,85\nCV 81,82,83\nCV 85,86,87\nCV 82,88,89\nCV 86,92,93\n"
"CV 83,90,91\nCV 87,94,95\nv_accvgpr_write_b32 a80,v80\nv_accvgpr_write_b32 a112,v84\nv_accvgpr_write_b32 a81,v81\nv_accvgpr_write_b32 a113,v85\n"
"v_accvgpr_write_b32 a82,v82\nv_accvgpr_write_b32 a114,v86\nv_accvgpr_write_b32 a83,v83\nv_accvgpr_write_b32 a115,v87\n"
"v_pk_mul_f32 v[96:97],v[96:97],v[236:237] op_sel:[0,0] op_sel_hi:[1,0]\nv_pk_mul_f32 v[100:101],v[100:101],v[236:237] op_sel:[0,1] op_sel_hi:[1,1]\n"
"v_pk_mul_f32 v[98:99],v[98:99],v[236:237] op_sel:[0,0] op_sel_hi:[1,0]\nv_pk_mul_f32 v[102:103],v[102:103],v[236:237] op_sel:[0,1] op_sel_hi:[1,1]\n"
"v_pk_mul_f32 v[104:105],v[104:105],v[236:237] op_sel:[0,0] op_sel_hi:[1,0]\nv_pk_mul_f32 v[108:109],v[108:109],v[236:237] op_sel:[0,1] op_sel_hi:[1,1]\n"
"v_pk_mul_f32 v[106:107],v[106:107],v[236:237] op_sel:[0,0] op_sel_hi:[1,0]\nv_pk_mul_f32 v[110:111],v[110:111],v[236:237] op_sel:[0,1] op_sel_hi:[1,1]\n"
"CV 96,96,97\nCV 100,100,101\nCV 97,98,99\nCV 101,102,103\nCV 98,104,105\nCV 102,108,109\nCV 99,106,107\nCV 103,110,111\nv_accvgpr_write_b32 a84,v96\n"
"v_accvgpr_write_b32 a116,v100\nv_accvgpr_write_b32 a85,v97\nv_accvgpr_write_b32 a117,v101\nv_accvgpr_write_b32 a86,v98\nv_accvgpr_write_b32 a118,v102\n"
"v_accvgpr_write_b32 a87,v99\nv_accvgpr_write_b32 a119,v103\nv_pk_mul_f32 v[112:113],v[112:113],v[236:237] op_sel:[0,0] op_sel_hi:[1,0]\n"
"v_pk_mul_f32 v[116:117],v[116:117],v[236:237] op_sel:[0,1] op_sel_hi:[1,1]\nv_pk_mul_f32 v[114:115],v[114:115],v[236:237] op_sel:[0,0] op_sel_hi:[1,0]\n"
"v_pk_mul_f32 v[118:119],v[118:119],v[236:237] op_sel:[0,1] op_sel_hi:[1,1]\nv_pk_mul_f32 v[120:121],v[120:121],v[236:237] op_sel:[0,0] op_sel_hi:[1,0]\n"
"v_pk_mul_f32 v[124:125],v[124:125],v[236:237] op_sel:[0,1] op_sel_hi:[1,1]\nv_pk_mul_f32 v[122:123],v[122:123],v[236:237] op_sel:[0,0] op_sel_hi:[1,0]\n"
"v_pk_mul_f32 v[126:127],v[126:127],v[236:237] op_sel:[0,1] op_sel_hi:[1,1]\nCV 112,112,113\nCV 116,116,117\nCV 113,114,115\nCV 117,118,119\n"
"CV 114,120,121\nCV 118,124,125\nCV 115,122,123\nCV 119,126,127\nv_accvgpr_write_b32 a88,v112\nv_accvgpr_write_b32 a120,v116\n"
"v_accvgpr_write_b32 a89,v113\nv_accvgpr_write_b32 a121,v117\nv_accvgpr_write_b32 a90,v114\nv_accvgpr_write_b32 a122,v118\nv_accvgpr_write_b32 a91,v115\n"
"v_accvgpr_write_b32 a123,v119\nv_pk_mul_f32 v[128:129],v[128:129],v[236:237] op_sel:[0,0] op_sel_hi:[1,0]\n"
"v_pk_mul_f32 v[132:133],v[132:133],v[236:237] op_sel:[0,1] op_sel_hi:[1,1]\nv_pk_mul_f32 v[130:131],v[130:131],v[236:237] op_sel:[0,0] op_sel_hi:[1,0]\n"
"v_pk_mul_f32 v[134:135],v[134:135],v[236:237] op_sel:[0,1] op_sel_hi:[1,1]\nv_pk_mul_f32 v[136:137],v[136:137],v[236:237] op_sel:[0,0] op_sel_hi:[1,0]\n"
"v_pk_mul_f32 v[140:141],v[140:141],v[236:237] op_sel:[0,1] op_sel_hi:[1,1]\nv_pk_mul_f32 v[138:139],v[138:139],v[236:237] op_sel:[0,0] op_sel_hi:[1,0]\n"
"v_pk_mul_f32 v[142:143],v[142:143],v[236:237] op_sel:[0,1] op_sel_hi:[1,1]\nCV 128,128,129\nCV 132,132,133\nCV 129,130,131\nCV 133,134,135\n"
"CV 130,136,137\nCV 134,140,141\nCV 131,138,139\nCV 135,142,143\nv_accvgpr_write_b32 a92,v128\nv_accvgpr_write_b32 a124,v132\n"
"v_accvgpr_write_b32 a93,v129\nv_accvgpr_write_b32 a125,v133\nv_accvgpr_write_b32 a94,v130\nv_accvgpr_write_b32 a126,v134\nv_accvgpr_write_b32 a95,v131\n"
"v_accvgpr_write_b32 a127,v135\ns_mov_b32 s69,1\ns_mov_b32 s61,s60\ns_add_u32 s50,s54,fused_op_tw-fused_op_pc\ns_addc_u32 s51,s55,0\ns_waitcnt vmcnt(8)\n"
"s_cmp_eq_u32 s61,2\ns_cbranch_scc1 fused_op_first2w\ns_branch fused_op_enter\nfused_op_tw:\ns_lshl_b32 s45,s10,13\n"
"s_add_u32 s45,s45,fused_op_tw-fused_op_pc\ns_min_u32 s45,s45,fused_op_end-fused_op_pc-8192\nv_lshlrev_b32 v12,3,v0\nv_add_u32 v12,s45,v12\n"
"global_load_dword a255,v12,s[54:55]\nNvaa 144,0,128,224\nRD 32,3,8192\nCV 208,176,177\nMvaa 144,4,132\nRD 36,3,9216\nCV 209,178,179\nMvaa 144,8,136\n"
"RD 40,3,10240\nv_add_u32 v1,0xffff8000,v1\nMvaa 144,12,140\nRD 44,3,11264\nCV 216,180,181\nMvaa 144,16,144\nRD 48,3,12288\nCV 217,182,183\n"
"Mvaa 144,20,148\nRD 52,3,13312\nv_add_f32 v234,v184,v234\nMvaa 144,24,152\nRD 56,3,14336\nv_add_f32 v234,v185,v234\nMvaa 144,28,156\n"
"s_add_u32 m0,s48,s44\nRD 60,3,15360\nCV 210,184,185\nNvaa 148,28,188,228\nv_exp_f32 v192,v192\nMvaa 148,24,184\nDS 1,20,36,0\nMvaa 148,20,180\n"
"v_exp_f32 v193,v193\nMvaa 148,16,176\nv_exp_f32 v194,v194\nMvaa 148,12,172\nDS 1,20,37,1024\nMvaa 148,8,168\nv_exp_f32 v195,v195\nMvaa 148,4,164\n"
"v_exp_f32 v196,v196\nMvaa 148,0,160\nWL 0\nDS 1,20,38,2048\nNvaa 152,32,128,224\nRD 0,3,16384\nv_add_f32 v234,v186,v234\nMvaa 152,36,132\nRD 4,3,17408\n"
"v_add_f32 v234,v187,v234\nMvaa 152,40,136\nRD 8,3,18432\nCV 211,186,187\nMvaa 152,44,140\nRD 12,3,19456\nv_add_f32 v235,v188,v235\nMvaa 152,48,144\n"
"RD 16,3,20480\nv_add_f32 v235,v189,v235\nMvaa 152,52,148\nRD 20,3,21504\nCV 218,188,189\nMvaa 152,56,152\nRD 24,3,22528\nv_add_f32 v235,v190,v235\n"
"Mvaa 152,60,156\nRD 28,3,23552\nv_add_f32 v235,v191,v235\nNvaa 156,60,188,228\nv_exp_f32 v197,v197\nMvaa 156,56,184\nDS 1,20,39,3072\nMvaa 156,52,180\n"
"s_add_u32 m0,m0,0x1000\nv_exp_f32 v198,v198\nMvaa 156,48,176\nv_exp_f32 v199,v199\nMvaa 156,44,172\nDS 1,20,40,0\nMvaa 156,40,168\nv_exp_f32 v200,v200\n"
"Mvaa 156,36,164\nv_exp_f32 v201,v201\nMvaa 156,32,160\nWL 0\nDS 1,20,41,1024\nNvaa 160,0,128,224\nRD 32,3,24576\nCV 219,190,191\nMvaa 160,4,132\n"
"s_mov_b32 s44,s47\nRD 36,3,25600\nv_add_f32 v234,v192,v234\nMvaa 160,8,136\ns_add_u32 s47,s47,0x8000\nRD 40,3,26624\nv_add_f32 v234,v193,v234\n"
"Mvaa 160,12,140\ns_cmp_eq_u32 s47,0x28000\nRD 44,3,27648\nCV 212,192,193\nMvaa 160,16,144\ns_cselect_b32 s47,0,s47\nRD 48,3,28672\n"
"v_add_f32 v234,v194,v234\nMvaa 160,20,148\nRD 52,3,29696\nv_add_f32 v234,v195,v234\nMvaa 160,24,152\nRD 56,3,30720\nCV 213,194,195\nMvaa 160,28,156\n"
"RD 60,3,31744\nv_add_f32 v235,v196,v235\nNvaa 164,28,188,228\nv_exp_f32 v202,v202\nMvaa 164,24,184\nDS 1,20,42,2048\nMvaa 164,20,180\n"
"v_exp_f32 v203,v203\nMvaa 164,16,176\nv_exp_f32 v204,v204\nMvaa 164,12,172\nDS 1,20,43,3072\nMvaa 164,8,168\ns_mul_i32 s45,s56,229376\n"
"v_lshrrev_b32 v13,7,v0\nv_add_f32 v235,v197,v235\nMvaa 164,4,164\ns_mul_i32 s49,s10,57344\nv_add_u32 v3,s47,v0\nv_and_b32 v1,6,v13\nMvaa 164,0,160\n"
"s_waitcnt vmcnt(25) lgkmcnt(0)\ns_barrier\nv_lshlrev_b32 v1,4,v1\nNvaa 168,32,128,224\ns_add_u32 s45,s45,s49\nRD 0,3,0\nv_xor_b32 v1,v1,v0\n"
"Mvaa 168,36,132\ns_add_u32 s20,s88,s45\ns_addc_u32 s21,s89,0\nRD 4,3,1024\nMvaa 168,40,136\ns_and_b32 s21,s21,0xffff\nRD 8,3,2048\n"
"v_and_b32 v1,0x70,v1\nMvaa 168,44,140\ns_sub_u32 s22,3670016,s45\nRD 12,3,3072\nv_mul_u32_u24 v13,896,v13\nMvaa 168,48,144\ns_mov_b32 s23,0x20000\n"
"RD 16,3,4096\nv_add_u32 v1,v1,v13\nMvaa 168,52,148\ns_mov_b32 s36,0\nRD 20,3,5120\nCV 220,196,197\nMvaa 168,56,152\ns_mov_b32 s37,6144\nRD 24,3,6144\n"
"v_add_f32 v235,v198,v235\nMvaa 168,60,156\ns_mov_b32 s38,12288\nRD 28,3,7168\nv_add_f32 v235,v199,v235\nNvaa 172,60,188,228\ns_mov_b32 s39,18432\n"
"v_exp_f32 v205,v205\nMvaa 172,56,184\ns_mov_b32 s40,28672\nv_exp_f32 v206,v206\nMvaa 172,52,180\ns_mov_b32 s41,34816\nv_exp_f32 v207,v207\n"
"Mvaa 172,48,176\ns_mov_b32 s42,40960\nCV 221,198,199\nv_add_f32 v234,v200,v234\nMvaa 172,44,172\ns_mov_b32 s43,47104\nv_add_f32 v234,v201,v234\n"
"CV 214,200,201\nMvaa 172,40,168\nv_add_f32 v234,v202,v234\nCV 215,202,203\nMvaa 172,36,164\nWL 0\nv_add_f32 v234,v203,v234\nv_add_f32 v235,v204,v235\n"
"Mvaa 172,32,160\nv_add_f32 v235,v205,v235\nCV 222,204,205\nv_add_f32 v235,v206,v235\nCV 223,206,207\nv_add_f32 v235,v207,v235\nMvav 16,0,208\n"
"RD 32,3,8192\nMvav 16,4,212\nRD 36,3,9216\nv_add_f32 v240,0x73840000,v246\nv_add_f32 v241,0x73780000,v246\nMvav 20,4,220\nRD 40,3,10240\n"
"v_min_f32 v148,v240,v148\nv_min_f32 v149,v241,v149\nMvav 20,0,216\nRD 44,3,11264\nv_add_f32 v242,0x73680000,v246\nv_add_f32 v243,0x73580000,v246\n"
"Mvav 24,8,208\nRD 48,3,12288\nv_min_f32 v150,v242,v150\nv_min_f32 v151,v243,v151\nMvav 24,12,212\nRD 52,3,13312\nv_add_f32 v240,0x71000000,v246\n"
"v_add_f32 v241,0xf1000000,v246\nMvav 28,12,220\nRD 56,3,14336\nv_min_f32 v144,v240,v144\nv_min_f32 v156,v240,v156\nMvav 28,8,216\ns_add_u32 m0,s48,s44\n"
"RD 60,3,15360\nv_min_f32 v145,v241,v145\nv_min_f32 v157,v241,v157\nMvav 32,16,208\nv_add_f32 v242,0xf1c00000,v246\nv_add_f32 v243,0xf2200000,v246\n"
"v_add_f32 v240,0xf3780000,v246\nMvav 32,20,212\nDS 1,20,36,0\nv_min_f32 v146,v242,v146\nMvav 36,20,220\nv_min_f32 v158,v242,v158\n"
"v_min_f32 v147,v243,v147\nv_min_f32 v159,v243,v159\nMvav 36,16,216\nv_min_f32 v152,v240,v152\nv_min_f32 v164,v240,v164\nv_add_f32 v241,0xf3840000,v246\n"
"Mvav 40,24,208\nDS 1,20,37,1024\nv_min_f32 v153,v241,v153\nMvav 40,28,212\nv_min_f32 v165,v241,v165\nv_add_f32 v242,0xf38c0000,v246\n"
"v_add_f32 v243,0xf3940000,v246\nMvav 44,28,220\nv_min_f32 v154,v242,v154\nv_min_f32 v166,v242,v166\nv_min_f32 v155,v243,v155\nMvav 44,24,216\nWL 0\n"
"DS 1,20,38,2048\nv_min_f32 v167,v243,v167\nMvav 48,32,208\nRD 0,3,16384\nv_add_f32 v240,0xf3fc0000,v246\nv_add_f32 v241,0xf4020000,v246\n"
"Mvav 48,36,212\nRD 4,3,17408\nv_min_f32 v160,v240,v160\nv_min_f32 v172,v240,v172\nMvav 52,36,220\nRD 8,3,18432\nv_min_f32 v161,v241,v161\n"
"v_min_f32 v173,v241,v173\nMvav 52,32,216\nRD 12,3,19456\nv_add_f32 v242,0xf4060000,v246\nv_add_f32 v243,0xf40a0000,v246\nMvav 56,40,208\nRD 16,3,20480\n"
"v_min_f32 v162,v242,v162\nv_min_f32 v174,v242,v174\nMvav 56,44,212\nRD 20,3,21504\nv_min_f32 v163,v243,v163\nv_min_f32 v175,v243,v175\nMvav 60,44,220\n"
"RD 24,3,22528\nv_add_f32 v240,0xf43e0000,v246\nv_add_f32 v241,0xf4420000,v246\nMvav 60,40,216\nRD 28,3,23552\nv_min_f32 v168,v240,v168\n"
"v_min_f32 v169,v241,v169\nMvav 64,48,208\nv_add_f32 v242,0xf4460000,v246\nv_add_f32 v243,0xf44a0000,v246\nv_max3_f32 v238,v144,v145,v146\n"
"Mvav 64,52,212\nDS 1,20,39,3072\nv_min_f32 v170,v242,v170\nMvav 68,52,220\ns_add_u32 m0,m0,0x1000\nv_min_f32 v171,v243,v171\n"
"v_add_f32 v246,0xf4800000,v246\nv_max3_f32 v239,v147,v148,v149\nMvav 68,48,216\nv_max3_f32 v238,v238,v150,v151\nv_max3_f32 v239,v239,v152,v153\n"
"Mvav 72,56,208\nDS 1,20,40,0\nv_max3_f32 v238,v238,v154,v155\nMvav 72,60,212\nv_max3_f32 v239,v239,v156,v157\nv_max3_f32 v238,v238,v158,v159\n"
"Mvav 76,60,220\nv_max3_f32 v239,v239,v160,v161\nv_max3_f32 v238,v238,v162,v163\nMvav 76,56,216\nWL 0\nDS 1,20,41,1024\nv_max3_f32 v239,v239,v164,v165\n"
"Mvav 80,0,208\nRD 32,3,24576\nv_max3_f32 v238,v238,v166,v167\nv_max3_f32 v239,v239,v168,v169\nMvav 80,4,212\ns_mov_b32 s44,s47\nRD 36,3,25600\n"
"v_max3_f32 v238,v238,v170,v171\nv_max3_f32 v239,v239,v172,v173\nMvav 84,4,220\ns_add_u32 s47,s47,0x8000\nRD 40,3,26624\nv_max3_f32 v238,v238,v174,v175\n"
"Mvav 84,0,216\ns_cmp_eq_u32 s47,0x28000\nRD 44,3,27648\nv_max_f32 v238,v238,v239\nMvav 88,8,208\ns_cselect_b32 s47,0,s47\nRD 48,3,28672\n"
"v_cmp_lt_f32 vcc,s67,v238\nMvav 88,12,212\ns_cbranch_vccnz fused_op_rm34\nfused_op_rmb35:\nRD 52,3,29696\nMvav 92,12,220\nRD 56,3,30720\nMvav 92,8,216\n"
"RD 60,3,31744\nMvav 96,16,208\nv_exp_f32 v144,v144\nMvav 96,20,212\nDS 1,20,42,2048\nMvav 100,20,220\nv_exp_f32 v145,v145\nMvav 100,16,216\n"
"v_exp_f32 v146,v146\nMvav 104,24,208\nDS 1,20,43,3072\nMvav 104,28,212\nv_add_u32 v1,0x80,v1\nv_add_f32 v234,v144,v234\nMvav 108,28,220\n"
"v_add_u32 v3,s47,v0\nv_add_f32 v234,v145,v234\nMvav 108,24,216\ns_waitcnt vmcnt(25) lgkmcnt(0)\ns_barrier\nv_add_f32 v234,v146,v234\nMvav 112,32,208\n"
"RD 0,3,0\nMvav 112,36,212\nRD 4,3,1024\nMvav 116,36,220\nRD 8,3,2048\nMvav 116,32,216\nRD 12,3,3072\nMvav 120,40,208\nRD 16,3,4096\nMvav 120,44,212\n"
"RD 20,3,5120\nMvav 124,44,220\nRD 24,3,6144\nMvav 124,40,216\nRD 28,3,7168\nMvav 128,48,208\nv_exp_f32 v147,v147\nMvav 128,52,212\nv_exp_f32 v148,v148\n"
"Mvav 132,52,220\nv_exp_f32 v149,v149\nMvav 132,48,216\nv_exp_f32 v150,v150\nMvav 136,56,208\nv_exp_f32 v151,v151\nMvav 136,60,212\ns_cmp_lg_u32 s64,0\n"
"v_exp_f32 v152,v152\nMvav 140,60,220\nWL 0\nv_exp_f32 v153,v153\nMvav 140,56,216\ns_cbranch_scc1 fused_op_rs36\nfused_op_rsb37:\nv_exp_f32 v154,v154\n"
"fused_op_t2w:\ns_cmp_lt_u32 s10,2\ns_cbranch_scc1 fused_op_tsw\nNvaa 176,0,128,224\nRD 32,3,8192\nCV 208,144,145\nMvaa 176,4,132\nRD 36,3,9216\n"
"v_add_f32 v234,v147,v234\nMvaa 176,8,136\nRD 40,3,10240\nCV 209,146,147\nMvaa 176,12,140\nRD 44,3,11264\nv_add_f32 v235,v148,v235\nMvaa 176,16,144\n"
"RD 48,3,12288\nv_add_f32 v235,v149,v235\nMvaa 176,20,148\nRD 52,3,13312\nCV 216,148,149\nMvaa 176,24,152\nRD 56,3,14336\nv_add_f32 v235,v150,v235\n"
"Mvaa 176,28,156\ns_add_u32 m0,s48,s44\nRD 60,3,15360\nv_add_f32 v235,v151,v235\nNvaa 180,28,188,228\nv_exp_f32 v155,v155\nMvaa 180,24,184\n"
"DS 1,20,36,0\nMvaa 180,20,180\nv_exp_f32 v156,v156\nMvaa 180,16,176\nv_exp_f32 v157,v157\nMvaa 180,12,172\nDS 1,20,37,1024\nMvaa 180,8,168\n"
"v_exp_f32 v158,v158\nMvaa 180,4,164\nv_exp_f32 v159,v159\nMvaa 180,0,160\nWL 0\nDS 1,20,38,2048\nNvaa 184,32,128,224\nRD 0,3,16384\nCV 217,150,151\n"
"Mvaa 184,36,132\nRD 4,3,17408\nv_add_f32 v234,v152,v234\nMvaa 184,40,136\nRD 8,3,18432\nv_add_f32 v234,v153,v234\nMvaa 184,44,140\nRD 12,3,19456\n"
"CV 210,152,153\nMvaa 184,48,144\nRD 16,3,20480\nv_add_f32 v234,v154,v234\nMvaa 184,52,148\nRD 20,3,21504\nv_add_f32 v234,v155,v234\nMvaa 184,56,152\n"
"RD 24,3,22528\nCV 211,154,155\nMvaa 184,60,156\nRD 28,3,23552\nv_add_f32 v235,v156,v235\nNvaa 188,60,188,228\nv_exp_f32 v160,v160\nMvaa 188,56,184\n"
"DS 1,20,39,3072\nMvaa 188,52,180\ns_add_u32 m0,m0,0x1000\nv_exp_f32 v161,v161\nMvaa 188,48,176\nv_exp_f32 v162,v162\nMvaa 188,44,172\nDS 1,20,40,0\n"
"Mvaa 188,40,168\nv_exp_f32 v163,v163\nMvaa 188,36,164\nv_exp_f32 v164,v164\nMvaa 188,32,160\nWL 0\nDS 1,20,41,1024\nNvaa 192,0,128,224\nRD 32,3,24576\n"
"v_add_f32 v235,v157,v235\nMvaa 192,4,132\ns_mov_b32 s44,s47\nRD 36,3,25600\nCV 218,156,157\nMvaa 192,8,136\ns_add_u32 s47,s47,0x8000\nRD 40,3,26624\n"
"v_add_f32 v235,v158,v235\nMvaa 192,12,140\ns_cmp_eq_u32 s47,0x28000\nRD 44,3,27648\nv_add_f32 v235,v159,v235\nMvaa 192,16,144\ns_cselect_b32 s47,0,s47\n"
"RD 48,3,28672\nCV 219,158,159\nMvaa 192,20,148\nRD 52,3,29696\nv_add_f32 v234,v160,v234\nMvaa 192,24,152\nRD 56,3,30720\nv_add_f32 v234,v161,v234\n"
"Mvaa 192,28,156\nRD 60,3,31744\nCV 212,160,161\nNvaa 196,28,188,228\nv_exp_f32 v165,v165\nMvaa 196,24,184\nDS 1,20,42,2048\nMvaa 196,20,180\n"
"v_exp_f32 v166,v166\nMvaa 196,16,176\nv_exp_f32 v167,v167\nMvaa 196,12,172\nDS 1,20,43,3072\nMvaa 196,8,168\nv_add_u32 v1,0x80,v1\n"
"v_add_f32 v234,v162,v234\nMvaa 196,4,164\nv_add_u32 v3,s47,v0\nv_add_f32 v234,v163,v234\nMvaa 196,0,160\ns_waitcnt vmcnt(25) lgkmcnt(0)\ns_barrier\n"
"CV 213,162,163\nNvaa 200,32,128,224\nRD 0,3,0\nv_add_f32 v235,v164,v235\nMvaa 200,36,132\nRD 4,3,1024\nv_add_f32 v235,v165,v235\nMvaa 200,40,136\n"
"RD 8,3,2048\nCV 220,164,165\nMvaa 200,44,140\nRD 12,3,3072\nv_add_f32 v235,v166,v235\nMvaa 200,48,144\nRD 16,3,4096\nv_add_f32 v235,v167,v235\n"
"Mvaa 200,52,148\nRD 20,3,5120\nCV 221,166,167\nMvaa 200,56,152\nRD 24,3,6144\nMvaa 200,60,156\nRD 28,3,7168\nNvaa 204,60,188,228\nv_exp_f32 v168,v168\n"
"Mvaa 204,56,184\nv_exp_f32 v169,v169\nMvaa 204,52,180\nv_exp_f32 v170,v170\nMvaa 204,48,176\nv_exp_f32 v171,v171\nMvaa 204,44,172\nv_exp_f32 v172,v172\n"
"Mvaa 204,40,168\nv_exp_f32 v173,v173\nMvaa 204,36,164\nWL 0\nMvaa 204,32,160\nv_exp_f32 v174,v174\nv_add_f32 v234,v168,v234\nCV 214,168,169\n"
"v_exp_f32 v175,v175\nv_add_f32 v234,v169,v234\nCV 215,170,171\nv_add_f32 v234,v170,v234\nv_add_f32 v235,v172,v235\nv_add_f32 v234,v171,v234\n"
"v_add_f32 v235,v173,v235\nCV 222,172,173\nv_add_f32 v235,v174,v235\nv_add_f32 v235,v175,v235\nCV 223,174,175\nMvav 16,0,208\nRD 32,3,8192\n"
"Mvav 16,4,212\nRD 36,3,9216\nv_add_f32 v240,0x73840000,v246\nv_add_f32 v241,0x73780000,v246\nMvav 20,4,220\nRD 40,3,10240\nv_min_f32 v180,v240,v180\n"
"v_min_f32 v181,v241,v181\nMvav 20,0,216\nRD 44,3,11264\nv_add_f32 v242,0x73680000,v246\nv_add_f32 v243,0x73580000,v246\nMvav 24,8,208\nRD 48,3,12288\n"
"v_min_f32 v182,v242,v182\nv_min_f32 v183,v243,v183\nMvav 24,12,212\nRD 52,3,13312\nv_add_f32 v240,0x71000000,v246\nv_add_f32 v241,0xf1000000,v246\n"
"Mvav 28,12,220\nRD 56,3,14336\nv_min_f32 v176,v240,v176\nv_min_f32 v188,v240,v188\nMvav 28,8,216\ns_add_u32 m0,s48,s44\nRD 60,3,15360\n"
"v_min_f32 v177,v241,v177\nv_min_f32 v189,v241,v189\nMvav 32,16,208\nv_add_f32 v242,0xf1c00000,v246\nv_add_f32 v243,0xf2200000,v246\n"
"v_add_f32 v240,0xf3780000,v246\nMvav 32,20,212\nDS 1,20,36,0\nv_min_f32 v178,v242,v178\nMvav 36,20,220\nv_min_f32 v190,v242,v190\n"
"v_min_f32 v179,v243,v179\nv_min_f32 v191,v243,v191\nMvav 36,16,216\nv_min_f32 v184,v240,v184\nv_min_f32 v196,v240,v196\nv_add_f32 v241,0xf3840000,v246\n"
"Mvav 40,24,208\nDS 1,20,37,1024\nv_min_f32 v185,v241,v185\nMvav 40,28,212\nv_min_f32 v197,v241,v197\nv_add_f32 v242,0xf38c0000,v246\n"
"v_add_f32 v243,0xf3940000,v246\nMvav 44,28,220\nv_min_f32 v186,v242,v186\nv_min_f32 v198,v242,v198\nv_min_f32 v187,v243,v187\nMvav 44,24,216\nWL 0\n"
"DS 1,20,38,2048\nv_min_f32 v199,v243,v199\nMvav 48,32,208\nRD 0,3,16384\nv_add_f32 v240,0xf3fc0000,v246\nv_add_f32 v241,0xf4020000,v246\n"
"Mvav 48,36,212\nRD 4,3,17408\nv_min_f32 v192,v240,v192\nv_min_f32 v204,v240,v204\nMvav 52,36,220\nRD 8,3,18432\nv_min_f32 v193,v241,v193\n"
"v_min_f32 v205,v241,v205\nMvav 52,32,216\nRD 12,3,19456\nv_add_f32 v242,0xf4060000,v246\nv_add_f32 v243,0xf40a0000,v246\nMvav 56,40,208\nRD 16,3,20480\n"
"v_min_f32 v194,v242,v194\nv_min_f32 v206,v242,v206\nMvav 56,44,212\nRD 20,3,21504\nv_min_f32 v195,v243,v195\nv_min_f32 v207,v243,v207\nMvav 60,44,220\n"
"RD 24,3,22528\nv_add_f32 v240,0xf43e0000,v246\nv_add_f32 v241,0xf4420000,v246\nMvav 60,40,216\nRD 28,3,23552\nv_min_f32 v200,v240,v200\n"
"v_min_f32 v201,v241,v201\nMvav 64,48,208\nv_add_f32 v242,0xf4460000,v246\nv_add_f32 v243,0xf44a0000,v246\nv_max3_f32 v238,v176,v177,v178\n"
"Mvav 64,52,212\nDS 1,20,39,3072\nv_min_f32 v202,v242,v202\nMvav 68,52,220\ns_add_u32 m0,m0,0x1000\nv_min_f32 v203,v243,v203\n"
"v_add_f32 v246,0xf4800000,v246\nv_max3_f32 v239,v179,v180,v181\nMvav 68,48,216\nv_max3_f32 v238,v238,v182,v183\nv_max3_f32 v239,v239,v184,v185\n"
"Mvav 72,56,208\nDS 1,20,40,0\nv_max3_f32 v238,v238,v186,v187\nMvav 72,60,212\nv_max3_f32 v239,v239,v188,v189\nv_max3_f32 v238,v238,v190,v191\n"
"Mvav 76,60,220\nv_max3_f32 v239,v239,v192,v193\nv_max3_f32 v238,v238,v194,v195\nMvav 76,56,216\nWL 0\nDS 1,20,41,1024\nv_max3_f32 v239,v239,v196,v197\n"
"Mvav 80,0,208\nRD 32,3,24576\nv_max3_f32 v238,v238,v198,v199\nv_max3_f32 v239,v239,v200,v201\nMvav 80,4,212\ns_mov_b32 s44,s47\nRD 36,3,25600\n"
"v_max3_f32 v238,v238,v202,v203\nv_max3_f32 v239,v239,v204,v205\nMvav 84,4,220\ns_add_u32 s47,s47,0x8000\nRD 40,3,26624\nv_max3_f32 v238,v238,v206,v207\n"
"Mvav 84,0,216\ns_cmp_eq_u32 s47,0x28000\nRD 44,3,27648\nv_max_f32 v238,v238,v239\nMvav 88,8,208\ns_cselect_b32 s47,0,s47\nRD 48,3,28672\n"
"v_cmp_lt_f32 vcc,s67,v238\nMvav 88,12,212\ns_cbranch_vccnz fused_op_rm38\nfused_op_rmb39:\nRD 52,3,29696\nMvav 92,12,220\nRD 56,3,30720\nMvav 92,8,216\n"
"RD 60,3,31744\nMvav 96,16,208\nv_exp_f32 v176,v176\nMvav 96,20,212\nDS 1,20,42,2048\nMvav 100,20,220\nv_exp_f32 v177,v177\nMvav 100,16,216\n"
"v_exp_f32 v178,v178\nMvav 104,24,208\nDS 1,20,43,3072\nMvav 104,28,212\nv_add_u32 v1,0x80,v1\nv_add_f32 v234,v176,v234\nMvav 108,28,220\n"
"v_add_u32 v3,s47,v0\nv_add_f32 v234,v177,v234\nMvav 108,24,216\ns_waitcnt vmcnt(24) lgkmcnt(0)\ns_barrier\nv_add_f32 v234,v178,v234\nMvav 112,32,208\n"
"RD 0,3,0\nMvav 112,36,212\nRD 4,3,1024\nMvav 116,36,220\nRD 8,3,2048\nMvav 116,32,216\nRD 12,3,3072\nMvav 120,40,208\nRD 16,3,4096\nMvav 120,44,212\n"
"RD 20,3,5120\nMvav 124,44,220\nRD 24,3,6144\nMvav 124,40,216\nRD 28,3,7168\nMvav 128,48,208\nv_exp_f32 v179,v179\nMvav 128,52,212\nv_exp_f32 v180,v180\n"
"Mvav 132,52,220\nv_exp_f32 v181,v181\nMvav 132,48,216\nv_exp_f32 v182,v182\nMvav 136,56,208\nv_exp_f32 v183,v183\nMvav 136,60,212\ns_cmp_lg_u32 s64,0\n"
"v_exp_f32 v184,v184\nMvav 140,60,220\nWL 0\nv_exp_f32 v185,v185\nMvav 140,56,216\ns_cbranch_scc1 fused_op_rs40\nfused_op_rsb41:\nv_exp_f32 v186,v186\n"
"v_exp_f32 v187,v187\nCV 208,176,177\nv_add_f32 v234,v179,v234\nv_exp_f32 v188,v188\nCV 209,178,179\nv_add_f32 v235,v180,v235\nv_exp_f32 v189,v189\n"
"v_add_f32 v235,v181,v235\nCV 216,180,181\nv_exp_f32 v190,v190\nv_add_f32 v235,v182,v235\nCV 217,182,183\nv_exp_f32 v191,v191\nv_add_f32 v235,v183,v235\n"
"v_add_f32 v234,v184,v234\nv_exp_f32 v192,v192\nv_add_f32 v234,v185,v234\nCV 210,184,185\nv_exp_f32 v193,v193\nv_add_f32 v234,v186,v234\nCV 211,186,187\n"
"v_exp_f32 v194,v194\nv_add_f32 v234,v187,v234\nv_add_f32 v235,v188,v235\nv_exp_f32 v195,v195\nv_add_f32 v235,v189,v235\nCV 218,188,189\n"
"v_exp_f32 v196,v196\nv_add_f32 v235,v190,v235\nCV 219,190,191\nv_exp_f32 v197,v197\nv_add_f32 v235,v191,v235\nv_add_f32 v234,v192,v234\n"
"v_exp_f32 v198,v198\nv_add_f32 v234,v193,v234\nCV 212,192,193\nv_exp_f32 v199,v199\nv_add_f32 v234,v194,v234\nCV 213,194,195\nv_exp_f32 v200,v200\n"
"v_add_f32 v234,v195,v234\nv_add_f32 v235,v196,v235\nv_exp_f32 v201,v201\nv_add_f32 v235,v197,v235\nCV 220,196,197\nv_exp_f32 v202,v202\n"
"v_add_f32 v235,v198,v235\nCV 221,198,199\nv_exp_f32 v203,v203\nv_add_f32 v235,v199,v235\nv_add_f32 v234,v200,v234\nv_exp_f32 v204,v204\n"
"v_add_f32 v234,v201,v234\nCV 214,200,201\nv_exp_f32 v205,v205\nv_add_f32 v234,v202,v234\nCV 215,202,203\nv_exp_f32 v206,v206\nv_add_f32 v234,v203,v234\n"
"v_add_f32 v235,v204,v235\nv_exp_f32 v207,v207\nv_add_f32 v235,v205,v235\nCV 222,204,205\nv_add_f32 v235,v206,v235\nCV 223,206,207\n"
"v_add_f32 v235,v207,v235\ns_nop 1\nMvav 16,0,208\nRD 32,3,8192\nMvav 16,4,212\nRD 36,3,9216\nMvav 20,4,220\nRD 40,3,10240\nMvav 20,0,216\n"
"RD 44,3,11264\nMvav 24,8,208\nRD 48,3,12288\nMvav 24,12,212\nRD 52,3,13312\nMvav 28,12,220\nRD 56,3,14336\nMvav 28,8,216\ns_add_u32 m0,s48,s44\n"
"RD 60,3,15360\nMvav 32,16,208\nMvav 32,20,212\nDS 1,20,36,0\nMvav 36,20,220\nMvav 36,16,216\nMvav 40,24,208\nDS 1,20,37,1024\nMvav 40,28,212\n"
"Mvav 44,28,220\nMvav 44,24,216\nWL 0\nDS 1,20,38,2048\nMvav 48,32,208\nRD 0,3,16384\nMvav 48,36,212\nRD 4,3,17408\nMvav 52,36,220\nRD 8,3,18432\n"
"Mvav 52,32,216\nRD 12,3,19456\nMvav 56,40,208\nRD 16,3,20480\nMvav 56,44,212\nRD 20,3,21504\nMvav 60,44,220\nRD 24,3,22528\nMvav 60,40,216\n"
"RD 28,3,23552\nMvav 64,48,208\nMvav 64,52,212\nDS 1,20,39,3072\nMvav 68,52,220\ns_add_u32 m0,m0,0x1000\nMvav 68,48,216\nMvav 72,56,208\nDS 1,20,40,0\n"
"Mvav 72,60,212\nMvav 76,60,220\nMvav 76,56,216\nWL 0\nDS 1,20,41,1024\nMvav 80,0,208\nRD 32,3,24576\nv_lshrrev_b32 v12,4,v0\nMvav 80,4,212\n"
"s_mov_b32 s44,s47\nRD 36,3,25600\nv_lshrrev_b32 v13,2,v12\nMvav 84,4,220\ns_add_u32 s47,s47,0x8000\nRD 40,3,26624\nv_lshlrev_b32 v10,7,v13\n"
"Mvav 84,0,216\ns_cmp_eq_u32 s47,0x28000\nRD 44,3,27648\nv_bfe_u32 v13,v12,3,2\nMvav 88,8,208\ns_cselect_b32 s47,0,s47\nRD 48,3,28672\n"
"v_lshl_add_u32 v10,v13,5,v10\nMvav 88,12,212\nRD 52,3,29696\nv_and_b32 v12,3,v12\nMvav 92,12,220\nRD 56,3,30720\nv_lshl_add_u32 v10,v12,3,v10\n"
"Mvav 92,8,216\nRD 60,3,31744\nMvav 96,16,208\nMvav 96,20,212\nDS 1,20,42,2048\nMvav 100,20,220\nv_add_u32 v5,s47,v10\nv_xor_b32 v6,32,v5\n"
"Mvav 100,16,216\nv_xor_b32 v7,64,v5\nv_xor_b32 v8,96,v5\nMvav 104,24,208\nDS 1,20,43,3072\nMvav 104,28,212\nv_add_u32 v1,0x80,v1\nMvav 108,28,220\n"
"Mvav 108,24,216\ns_waitcnt vmcnt(24) lgkmcnt(0)\ns_barrier\nMvav 112,32,208\nTR 0,5,0\nMvav 112,36,212\nTR 2,5,2048\nMvav 116,36,220\nTR 4,5,4096\n"
"Mvav 116,32,216\nTR 6,5,6144\nMvav 120,40,208\nTR 8,5,8192\nMvav 120,44,212\nTR 10,5,10240\nMvav 124,44,220\nTR 12,5,12288\nMvav 124,40,216\n"
"TR 14,5,14336\nMvav 128,48,208\nTR 16,5,16384\nMvav 128,52,212\nTR 18,5,18432\nMvav 132,52,220\nTR 20,5,20480\nMvav 132,48,216\nTR 22,5,22528\n"
"Mvav 136,56,208\nTR 24,5,24576\nMvav 136,60,212\nTR 26,5,26624\nMvav 140,60,220\nTR 28,5,28672\nMvav 140,56,216\nTR 30,5,30720\nfused_op_tew:\n"
"v_mov_b32 v238,v234\nv_mov_b32 v239,v235\ns_nop 0\nv_permlane32_swap_b32 v234,v238\nv_permlane32_swap_b32 v235,v239\nv_add_f32 v234,v234,v238\n"
"v_add_f32 v235,v235,v239\nv_mov_b32 v238,v234\nv_mov_b32 v239,v235\ns_nop 0\nv_permlane16_swap_b32 v234,v238\nv_permlane16_swap_b32 v235,v239\n"
"v_add_f32 v234,v234,v238\nv_add_f32 v235,v235,v239\ns_nop 1\ns_cmp_eq_u32 s58,0\ns_cbranch_scc0 fused_op_endb\nv_rcp_f32 v236,v234\n"
"v_rcp_f32 v237,v235\ns_nop 0\nv_pk_mul_f32 v[16:17],v[16:17],v[236:237] op_sel:[0,0] op_sel_hi:[1,0]\n"
"v_pk_mul_f32 v[20:21],v[20:21],v[236:237] op_sel:[0,1] op_sel_hi:[1,1]\nv_pk_mul_f32 v[18:19],v[18:19],v[236:237] op_sel:[0,0] op_sel_hi:[1,0]\n"
"v_pk_mul_f32 v[22:23],v[22:23],v[236:237] op_sel:[0,1] op_sel_hi:[1,1]\nv_pk_mul_f32 v[24:25],v[24:25],v[236:237] op_sel:[0,0] op_sel_hi:[1,0]\n"
"v_pk_mul_f32 v[28:29],v[28:29],v[236:237] op_sel:[0,1] op_sel_hi:[1,1]\nv_pk_mul_f32 v[26:27],v[26:27],v[236:237] op_sel:[0,0] op_sel_hi:[1,0]\n"
"v_pk_mul_f32 v[30:31],v[30:31],v[236:237] op_sel:[0,1] op_sel_hi:[1,1]\nCV 16,16,17\nCV 20,20,21\nCV 17,18,19\nCV 21,22,23\nCV 18,24,25\nCV 22,28,29\n"
"CV 19,26,27\nCV 23,30,31\nv_pk_mul_f32 v[32:33],v[32:33],v[236:237] op_sel:[0,0] op_sel_hi:[1,0]\n"
"v_pk_mul_f32 v[36:37],v[36:37],v[236:237] op_sel:[0,1] op_sel_hi:[1,1]\nv_pk_mul_f32 v[34:35],v[34:35],v[236:237] op_sel:[0,0] op_sel_hi:[1,0]\n"
"v_pk_mul_f32 v[38:39],v[38:39],v[236:237] op_sel:[0,1] op_sel_hi:[1,1]\nv_pk_mul_f32 v[40:41],v[40:41],v[236:237] op_sel:[0,0] op_sel_hi:[1,0]\n"
"v_pk_mul_f32 v[44:45],v[44:45],v[236:237] op_sel:[0,1] op_sel_hi:[1,1]\nv_pk_mul_f32 v[42:43],v[42:43],v[236:237] op_sel:[0,0] op_sel_hi:[1,0]\n"
"v_pk_mul_f32 v[46:47],v[46:47],v[236:237] op_sel:[0,1] op_sel_hi:[1,1]\nCV 32,32,33\nCV 36,36,37\nCV 33,34,35\nCV 37,38,39\nCV 34,40,41\nCV 38,44,45\n"
"CV 35,42,43\nCV 39,46,47\nv_pk_mul_f32 v[48:49],v[48:49],v[236:237] op_sel:[0,0] op_sel_hi:[1,0]\n"
"v_pk_mul_f32 v[52:53],v[52:53],v[236:237] op_sel:[0,1] op_sel_hi:[1,1]\nv_pk_mul_f32 v[50:51],v[50:51],v[236:237] op_sel:[0,0] op_sel_hi:[1,0]\n"
"v_pk_mul_f32 v[54:55],v[54:55],v[236:237] op_sel:[0,1] op_sel_hi:[1,1]\nv_pk_mul_f32 v[56:57],v[56:57],v[236:237] op_sel:[0,0] op_sel_hi:[1,0]\n"
"v_pk_mul_f32 v[60:61],v[60:61],v[236:237] op_sel:[0,1] op_sel_hi:[1,1]\nv_pk_mul_f32 v[58:59],v[58:59],v[236:237] op_sel:[0,0] op_sel_hi:[1,0]\n"
"v_pk_mul_f32 v[62:63],v[62:63],v[236:237] op_sel:[0,1] op_sel_hi:[1,1]\nCV 48,48,49\nCV 52,52,53\nCV 49,50,51\nCV 53,54,55\nCV 50,56,57\nCV 54,60,61\n"
"CV 51,58,59\nCV 55,62,63\nv_pk_mul_f32 v[64:65],v[64:65],v[236:237] op_sel:[0,0] op_sel_hi:[1,0]\n"
"v_pk_mul_f32 v[68:69],v[68:69],v[236:237] op_sel:[0,1] op_sel_hi:[1,1]\nv_pk_mul_f32 v[66:67],v[66:67],v[236:237] op_sel:[0,0] op_sel_hi:[1,0]\n"
"v_pk_mul_f32 v[70:71],v[70:71],v[236:237] op_sel:[0,1] op_sel_hi:[1,1]\nv_pk_mul_f32 v[72:73],v[72:73],v[236:237] op_sel:[0,0] op_sel_hi:[1,0]\n"
"v_pk_mul_f32 v[76:77],v[76:77],v[236:237] op_sel:[0,1] op_sel_hi:[1,1]\nv_pk_mul_f32 v[74:75],v[74:75],v[236:237] op_sel:[0,0] op_sel_hi:[1,0]\n"
"v_pk_mul_f32 v[78:79],v[78:79],v[236:237] op_sel:[0,1] op_sel_hi:[1,1]\nCV 64,64,65\nCV 68,68,69\nCV 65,66,67\nCV 69,70,71\nCV 66,72,73\nCV 70,76,77\n"
"CV 67,74,75\nCV 71,78,79\nv_pk_mul_f32 v[80:81],v[80:81],v[236:237] op_sel:[0,0] op_sel_hi:[1,0]\n"
"v_pk_mul_f32 v[84:85],v[84:85],v[236:237] op_sel:[0,1] op_sel_hi:[1,1]\nv_pk_mul_f32 v[82:83],v[82:83],v[236:237] op_sel:[0,0] op_sel_hi:[1,0]\n"
"v_pk_mul_f32 v[86:87],v[86:87],v[236:237] op_sel:[0,1] op_sel_hi:[1,1]\nv_pk_mul_f32 v[88:89],v[88:89],v[236:237] op_sel:[0,0] op_sel_hi:[1,0]\n"
"v_pk_mul_f32 v[92:93],v[92:93],v[236:237] op_sel:[0,1] op_sel_hi:[1,1]\nv_pk_mul_f32 v[90:91],v[90:91],v[236:237] op_sel:[0,0] op_sel_hi:[1,0]\n"
"v_pk_mul_f32 v[94:95],v[94:95],v[236:237] op_sel:[0,1] op_sel_hi:[1,1]\nCV 80,80,81\nCV 84,84,85\nCV 81,82,83\nCV 85,86,87\nCV 82,88,89\nCV 86,92,93\n"
"CV 83,90,91\nCV 87,94,95\nv_pk_mul_f32 v[96:97],v[96:97],v[236:237] op_sel:[0,0] op_sel_hi:[1,0]\n"
"v_pk_mul_f32 v[100:101],v[100:101],v[236:237] op_sel:[0,1] op_sel_hi:[1,1]\nv_pk_mul_f32 v[98:99],v[98:99],v[236:237] op_sel:[0,0] op_sel_hi:[1,0]\n"
"v_pk_mul_f32 v[102:103],v[102:103],v[236:237] op_sel:[0,1] op_sel_hi:[1,1]\nv_pk_mul_f32 v[104:105],v[104:105],v[236:237] op_sel:[0,0] op_sel_hi:[1,0]\n"
"v_pk_mul_f32 v[108:109],v[108:109],v[236:237] op_sel:[0,1] op_sel_hi:[1,1]\nv_pk_mul_f32 v[106:107],v[106:107],v[236:237] op_sel:[0,0] op_sel_hi:[1,0]\n"
"v_pk_mul_f32 v[110:111],v[110:111],v[236:237] op_sel:[0,1] op_sel_hi:[1,1]\nCV 96,96,97\nCV 100,100,101\nCV 97,98,99\nCV 101,102,103\nCV 98,104,105\n"
"CV 102,108,109\nCV 99,106,107\nCV 103,110,111\nv_pk_mul_f32 v[112:113],v[112:113],v[236:237] op_sel:[0,0] op_sel_hi:[1,0]\n"
"v_pk_mul_f32 v[116:117],v[116:117],v[236:237] op_sel:[0,1] op_sel_hi:[1,1]\nv_pk_mul_f32 v[114:115],v[114:115],v[236:237] op_sel:[0,0] op_sel_hi:[1,0]\n"
"v_pk_mul_f32 v[118:119],v[118:119],v[236:237] op_sel:[0,1] op_sel_hi:[1,1]\nv_pk_mul_f32 v[120:121],v[120:121],v[236:237] op_sel:[0,0] op_sel_hi:[1,0]\n"
"v_pk_mul_f32 v[124:125],v[124:125],v[236:237] op_sel:[0,1] op_sel_hi:[1,1]\nv_pk_mul_f32 v[122:123],v[122:123],v[236:237] op_sel:[0,0] op_sel_hi:[1,0]\n"
"v_pk_mul_f32 v[126:127],v[126:127],v[236:237] op_sel:[0,1] op_sel_hi:[1,1]\nCV 112,112,113\nCV 116,116,117\nCV 113,114,115\nCV 117,118,119\n"
"CV 114,120,121\nCV 118,124,125\nCV 115,122,123\nCV 119,126,127\nv_pk_mul_f32 v[128:129],v[128:129],v[236:237] op_sel:[0,0] op_sel_hi:[1,0]\n"
"v_pk_mul_f32 v[132:133],v[132:133],v[236:237] op_sel:[0,1] op_sel_hi:[1,1]\nv_pk_mul_f32 v[130:131],v[130:131],v[236:237] op_sel:[0,0] op_sel_hi:[1,0]\n"
"v_pk_mul_f32 v[134:135],v[134:135],v[236:237] op_sel:[0,1] op_sel_hi:[1,1]\nv_pk_mul_f32 v[136:137],v[136:137],v[236:237] op_sel:[0,0] op_sel_hi:[1,0]\n"
"v_pk_mul_f32 v[140:141],v[140:141],v[236:237] op_sel:[0,1] op_sel_hi:[1,1]\nv_pk_mul_f32 v[138:139],v[138:139],v[236:237] op_sel:[0,0] op_sel_hi:[1,0]\n"
"v_pk_mul_f32 v[142:143],v[142:143],v[236:237] op_sel:[0,1] op_sel_hi:[1,1]\nCV 128,128,129\nCV 132,132,133\nCV 129,130,131\nCV 133,134,135\n"
"CV 130,136,137\nCV 134,140,141\nCV 131,138,139\nCV 135,142,143\ns_branch fused_op_opa\nfused_op_endb:\ns_cmp_eq_u32 s69,0\n"
"s_cbranch_scc0 fused_op_helper_end\nv_rcp_f32 v236,v234\nv_rcp_f32 v237,v235\ns_nop 0\n"
"v_pk_mul_f32 v[16:17],v[16:17],v[236:237] op_sel:[0,0] op_sel_hi:[1,0]\nv_pk_mul_f32 v[20:21],v[20:21],v[236:237] op_sel:[0,1] op_sel_hi:[1,1]\n"
"v_pk_mul_f32 v[18:19],v[18:19],v[236:237] op_sel:[0,0] op_sel_hi:[1,0]\nv_pk_mul_f32 v[22:23],v[22:23],v[236:237] op_sel:[0,1] op_sel_hi:[1,1]\n"
"v_pk_mul_f32 v[24:25],v[24:25],v[236:237] op_sel:[0,0] op_sel_hi:[1,0]\nv_pk_mul_f32 v[28:29],v[28:29],v[236:237] op_sel:[0,1] op_sel_hi:[1,1]\n"
"v_pk_mul_f32 v[26:27],v[26:27],v[236:237] op_sel:[0,0] op_sel_hi:[1,0]\nv_pk_mul_f32 v[30:31],v[30:31],v[236:237] op_sel:[0,1] op_sel_hi:[1,1]\n"
"CV 16,16,17\nCV 20,20,21\nCV 17,18,19\nCV 21,22,23\nCV 18,24,25\nCV 22,28,29\nCV 19,26,27\nCV 23,30,31\nv_accvgpr_write_b32 a64,v16\n"
"v_accvgpr_write_b32 a96,v20\nv_accvgpr_write_b32 a65,v17\nv_accvgpr_write_b32 a97,v21\nv_accvgpr_write_b32 a66,v18\nv_accvgpr_write_b32 a98,v22\n"
"v_accvgpr_write_b32 a67,v19\nv_accvgpr_write_b32 a99,v23\nv_pk_mul_f32 v[32:33],v[32:33],v[236:237] op_sel:[0,0] op_sel_hi:[1,0]\n"
"v_pk_mul_f32 v[36:37],v[36:37],v[236:237] op_sel:[0,1] op_sel_hi:[1,1]\nv_pk_mul_f32 v[34:35],v[34:35],v[236:237] op_sel:[0,0] op_sel_hi:[1,0]\n"
"v_pk_mul_f32 v[38:39],v[38:39],v[236:237] op_sel:[0,1] op_sel_hi:[1,1]\nv_pk_mul_f32 v[40:41],v[40:41],v[236:237] op_sel:[0,0] op_sel_hi:[1,0]\n"
"v_pk_mul_f32 v[44:45],v[44:45],v[236:237] op_sel:[0,1] op_sel_hi:[1,1]\nv_pk_mul_f32 v[42:43],v[42:43],v[236:237] op_sel:[0,0] op_sel_hi:[1,0]\n"
"v_pk_mul_f32 v[46:47],v[46:47],v[236:237] op_sel:[0,1] op_sel_hi:[1,1]\nCV 32,32,33\nCV 36,36,37\nCV 33,34,35\nCV 37,38,39\nCV 34,40,41\nCV 38,44,45\n"
"CV 35,42,43\nCV 39,46,47\nv_accvgpr_write_b32 a68,v32\nv_accvgpr_write_b32 a100,v36\nv_accvgpr_write_b32 a69,v33\nv_accvgpr_write_b32 a101,v37\n"
"v_accvgpr_write_b32 a70,v34\nv_accvgpr_write_b32 a102,v38\nv_accvgpr_write_b32 a71,v35\nv_accvgpr_write_b32 a103,v39\n"
"v_pk_mul_f32 v[48:49],v[48:49],v[236:237] op_sel:[0,0] op_sel_hi:[1,0]\nv_pk_mul_f32 v[52:53],v[52:53],v[236:237] op_sel:[0,1] op_sel_hi:[1,1]\n"
"v_pk_mul_f32 v[50:51],v[50:51],v[236:237] op_sel:[0,0] op_sel_hi:[1,0]\nv_pk_mul_f32 v[54:55],v[54:55],v[236:237] op_sel:[0,1] op_sel_hi:[1,1]\n"
"v_pk_mul_f32 v[56:57],v[56:57],v[236:237] op_sel:[0,0] op_sel_hi:[1,0]\nv_pk_mul_f32 v[60:61],v[60:61],v[236:237] op_sel:[0,1] op_sel_hi:[1,1]\n"
"v_pk_mul_f32 v[58:59],v[58:59],v[236:237] op_sel:[0,0] op_sel_hi:[1,0]\nv_pk_mul_f32 v[62:63],v[62:63],v[236:237] op_sel:[0,1] op_sel_hi:[1,1]\n"
"CV 48,48,49\nCV 52,52,53\nCV 49,50,51\nCV 53,54,55\nCV 50,56,57\nCV 54,60,61\nCV 51,58,59\nCV 55,62,63\nv_accvgpr_write_b32 a72,v48\n"
"v_accvgpr_write_b32 a104,v52\nv_accvgpr_write_b32 a73,v49\nv_accvgpr_write_b32 a105,v53\nv_accvgpr_write_b32 a74,v50\nv_accvgpr_write_b32 a106,v54\n"
"v_accvgpr_write_b32 a75,v51\nv_accvgpr_write_b32 a107,v55\nv_pk_mul_f32 v[64:65],v[64:65],v[236:237] op_sel:[0,0] op_sel_hi:[1,0]\n"
"v_pk_mul_f32 v[68:69],v[68:69],v[236:237] op_sel:[0,1] op_sel_hi:[1,1]\nv_pk_mul_f32 v[66:67],v[66:67],v[236:237] op_sel:[0,0] op_sel_hi:[1,0]\n"
"v_pk_mul_f32 v[70:71],v[70:71],v[236:237] op_sel:[0,1] op_sel_hi:[1,1]\nv_pk_mul_f32 v[72:73],v[72:73],v[236:237] op_sel:[0,0] op_sel_hi:[1,0]\n"
"v_pk_mul_f32 v[76:77],v[76:77],v[236:237] op_sel:[0,1] op_sel_hi:[1,1]\nv_pk_mul_f32 v[74:75],v[74:75],v[236:237] op_sel:[0,0] op_sel_hi:[1,0]\n"
"v_pk_mul_f32 v[78:79],v[78:79],v[236:237] op_sel:[0,1] op_sel_hi:[1,1]\nCV 64,64,65\nCV 68,68,69\nCV 65,66,67\nCV 69,70,71\nCV 66,72,73\nCV 70,76,77\n"
"CV 67,74,75\nCV 71,78,79\nv_accvgpr_write_b32 a76,v64\nv_accvgpr_write_b32 a108,v68\nv_accvgpr_write_b32 a77,v65\nv_accvgpr_write_b32 a109,v69\n"
"v_accvgpr_write_b32 a78,v66\nv_accvgpr_write_b32 a110,v70\nv_accvgpr_write_b32 a79,v67\nv_accvgpr_write_b32 a111,v71\n"
"v_pk_mul_f32 v[80:81],v[80:81],v[236:237] op_sel:[0,0] op_sel_hi:[1,0]\nv_pk_mul_f32 v[84:85],v[84:85],v[236:237] op_sel:[0,1] op_sel_hi:[1,1]\n"
"v_pk_mul_f32 v[82:83],v[82:83],v[236:237] op_sel:[0,0] op_sel_hi:[1,0]\nv_pk_mul_f32 v[86:87],v[86:87],v[236:237] op_sel:[0,1] op_sel_hi:[1,1]\n"
"v_pk_mul_f32 v[88:89],v[88:89],v[236:237] op_sel:[0,0] op_sel_hi:[1,0]\nv_pk_mul_f32 v[92:93],v[92:93],v[236:237] op_sel:[0,1] op_sel_hi:[1,1]\n"
"v_pk_mul_f32 v[90:91],v[90:91],v[236:237] op_sel:[0,0] op_sel_hi:[1,0]\nv_pk_mul_f32 v[94:95],v[94:95],v[236:237] op_sel:[0,1] op_sel_hi:[1,1]\n"
"CV 80,80,81\nCV 84,84,85\nCV 81,82,83\nCV 85,86,87\nCV 82,88,89\nCV 86,92,93\nCV 83,90,91\nCV 87,94,95\nv_accvgpr_write_b32 a80,v80\n"
"v_accvgpr_write_b32 a112,v84\nv_accvgpr_write_b32 a81,v81\nv_accvgpr_write_b32 a113,v85\nv_accvgpr_write_b32 a82,v82\nv_accvgpr_write_b32 a114,v86\n"
"v_accvgpr_write_b32 a83,v83\nv_accvgpr_write_b32 a115,v87\nv_pk_mul_f32 v[96:97],v[96:97],v[236:237] op_sel:[0,0] op_sel_hi:[1,0]\n"
"v_pk_mul_f32 v[100:101],v[100:101],v[236:237] op_sel:[0,1] op_sel_hi:[1,1]\nv_pk_mul_f32 v[98:99],v[98:99],v[236:237] op_sel:[0,0] op_sel_hi:[1,0]\n"
"v_pk_mul_f32 v[102:103],v[102:103],v[236:237] op_sel:[0,1] op_sel_hi:[1,1]\nv_pk_mul_f32 v[104:105],v[104:105],v[236:237] op_sel:[0,0] op_sel_hi:[1,0]\n"
"v_pk_mul_f32 v[108:109],v[108:109],v[236:237] op_sel:[0,1] op_sel_hi:[1,1]\nv_pk_mul_f32 v[106:107],v[106:107],v[236:237] op_sel:[0,0] op_sel_hi:[1,0]\n"
"v_pk_mul_f32 v[110:111],v[110:111],v[236:237] op_sel:[0,1] op_sel_hi:[1,1]\nCV 96,96,97\nCV 100,100,101\nCV 97,98,99\nCV 101,102,103\nCV 98,104,105\n"
"CV 102,108,109\nCV 99,106,107\nCV 103,110,111\nv_accvgpr_write_b32 a84,v96\nv_accvgpr_write_b32 a116,v100\nv_accvgpr_write_b32 a85,v97\n"
"v_accvgpr_write_b32 a117,v101\nv_accvgpr_write_b32 a86,v98\nv_accvgpr_write_b32 a118,v102\nv_accvgpr_write_b32 a87,v99\nv_accvgpr_write_b32 a119,v103\n"
"v_pk_mul_f32 v[112:113],v[112:113],v[236:237] op_sel:[0,0] op_sel_hi:[1,0]\nv_pk_mul_f32 v[116:117],v[116:117],v[236:237] op_sel:[0,1] op_sel_hi:[1,1]\n"
"v_pk_mul_f32 v[114:115],v[114:115],v[236:237] op_sel:[0,0] op_sel_hi:[1,0]\nv_pk_mul_f32 v[118:119],v[118:119],v[236:237] op_sel:[0,1] op_sel_hi:[1,1]\n"
"v_pk_mul_f32 v[120:121],v[120:121],v[236:237] op_sel:[0,0] op_sel_hi:[1,0]\nv_pk_mul_f32 v[124:125],v[124:125],v[236:237] op_sel:[0,1] op_sel_hi:[1,1]\n"
"v_pk_mul_f32 v[122:123],v[122:123],v[236:237] op_sel:[0,0] op_sel_hi:[1,0]\nv_pk_mul_f32 v[126:127],v[126:127],v[236:237] op_sel:[0,1] op_sel_hi:[1,1]\n"
"CV 112,112,113\nCV 116,116,117\nCV 113,114,115\nCV 117,118,119\nCV 114,120,121\nCV 118,124,125\nCV 115,122,123\nCV 119,126,127\n"
"v_accvgpr_write_b32 a88,v112\nv_accvgpr_write_b32 a120,v116\nv_accvgpr_write_b32 a89,v113\nv_accvgpr_write_b32 a121,v117\nv_accvgpr_write_b32 a90,v114\n"
"v_accvgpr_write_b32 a122,v118\nv_accvgpr_write_b32 a91,v115\nv_accvgpr_write_b32 a123,v119\n"
"v_pk_mul_f32 v[128:129],v[128:129],v[236:237] op_sel:[0,0] op_sel_hi:[1,0]\nv_pk_mul_f32 v[132:133],v[132:133],v[236:237] op_sel:[0,1] op_sel_hi:[1,1]\n"
"v_pk_mul_f32 v[130:131],v[130:131],v[236:237] op_sel:[0,0] op_sel_hi:[1,0]\nv_pk_mul_f32 v[134:135],v[134:135],v[236:237] op_sel:[0,1] op_sel_hi:[1,1]\n"
"v_pk_mul_f32 v[136:137],v[136:137],v[236:237] op_sel:[0,0] op_sel_hi:[1,0]\nv_pk_mul_f32 v[140:141],v[140:141],v[236:237] op_sel:[0,1] op_sel_hi:[1,1]\n"
"v_pk_mul_f32 v[138:139],v[138:139],v[236:237] op_sel:[0,0] op_sel_hi:[1,0]\nv_pk_mul_f32 v[142:143],v[142:143],v[236:237] op_sel:[0,1] op_sel_hi:[1,1]\n"
"CV 128,128,129\nCV 132,132,133\nCV 129,130,131\nCV 133,134,135\nCV 130,136,137\nCV 134,140,141\nCV 131,138,139\nCV 135,142,143\n"
"v_accvgpr_write_b32 a92,v128\nv_accvgpr_write_b32 a124,v132\nv_accvgpr_write_b32 a93,v129\nv_accvgpr_write_b32 a125,v133\nv_accvgpr_write_b32 a94,v130\n"
"v_accvgpr_write_b32 a126,v134\nv_accvgpr_write_b32 a95,v131\nv_accvgpr_write_b32 a127,v135\ns_branch fused_op_opb\nfused_op_helper_end:\n"
"v_rcp_f32 v236,v234\nv_rcp_f32 v237,v235\ns_nop 0\nv_pk_mul_f32 v[16:17],v[16:17],v[236:237] op_sel:[0,0] op_sel_hi:[1,0]\n"
"v_pk_mul_f32 v[20:21],v[20:21],v[236:237] op_sel:[0,1] op_sel_hi:[1,1]\nv_pk_mul_f32 v[18:19],v[18:19],v[236:237] op_sel:[0,0] op_sel_hi:[1,0]\n"
"v_pk_mul_f32 v[22:23],v[22:23],v[236:237] op_sel:[0,1] op_sel_hi:[1,1]\nv_pk_mul_f32 v[24:25],v[24:25],v[236:237] op_sel:[0,0] op_sel_hi:[1,0]\n"
"v_pk_mul_f32 v[28:29],v[28:29],v[236:237] op_sel:[0,1] op_sel_hi:[1,1]\nv_pk_mul_f32 v[26:27],v[26:27],v[236:237] op_sel:[0,0] op_sel_hi:[1,0]\n"
"v_pk_mul_f32 v[30:31],v[30:31],v[236:237] op_sel:[0,1] op_sel_hi:[1,1]\nCV 16,16,17\nCV 20,20,21\nCV 17,18,19\nCV 21,22,23\nCV 18,24,25\nCV 22,28,29\n"
"CV 19,26,27\nCV 23,30,31\nv_pk_mul_f32 v[32:33],v[32:33],v[236:237] op_sel:[0,0] op_sel_hi:[1,0]\n"
"v_pk_mul_f32 v[36:37],v[36:37],v[236:237] op_sel:[0,1] op_sel_hi:[1,1]\nv_pk_mul_f32 v[34:35],v[34:35],v[236:237] op_sel:[0,0] op_sel_hi:[1,0]\n"
"v_pk_mul_f32 v[38:39],v[38:39],v[236:237] op_sel:[0,1] op_sel_hi:[1,1]\nv_pk_mul_f32 v[40:41],v[40:41],v[236:237] op_sel:[0,0] op_sel_hi:[1,0]\n"
"v_pk_mul_f32 v[44:45],v[44:45],v[236:237] op_sel:[0,1] op_sel_hi:[1,1]\nv_pk_mul_f32 v[42:43],v[42:43],v[236:237] op_sel:[0,0] op_sel_hi:[1,0]\n"
"v_pk_mul_f32 v[46:47],v[46:47],v[236:237] op_sel:[0,1] op_sel_hi:[1,1]\nCV 32,32,33\nCV 36,36,37\nCV 33,34,35\nCV 37,38,39\nCV 34,40,41\nCV 38,44,45\n"
"CV 35,42,43\nCV 39,46,47\nv_pk_mul_f32 v[48:49],v[48:49],v[236:237] op_sel:[0,0] op_sel_hi:[1,0]\n"
"v_pk_mul_f32 v[52:53],v[52:53],v[236:237] op_sel:[0,1] op_sel_hi:[1,1]\nv_pk_mul_f32 v[50:51],v[50:51],v[236:237] op_sel:[0,0] op_sel_hi:[1,0]\n"
"v_pk_mul_f32 v[54:55],v[54:55],v[236:237] op_sel:[0,1] op_sel_hi:[1,1]\nv_pk_mul_f32 v[56:57],v[56:57],v[236:237] op_sel:[0,0] op_sel_hi:[1,0]\n"
"v_pk_mul_f32 v[60:61],v[60:61],v[236:237] op_sel:[0,1] op_sel_hi:[1,1]\nv_pk_mul_f32 v[58:59],v[58:59],v[236:237] op_sel:[0,0] op_sel_hi:[1,0]\n"
"v_pk_mul_f32 v[62:63],v[62:63],v[236:237] op_sel:[0,1] op_sel_hi:[1,1]\nCV 48,48,49\nCV 52,52,53\nCV 49,50,51\nCV 53,54,55\nCV 50,56,57\nCV 54,60,61\n"
"CV 51,58,59\nCV 55,62,63\nv_pk_mul_f32 v[64:65],v[64:65],v[236:237] op_sel:[0,0] op_sel_hi:[1,0]\n"
"v_pk_mul_f32 v[68:69],v[68:69],v[236:237] op_sel:[0,1] op_sel_hi:[1,1]\nv_pk_mul_f32 v[66:67],v[66:67],v[236:237] op_sel:[0,0] op_sel_hi:[1,0]\n"
"v_pk_mul_f32 v[70:71],v[70:71],v[236:237] op_sel:[0,1] op_sel_hi:[1,1]\nv_pk_mul_f32 v[72:73],v[72:73],v[236:237] op_sel:[0,0] op_sel_hi:[1,0]\n"
"v_pk_mul_f32 v[76:77],v[76:77],v[236:237] op_sel:[0,1] op_sel_hi:[1,1]\nv_pk_mul_f32 v[74:75],v[74:75],v[236:237] op_sel:[0,0] op_sel_hi:[1,0]\n"
"v_pk_mul_f32 v[78:79],v[78:79],v[236:237] op_sel:[0,1] op_sel_hi:[1,1]\nCV 64,64,65\nCV 68,68,69\nCV 65,66,67\nCV 69,70,71\nCV 66,72,73\nCV 70,76,77\n"
"CV 67,74,75\nCV 71,78,79\nv_pk_mul_f32 v[80:81],v[80:81],v[236:237] op_sel:[0,0] op_sel_hi:[1,0]\n"
"v_pk_mul_f32 v[84:85],v[84:85],v[236:237] op_sel:[0,1] op_sel_hi:[1,1]\nv_pk_mul_f32 v[82:83],v[82:83],v[236:237] op_sel:[0,0] op_sel_hi:[1,0]\n"
"v_pk_mul_f32 v[86:87],v[86:87],v[236:237] op_sel:[0,1] op_sel_hi:[1,1]\nv_pk_mul_f32 v[88:89],v[88:89],v[236:237] op_sel:[0,0] op_sel_hi:[1,0]\n"
"v_pk_mul_f32 v[92:93],v[92:93],v[236:237] op_sel:[0,1] op_sel_hi:[1,1]\nv_pk_mul_f32 v[90:91],v[90:91],v[236:237] op_sel:[0,0] op_sel_hi:[1,0]\n"
"v_pk_mul_f32 v[94:95],v[94:95],v[236:237] op_sel:[0,1] op_sel_hi:[1,1]\nCV 80,80,81\nCV 84,84,85\nCV 81,82,83\nCV 85,86,87\nCV 82,88,89\nCV 86,92,93\n"
"CV 83,90,91\nCV 87,94,95\nv_pk_mul_f32 v[96:97],v[96:97],v[236:237] op_sel:[0,0] op_sel_hi:[1,0]\n"
"v_pk_mul_f32 v[100:101],v[100:101],v[236:237] op_sel:[0,1] op_sel_hi:[1,1]\nv_pk_mul_f32 v[98:99],v[98:99],v[236:237] op_sel:[0,0] op_sel_hi:[1,0]\n"
"v_pk_mul_f32 v[102:103],v[102:103],v[236:237] op_sel:[0,1] op_sel_hi:[1,1]\nv_pk_mul_f32 v[104:105],v[104:105],v[236:237] op_sel:[0,0] op_sel_hi:[1,0]\n"
"v_pk_mul_f32 v[108:109],v[108:109],v[236:237] op_sel:[0,1] op_sel_hi:[1,1]\nv_pk_mul_f32 v[106:107],v[106:107],v[236:237] op_sel:[0,0] op_sel_hi:[1,0]\n"
"v_pk_mul_f32 v[110:111],v[110:111],v[236:237] op_sel:[0,1] op_sel_hi:[1,1]\nCV 96,96,97\nCV 100,100,101\nCV 97,98,99\nCV 101,102,103\nCV 98,104,105\n"
"CV 102,108,109\nCV 99,106,107\nCV 103,110,111\nv_pk_mul_f32 v[112:113],v[112:113],v[236:237] op_sel:[0,0] op_sel_hi:[1,0]\n"
"v_pk_mul_f32 v[116:117],v[116:117],v[236:237] op_sel:[0,1] op_sel_hi:[1,1]\nv_pk_mul_f32 v[114:115],v[114:115],v[236:237] op_sel:[0,0] op_sel_hi:[1,0]\n"
"v_pk_mul_f32 v[118:119],v[118:119],v[236:237] op_sel:[0,1] op_sel_hi:[1,1]\nv_pk_mul_f32 v[120:121],v[120:121],v[236:237] op_sel:[0,0] op_sel_hi:[1,0]\n"
"v_pk_mul_f32 v[124:125],v[124:125],v[236:237] op_sel:[0,1] op_sel_hi:[1,1]\nv_pk_mul_f32 v[122:123],v[122:123],v[236:237] op_sel:[0,0] op_sel_hi:[1,0]\n"
"v_pk_mul_f32 v[126:127],v[126:127],v[236:237] op_sel:[0,1] op_sel_hi:[1,1]\nCV 112,112,113\nCV 116,116,117\nCV 113,114,115\nCV 117,118,119\n"
"CV 114,120,121\nCV 118,124,125\nCV 115,122,123\nCV 119,126,127\nv_pk_mul_f32 v[128:129],v[128:129],v[236:237] op_sel:[0,0] op_sel_hi:[1,0]\n"
"v_pk_mul_f32 v[132:133],v[132:133],v[236:237] op_sel:[0,1] op_sel_hi:[1,1]\nv_pk_mul_f32 v[130:131],v[130:131],v[236:237] op_sel:[0,0] op_sel_hi:[1,0]\n"
"v_pk_mul_f32 v[134:135],v[134:135],v[236:237] op_sel:[0,1] op_sel_hi:[1,1]\nv_pk_mul_f32 v[136:137],v[136:137],v[236:237] op_sel:[0,0] op_sel_hi:[1,0]\n"
"v_pk_mul_f32 v[140:141],v[140:141],v[236:237] op_sel:[0,1] op_sel_hi:[1,1]\nv_pk_mul_f32 v[138:139],v[138:139],v[236:237] op_sel:[0,0] op_sel_hi:[1,0]\n"
"v_pk_mul_f32 v[142:143],v[142:143],v[236:237] op_sel:[0,1] op_sel_hi:[1,1]\nCV 128,128,129\nCV 132,132,133\nCV 129,130,131\nCV 133,134,135\n"
"CV 130,136,137\nCV 134,140,141\nCV 131,138,139\nCV 135,142,143\ns_mul_i32 s70,s68,131072\ns_mul_i32 s49,s10,32768\ns_add_u32 s70,s70,s49\n"
"s_add_u32 s70,s70,0x3100000\nbuffer_store_dwordx4 v[16:19],v0,s[24:27],s70 offen offset:0 sc1\n"
"buffer_store_dwordx4 v[32:35],v0,s[24:27],s70 offen offset:1024 sc1\nbuffer_store_dwordx4 v[48:51],v0,s[24:27],s70 offen offset:2048 sc1\n"
"buffer_store_dwordx4 v[64:67],v0,s[24:27],s70 offen offset:3072 sc1\ns_add_u32 s70,s70,4096\n"
"buffer_store_dwordx4 v[80:83],v0,s[24:27],s70 offen offset:0 sc1\nbuffer_store_dwordx4 v[96:99],v0,s[24:27],s70 offen offset:1024 sc1\n"
"buffer_store_dwordx4 v[112:115],v0,s[24:27],s70 offen offset:2048 sc1\nbuffer_store_dwordx4 v[128:131],v0,s[24:27],s70 offen offset:3072 sc1\n"
"s_add_u32 s70,s70,4096\nbuffer_store_dwordx4 v[20:23],v0,s[24:27],s70 offen offset:0 sc1\n"
"buffer_store_dwordx4 v[36:39],v0,s[24:27],s70 offen offset:1024 sc1\nbuffer_store_dwordx4 v[52:55],v0,s[24:27],s70 offen offset:2048 sc1\n"
"buffer_store_dwordx4 v[68:71],v0,s[24:27],s70 offen offset:3072 sc1\ns_add_u32 s70,s70,4096\n"
"buffer_store_dwordx4 v[84:87],v0,s[24:27],s70 offen offset:0 sc1\nbuffer_store_dwordx4 v[100:103],v0,s[24:27],s70 offen offset:1024 sc1\n"
"buffer_store_dwordx4 v[116:119],v0,s[24:27],s70 offen offset:2048 sc1\nbuffer_store_dwordx4 v[132:135],v0,s[24:27],s70 offen offset:3072 sc1\n"
"s_add_u32 s70,s70,4096\nv_mov_b32 v248,v232\nv_mov_b32 v249,v234\nv_mov_b32 v250,v233\nv_mov_b32 v251,v235\n"
"buffer_store_dwordx4 v[248:251],v0,s[24:27],s70 offen sc1\nfused_op_opb:\ns_mul_i32 s7,s68,512\ns_mul_i32 s49,s10,128\ns_add_u32 s7,s7,s49\n"
"s_add_u32 s7,s7,0x40000\ns_mul_i32 s70,s68,131072\ns_mul_i32 s49,s10,32768\ns_add_u32 s70,s70,s49\ns_mul_i32 s49,s58,-16777216\ns_add_u32 s70,s70,s49\n"
"s_add_u32 s70,s70,0x3100000\ns_add_u32 s71,s70,14336\ns_mul_i32 s9,s59,8192\ns_mul_i32 s49,s56,512\ns_add_u32 s9,s9,s49\ns_mul_i32 s49,s10,128\n"
"s_add_u32 s9,s9,s49\ns_add_u32 s9,s9,0x50000\ns_add_u32 s63,s9,131072\nWL 0\ns_mul_i32 s8,s56,4096\ns_mul_i32 s49,s10,128\ns_add_u32 s8,s8,s49\n"
"s_add_u32 s8,s8,0x90000\nv_and_b32 v15,0x70,v0\nv_lshlrev_b32 v15,5,v15\nbuffer_load_dword v14,v15,s[24:27],s8 offen sc1\ns_mul_i32 s94,s56,131072\n"
"s_mul_i32 s49,s10,32768\ns_add_u32 s94,s94,s49\ns_add_u32 s94,s94,0x4100000\nZvaa 144,0,64\nTR 32,6,0\nTR 34,6,2048\nMvaa 144,4,68\nTR 36,6,4096\n"
"TR 38,6,6144\nMvaa 144,8,72\nTR 40,6,8192\nTR 42,6,10240\nMvaa 144,12,76\nTR 44,6,12288\nMvaa 144,16,80\nTR 46,6,14336\nMvaa 144,20,84\nTR 48,6,16384\n"
"Mvaa 144,24,88\nTR 50,6,18432\nMvaa 144,28,92\ns_add_u32 m0,s48,s44\nTR 52,6,20480\nZvaa 148,28,124\nTR 54,6,22528\nMvaa 148,24,120\nDP 1,20,36,0\n"
"Mvaa 148,20,116\nTR 56,6,24576\nMvaa 148,16,112\nTR 58,6,26624\nMvaa 148,12,108\nDP 1,20,37,1024\nMvaa 148,8,104\nTR 60,6,28672\nMvaa 148,4,100\n"
"TR 62,6,30720\nMvaa 148,0,96\nWL 14\nDP 1,20,38,2048\nZvaa 152,32,64\nWL 12\nTR 0,7,0\nTR 2,7,2048\nMvaa 152,36,68\nWL 12\nTR 4,7,4096\nTR 6,7,6144\n"
"Mvaa 152,40,72\nWL 12\nTR 8,7,8192\nTR 10,7,10240\nMvaa 152,44,76\nWL 12\nTR 12,7,12288\nMvaa 152,48,80\nWL 11\nTR 14,7,14336\nMvaa 152,52,84\nWL 10\n"
"TR 16,7,16384\nMvaa 152,56,88\nWL 9\nTR 18,7,18432\nMvaa 152,60,92\nTR 20,7,20480\nZvaa 156,60,124\nTR 22,7,22528\nMvaa 156,56,120\nDP 1,20,39,3072\n"
"Mvaa 156,52,116\ns_add_u32 m0,m0,0x1000\nTR 24,7,24576\nMvaa 156,48,112\nTR 26,7,26624\nMvaa 156,44,108\nDP 1,20,40,0\nMvaa 156,40,104\nTR 28,7,28672\n"
"Mvaa 156,36,100\nTR 30,7,30720\ns_waitcnt vmcnt(5)\nv_cmp_ne_u32 vcc,s65,v14\ns_nop 0\ns_cbranch_vccnz fused_op_sp54\nfused_op_spb55:\n"
"v_mov_b32 v13,s65\ns_mov_b64 exec,1\nbuffer_store_dword v13,off,s[24:27],s7 sc1\ns_mov_b64 exec,-1\nMvaa 156,32,96\nWL 14\nDP 1,20,41,1024\n"
"Zvaa 160,0,64\nWL 12\nTR 32,8,0\nTR 34,8,2048\nMvaa 160,4,68\nWL 12\ns_mov_b32 s44,s47\nTR 36,8,4096\nTR 38,8,6144\nMvaa 160,8,72\nWL 12\n"
"s_add_u32 s47,s47,0x8000\nTR 40,8,8192\nTR 42,8,10240\nMvaa 160,12,76\nWL 12\ns_cmp_eq_u32 s47,0x28000\nTR 44,8,12288\nTR 46,8,14336\nMvaa 160,16,80\n"
"WL 12\ns_cselect_b32 s47,0,s47\nTR 48,8,16384\nTR 50,8,18432\nMvaa 160,20,84\nWL 12\nTR 52,8,20480\nTR 54,8,22528\nMvaa 160,24,88\nWL 12\n"
"TR 56,8,24576\nTR 58,8,26624\nMvaa 160,28,92\nTR 60,8,28672\nbuffer_load_dwordx4 v[16:19],v0,s[24:27],s94 offen offset:0 sc1\nZvaa 164,28,124\n"
"TR 62,8,30720\nbuffer_load_dwordx4 v[20:23],v0,s[24:27],s94 offen offset:1024 sc1\nMvaa 164,24,120\nDP 1,20,42,2048\nMvaa 164,20,116\n"
"v_add_u32 v5,s47,v10\nv_xor_b32 v6,32,v5\nMvaa 164,16,112\nv_xor_b32 v7,64,v5\nv_xor_b32 v8,96,v5\nMvaa 164,12,108\nDP 1,20,43,3072\nMvaa 164,8,104\n"
"v_add_u32 v1,0x80,v1\nMvaa 164,4,100\nbuffer_load_dwordx4 v[24:27],v0,s[24:27],s94 offen offset:2048 sc1\nMvaa 164,0,96\n"
"s_waitcnt vmcnt(29) lgkmcnt(0)\ns_barrier\nZvaa 168,32,64\nTR 0,5,0\nMvaa 168,36,68\nTR 2,5,2048\nMvaa 168,40,72\nTR 4,5,4096\nMvaa 168,44,76\n"
"TR 6,5,6144\nMvaa 168,48,80\nTR 8,5,8192\nbuffer_load_dwordx4 v[28:31],v0,s[24:27],s94 offen offset:3072 sc1\nMvaa 168,52,84\ns_add_u32 s94,s94,4096\n"
"TR 10,5,10240\nMvaa 168,56,88\nTR 12,5,12288\nMvaa 168,60,92\nTR 14,5,14336\nZvaa 172,60,124\nTR 16,5,16384\nMvaa 172,56,120\nTR 18,5,18432\n"
"Mvaa 172,52,116\nTR 20,5,20480\nMvaa 172,48,112\nTR 22,5,22528\nMvaa 172,44,108\nTR 24,5,24576\nMvaa 172,40,104\nTR 26,5,26624\nMvaa 172,36,100\n"
"TR 28,5,28672\nMvaa 172,32,96\nWL 13\nTR 30,5,30720\nZvaa 176,0,64\nWL 12\nTR 32,6,0\nTR 34,6,2048\nMvaa 176,4,68\nWL 12\nTR 36,6,4096\nTR 38,6,6144\n"
"Mvaa 176,8,72\nWL 12\nTR 40,6,8192\nTR 42,6,10240\nMvaa 176,12,76\nWL 12\nTR 44,6,12288\nMvaa 176,16,80\nWL 11\nTR 46,6,14336\nCV 144,144,145\n"
"Mvaa 176,20,84\nWL 10\nTR 48,6,16384\nCV 145,146,147\nMvaa 176,24,88\nWL 9\nTR 50,6,18432\nCV 146,152,153\nMvaa 176,28,92\ns_add_u32 m0,s48,s44\n"
"TR 52,6,20480\nCV 147,154,155\nZvaa 180,28,124\nTR 54,6,22528\nbuffer_store_dwordx4 v[144:147],v0,s[24:27],s70 offen sc1\nMvaa 180,24,120\n"
"s_add_u32 s70,s70,1024\nDP 1,20,36,0\nMvaa 180,20,116\nTR 56,6,24576\nbuffer_load_dwordx4 v[32:35],v0,s[24:27],s94 offen offset:0 sc1\nCV 148,148,149\n"
"Mvaa 180,16,112\nTR 58,6,26624\nCV 149,150,151\nMvaa 180,12,108\nDP 1,20,37,1024\nMvaa 180,8,104\nTR 60,6,28672\nCV 150,156,157\nMvaa 180,4,100\n"
"TR 62,6,30720\nCV 151,158,159\nMvaa 180,0,96\nWL 14\nDP 1,20,38,2048\nZvaa 184,32,64\nWL 12\nTR 0,7,0\nTR 2,7,2048\n"
"buffer_store_dwordx4 v[148:151],v0,s[24:27],s71 offen sc1\nMvaa 184,36,68\nWL 12\ns_add_u32 s71,s71,1024\nTR 4,7,4096\nTR 6,7,6144\nMvaa 184,40,72\n"
"WL 12\nTR 8,7,8192\nTR 10,7,10240\nMvaa 184,44,76\nWL 12\nTR 12,7,12288\nCV 160,160,161\nMvaa 184,48,80\nWL 11\nTR 14,7,14336\nCV 161,162,163\n"
"Mvaa 184,52,84\nWL 10\nTR 16,7,16384\nbuffer_load_dwordx4 v[36:39],v0,s[24:27],s94 offen offset:1024 sc1\nCV 162,168,169\nMvaa 184,56,88\nWL 9\n"
"TR 18,7,18432\nCV 163,170,171\nMvaa 184,60,92\nTR 20,7,20480\nbuffer_store_dwordx4 v[160:163],v0,s[24:27],s70 offen sc1\nZvaa 188,60,124\n"
"s_add_u32 s70,s70,1024\nTR 22,7,22528\nCV 164,164,165\nMvaa 188,56,120\nDP 1,20,39,3072\nMvaa 188,52,116\ns_add_u32 m0,m0,0x1000\nTR 24,7,24576\n"
"CV 165,166,167\nMvaa 188,48,112\nTR 26,7,26624\nCV 166,172,173\nMvaa 188,44,108\nDP 1,20,40,0\nMvaa 188,40,104\nTR 28,7,28672\nCV 167,174,175\n"
"Mvaa 188,36,100\nTR 30,7,30720\nbuffer_store_dwordx4 v[164:167],v0,s[24:27],s71 offen sc1\nMvaa 188,32,96\nWL 14\ns_add_u32 s71,s71,1024\n"
"DP 1,20,41,1024\nZvaa 192,0,64\nWL 12\nTR 32,8,0\nTR 34,8,2048\nMvaa 192,4,68\nWL 12\ns_mov_b32 s44,s47\nTR 36,8,4096\nTR 38,8,6144\nMvaa 192,8,72\n"
"WL 12\ns_add_u32 s47,s47,0x8000\nTR 40,8,8192\nTR 42,8,10240\nMvaa 192,12,76\nWL 12\ns_cmp_eq_u32 s47,0x28000\nTR 44,8,12288\nTR 46,8,14336\n"
"Mvaa 192,16,80\nWL 12\ns_cselect_b32 s47,0,s47\nTR 48,8,16384\nTR 50,8,18432\nMvaa 192,20,84\nWL 12\nTR 52,8,20480\nTR 54,8,22528\nMvaa 192,24,88\n"
"WL 12\nTR 56,8,24576\nTR 58,8,26624\nMvaa 192,28,92\nTR 60,8,28672\nZvaa 196,28,124\nTR 62,8,30720\nMvaa 196,24,120\nDP 1,20,42,2048\nMvaa 196,20,116\n"
"v_add_u32 v5,s47,v10\nv_xor_b32 v6,32,v5\nMvaa 196,16,112\nv_xor_b32 v7,64,v5\nv_xor_b32 v8,96,v5\nMvaa 196,12,108\nDP 1,20,43,3072\nMvaa 196,8,104\n"
"v_add_u32 v1,0x80,v1\nMvaa 196,4,100\nbuffer_load_dwordx4 v[40:43],v0,s[24:27],s94 offen offset:2048 sc1\nMvaa 196,0,96\n"
"s_waitcnt vmcnt(37) lgkmcnt(0)\ns_barrier\nbuffer_load_dwordx4 v[44:47],v0,s[24:27],s94 offen offset:3072 sc1\nZvaa 200,32,64\ns_add_u32 s94,s94,4096\n"
"TR 0,5,0\nMvaa 200,36,68\nTR 2,5,2048\nMvaa 200,40,72\nTR 4,5,4096\nMvaa 200,44,76\nTR 6,5,6144\nMvaa 200,48,80\nTR 8,5,8192\nMvaa 200,52,84\n"
"TR 10,5,10240\nMvaa 200,56,88\nTR 12,5,12288\nMvaa 200,60,92\nTR 14,5,14336\nZvaa 204,60,124\nTR 16,5,16384\nMvaa 204,56,120\nTR 18,5,18432\n"
"Mvaa 204,52,116\nTR 20,5,20480\nMvaa 204,48,112\nTR 22,5,22528\nMvaa 204,44,108\nTR 24,5,24576\nMvaa 204,40,104\nTR 26,5,26624\nMvaa 204,36,100\n"
"TR 28,5,28672\nMvaa 204,32,96\nWL 13\nTR 30,5,30720\nZvaa 144,0,64\nWL 12\nTR 32,6,0\nTR 34,6,2048\nMvaa 144,4,68\nWL 12\nTR 36,6,4096\nTR 38,6,6144\n"
"Mvaa 144,8,72\nWL 12\nTR 40,6,8192\nTR 42,6,10240\nMvaa 144,12,76\nWL 12\nTR 44,6,12288\nMvaa 144,16,80\nWL 11\nTR 46,6,14336\nCV 176,176,177\n"
"Mvaa 144,20,84\nWL 10\nTR 48,6,16384\nCV 177,178,179\nMvaa 144,24,88\nWL 9\nTR 50,6,18432\nCV 178,184,185\nMvaa 144,28,92\ns_add_u32 m0,s48,s44\n"
"TR 52,6,20480\nCV 179,186,187\nZvaa 148,28,124\nTR 54,6,22528\nbuffer_store_dwordx4 v[176:179],v0,s[24:27],s70 offen sc1\nMvaa 148,24,120\n"
"s_add_u32 s70,s70,1024\nDP 1,20,36,0\nMvaa 148,20,116\nTR 56,6,24576\nbuffer_load_dwordx4 v[48:51],v0,s[24:27],s94 offen offset:0 sc1\nCV 180,180,181\n"
"Mvaa 148,16,112\nTR 58,6,26624\nCV 181,182,183\nMvaa 148,12,108\nDP 1,20,37,1024\nMvaa 148,8,104\nTR 60,6,28672\nCV 182,188,189\nMvaa 148,4,100\n"
"TR 62,6,30720\nCV 183,190,191\nMvaa 148,0,96\nWL 14\nDP 1,20,38,2048\nZvaa 152,32,64\nWL 12\nTR 0,7,0\nTR 2,7,2048\n"
"buffer_store_dwordx4 v[180:183],v0,s[24:27],s71 offen sc1\nMvaa 152,36,68\nWL 12\ns_add_u32 s71,s71,1024\nTR 4,7,4096\nTR 6,7,6144\n"
"buffer_load_dwordx4 v[52:55],v0,s[24:27],s94 offen offset:1024 sc1\nMvaa 152,40,72\nWL 12\nTR 8,7,8192\nTR 10,7,10240\nMvaa 152,44,76\nWL 12\n"
"TR 12,7,12288\nCV 192,192,193\nMvaa 152,48,80\nWL 11\nTR 14,7,14336\nCV 193,194,195\nMvaa 152,52,84\nWL 10\nTR 16,7,16384\nCV 194,200,201\n"
"Mvaa 152,56,88\nWL 9\nTR 18,7,18432\nCV 195,202,203\nMvaa 152,60,92\nTR 20,7,20480\nbuffer_store_dwordx4 v[192:195],v0,s[24:27],s70 offen sc1\n"
"Zvaa 156,60,124\ns_add_u32 s70,s70,1024\nTR 22,7,22528\nCV 196,196,197\nMvaa 156,56,120\nDP 1,20,39,3072\nMvaa 156,52,116\ns_add_u32 m0,m0,0x1000\n"
"TR 24,7,24576\nbuffer_load_dwordx4 v[56:59],v0,s[24:27],s94 offen offset:2048 sc1\nCV 197,198,199\nMvaa 156,48,112\nTR 26,7,26624\nCV 198,204,205\n"
"Mvaa 156,44,108\nDP 1,20,40,0\nMvaa 156,40,104\nTR 28,7,28672\nCV 199,206,207\nMvaa 156,36,100\nTR 30,7,30720\n"
"buffer_store_dwordx4 v[196:199],v0,s[24:27],s71 offen sc1\nMvaa 156,32,96\nWL 14\ns_add_u32 s71,s71,1024\nDP 1,20,41,1024\nZvaa 160,0,64\nWL 12\n"
"TR 32,8,0\nTR 34,8,2048\nMvaa 160,4,68\nWL 12\ns_mov_b32 s44,s47\nTR 36,8,4096\nTR 38,8,6144\nMvaa 160,8,72\nWL 12\ns_add_u32 s47,s47,0x8000\n"
"TR 40,8,8192\nTR 42,8,10240\nMvaa 160,12,76\nWL 12\ns_cmp_eq_u32 s47,0x28000\nTR 44,8,12288\nTR 46,8,14336\nMvaa 160,16,80\nWL 12\n"
"s_cselect_b32 s47,0,s47\nTR 48,8,16384\nTR 50,8,18432\nMvaa 160,20,84\nWL 12\nTR 52,8,20480\nTR 54,8,22528\nMvaa 160,24,88\nWL 12\nTR 56,8,24576\n"
"TR 58,8,26624\nMvaa 160,28,92\nTR 60,8,28672\nbuffer_load_dwordx4 v[60:63],v0,s[24:27],s94 offen offset:3072 sc1\nZvaa 164,28,124\n"
"s_add_u32 s94,s94,4096\nTR 62,8,30720\nMvaa 164,24,120\nDP 1,20,42,2048\nMvaa 164,20,116\nv_add_u32 v5,s47,v10\nv_xor_b32 v6,32,v5\nMvaa 164,16,112\n"
"v_xor_b32 v7,64,v5\nv_xor_b32 v8,96,v5\nMvaa 164,12,108\nDP 1,20,43,3072\nMvaa 164,8,104\nMvaa 164,4,100\ns_mul_i32 s45,s57,128\n"
"v_lshrrev_b32 v13,7,v0\nMvaa 164,0,96\ns_waitcnt vmcnt(46) lgkmcnt(0)\ns_barrier\nv_and_b32 v1,6,v13\nZvaa 168,32,64\ns_mul_i32 s49,s10,50176\n"
"TR 0,5,0\nv_lshlrev_b32 v1,4,v1\nMvaa 168,36,68\ns_add_u32 s45,s45,s49\nTR 2,5,2048\nv_xor_b32 v1,v1,v0\nMvaa 168,40,72\ns_add_u32 s20,s90,s45\n"
"s_addc_u32 s21,s91,0\nTR 4,5,4096\nMvaa 168,44,76\ns_and_b32 s21,s21,0xffff\nTR 6,5,6144\nv_and_b32 v1,0x70,v1\nMvaa 168,48,80\n"
"s_sub_u32 s22,401408,s45\nTR 8,5,8192\nv_mul_u32_u24 v13,896,v13\nMvaa 168,52,84\ns_mov_b32 s23,0x20000\nTR 10,5,10240\nv_add_u32 v1,v1,v13\n"
"Mvaa 168,56,88\ns_mov_b32 s36,0\nTR 12,5,12288\nMvaa 168,60,92\ns_mov_b32 s37,6144\nTR 14,5,14336\nZvaa 172,60,124\ns_mov_b32 s38,12288\nTR 16,5,16384\n"
"Mvaa 172,56,120\ns_mov_b32 s39,18432\nTR 18,5,18432\nMvaa 172,52,116\ns_mov_b32 s40,28672\nTR 20,5,20480\nMvaa 172,48,112\ns_mov_b32 s41,34816\n"
"TR 22,5,22528\nMvaa 172,44,108\ns_mov_b32 s42,40960\nTR 24,5,24576\nMvaa 172,40,104\ns_mov_b32 s43,47104\nTR 26,5,26624\nMvaa 172,36,100\n"
"s_cmp_eq_u32 s57,7\ns_cselect_b32 s22,0,s22\nTR 28,5,28672\nMvaa 172,32,96\nWL 13\nTR 30,5,30720\nZvaa 176,0,64\nWL 12\nTR 32,6,0\nTR 34,6,2048\n"
"Mvaa 176,4,68\nWL 12\nTR 36,6,4096\nTR 38,6,6144\nMvaa 176,8,72\nWL 12\nTR 40,6,8192\nTR 42,6,10240\nMvaa 176,12,76\nWL 12\nTR 44,6,12288\n"
"Mvaa 176,16,80\nWL 11\nTR 46,6,14336\nCV 144,144,145\nMvaa 176,20,84\nWL 10\nTR 48,6,16384\nCV 145,146,147\nMvaa 176,24,88\nWL 9\nTR 50,6,18432\n"
"CV 146,152,153\nMvaa 176,28,92\ns_add_u32 m0,s48,s44\nTR 52,6,20480\nCV 147,154,155\nZvaa 180,28,124\nTR 54,6,22528\n"
"buffer_store_dwordx4 v[144:147],v0,s[24:27],s70 offen sc1\nMvaa 180,24,120\ns_add_u32 s70,s70,1024\nDP 1,20,36,0\nMvaa 180,20,116\nTR 56,6,24576\n"
"buffer_load_dwordx4 v[64:67],v0,s[24:27],s94 offen offset:0 sc1\nCV 148,148,149\nMvaa 180,16,112\nTR 58,6,26624\nCV 149,150,151\nMvaa 180,12,108\n"
"DP 1,20,37,1024\nMvaa 180,8,104\nTR 60,6,28672\nCV 150,156,157\nMvaa 180,4,100\nTR 62,6,30720\nCV 151,158,159\nMvaa 180,0,96\nWL 14\nDP 1,20,38,2048\n"
"Zvaa 184,32,64\nWL 12\nTR 0,7,0\nTR 2,7,2048\nbuffer_store_dwordx4 v[148:151],v0,s[24:27],s71 offen sc1\nMvaa 184,36,68\nWL 12\ns_add_u32 s71,s71,1024\n"
"TR 4,7,4096\nTR 6,7,6144\nMvaa 184,40,72\nWL 12\nTR 8,7,8192\nTR 10,7,10240\nMvaa 184,44,76\nWL 12\nTR 12,7,12288\nCV 160,160,161\nMvaa 184,48,80\n"
"WL 11\nTR 14,7,14336\nCV 161,162,163\nMvaa 184,52,84\nWL 10\nTR 16,7,16384\nCV 162,168,169\nMvaa 184,56,88\nWL 9\nTR 18,7,18432\nCV 163,170,171\n"
"Mvaa 184,60,92\nTR 20,7,20480\nbuffer_store_dwordx4 v[160:163],v0,s[24:27],s70 offen sc1\nZvaa 188,60,124\ns_add_u32 s70,s70,1024\nTR 22,7,22528\n"
"CV 164,164,165\nMvaa 188,56,120\nDP 1,20,39,3072\nMvaa 188,52,116\ns_add_u32 m0,m0,0x1000\nTR 24,7,24576\nCV 165,166,167\nMvaa 188,48,112\n"
"TR 26,7,26624\nCV 166,172,173\nMvaa 188,44,108\nDP 1,20,40,0\nMvaa 188,40,104\nTR 28,7,28672\nCV 167,174,175\nMvaa 188,36,100\nTR 30,7,30720\n"
"buffer_store_dwordx4 v[164:167],v0,s[24:27],s71 offen sc1\nMvaa 188,32,96\nWL 14\ns_add_u32 s71,s71,1024\nDP 1,20,41,1024\nZvaa 192,0,64\nWL 12\n"
"TR 32,8,0\nTR 34,8,2048\nMvaa 192,4,68\nWL 12\ns_mov_b32 s44,s47\nTR 36,8,4096\nTR 38,8,6144\nMvaa 192,8,72\nWL 12\ns_add_u32 s47,s47,0x8000\n"
"TR 40,8,8192\nTR 42,8,10240\nMvaa 192,12,76\nWL 12\ns_cmp_eq_u32 s47,0x28000\nTR 44,8,12288\nTR 46,8,14336\nMvaa 192,16,80\nWL 12\n"
"s_cselect_b32 s47,0,s47\nTR 48,8,16384\nTR 50,8,18432\nMvaa 192,20,84\nWL 12\nTR 52,8,20480\nTR 54,8,22528\nMvaa 192,24,88\nWL 12\nTR 56,8,24576\n"
"TR 58,8,26624\nMvaa 192,28,92\nTR 60,8,28672\nbuffer_load_dwordx4 v[68:71],v0,s[24:27],s94 offen offset:1024 sc1\nZvaa 196,28,124\nTR 62,8,30720\n"
"Mvaa 196,24,120\nDP 1,20,42,2048\nMvaa 196,20,116\nv_add_u32 v5,s47,v10\nv_xor_b32 v6,32,v5\nMvaa 196,16,112\nv_xor_b32 v7,64,v5\nv_xor_b32 v8,96,v5\n"
"Mvaa 196,12,108\nDP 1,20,43,3072\nMvaa 196,8,104\nv_add_u32 v1,0x31000,v1\nMvaa 196,4,100\n"
"buffer_load_dwordx4 v[72:75],v0,s[24:27],s94 offen offset:2048 sc1\nMvaa 196,0,96\ns_waitcnt vmcnt(49) lgkmcnt(0)\ns_barrier\nZvaa 200,32,64\nTR 0,5,0\n"
"Mvaa 200,36,68\nTR 2,5,2048\nMvaa 200,40,72\nTR 4,5,4096\nMvaa 200,44,76\nTR 6,5,6144\nMvaa 200,48,80\nTR 8,5,8192\nMvaa 200,52,84\nTR 10,5,10240\n"
"Mvaa 200,56,88\nTR 12,5,12288\nMvaa 200,60,92\nTR 14,5,14336\nZvaa 204,60,124\nTR 16,5,16384\nMvaa 204,56,120\nTR 18,5,18432\nMvaa 204,52,116\n"
"TR 20,5,20480\nMvaa 204,48,112\nTR 22,5,22528\nMvaa 204,44,108\nTR 24,5,24576\nMvaa 204,40,104\nTR 26,5,26624\nMvaa 204,36,100\nTR 28,5,28672\n"
"Mvaa 204,32,96\nWL 13\nTR 30,5,30720\nZvaa 144,0,64\nWL 12\nTR 32,6,0\nTR 34,6,2048\nMvaa 144,4,68\nWL 12\nTR 36,6,4096\nTR 38,6,6144\nMvaa 144,8,72\n"
"WL 12\nTR 40,6,8192\nTR 42,6,10240\nMvaa 144,12,76\nWL 12\nTR 44,6,12288\nMvaa 144,16,80\nWL 11\nTR 46,6,14336\nCV 176,176,177\nMvaa 144,20,84\nWL 10\n"
"TR 48,6,16384\nCV 177,178,179\nMvaa 144,24,88\nWL 9\nTR 50,6,18432\nCV 178,184,185\nMvaa 144,28,92\ns_add_u32 m0,s48,s44\nTR 52,6,20480\n"
"CV 179,186,187\nZvaa 148,28,124\nTR 54,6,22528\nbuffer_store_dwordx4 v[176:179],v0,s[24:27],s70 offen sc1\nMvaa 148,24,120\ns_add_u32 s70,s70,1024\n"
"DP 1,20,36,0\nMvaa 148,20,116\nTR 56,6,24576\nbuffer_load_dwordx4 v[76:79],v0,s[24:27],s94 offen offset:3072 sc1\nCV 180,180,181\nMvaa 148,16,112\n"
"s_add_u32 s94,s94,4096\nTR 58,6,26624\nCV 181,182,183\nMvaa 148,12,108\nDP 1,20,37,1024\nMvaa 148,8,104\nTR 60,6,28672\nCV 182,188,189\nMvaa 148,4,100\n"
"TR 62,6,30720\nCV 183,190,191\nMvaa 148,0,96\nWL 14\nDP 1,20,38,2048\nZvaa 152,32,64\nWL 12\nTR 0,7,0\nTR 2,7,2048\n"
"buffer_store_dwordx4 v[180:183],v0,s[24:27],s71 offen sc1\nMvaa 152,36,68\nWL 12\ns_add_u32 s71,s71,1024\nTR 4,7,4096\nTR 6,7,6144\nMvaa 152,40,72\n"
"WL 12\nTR 8,7,8192\nTR 10,7,10240\nMvaa 152,44,76\nWL 12\nTR 12,7,12288\nCV 192,192,193\nMvaa 152,48,80\nWL 11\nTR 14,7,14336\nCV 193,194,195\n"
"Mvaa 152,52,84\nWL 10\nTR 16,7,16384\nCV 194,200,201\nMvaa 152,56,88\nWL 9\nTR 18,7,18432\nCV 195,202,203\nMvaa 152,60,92\nTR 20,7,20480\n"
"buffer_store_dwordx4 v[192:195],v0,s[24:27],s70 offen sc1\nZvaa 156,60,124\ns_add_u32 s70,s70,1024\nTR 22,7,22528\nCV 196,196,197\nMvaa 156,56,120\n"
"DP 1,20,39,3072\nMvaa 156,52,116\ns_add_u32 m0,m0,0x1000\nTR 24,7,24576\nCV 197,198,199\nMvaa 156,48,112\nTR 26,7,26624\nCV 198,204,205\n"
"Mvaa 156,44,108\nDP 1,20,40,0\nMvaa 156,40,104\nTR 28,7,28672\nCV 199,206,207\nMvaa 156,36,100\nTR 30,7,30720\n"
"buffer_store_dwordx4 v[196:199],v0,s[24:27],s71 offen sc1\nMvaa 156,32,96\nWL 14\ns_add_u32 s71,s71,1024\nDP 1,20,41,1024\nZvaa 160,0,64\nWL 12\n"
"TR 32,8,0\nTR 34,8,2048\nMvaa 160,4,68\nWL 12\ns_mov_b32 s44,s47\nTR 36,8,4096\nTR 38,8,6144\nMvaa 160,8,72\nWL 12\ns_add_u32 s47,s47,0x8000\n"
"TR 40,8,8192\nTR 42,8,10240\nMvaa 160,12,76\nWL 12\ns_cmp_eq_u32 s47,0x28000\nTR 44,8,12288\nTR 46,8,14336\nMvaa 160,16,80\nWL 12\n"
"s_cselect_b32 s47,0,s47\nTR 48,8,16384\nbuffer_load_dwordx4 v[80:83],v0,s[24:27],s94 offen offset:0 sc1\nMvaa 160,20,84\nWL 11\nTR 50,8,18432\n"
"Mvaa 160,24,88\nWL 10\nTR 52,8,20480\nMvaa 160,28,92\nTR 54,8,22528\nZvaa 164,28,124\nTR 56,8,24576\nMvaa 164,24,120\nDP 1,20,42,2048\nMvaa 164,20,116\n"
"TR 58,8,26624\nbuffer_load_dwordx4 v[84:87],v0,s[24:27],s94 offen offset:1024 sc1\nMvaa 164,16,112\nTR 60,8,28672\nMvaa 164,12,108\nDP 1,20,43,3072\n"
"Mvaa 164,8,104\nTR 62,8,30720\nMvaa 164,4,100\ns_waitcnt vmcnt(48)\nv_add_u32 v5,s47,v10\nv_xor_b32 v6,32,v5\nMvaa 164,0,96\ns_barrier\n"
"v_xor_b32 v7,64,v5\nv_xor_b32 v8,96,v5\nWL 14\nZvaa 168,32,64\nWL 12\nTR 0,5,0\nMvaa 168,36,68\nWL 11\nTR 2,5,2048\nMvaa 168,40,72\nWL 10\nTR 4,5,4096\n"
"Mvaa 168,44,76\nWL 9\nTR 6,5,6144\nMvaa 168,48,80\nWL 8\nTR 8,5,8192\nMvaa 168,52,84\nWL 7\nTR 10,5,10240\nMvaa 168,56,88\nWL 6\nTR 12,5,12288\n"
"Mvaa 168,60,92\nTR 14,5,14336\nZvaa 172,60,124\nTR 16,5,16384\nMvaa 172,56,120\nTR 18,5,18432\nMvaa 172,52,116\nTR 20,5,20480\nMvaa 172,48,112\n"
"TR 22,5,22528\nMvaa 172,44,108\nTR 24,5,24576\nMvaa 172,40,104\nTR 26,5,26624\nMvaa 172,36,100\nTR 28,5,28672\nMvaa 172,32,96\nWL 13\nTR 30,5,30720\n"
"Zvaa 176,0,64\nWL 12\nTR 32,6,0\nMvaa 176,4,68\nWL 11\nTR 34,6,2048\nMvaa 176,8,72\nWL 10\nTR 36,6,4096\nMvaa 176,12,76\nWL 9\nTR 38,6,6144\n"
"Mvaa 176,16,80\nWL 8\nTR 40,6,8192\nCV 144,144,145\nMvaa 176,20,84\nWL 7\nTR 42,6,10240\nCV 145,146,147\nMvaa 176,24,88\nWL 6\nTR 44,6,12288\n"
"CV 146,152,153\nMvaa 176,28,92\nTR 46,6,14336\nCV 147,154,155\nZvaa 180,28,124\nTR 48,6,16384\n"
"buffer_store_dwordx4 v[144:147],v0,s[24:27],s70 offen sc1\nMvaa 180,24,120\ns_add_u32 s70,s70,1024\nTR 50,6,18432\n"
"buffer_load_dwordx4 v[88:91],v0,s[24:27],s94 offen offset:2048 sc1\nCV 148,148,149\nMvaa 180,20,116\nTR 52,6,20480\nCV 149,150,151\nMvaa 180,16,112\n"
"TR 54,6,22528\nCV 150,156,157\nMvaa 180,12,108\nTR 56,6,24576\nCV 151,158,159\nMvaa 180,8,104\nTR 58,6,26624\n"
"buffer_store_dwordx4 v[148:151],v0,s[24:27],s71 offen sc1\nMvaa 180,4,100\ns_add_u32 s71,s71,1024\nTR 60,6,28672\nCV 160,160,161\nMvaa 180,0,96\nWL 13\n"
"TR 62,6,30720\nbuffer_load_dwordx4 v[92:95],v0,s[24:27],s94 offen offset:3072 sc1\nCV 161,162,163\nZvaa 184,32,64\nWL 12\ns_add_u32 s94,s94,4096\n"
"TR 0,7,0\nCV 162,168,169\nMvaa 184,36,68\nWL 11\nTR 2,7,2048\nCV 163,170,171\nMvaa 184,40,72\nWL 10\nTR 4,7,4096\n"
"buffer_store_dwordx4 v[160:163],v0,s[24:27],s70 offen sc1\nMvaa 184,44,76\nWL 9\ns_add_u32 s70,s70,1024\nTR 6,7,6144\nCV 164,164,165\nMvaa 184,48,80\n"
"WL 8\nTR 8,7,8192\nCV 165,166,167\nMvaa 184,52,84\nWL 7\nTR 10,7,10240\nCV 166,172,173\nMvaa 184,56,88\nWL 6\nTR 12,7,12288\nCV 167,174,175\n"
"Mvaa 184,60,92\nTR 14,7,14336\nbuffer_store_dwordx4 v[164:167],v0,s[24:27],s71 offen sc1\nZvaa 188,60,124\ns_add_u32 s71,s71,1024\nTR 16,7,16384\n"
"buffer_load_dwordx4 v[96:99],v0,s[24:27],s94 offen offset:0 sc1\nMvaa 188,56,120\nTR 18,7,18432\nMvaa 188,52,116\nTR 20,7,20480\nMvaa 188,48,112\n"
"TR 22,7,22528\nMvaa 188,44,108\nTR 24,7,24576\nMvaa 188,40,104\nTR 26,7,26624\nbuffer_load_dwordx4 v[100:103],v0,s[24:27],s94 offen offset:1024 sc1\n"
"Mvaa 188,36,100\nTR 28,7,28672\nMvaa 188,32,96\nWL 13\nTR 30,7,30720\nZvaa 192,0,64\nWL 12\nTR 32,8,0\nTR 34,8,2048\nMvaa 192,4,68\nWL 12\n"
"s_mov_b32 s44,s47\nTR 36,8,4096\nTR 38,8,6144\nMvaa 192,8,72\nWL 12\ns_add_u32 s47,s47,0x8000\nTR 40,8,8192\nMvaa 192,12,76\nWL 11\n"
"s_cmp_eq_u32 s47,0x28000\nTR 42,8,10240\nMvaa 192,16,80\nWL 10\ns_cselect_b32 s47,0,s47\nTR 44,8,12288\n"
"buffer_load_dwordx4 v[104:107],v0,s[24:27],s94 offen offset:2048 sc1\nMvaa 192,20,84\nWL 9\nTR 46,8,14336\nMvaa 192,24,88\nWL 8\nTR 48,8,16384\n"
"Mvaa 192,28,92\nTR 50,8,18432\nZvaa 196,28,124\nTR 52,8,20480\nMvaa 196,24,120\nTR 54,8,22528\nMvaa 196,20,116\nTR 56,8,24576\nMvaa 196,16,112\n"
"TR 58,8,26624\nbuffer_load_dwordx4 v[108:111],v0,s[24:27],s94 offen offset:3072 sc1\nMvaa 196,12,108\ns_add_u32 s94,s94,4096\nTR 60,8,28672\n"
"Mvaa 196,8,104\nTR 62,8,30720\nMvaa 196,4,100\ns_waitcnt vmcnt(10)\nv_add_u32 v5,s47,v10\nv_xor_b32 v6,32,v5\nMvaa 196,0,96\ns_barrier\n"
"v_xor_b32 v7,64,v5\nv_xor_b32 v8,96,v5\nWL 14\nZvaa 200,32,64\nWL 12\nTR 0,5,0\nMvaa 200,36,68\nWL 11\nTR 2,5,2048\nMvaa 200,40,72\nWL 10\nTR 4,5,4096\n"
"buffer_load_dwordx4 v[112:115],v0,s[24:27],s94 offen offset:0 sc1\nMvaa 200,44,76\nWL 9\nTR 6,5,6144\nMvaa 200,48,80\nWL 8\nTR 8,5,8192\n"
"Mvaa 200,52,84\nWL 7\nTR 10,5,10240\nMvaa 200,56,88\nWL 6\nTR 12,5,12288\nMvaa 200,60,92\nTR 14,5,14336\nZvaa 204,60,124\nTR 16,5,16384\n"
"Mvaa 204,56,120\nTR 18,5,18432\nMvaa 204,52,116\nTR 20,5,20480\nMvaa 204,48,112\nTR 22,5,22528\nMvaa 204,44,108\nTR 24,5,24576\nMvaa 204,40,104\n"
"TR 26,5,26624\nMvaa 204,36,100\nTR 28,5,28672\nMvaa 204,32,96\nWL 13\nTR 30,5,30720\nZvaa 144,0,64\nWL 12\nTR 32,6,0\nMvaa 144,4,68\nWL 11\n"
"TR 34,6,2048\nMvaa 144,8,72\nWL 10\nTR 36,6,4096\nMvaa 144,12,76\nWL 9\nTR 38,6,6144\nMvaa 144,16,80\nWL 8\nTR 40,6,8192\nCV 176,176,177\n"
"Mvaa 144,20,84\nWL 7\nTR 42,6,10240\nCV 177,178,179\nMvaa 144,24,88\nWL 6\nTR 44,6,12288\nCV 178,184,185\nMvaa 144,28,92\nTR 46,6,14336\n"
"CV 179,186,187\nZvaa 148,28,124\nTR 48,6,16384\nbuffer_store_dwordx4 v[176:179],v0,s[24:27],s70 offen sc1\nMvaa 148,24,120\ns_add_u32 s70,s70,1024\n"
"TR 50,6,18432\nbuffer_load_dwordx4 v[116:119],v0,s[24:27],s94 offen offset:1024 sc1\nCV 180,180,181\nMvaa 148,20,116\nTR 52,6,20480\nCV 181,182,183\n"
"Mvaa 148,16,112\nTR 54,6,22528\nCV 182,188,189\nMvaa 148,12,108\nTR 56,6,24576\nCV 183,190,191\nMvaa 148,8,104\nTR 58,6,26624\n"
"buffer_store_dwordx4 v[180:183],v0,s[24:27],s71 offen sc1\nMvaa 148,4,100\ns_add_u32 s71,s71,1024\nTR 60,6,28672\nCV 192,192,193\nMvaa 148,0,96\nWL 13\n"
"TR 62,6,30720\nbuffer_load_dwordx4 v[120:123],v0,s[24:27],s94 offen offset:2048 sc1\nCV 193,194,195\nZvaa 152,32,64\nWL 12\nTR 0,7,0\nCV 194,200,201\n"
"Mvaa 152,36,68\nWL 11\nTR 2,7,2048\nCV 195,202,203\nMvaa 152,40,72\nWL 10\nTR 4,7,4096\nbuffer_store_dwordx4 v[192:195],v0,s[24:27],s70 offen sc1\n"
"Mvaa 152,44,76\nWL 9\ns_add_u32 s70,s70,1024\nTR 6,7,6144\nCV 196,196,197\nMvaa 152,48,80\nWL 8\nTR 8,7,8192\nCV 197,198,199\nMvaa 152,52,84\nWL 7\n"
"TR 10,7,10240\nCV 198,204,205\nMvaa 152,56,88\nWL 6\nTR 12,7,12288\nCV 199,206,207\nMvaa 152,60,92\nTR 14,7,14336\n"
"buffer_store_dwordx4 v[196:199],v0,s[24:27],s71 offen sc1\nZvaa 156,60,124\ns_add_u32 s71,s71,1024\nTR 16,7,16384\n"
"buffer_load_dwordx4 v[124:127],v0,s[24:27],s94 offen offset:3072 sc1\nMvaa 156,56,120\ns_add_u32 s94,s94,4096\nTR 18,7,18432\nMvaa 156,52,116\n"
"TR 20,7,20480\nMvaa 156,48,112\nTR 22,7,22528\nMvaa 156,44,108\nTR 24,7,24576\nMvaa 156,40,104\nTR 26,7,26624\n"
"buffer_load_dwordx4 v[128:131],v0,s[24:27],s94 offen offset:0 sc1\nMvaa 156,36,100\nTR 28,7,28672\nMvaa 156,32,96\nWL 13\nTR 30,7,30720\nZvaa 160,0,64\n"
"WL 12\nTR 32,8,0\nTR 34,8,2048\nMvaa 160,4,68\nWL 12\ns_mov_b32 s44,s47\nTR 36,8,4096\nTR 38,8,6144\nMvaa 160,8,72\nWL 12\ns_add_u32 s47,s47,0x8000\n"
"TR 40,8,8192\nMvaa 160,12,76\nWL 11\ns_cmp_eq_u32 s47,0x28000\nTR 42,8,10240\nMvaa 160,16,80\nWL 10\ns_cselect_b32 s47,0,s47\nTR 44,8,12288\n"
"buffer_load_dwordx4 v[132:135],v0,s[24:27],s94 offen offset:1024 sc1\nMvaa 160,20,84\nWL 9\nTR 46,8,14336\nMvaa 160,24,88\nWL 8\nTR 48,8,16384\n"
"Mvaa 160,28,92\nTR 50,8,18432\nZvaa 164,28,124\nTR 52,8,20480\nMvaa 164,24,120\nTR 54,8,22528\nMvaa 164,20,116\nTR 56,8,24576\nMvaa 164,16,112\n"
"TR 58,8,26624\nbuffer_load_dwordx4 v[136:139],v0,s[24:27],s94 offen offset:2048 sc1\nMvaa 164,12,108\nTR 60,8,28672\nMvaa 164,8,104\nTR 62,8,30720\n"
"Mvaa 164,4,100\nv_add_u32 v5,s47,v10\nv_xor_b32 v6,32,v5\nMvaa 164,0,96\nWL 14\nv_xor_b32 v7,64,v5\nv_xor_b32 v8,96,v5\nZvaa 168,32,64\nWL 12\n"
"TR 0,5,0\nMvaa 168,36,68\nWL 11\nTR 2,5,2048\nMvaa 168,40,72\nWL 10\nTR 4,7,3584\nbuffer_load_dwordx4 v[140:143],v0,s[24:27],s94 offen offset:3072 sc1\n"
"Mvaa 168,44,76\nWL 9\ns_add_u32 s94,s94,4096\nTR 6,7,5632\nMvaa 168,48,80\nWL 8\nTR 8,5,8192\nMvaa 168,52,84\nWL 7\nTR 10,5,10240\nMvaa 168,56,88\n"
"WL 6\nTR 12,7,11776\nMvaa 168,60,92\nTR 14,7,13824\nZvaa 172,60,124\nTR 16,5,16384\nMvaa 172,56,120\nTR 18,5,18432\nMvaa 172,52,116\nTR 20,7,19968\n"
"Mvaa 172,48,112\nTR 22,7,22016\nMvaa 172,44,108\nTR 24,5,24576\nMvaa 172,40,104\nTR 26,5,26624\nMvaa 172,36,100\nTR 28,7,28160\nMvaa 172,32,96\n"
"TR 30,7,30208\nv_lshrrev_b32 v12,4,v0\nv_and_b32 v13,15,v12\nv_mul_u32_u24 v13,1792,v13\nv_bfe_u32 v11,v12,4,1\nv_lshl_add_u32 v13,v11,5,v13\n"
"v_lshrrev_b32 v12,5,v12\nv_lshl_add_u32 v12,v12,4,v13\ns_mov_b32 s16,s92\ns_and_b32 s17,s93,0xffff\ns_mov_b32 s18,3670016\ns_mov_b32 s19,0x20000\n"
"s_mov_b32 s28,0x3f80\ns_mov_b32 s29,0x3f800000\ns_cmp_eq_u32 s57,7\ns_cbranch_scc1 fused_op_noa2\nWL 14\nZvav 176,0,16\nWL 12\nTR 32,6,0\n"
"Mvav 176,4,20\nWL 11\nTR 34,6,2048\nMvav 176,8,24\nWL 10\nTR 36,8,3584\nMvav 176,12,28\nWL 9\nTR 38,8,5632\nMvav 176,16,32\nWL 8\nTR 40,6,8192\n"
"CV 144,144,145\nMvav 176,20,36\nWL 7\nTR 42,6,10240\nCV 145,146,147\nMvav 176,24,40\nWL 6\nTR 44,8,11776\nCV 146,152,153\nMvav 176,28,44\n"
"TR 46,8,13824\nCV 147,154,155\ns_waitcnt vmcnt(12)\nZvav 180,28,108\nTR 48,6,16384\nbuffer_store_dwordx4 v[144:147],v0,s[24:27],s70 offen sc1\n"
"Mvav 180,24,104\ns_add_u32 s70,s70,1024\nTR 50,6,18432\nCV 148,148,149\nMvav 180,20,100\nTR 52,8,19968\nCV 149,150,151\nMvav 180,16,96\nTR 54,8,22016\n"
"CV 150,156,157\nMvav 180,12,92\nTR 56,6,24576\nCV 151,158,159\nMvav 180,8,88\nTR 58,6,26624\nbuffer_store_dwordx4 v[148:151],v0,s[24:27],s71 offen sc1\n"
"Mvav 180,4,84\ns_add_u32 s71,s71,1024\nTR 60,8,28160\nCV 160,160,161\nMvav 180,0,80\nWL 13\nTR 62,8,30208\nCV 161,162,163\nZvav 184,32,16\nWL 12\n"
"TR 0,7,0\nCV 162,168,169\nMvav 184,36,20\nWL 11\nTR 2,7,2048\nCV 163,170,171\nMvav 184,40,24\nWL 10\nTR 4,5,3584\n"
"buffer_store_dwordx4 v[160:163],v0,s[24:27],s70 offen sc1\nMvav 184,44,28\nWL 9\ns_add_u32 s70,s70,1024\nTR 6,5,5632\nCV 164,164,165\nMvav 184,48,32\n"
"WL 8\nTR 8,7,8192\nCV 165,166,167\nMvav 184,52,36\nWL 7\nTR 10,7,10240\nCV 166,172,173\nMvav 184,56,40\nWL 6\nTR 12,5,11776\nCV 167,174,175\n"
"Mvav 184,60,44\nTR 14,5,13824\nbuffer_store_dwordx4 v[164:167],v0,s[24:27],s71 offen sc1\nZvav 188,60,108\ns_add_u32 s71,s71,1024\nTR 16,7,16384\n"
"Mvav 188,56,104\nTR 18,7,18432\nMvav 188,52,100\nTR 20,5,19968\nMvav 188,48,96\nTR 22,5,22016\nMvav 188,44,92\nTR 24,7,24576\nMvav 188,40,88\n"
"TR 26,7,26624\nMvav 188,36,84\nTR 28,5,28160\nMvav 188,32,80\nWL 13\nTR 30,5,30208\nZvav 192,0,16\nWL 12\nTR 32,8,0\nTR 34,8,2048\nMvav 192,4,20\n"
"WL 12\ns_mov_b32 s44,s47\nTR 36,6,3584\nTR 38,6,5632\nMvav 192,8,24\nWL 12\ns_add_u32 s47,s47,0x8000\nTR 40,8,8192\nMvav 192,12,28\nWL 11\n"
"s_cmp_eq_u32 s47,0x28000\nTR 42,8,10240\nMvav 192,16,32\nWL 10\ns_cselect_b32 s47,0,s47\nTR 44,6,11776\nMvav 192,20,36\nWL 9\nTR 46,6,13824\n"
"Mvav 192,24,40\nWL 8\nTR 48,8,16384\nMvav 192,28,44\nTR 50,8,18432\nZvav 196,28,108\nTR 52,6,19968\nMvav 196,24,104\nTR 54,6,22016\nMvav 196,20,100\n"
"TR 56,8,24576\nMvav 196,16,96\nTR 58,8,26624\nMvav 196,12,92\nTR 60,6,28160\nMvav 196,8,88\nTR 62,6,30208\nMvav 196,4,84\nv_add_u32 v5,s47,v10\n"
"v_xor_b32 v6,32,v5\nMvav 196,0,80\nWL 14\nv_xor_b32 v7,64,v5\nv_xor_b32 v8,96,v5\nZvav 200,32,16\nWL 12\nTR 0,5,0\nMvav 200,36,20\nWL 11\nTR 2,5,2048\n"
"Mvav 200,40,24\nWL 10\nTR 4,7,3584\nMvav 200,44,28\nWL 9\nTR 6,7,5632\nMvav 200,48,32\nWL 8\nTR 8,5,8192\nMvav 200,52,36\nWL 7\nTR 10,5,10240\n"
"Mvav 200,56,40\nWL 6\nTR 12,7,11776\nMvav 200,60,44\nTR 14,7,13824\nZvav 204,60,108\nTR 16,5,16384\nMvav 204,56,104\nTR 18,5,18432\nMvav 204,52,100\n"
"TR 20,7,19968\nMvav 204,48,96\nTR 22,7,22016\nMvav 204,44,92\nTR 24,5,24576\nMvav 204,40,88\nTR 26,5,26624\nMvav 204,36,84\nTR 28,7,28160\n"
"Mvav 204,32,80\nWL 13\nTR 30,7,30208\nMvav 176,0,48\nWL 12\nTR 32,6,0\nMvav 176,4,52\nWL 11\nTR 34,6,2048\nMvav 176,8,56\nWL 10\nTR 36,8,3584\n"
"Mvav 176,12,60\nWL 9\nTR 38,8,5632\nMvav 176,16,64\nWL 8\nTR 40,6,8192\nMvav 176,20,68\nWL 7\nTR 42,6,10240\nMvav 176,24,72\nWL 6\nTR 44,8,11776\n"
"Mvav 176,28,76\nTR 46,8,13824\ns_waitcnt vmcnt(4)\nMvav 180,28,140\nTR 48,6,16384\nMvav 180,24,136\nTR 50,6,18432\nMvav 180,20,132\nTR 52,8,19968\n"
"Mvav 180,16,128\nTR 54,8,22016\nMvav 180,12,124\nTR 56,6,24576\nMvav 180,8,120\nTR 58,6,26624\nMvav 180,4,116\nTR 60,8,28160\nMvav 180,0,112\nWL 13\n"
"TR 62,8,30208\nMvav 184,32,48\nWL 12\nTR 0,7,0\nMvav 184,36,52\nWL 11\nTR 2,7,2048\nMvav 184,40,56\nWL 10\nTR 4,5,3584\nMvav 184,44,60\nWL 9\n"
"TR 6,5,5632\nMvav 184,48,64\nWL 8\nTR 8,7,8192\nMvav 184,52,68\nWL 7\nTR 10,7,10240\nMvav 184,56,72\nWL 6\nTR 12,5,11776\nMvav 184,60,76\n"
"TR 14,5,13824\nMvav 188,60,140\nTR 16,7,16384\nMvav 188,56,136\nTR 18,7,18432\nMvav 188,52,132\nTR 20,5,19968\nMvav 188,48,128\nTR 22,5,22016\n"
"Mvav 188,44,124\nTR 24,7,24576\nMvav 188,40,120\nTR 26,7,26624\nMvav 188,36,116\nTR 28,5,28160\nMvav 188,32,112\nWL 13\nTR 30,5,30208\nMvav 192,0,48\n"
"WL 12\nTR 32,8,0\nTR 34,8,2048\nMvav 192,4,52\nWL 12\nTR 36,6,3584\nTR 38,6,5632\nMvav 192,8,56\nWL 12\nTR 40,8,8192\nMvav 192,12,60\nWL 11\n"
"TR 42,8,10240\nMvav 192,16,64\nWL 10\nTR 44,6,11776\nMvav 192,20,68\nWL 9\nTR 46,6,13824\nMvav 192,24,72\nWL 8\nTR 48,8,16384\nMvav 192,28,76\n"
"TR 50,8,18432\nMvav 196,28,140\ns_waitcnt vmcnt(0)\nTR 52,6,19968\nv_mov_b32 v13,s65\ns_mov_b64 exec,1\nbuffer_store_dword v13,off,s[24:27],s9 sc1\n"
"s_mov_b64 exec,-1\nMvav 196,24,136\nTR 54,6,22016\nMvav 196,20,132\nTR 56,8,24576\nMvav 196,16,128\nTR 58,8,26624\nMvav 196,12,124\nTR 60,6,28160\n"
"Mvav 196,8,120\nTR 62,6,30208\nMvav 196,4,116\nMvav 196,0,112\nWL 14\nMvav 200,32,48\nWL 12\nMvav 200,36,52\nWL 10\nMvav 200,40,56\nWL 8\n"
"Mvav 200,44,60\nWL 6\nMvav 200,48,64\nWL 4\nMvav 200,52,68\nWL 2\nMvav 200,56,72\nWL 0\nMvav 200,60,76\nMvav 204,60,140\nMvav 204,56,136\n"
"Mvav 204,52,132\nMvav 204,48,128\nMvav 204,44,124\nMvav 204,40,120\nMvav 204,36,116\nMvav 204,32,112\ns_nop 7\ns_mul_i32 s45,s56,229376\n"
"s_mul_i32 s49,s10,57344\ns_add_u32 s45,s45,s49\ns_mul_i32 s49,s57,128\ns_add_u32 s45,s45,s49\ns_add_u32 s49,s45,28672\nCV 176,176,177\nCV 177,178,179\n"
"CV 178,184,185\nCV 179,186,187\ns_nop 0\nv_permlane16_swap_b32 v176,v178\nv_permlane16_swap_b32 v177,v179\n"
"buffer_store_dwordx4 v[176:179],v12,s[16:19],s45 offen offset:0\nCV 180,180,181\nCV 181,182,183\nCV 182,188,189\nCV 183,190,191\ns_nop 0\n"
"v_permlane16_swap_b32 v180,v182\nv_permlane16_swap_b32 v181,v183\nbuffer_store_dwordx4 v[180:183],v12,s[16:19],s49 offen offset:0\nCV 192,192,193\n"
"CV 193,194,195\nCV 194,200,201\nCV 195,202,203\ns_nop 0\nv_permlane16_swap_b32 v192,v194\nv_permlane16_swap_b32 v193,v195\n"
"buffer_store_dwordx4 v[192:195],v12,s[16:19],s45 offen offset:64\nCV 196,196,197\nCV 197,198,199\nCV 198,204,205\nCV 199,206,207\ns_nop 0\n"
"v_permlane16_swap_b32 v196,v198\nv_permlane16_swap_b32 v197,v199\nbuffer_store_dwordx4 v[196:199],v12,s[16:19],s49 offen offset:64\n"
"s_branch fused_op_early\nfused_op_noa2:\ns_nop 7\nCV 144,144,145\nCV 145,146,147\nCV 146,152,153\nCV 147,154,155\n"
"buffer_store_dwordx4 v[144:147],v0,s[24:27],s70 offen sc1\ns_add_u32 s70,s70,1024\nCV 148,148,149\nCV 149,150,151\nCV 150,156,157\nCV 151,158,159\n"
"buffer_store_dwordx4 v[148:151],v0,s[24:27],s71 offen sc1\ns_add_u32 s71,s71,1024\nCV 160,160,161\nCV 161,162,163\nCV 162,168,169\nCV 163,170,171\n"
"buffer_store_dwordx4 v[160:163],v0,s[24:27],s70 offen sc1\ns_add_u32 s70,s70,1024\nCV 164,164,165\nCV 165,166,167\nCV 166,172,173\nCV 167,174,175\n"
"buffer_store_dwordx4 v[164:167],v0,s[24:27],s71 offen sc1\ns_add_u32 s71,s71,1024\ns_waitcnt vmcnt(0) lgkmcnt(0)\nv_mov_b32 v13,s65\ns_mov_b64 exec,1\n"
"buffer_store_dword v13,off,s[24:27],s9 sc1\ns_mov_b64 exec,-1\nfused_op_early:\ns_mul_i32 s31,s57,64\ns_mul_i32 s49,s56,4\ns_add_u32 s31,s31,s49\n"
"s_mul_i32 s49,s10,1\ns_add_u32 s31,s31,s49\ns_mul_hi_u32 s94,s31,0x12492493\ns_mul_i32 s95,s94,14\ns_sub_u32 s95,s31,s95\ns_add_u32 s45,s31,512\n"
"s_cmp_ge_u32 s57,6\ns_cselect_b32 s45,s31,s45\ns_mul_hi_u32 s96,s45,0x12492493\ns_mul_i32 s97,s96,14\ns_sub_u32 s97,s45,s97\ns_lshr_b32 s8,s94,3\n"
"s_lshl_b32 s8,s8,13\ns_bfe_u32 s49,s94,0x20001\ns_lshl_b32 s49,s49,7\ns_add_u32 s8,s8,s49\ns_add_u32 s8,s8,327680\ns_lshr_b32 s9,s96,3\n"
"s_lshl_b32 s9,s9,13\ns_bfe_u32 s49,s96,0x20001\ns_lshl_b32 s49,s49,7\ns_add_u32 s9,s9,s49\ns_add_u32 s9,s9,327680\nv_and_b32 v15,0xf0,v0\n"
"v_lshlrev_b32 v15,5,v15\ns_sub_u32 s45,s9,s8\nv_bfe_u32 v13,v0,8,1\nv_mul_lo_u32 v13,s45,v13\nv_add_u32 v15,v15,v13\n"
"buffer_load_dword v14,v15,s[24:27],s8 offen sc1\ns_waitcnt vmcnt(0)\nv_cmp_ne_u32 vcc,s65,v14\ns_nop 0\ns_cbranch_vccnz fused_op_sp56\nfused_op_spb57:\n"
"s_lshr_b32 s45,s94,3\ns_lshl_b32 s45,s45,17\ns_bfe_u32 s49,s94,0x20001\ns_lshl_b32 s49,s49,15\ns_add_u32 s45,s45,s49\ns_and_b32 s49,s94,1\n"
"s_mul_i32 s49,s49,14\ns_add_u32 s49,s49,s95\ns_lshl_b32 s49,s49,10\ns_add_u32 s45,s45,s49\ns_add_u32 s45,s45,34603008\n"
"buffer_load_dwordx4 v[16:19],v0,s[24:27],s45 offen sc1\ns_add_u32 s45,s45,0x100000\nbuffer_load_dwordx4 v[20:23],v0,s[24:27],s45 offen sc1\n"
"s_add_u32 s45,s45,0x100000\nbuffer_load_dwordx4 v[24:27],v0,s[24:27],s45 offen sc1\ns_add_u32 s45,s45,0x100000\n"
"buffer_load_dwordx4 v[28:31],v0,s[24:27],s45 offen sc1\ns_add_u32 s45,s45,0x100000\nbuffer_load_dwordx4 v[32:35],v0,s[24:27],s45 offen sc1\n"
"s_add_u32 s45,s45,0x100000\nbuffer_load_dwordx4 v[36:39],v0,s[24:27],s45 offen sc1\ns_add_u32 s45,s45,0x100000\n"
"buffer_load_dwordx4 v[40:43],v0,s[24:27],s45 offen sc1\ns_add_u32 s45,s45,0x100000\nbuffer_load_dwordx4 v[44:47],v0,s[24:27],s45 offen sc1\n"
"s_add_u32 s45,s45,0x100000\nbuffer_load_dwordx4 v[48:51],v0,s[24:27],s45 offen sc1\ns_add_u32 s45,s45,0x100000\n"
"buffer_load_dwordx4 v[52:55],v0,s[24:27],s45 offen sc1\ns_add_u32 s45,s45,0x100000\nbuffer_load_dwordx4 v[56:59],v0,s[24:27],s45 offen sc1\n"
"s_add_u32 s45,s45,0x100000\nbuffer_load_dwordx4 v[60:63],v0,s[24:27],s45 offen sc1\ns_add_u32 s45,s45,0x100000\n"
"buffer_load_dwordx4 v[64:67],v0,s[24:27],s45 offen sc1\ns_add_u32 s45,s45,0x100000\nbuffer_load_dwordx4 v[68:71],v0,s[24:27],s45 offen sc1\n"
"s_add_u32 s45,s45,0x100000\nbuffer_load_dwordx4 v[72:75],v0,s[24:27],s45 offen sc1\ns_add_u32 s45,s45,0x100000\n"
"buffer_load_dwordx4 v[76:79],v0,s[24:27],s45 offen sc1\ns_add_u32 s45,s45,0x100000\ns_cmp_ge_u32 s57,6\ns_cbranch_scc1 fused_op_early1\n"
"s_lshr_b32 s45,s96,3\ns_lshl_b32 s45,s45,17\ns_bfe_u32 s49,s96,0x20001\ns_lshl_b32 s49,s49,15\ns_add_u32 s45,s45,s49\ns_and_b32 s49,s96,1\n"
"s_mul_i32 s49,s49,14\ns_add_u32 s49,s49,s97\ns_lshl_b32 s49,s49,10\ns_add_u32 s45,s45,s49\ns_add_u32 s45,s45,34603008\n"
"buffer_load_dwordx4 v[80:83],v0,s[24:27],s45 offen sc1\ns_add_u32 s45,s45,0x100000\nbuffer_load_dwordx4 v[84:87],v0,s[24:27],s45 offen sc1\n"
"s_add_u32 s45,s45,0x100000\nbuffer_load_dwordx4 v[88:91],v0,s[24:27],s45 offen sc1\ns_add_u32 s45,s45,0x100000\n"
"buffer_load_dwordx4 v[92:95],v0,s[24:27],s45 offen sc1\ns_add_u32 s45,s45,0x100000\nbuffer_load_dwordx4 v[96:99],v0,s[24:27],s45 offen sc1\n"
"s_add_u32 s45,s45,0x100000\nbuffer_load_dwordx4 v[100:103],v0,s[24:27],s45 offen sc1\ns_add_u32 s45,s45,0x100000\n"
"buffer_load_dwordx4 v[104:107],v0,s[24:27],s45 offen sc1\ns_add_u32 s45,s45,0x100000\nbuffer_load_dwordx4 v[108:111],v0,s[24:27],s45 offen sc1\n"
"s_add_u32 s45,s45,0x100000\nbuffer_load_dwordx4 v[112:115],v0,s[24:27],s45 offen sc1\ns_add_u32 s45,s45,0x100000\n"
"buffer_load_dwordx4 v[116:119],v0,s[24:27],s45 offen sc1\ns_add_u32 s45,s45,0x100000\nbuffer_load_dwordx4 v[120:123],v0,s[24:27],s45 offen sc1\n"
"s_add_u32 s45,s45,0x100000\nbuffer_load_dwordx4 v[124:127],v0,s[24:27],s45 offen sc1\ns_add_u32 s45,s45,0x100000\n"
"buffer_load_dwordx4 v[128:131],v0,s[24:27],s45 offen sc1\ns_add_u32 s45,s45,0x100000\nbuffer_load_dwordx4 v[132:135],v0,s[24:27],s45 offen sc1\n"
"s_add_u32 s45,s45,0x100000\nbuffer_load_dwordx4 v[136:139],v0,s[24:27],s45 offen sc1\ns_add_u32 s45,s45,0x100000\n"
"buffer_load_dwordx4 v[140:143],v0,s[24:27],s45 offen sc1\ns_add_u32 s45,s45,0x100000\ns_mul_i32 s45,s94,28672\ns_mul_i32 s49,s95,64\n"
"s_add_u32 s45,s45,s49\ns_waitcnt vmcnt(28)\nv_dot2_f32_bf16 v144,v16,s28,0\nv_dot2_f32_bf16 v145,v16,s29,0\nv_dot2_f32_bf16 v146,v17,s28,0\n"
"v_dot2_f32_bf16 v147,v17,s29,0\nv_dot2_f32_bf16 v148,v18,s28,0\nv_dot2_f32_bf16 v149,v18,s29,0\nv_dot2_f32_bf16 v150,v19,s28,0\n"
"v_dot2_f32_bf16 v151,v19,s29,0\nv_dot2_f32_bf16 v144,v20,s28,v144\nv_dot2_f32_bf16 v145,v20,s29,v145\nv_dot2_f32_bf16 v146,v21,s28,v146\n"
"v_dot2_f32_bf16 v147,v21,s29,v147\nv_dot2_f32_bf16 v148,v22,s28,v148\nv_dot2_f32_bf16 v149,v22,s29,v149\nv_dot2_f32_bf16 v150,v23,s28,v150\n"
"v_dot2_f32_bf16 v151,v23,s29,v151\nv_dot2_f32_bf16 v144,v24,s28,v144\nv_dot2_f32_bf16 v145,v24,s29,v145\nv_dot2_f32_bf16 v146,v25,s28,v146\n"
"v_dot2_f32_bf16 v147,v25,s29,v147\nv_dot2_f32_bf16 v148,v26,s28,v148\nv_dot2_f32_bf16 v149,v26,s29,v149\nv_dot2_f32_bf16 v150,v27,s28,v150\n"
"v_dot2_f32_bf16 v151,v27,s29,v151\nv_dot2_f32_bf16 v144,v28,s28,v144\nv_dot2_f32_bf16 v145,v28,s29,v145\nv_dot2_f32_bf16 v146,v29,s28,v146\n"
"v_dot2_f32_bf16 v147,v29,s29,v147\nv_dot2_f32_bf16 v148,v30,s28,v148\nv_dot2_f32_bf16 v149,v30,s29,v149\nv_dot2_f32_bf16 v150,v31,s28,v150\n"
"v_dot2_f32_bf16 v151,v31,s29,v151\ns_waitcnt vmcnt(24)\nv_dot2_f32_bf16 v144,v32,s28,v144\nv_dot2_f32_bf16 v145,v32,s29,v145\n"
"v_dot2_f32_bf16 v146,v33,s28,v146\nv_dot2_f32_bf16 v147,v33,s29,v147\nv_dot2_f32_bf16 v148,v34,s28,v148\nv_dot2_f32_bf16 v149,v34,s29,v149\n"
"v_dot2_f32_bf16 v150,v35,s28,v150\nv_dot2_f32_bf16 v151,v35,s29,v151\nv_dot2_f32_bf16 v144,v36,s28,v144\nv_dot2_f32_bf16 v145,v36,s29,v145\n"
"v_dot2_f32_bf16 v146,v37,s28,v146\nv_dot2_f32_bf16 v147,v37,s29,v147\nv_dot2_f32_bf16 v148,v38,s28,v148\nv_dot2_f32_bf16 v149,v38,s29,v149\n"
"v_dot2_f32_bf16 v150,v39,s28,v150\nv_dot2_f32_bf16 v151,v39,s29,v151\nv_dot2_f32_bf16 v144,v40,s28,v144\nv_dot2_f32_bf16 v145,v40,s29,v145\n"
"v_dot2_f32_bf16 v146,v41,s28,v146\nv_dot2_f32_bf16 v147,v41,s29,v147\nv_dot2_f32_bf16 v148,v42,s28,v148\nv_dot2_f32_bf16 v149,v42,s29,v149\n"
"v_dot2_f32_bf16 v150,v43,s28,v150\nv_dot2_f32_bf16 v151,v43,s29,v151\nv_dot2_f32_bf16 v144,v44,s28,v144\nv_dot2_f32_bf16 v145,v44,s29,v145\n"
"v_dot2_f32_bf16 v146,v45,s28,v146\nv_dot2_f32_bf16 v147,v45,s29,v147\nv_dot2_f32_bf16 v148,v46,s28,v148\nv_dot2_f32_bf16 v149,v46,s29,v149\n"
"v_dot2_f32_bf16 v150,v47,s28,v150\nv_dot2_f32_bf16 v151,v47,s29,v151\ns_waitcnt vmcnt(20)\nv_dot2_f32_bf16 v144,v48,s28,v144\n"
"v_dot2_f32_bf16 v145,v48,s29,v145\nv_dot2_f32_bf16 v146,v49,s28,v146\nv_dot2_f32_bf16 v147,v49,s29,v147\nv_dot2_f32_bf16 v148,v50,s28,v148\n"
"v_dot2_f32_bf16 v149,v50,s29,v149\nv_dot2_f32_bf16 v150,v51,s28,v150\nv_dot2_f32_bf16 v151,v51,s29,v151\nv_dot2_f32_bf16 v144,v52,s28,v144\n"
"v_dot2_f32_bf16 v145,v52,s29,v145\nv_dot2_f32_bf16 v146,v53,s28,v146\nv_dot2_f32_bf16 v147,v53,s29,v147\nv_dot2_f32_bf16 v148,v54,s28,v148\n"
"v_dot2_f32_bf16 v149,v54,s29,v149\nv_dot2_f32_bf16 v150,v55,s28,v150\nv_dot2_f32_bf16 v151,v55,s29,v151\nv_dot2_f32_bf16 v144,v56,s28,v144\n"
"v_dot2_f32_bf16 v145,v56,s29,v145\nv_dot2_f32_bf16 v146,v57,s28,v146\nv_dot2_f32_bf16 v147,v57,s29,v147\nv_dot2_f32_bf16 v148,v58,s28,v148\n"
"v_dot2_f32_bf16 v149,v58,s29,v149\nv_dot2_f32_bf16 v150,v59,s28,v150\nv_dot2_f32_bf16 v151,v59,s29,v151\nv_dot2_f32_bf16 v144,v60,s28,v144\n"
"v_dot2_f32_bf16 v145,v60,s29,v145\nv_dot2_f32_bf16 v146,v61,s28,v146\nv_dot2_f32_bf16 v147,v61,s29,v147\nv_dot2_f32_bf16 v148,v62,s28,v148\n"
"v_dot2_f32_bf16 v149,v62,s29,v149\nv_dot2_f32_bf16 v150,v63,s28,v150\nv_dot2_f32_bf16 v151,v63,s29,v151\ns_waitcnt vmcnt(16)\n"
"v_dot2_f32_bf16 v144,v64,s28,v144\nv_dot2_f32_bf16 v145,v64,s29,v145\nv_dot2_f32_bf16 v146,v65,s28,v146\nv_dot2_f32_bf16 v147,v65,s29,v147\n"
"v_dot2_f32_bf16 v148,v66,s28,v148\nv_dot2_f32_bf16 v149,v66,s29,v149\nv_dot2_f32_bf16 v150,v67,s28,v150\nv_dot2_f32_bf16 v151,v67,s29,v151\n"
"v_dot2_f32_bf16 v144,v68,s28,v144\nv_dot2_f32_bf16 v145,v68,s29,v145\nv_dot2_f32_bf16 v146,v69,s28,v146\nv_dot2_f32_bf16 v147,v69,s29,v147\n"
"v_dot2_f32_bf16 v148,v70,s28,v148\nv_dot2_f32_bf16 v149,v70,s29,v149\nv_dot2_f32_bf16 v150,v71,s28,v150\nv_dot2_f32_bf16 v151,v71,s29,v151\n"
"v_dot2_f32_bf16 v144,v72,s28,v144\nv_dot2_f32_bf16 v145,v72,s29,v145\nv_dot2_f32_bf16 v146,v73,s28,v146\nv_dot2_f32_bf16 v147,v73,s29,v147\n"
"v_dot2_f32_bf16 v148,v74,s28,v148\nv_dot2_f32_bf16 v149,v74,s29,v149\nv_dot2_f32_bf16 v150,v75,s28,v150\nv_dot2_f32_bf16 v151,v75,s29,v151\n"
"v_dot2_f32_bf16 v144,v76,s28,v144\nv_dot2_f32_bf16 v145,v76,s29,v145\nv_dot2_f32_bf16 v146,v77,s28,v146\nv_dot2_f32_bf16 v147,v77,s29,v147\n"
"v_dot2_f32_bf16 v148,v78,s28,v148\nv_dot2_f32_bf16 v149,v78,s29,v149\nv_dot2_f32_bf16 v150,v79,s28,v150\nv_dot2_f32_bf16 v151,v79,s29,v151\ns_nop 2\n"
"CV 144,144,145\nCV 145,146,147\nCV 146,148,149\nCV 147,150,151\ns_nop 0\nv_permlane16_swap_b32 v144,v146\nv_permlane16_swap_b32 v145,v147\n"
"buffer_store_dwordx4 v[144:147],v12,s[16:19],s45 offen offset:896\ns_mul_i32 s45,s96,28672\ns_mul_i32 s49,s97,64\ns_add_u32 s45,s45,s49\n"
"s_waitcnt vmcnt(13)\nv_dot2_f32_bf16 v144,v80,s28,0\nv_dot2_f32_bf16 v145,v80,s29,0\nv_dot2_f32_bf16 v146,v81,s28,0\nv_dot2_f32_bf16 v147,v81,s29,0\n"
"v_dot2_f32_bf16 v148,v82,s28,0\nv_dot2_f32_bf16 v149,v82,s29,0\nv_dot2_f32_bf16 v150,v83,s28,0\nv_dot2_f32_bf16 v151,v83,s29,0\n"
"v_dot2_f32_bf16 v144,v84,s28,v144\nv_dot2_f32_bf16 v145,v84,s29,v145\nv_dot2_f32_bf16 v146,v85,s28,v146\nv_dot2_f32_bf16 v147,v85,s29,v147\n"
"v_dot2_f32_bf16 v148,v86,s28,v148\nv_dot2_f32_bf16 v149,v86,s29,v149\nv_dot2_f32_bf16 v150,v87,s28,v150\nv_dot2_f32_bf16 v151,v87,s29,v151\n"
"v_dot2_f32_bf16 v144,v88,s28,v144\nv_dot2_f32_bf16 v145,v88,s29,v145\nv_dot2_f32_bf16 v146,v89,s28,v146\nv_dot2_f32_bf16 v147,v89,s29,v147\n"
"v_dot2_f32_bf16 v148,v90,s28,v148\nv_dot2_f32_bf16 v149,v90,s29,v149\nv_dot2_f32_bf16 v150,v91,s28,v150\nv_dot2_f32_bf16 v151,v91,s29,v151\n"
"v_dot2_f32_bf16 v144,v92,s28,v144\nv_dot2_f32_bf16 v145,v92,s29,v145\nv_dot2_f32_bf16 v146,v93,s28,v146\nv_dot2_f32_bf16 v147,v93,s29,v147\n"
"v_dot2_f32_bf16 v148,v94,s28,v148\nv_dot2_f32_bf16 v149,v94,s29,v149\nv_dot2_f32_bf16 v150,v95,s28,v150\nv_dot2_f32_bf16 v151,v95,s29,v151\n"
"s_waitcnt vmcnt(9)\nv_dot2_f32_bf16 v144,v96,s28,v144\nv_dot2_f32_bf16 v145,v96,s29,v145\nv_dot2_f32_bf16 v146,v97,s28,v146\n"
"v_dot2_f32_bf16 v147,v97,s29,v147\nv_dot2_f32_bf16 v148,v98,s28,v148\nv_dot2_f32_bf16 v149,v98,s29,v149\nv_dot2_f32_bf16 v150,v99,s28,v150\n"
"v_dot2_f32_bf16 v151,v99,s29,v151\nv_dot2_f32_bf16 v144,v100,s28,v144\nv_dot2_f32_bf16 v145,v100,s29,v145\nv_dot2_f32_bf16 v146,v101,s28,v146\n"
"v_dot2_f32_bf16 v147,v101,s29,v147\nv_dot2_f32_bf16 v148,v102,s28,v148\nv_dot2_f32_bf16 v149,v102,s29,v149\nv_dot2_f32_bf16 v150,v103,s28,v150\n"
"v_dot2_f32_bf16 v151,v103,s29,v151\nv_dot2_f32_bf16 v144,v104,s28,v144\nv_dot2_f32_bf16 v145,v104,s29,v145\nv_dot2_f32_bf16 v146,v105,s28,v146\n"
"v_dot2_f32_bf16 v147,v105,s29,v147\nv_dot2_f32_bf16 v148,v106,s28,v148\nv_dot2_f32_bf16 v149,v106,s29,v149\nv_dot2_f32_bf16 v150,v107,s28,v150\n"
"v_dot2_f32_bf16 v151,v107,s29,v151\nv_dot2_f32_bf16 v144,v108,s28,v144\nv_dot2_f32_bf16 v145,v108,s29,v145\nv_dot2_f32_bf16 v146,v109,s28,v146\n"
"v_dot2_f32_bf16 v147,v109,s29,v147\nv_dot2_f32_bf16 v148,v110,s28,v148\nv_dot2_f32_bf16 v149,v110,s29,v149\nv_dot2_f32_bf16 v150,v111,s28,v150\n"
"v_dot2_f32_bf16 v151,v111,s29,v151\ns_waitcnt vmcnt(5)\nv_dot2_f32_bf16 v144,v112,s28,v144\nv_dot2_f32_bf16 v145,v112,s29,v145\n"
"v_dot2_f32_bf16 v146,v113,s28,v146\nv_dot2_f32_bf16 v147,v113,s29,v147\nv_dot2_f32_bf16 v148,v114,s28,v148\nv_dot2_f32_bf16 v149,v114,s29,v149\n"
"v_dot2_f32_bf16 v150,v115,s28,v150\nv_dot2_f32_bf16 v151,v115,s29,v151\nv_dot2_f32_bf16 v144,v116,s28,v144\nv_dot2_f32_bf16 v145,v116,s29,v145\n"
"v_dot2_f32_bf16 v146,v117,s28,v146\nv_dot2_f32_bf16 v147,v117,s29,v147\nv_dot2_f32_bf16 v148,v118,s28,v148\nv_dot2_f32_bf16 v149,v118,s29,v149\n"
"v_dot2_f32_bf16 v150,v119,s28,v150\nv_dot2_f32_bf16 v151,v119,s29,v151\nv_dot2_f32_bf16 v144,v120,s28,v144\nv_dot2_f32_bf16 v145,v120,s29,v145\n"
"v_dot2_f32_bf16 v146,v121,s28,v146\nv_dot2_f32_bf16 v147,v121,s29,v147\nv_dot2_f32_bf16 v148,v122,s28,v148\nv_dot2_f32_bf16 v149,v122,s29,v149\n"
"v_dot2_f32_bf16 v150,v123,s28,v150\nv_dot2_f32_bf16 v151,v123,s29,v151\nv_dot2_f32_bf16 v144,v124,s28,v144\nv_dot2_f32_bf16 v145,v124,s29,v145\n"
"v_dot2_f32_bf16 v146,v125,s28,v146\nv_dot2_f32_bf16 v147,v125,s29,v147\nv_dot2_f32_bf16 v148,v126,s28,v148\nv_dot2_f32_bf16 v149,v126,s29,v149\n"
"v_dot2_f32_bf16 v150,v127,s28,v150\nv_dot2_f32_bf16 v151,v127,s29,v151\ns_waitcnt vmcnt(1)\nv_dot2_f32_bf16 v144,v128,s28,v144\n"
"v_dot2_f32_bf16 v145,v128,s29,v145\nv_dot2_f32_bf16 v146,v129,s28,v146\nv_dot2_f32_bf16 v147,v129,s29,v147\nv_dot2_f32_bf16 v148,v130,s28,v148\n"
"v_dot2_f32_bf16 v149,v130,s29,v149\nv_dot2_f32_bf16 v150,v131,s28,v150\nv_dot2_f32_bf16 v151,v131,s29,v151\nv_dot2_f32_bf16 v144,v132,s28,v144\n"
"v_dot2_f32_bf16 v145,v132,s29,v145\nv_dot2_f32_bf16 v146,v133,s28,v146\nv_dot2_f32_bf16 v147,v133,s29,v147\nv_dot2_f32_bf16 v148,v134,s28,v148\n"
"v_dot2_f32_bf16 v149,v134,s29,v149\nv_dot2_f32_bf16 v150,v135,s28,v150\nv_dot2_f32_bf16 v151,v135,s29,v151\nv_dot2_f32_bf16 v144,v136,s28,v144\n"
"v_dot2_f32_bf16 v145,v136,s29,v145\nv_dot2_f32_bf16 v146,v137,s28,v146\nv_dot2_f32_bf16 v147,v137,s29,v147\nv_dot2_f32_bf16 v148,v138,s28,v148\n"
"v_dot2_f32_bf16 v149,v138,s29,v149\nv_dot2_f32_bf16 v150,v139,s28,v150\nv_dot2_f32_bf16 v151,v139,s29,v151\nv_dot2_f32_bf16 v144,v140,s28,v144\n"
"v_dot2_f32_bf16 v145,v140,s29,v145\nv_dot2_f32_bf16 v146,v141,s28,v146\nv_dot2_f32_bf16 v147,v141,s29,v147\nv_dot2_f32_bf16 v148,v142,s28,v148\n"
"v_dot2_f32_bf16 v149,v142,s29,v149\nv_dot2_f32_bf16 v150,v143,s28,v150\nv_dot2_f32_bf16 v151,v143,s29,v151\ns_nop 2\nCV 144,144,145\nCV 145,146,147\n"
"CV 146,148,149\nCV 147,150,151\ns_nop 0\nv_permlane16_swap_b32 v144,v146\nv_permlane16_swap_b32 v145,v147\n"
"buffer_store_dwordx4 v[144:147],v12,s[16:19],s45 offen offset:896\ns_branch fused_op_late\nfused_op_early1:\ns_mul_i32 s45,s94,28672\n"
"s_mul_i32 s49,s95,64\ns_add_u32 s45,s45,s49\ns_waitcnt vmcnt(12)\nv_dot2_f32_bf16 v144,v16,s28,0\nv_dot2_f32_bf16 v145,v16,s29,0\n"
"v_dot2_f32_bf16 v146,v17,s28,0\nv_dot2_f32_bf16 v147,v17,s29,0\nv_dot2_f32_bf16 v148,v18,s28,0\nv_dot2_f32_bf16 v149,v18,s29,0\n"
"v_dot2_f32_bf16 v150,v19,s28,0\nv_dot2_f32_bf16 v151,v19,s29,0\nv_dot2_f32_bf16 v144,v20,s28,v144\nv_dot2_f32_bf16 v145,v20,s29,v145\n"
"v_dot2_f32_bf16 v146,v21,s28,v146\nv_dot2_f32_bf16 v147,v21,s29,v147\nv_dot2_f32_bf16 v148,v22,s28,v148\nv_dot2_f32_bf16 v149,v22,s29,v149\n"
"v_dot2_f32_bf16 v150,v23,s28,v150\nv_dot2_f32_bf16 v151,v23,s29,v151\nv_dot2_f32_bf16 v144,v24,s28,v144\nv_dot2_f32_bf16 v145,v24,s29,v145\n"
"v_dot2_f32_bf16 v146,v25,s28,v146\nv_dot2_f32_bf16 v147,v25,s29,v147\nv_dot2_f32_bf16 v148,v26,s28,v148\nv_dot2_f32_bf16 v149,v26,s29,v149\n"
"v_dot2_f32_bf16 v150,v27,s28,v150\nv_dot2_f32_bf16 v151,v27,s29,v151\nv_dot2_f32_bf16 v144,v28,s28,v144\nv_dot2_f32_bf16 v145,v28,s29,v145\n"
"v_dot2_f32_bf16 v146,v29,s28,v146\nv_dot2_f32_bf16 v147,v29,s29,v147\nv_dot2_f32_bf16 v148,v30,s28,v148\nv_dot2_f32_bf16 v149,v30,s29,v149\n"
"v_dot2_f32_bf16 v150,v31,s28,v150\nv_dot2_f32_bf16 v151,v31,s29,v151\ns_waitcnt vmcnt(8)\nv_dot2_f32_bf16 v144,v32,s28,v144\n"
"v_dot2_f32_bf16 v145,v32,s29,v145\nv_dot2_f32_bf16 v146,v33,s28,v146\nv_dot2_f32_bf16 v147,v33,s29,v147\nv_dot2_f32_bf16 v148,v34,s28,v148\n"
"v_dot2_f32_bf16 v149,v34,s29,v149\nv_dot2_f32_bf16 v150,v35,s28,v150\nv_dot2_f32_bf16 v151,v35,s29,v151\nv_dot2_f32_bf16 v144,v36,s28,v144\n"
"v_dot2_f32_bf16 v145,v36,s29,v145\nv_dot2_f32_bf16 v146,v37,s28,v146\nv_dot2_f32_bf16 v147,v37,s29,v147\nv_dot2_f32_bf16 v148,v38,s28,v148\n"
"v_dot2_f32_bf16 v149,v38,s29,v149\nv_dot2_f32_bf16 v150,v39,s28,v150\nv_dot2_f32_bf16 v151,v39,s29,v151\nv_dot2_f32_bf16 v144,v40,s28,v144\n"
"v_dot2_f32_bf16 v145,v40,s29,v145\nv_dot2_f32_bf16 v146,v41,s28,v146\nv_dot2_f32_bf16 v147,v41,s29,v147\nv_dot2_f32_bf16 v148,v42,s28,v148\n"
"v_dot2_f32_bf16 v149,v42,s29,v149\nv_dot2_f32_bf16 v150,v43,s28,v150\nv_dot2_f32_bf16 v151,v43,s29,v151\nv_dot2_f32_bf16 v144,v44,s28,v144\n"
"v_dot2_f32_bf16 v145,v44,s29,v145\nv_dot2_f32_bf16 v146,v45,s28,v146\nv_dot2_f32_bf16 v147,v45,s29,v147\nv_dot2_f32_bf16 v148,v46,s28,v148\n"
"v_dot2_f32_bf16 v149,v46,s29,v149\nv_dot2_f32_bf16 v150,v47,s28,v150\nv_dot2_f32_bf16 v151,v47,s29,v151\ns_waitcnt vmcnt(4)\n"
"v_dot2_f32_bf16 v144,v48,s28,v144\nv_dot2_f32_bf16 v145,v48,s29,v145\nv_dot2_f32_bf16 v146,v49,s28,v146\nv_dot2_f32_bf16 v147,v49,s29,v147\n"
"v_dot2_f32_bf16 v148,v50,s28,v148\nv_dot2_f32_bf16 v149,v50,s29,v149\nv_dot2_f32_bf16 v150,v51,s28,v150\nv_dot2_f32_bf16 v151,v51,s29,v151\n"
"v_dot2_f32_bf16 v144,v52,s28,v144\nv_dot2_f32_bf16 v145,v52,s29,v145\nv_dot2_f32_bf16 v146,v53,s28,v146\nv_dot2_f32_bf16 v147,v53,s29,v147\n"
"v_dot2_f32_bf16 v148,v54,s28,v148\nv_dot2_f32_bf16 v149,v54,s29,v149\nv_dot2_f32_bf16 v150,v55,s28,v150\nv_dot2_f32_bf16 v151,v55,s29,v151\n"
"v_dot2_f32_bf16 v144,v56,s28,v144\nv_dot2_f32_bf16 v145,v56,s29,v145\nv_dot2_f32_bf16 v146,v57,s28,v146\nv_dot2_f32_bf16 v147,v57,s29,v147\n"
"v_dot2_f32_bf16 v148,v58,s28,v148\nv_dot2_f32_bf16 v149,v58,s29,v149\nv_dot2_f32_bf16 v150,v59,s28,v150\nv_dot2_f32_bf16 v151,v59,s29,v151\n"
"v_dot2_f32_bf16 v144,v60,s28,v144\nv_dot2_f32_bf16 v145,v60,s29,v145\nv_dot2_f32_bf16 v146,v61,s28,v146\nv_dot2_f32_bf16 v147,v61,s29,v147\n"
"v_dot2_f32_bf16 v148,v62,s28,v148\nv_dot2_f32_bf16 v149,v62,s29,v149\nv_dot2_f32_bf16 v150,v63,s28,v150\nv_dot2_f32_bf16 v151,v63,s29,v151\n"
"s_waitcnt vmcnt(0)\nv_dot2_f32_bf16 v144,v64,s28,v144\nv_dot2_f32_bf16 v145,v64,s29,v145\nv_dot2_f32_bf16 v146,v65,s28,v146\n"
"v_dot2_f32_bf16 v147,v65,s29,v147\nv_dot2_f32_bf16 v148,v66,s28,v148\nv_dot2_f32_bf16 v149,v66,s29,v149\nv_dot2_f32_bf16 v150,v67,s28,v150\n"
"v_dot2_f32_bf16 v151,v67,s29,v151\nv_dot2_f32_bf16 v144,v68,s28,v144\nv_dot2_f32_bf16 v145,v68,s29,v145\nv_dot2_f32_bf16 v146,v69,s28,v146\n"
"v_dot2_f32_bf16 v147,v69,s29,v147\nv_dot2_f32_bf16 v148,v70,s28,v148\nv_dot2_f32_bf16 v149,v70,s29,v149\nv_dot2_f32_bf16 v150,v71,s28,v150\n"
"v_dot2_f32_bf16 v151,v71,s29,v151\nv_dot2_f32_bf16 v144,v72,s28,v144\nv_dot2_f32_bf16 v145,v72,s29,v145\nv_dot2_f32_bf16 v146,v73,s28,v146\n"
"v_dot2_f32_bf16 v147,v73,s29,v147\nv_dot2_f32_bf16 v148,v74,s28,v148\nv_dot2_f32_bf16 v149,v74,s29,v149\nv_dot2_f32_bf16 v150,v75,s28,v150\n"
"v_dot2_f32_bf16 v151,v75,s29,v151\nv_dot2_f32_bf16 v144,v76,s28,v144\nv_dot2_f32_bf16 v145,v76,s29,v145\nv_dot2_f32_bf16 v146,v77,s28,v146\n"
"v_dot2_f32_bf16 v147,v77,s29,v147\nv_dot2_f32_bf16 v148,v78,s28,v148\nv_dot2_f32_bf16 v149,v78,s29,v149\nv_dot2_f32_bf16 v150,v79,s28,v150\n"
"v_dot2_f32_bf16 v151,v79,s29,v151\ns_nop 2\nCV 144,144,145\nCV 145,146,147\nCV 146,148,149\nCV 147,150,151\ns_nop 0\nv_permlane16_swap_b32 v144,v146\n"
"v_permlane16_swap_b32 v145,v147\nbuffer_store_dwordx4 v[144:147],v12,s[16:19],s45 offen offset:896\nfused_op_late:\ns_lshr_b32 s94,s31,3\n"
"s_add_u32 s94,s94,64\ns_and_b32 s95,s31,7\ns_lshr_b32 s8,s94,3\ns_lshl_b32 s8,s8,13\ns_bfe_u32 s49,s94,0x20001\ns_lshl_b32 s49,s49,7\n"
"s_add_u32 s8,s8,s49\ns_add_u32 s8,s8,458752\nv_and_b32 v15,0xf0,v0\nv_lshlrev_b32 v15,5,v15\nbuffer_load_dword v14,v15,s[24:27],s8 offen sc1\n"
"s_waitcnt vmcnt(0)\nv_cmp_ne_u32 vcc,s65,v14\ns_nop 0\ns_cbranch_vccnz fused_op_sp58\nfused_op_spb59:\ns_lshr_b32 s45,s94,3\ns_sub_u32 s45,15,s45\n"
"s_lshl_b32 s45,s45,17\ns_bfe_u32 s49,s94,0x20001\ns_lshl_b32 s49,s49,15\ns_add_u32 s45,s45,s49\ns_and_b32 s49,s94,1\ns_mul_i32 s49,s49,14\n"
"s_add_u32 s49,s49,s95\ns_lshl_b32 s49,s49,10\ns_add_u32 s45,s45,s49\ns_add_u32 s45,s45,51380224\n"
"buffer_load_dwordx4 v[16:19],v0,s[24:27],s45 offen sc1\ns_add_u32 s45,s45,0x100000\nbuffer_load_dwordx4 v[20:23],v0,s[24:27],s45 offen sc1\n"
"s_add_u32 s45,s45,0x100000\nbuffer_load_dwordx4 v[24:27],v0,s[24:27],s45 offen sc1\ns_add_u32 s45,s45,0x100000\n"
"buffer_load_dwordx4 v[28:31],v0,s[24:27],s45 offen sc1\ns_add_u32 s45,s45,0x100000\nbuffer_load_dwordx4 v[32:35],v0,s[24:27],s45 offen sc1\n"
"s_add_u32 s45,s45,0x100000\nbuffer_load_dwordx4 v[36:39],v0,s[24:27],s45 offen sc1\ns_add_u32 s45,s45,0x100000\n"
"buffer_load_dwordx4 v[40:43],v0,s[24:27],s45 offen sc1\ns_add_u32 s45,s45,0x100000\nbuffer_load_dwordx4 v[44:47],v0,s[24:27],s45 offen sc1\n"
"s_add_u32 s45,s45,0x100000\nbuffer_load_dwordx4 v[48:51],v0,s[24:27],s45 offen sc1\ns_add_u32 s45,s45,0x100000\n"
"buffer_load_dwordx4 v[52:55],v0,s[24:27],s45 offen sc1\ns_add_u32 s45,s45,0x100000\nbuffer_load_dwordx4 v[56:59],v0,s[24:27],s45 offen sc1\n"
"s_add_u32 s45,s45,0x100000\nbuffer_load_dwordx4 v[60:63],v0,s[24:27],s45 offen sc1\ns_add_u32 s45,s45,0x100000\n"
"buffer_load_dwordx4 v[64:67],v0,s[24:27],s45 offen sc1\ns_add_u32 s45,s45,0x100000\nbuffer_load_dwordx4 v[68:71],v0,s[24:27],s45 offen sc1\n"
"s_add_u32 s45,s45,0x100000\nbuffer_load_dwordx4 v[72:75],v0,s[24:27],s45 offen sc1\ns_add_u32 s45,s45,0x100000\n"
"buffer_load_dwordx4 v[76:79],v0,s[24:27],s45 offen sc1\ns_add_u32 s45,s45,0x100000\ns_mul_i32 s45,s94,28672\ns_mul_i32 s49,s95,64\n"
"s_add_u32 s45,s45,s49\ns_waitcnt vmcnt(12)\nv_dot2_f32_bf16 v144,v16,s28,0\nv_dot2_f32_bf16 v145,v16,s29,0\nv_dot2_f32_bf16 v146,v17,s28,0\n"
"v_dot2_f32_bf16 v147,v17,s29,0\nv_dot2_f32_bf16 v148,v18,s28,0\nv_dot2_f32_bf16 v149,v18,s29,0\nv_dot2_f32_bf16 v150,v19,s28,0\n"
"v_dot2_f32_bf16 v151,v19,s29,0\nv_dot2_f32_bf16 v144,v20,s28,v144\nv_dot2_f32_bf16 v145,v20,s29,v145\nv_dot2_f32_bf16 v146,v21,s28,v146\n"
"v_dot2_f32_bf16 v147,v21,s29,v147\nv_dot2_f32_bf16 v148,v22,s28,v148\nv_dot2_f32_bf16 v149,v22,s29,v149\nv_dot2_f32_bf16 v150,v23,s28,v150\n"
"v_dot2_f32_bf16 v151,v23,s29,v151\nv_dot2_f32_bf16 v144,v24,s28,v144\nv_dot2_f32_bf16 v145,v24,s29,v145\nv_dot2_f32_bf16 v146,v25,s28,v146\n"
"v_dot2_f32_bf16 v147,v25,s29,v147\nv_dot2_f32_bf16 v148,v26,s28,v148\nv_dot2_f32_bf16 v149,v26,s29,v149\nv_dot2_f32_bf16 v150,v27,s28,v150\n"
"v_dot2_f32_bf16 v151,v27,s29,v151\nv_dot2_f32_bf16 v144,v28,s28,v144\nv_dot2_f32_bf16 v145,v28,s29,v145\nv_dot2_f32_bf16 v146,v29,s28,v146\n"
"v_dot2_f32_bf16 v147,v29,s29,v147\nv_dot2_f32_bf16 v148,v30,s28,v148\nv_dot2_f32_bf16 v149,v30,s29,v149\nv_dot2_f32_bf16 v150,v31,s28,v150\n"
"v_dot2_f32_bf16 v151,v31,s29,v151\ns_waitcnt vmcnt(8)\nv_dot2_f32_bf16 v144,v32,s28,v144\nv_dot2_f32_bf16 v145,v32,s29,v145\n"
"v_dot2_f32_bf16 v146,v33,s28,v146\nv_dot2_f32_bf16 v147,v33,s29,v147\nv_dot2_f32_bf16 v148,v34,s28,v148\nv_dot2_f32_bf16 v149,v34,s29,v149\n"
"v_dot2_f32_bf16 v150,v35,s28,v150\nv_dot2_f32_bf16 v151,v35,s29,v151\nv_dot2_f32_bf16 v144,v36,s28,v144\nv_dot2_f32_bf16 v145,v36,s29,v145\n"
"v_dot2_f32_bf16 v146,v37,s28,v146\nv_dot2_f32_bf16 v147,v37,s29,v147\nv_dot2_f32_bf16 v148,v38,s28,v148\nv_dot2_f32_bf16 v149,v38,s29,v149\n"
"v_dot2_f32_bf16 v150,v39,s28,v150\nv_dot2_f32_bf16 v151,v39,s29,v151\nv_dot2_f32_bf16 v144,v40,s28,v144\nv_dot2_f32_bf16 v145,v40,s29,v145\n"
"v_dot2_f32_bf16 v146,v41,s28,v146\nv_dot2_f32_bf16 v147,v41,s29,v147\nv_dot2_f32_bf16 v148,v42,s28,v148\nv_dot2_f32_bf16 v149,v42,s29,v149\n"
"v_dot2_f32_bf16 v150,v43,s28,v150\nv_dot2_f32_bf16 v151,v43,s29,v151\nv_dot2_f32_bf16 v144,v44,s28,v144\nv_dot2_f32_bf16 v145,v44,s29,v145\n"
"v_dot2_f32_bf16 v146,v45,s28,v146\nv_dot2_f32_bf16 v147,v45,s29,v147\nv_dot2_f32_bf16 v148,v46,s28,v148\nv_dot2_f32_bf16 v149,v46,s29,v149\n"
"v_dot2_f32_bf16 v150,v47,s28,v150\nv_dot2_f32_bf16 v151,v47,s29,v151\ns_waitcnt vmcnt(4)\nv_dot2_f32_bf16 v144,v48,s28,v144\n"
"v_dot2_f32_bf16 v145,v48,s29,v145\nv_dot2_f32_bf16 v146,v49,s28,v146\nv_dot2_f32_bf16 v147,v49,s29,v147\nv_dot2_f32_bf16 v148,v50,s28,v148\n"
"v_dot2_f32_bf16 v149,v50,s29,v149\nv_dot2_f32_bf16 v150,v51,s28,v150\nv_dot2_f32_bf16 v151,v51,s29,v151\nv_dot2_f32_bf16 v144,v52,s28,v144\n"
"v_dot2_f32_bf16 v145,v52,s29,v145\nv_dot2_f32_bf16 v146,v53,s28,v146\nv_dot2_f32_bf16 v147,v53,s29,v147\nv_dot2_f32_bf16 v148,v54,s28,v148\n"
"v_dot2_f32_bf16 v149,v54,s29,v149\nv_dot2_f32_bf16 v150,v55,s28,v150\nv_dot2_f32_bf16 v151,v55,s29,v151\nv_dot2_f32_bf16 v144,v56,s28,v144\n"
"v_dot2_f32_bf16 v145,v56,s29,v145\nv_dot2_f32_bf16 v146,v57,s28,v146\nv_dot2_f32_bf16 v147,v57,s29,v147\nv_dot2_f32_bf16 v148,v58,s28,v148\n"
"v_dot2_f32_bf16 v149,v58,s29,v149\nv_dot2_f32_bf16 v150,v59,s28,v150\nv_dot2_f32_bf16 v151,v59,s29,v151\nv_dot2_f32_bf16 v144,v60,s28,v144\n"
"v_dot2_f32_bf16 v145,v60,s29,v145\nv_dot2_f32_bf16 v146,v61,s28,v146\nv_dot2_f32_bf16 v147,v61,s29,v147\nv_dot2_f32_bf16 v148,v62,s28,v148\n"
"v_dot2_f32_bf16 v149,v62,s29,v149\nv_dot2_f32_bf16 v150,v63,s28,v150\nv_dot2_f32_bf16 v151,v63,s29,v151\ns_waitcnt vmcnt(0)\n"
"v_dot2_f32_bf16 v144,v64,s28,v144\nv_dot2_f32_bf16 v145,v64,s29,v145\nv_dot2_f32_bf16 v146,v65,s28,v146\nv_dot2_f32_bf16 v147,v65,s29,v147\n"
"v_dot2_f32_bf16 v148,v66,s28,v148\nv_dot2_f32_bf16 v149,v66,s29,v149\nv_dot2_f32_bf16 v150,v67,s28,v150\nv_dot2_f32_bf16 v151,v67,s29,v151\n"
"v_dot2_f32_bf16 v144,v68,s28,v144\nv_dot2_f32_bf16 v145,v68,s29,v145\nv_dot2_f32_bf16 v146,v69,s28,v146\nv_dot2_f32_bf16 v147,v69,s29,v147\n"
"v_dot2_f32_bf16 v148,v70,s28,v148\nv_dot2_f32_bf16 v149,v70,s29,v149\nv_dot2_f32_bf16 v150,v71,s28,v150\nv_dot2_f32_bf16 v151,v71,s29,v151\n"
"v_dot2_f32_bf16 v144,v72,s28,v144\nv_dot2_f32_bf16 v145,v72,s29,v145\nv_dot2_f32_bf16 v146,v73,s28,v146\nv_dot2_f32_bf16 v147,v73,s29,v147\n"
"v_dot2_f32_bf16 v148,v74,s28,v148\nv_dot2_f32_bf16 v149,v74,s29,v149\nv_dot2_f32_bf16 v150,v75,s28,v150\nv_dot2_f32_bf16 v151,v75,s29,v151\n"
"v_dot2_f32_bf16 v144,v76,s28,v144\nv_dot2_f32_bf16 v145,v76,s29,v145\nv_dot2_f32_bf16 v146,v77,s28,v146\nv_dot2_f32_bf16 v147,v77,s29,v147\n"
"v_dot2_f32_bf16 v148,v78,s28,v148\nv_dot2_f32_bf16 v149,v78,s29,v149\nv_dot2_f32_bf16 v150,v79,s28,v150\nv_dot2_f32_bf16 v151,v79,s29,v151\ns_nop 2\n"
"CV 144,144,145\nCV 145,146,147\nCV 146,148,149\nCV 147,150,151\ns_nop 0\nv_permlane16_swap_b32 v144,v146\nv_permlane16_swap_b32 v145,v147\n"
"buffer_store_dwordx4 v[144:147],v12,s[16:19],s45 offen offset:896\ns_mul_i32 s31,s11,1\ns_mul_i32 s49,s10,1\ns_add_u32 s31,s31,s49\n"
"s_add_u32 s31,s31,0x7d\ns_cmp_le_u32 s10,2\ns_cbranch_scc1 fused_op_end\ns_mul_hi_u32 s94,s31,0x2aaaaaab\ns_mul_i32 s95,s94,6\ns_sub_u32 s95,s31,s95\n"
"s_add_u32 s94,s94,64\ns_add_u32 s95,s95,8\ns_lshr_b32 s8,s94,3\ns_lshl_b32 s8,s8,13\ns_bfe_u32 s49,s94,0x20001\ns_lshl_b32 s49,s49,7\n"
"s_add_u32 s8,s8,s49\ns_add_u32 s8,s8,327680\nfused_op_item:\nv_and_b32 v15,0xf0,v0\nv_lshlrev_b32 v15,5,v15\n"
"buffer_load_dword v14,v15,s[24:27],s8 offen sc1\ns_waitcnt vmcnt(0)\nv_cmp_ne_u32 vcc,s65,v14\ns_nop 0\ns_cbranch_vccnz fused_op_sp60\nfused_op_spb61:\n"
"s_lshr_b32 s45,s94,3\ns_sub_u32 s45,15,s45\ns_lshl_b32 s45,s45,17\ns_bfe_u32 s49,s94,0x20001\ns_lshl_b32 s49,s49,15\ns_add_u32 s45,s45,s49\n"
"s_and_b32 s49,s94,1\ns_mul_i32 s49,s49,14\ns_add_u32 s49,s49,s95\ns_lshl_b32 s49,s49,10\ns_add_u32 s45,s45,s49\ns_add_u32 s45,s45,51380224\n"
"buffer_load_dwordx4 v[16:19],v0,s[24:27],s45 offen sc1\ns_add_u32 s45,s45,0x100000\nbuffer_load_dwordx4 v[20:23],v0,s[24:27],s45 offen sc1\n"
"s_add_u32 s45,s45,0x100000\nbuffer_load_dwordx4 v[24:27],v0,s[24:27],s45 offen sc1\ns_add_u32 s45,s45,0x100000\n"
"buffer_load_dwordx4 v[28:31],v0,s[24:27],s45 offen sc1\ns_add_u32 s45,s45,0x100000\nbuffer_load_dwordx4 v[32:35],v0,s[24:27],s45 offen sc1\n"
"s_add_u32 s45,s45,0x100000\nbuffer_load_dwordx4 v[36:39],v0,s[24:27],s45 offen sc1\ns_add_u32 s45,s45,0x100000\n"
"buffer_load_dwordx4 v[40:43],v0,s[24:27],s45 offen sc1\ns_add_u32 s45,s45,0x100000\nbuffer_load_dwordx4 v[44:47],v0,s[24:27],s45 offen sc1\n"
"s_add_u32 s45,s45,0x100000\nbuffer_load_dwordx4 v[48:51],v0,s[24:27],s45 offen sc1\ns_add_u32 s45,s45,0x100000\n"
"buffer_load_dwordx4 v[52:55],v0,s[24:27],s45 offen sc1\ns_add_u32 s45,s45,0x100000\nbuffer_load_dwordx4 v[56:59],v0,s[24:27],s45 offen sc1\n"
"s_add_u32 s45,s45,0x100000\nbuffer_load_dwordx4 v[60:63],v0,s[24:27],s45 offen sc1\ns_add_u32 s45,s45,0x100000\n"
"buffer_load_dwordx4 v[64:67],v0,s[24:27],s45 offen sc1\ns_add_u32 s45,s45,0x100000\nbuffer_load_dwordx4 v[68:71],v0,s[24:27],s45 offen sc1\n"
"s_add_u32 s45,s45,0x100000\nbuffer_load_dwordx4 v[72:75],v0,s[24:27],s45 offen sc1\ns_add_u32 s45,s45,0x100000\n"
"buffer_load_dwordx4 v[76:79],v0,s[24:27],s45 offen sc1\ns_add_u32 s45,s45,0x100000\ns_mul_i32 s45,s94,28672\ns_mul_i32 s49,s95,64\n"
"s_add_u32 s45,s45,s49\ns_waitcnt vmcnt(12)\nv_dot2_f32_bf16 v144,v16,s28,0\nv_dot2_f32_bf16 v145,v16,s29,0\nv_dot2_f32_bf16 v146,v17,s28,0\n"
"v_dot2_f32_bf16 v147,v17,s29,0\nv_dot2_f32_bf16 v148,v18,s28,0\nv_dot2_f32_bf16 v149,v18,s29,0\nv_dot2_f32_bf16 v150,v19,s28,0\n"
"v_dot2_f32_bf16 v151,v19,s29,0\nv_dot2_f32_bf16 v144,v20,s28,v144\nv_dot2_f32_bf16 v145,v20,s29,v145\nv_dot2_f32_bf16 v146,v21,s28,v146\n"
"v_dot2_f32_bf16 v147,v21,s29,v147\nv_dot2_f32_bf16 v148,v22,s28,v148\nv_dot2_f32_bf16 v149,v22,s29,v149\nv_dot2_f32_bf16 v150,v23,s28,v150\n"
"v_dot2_f32_bf16 v151,v23,s29,v151\nv_dot2_f32_bf16 v144,v24,s28,v144\nv_dot2_f32_bf16 v145,v24,s29,v145\nv_dot2_f32_bf16 v146,v25,s28,v146\n"
"v_dot2_f32_bf16 v147,v25,s29,v147\nv_dot2_f32_bf16 v148,v26,s28,v148\nv_dot2_f32_bf16 v149,v26,s29,v149\nv_dot2_f32_bf16 v150,v27,s28,v150\n"
"v_dot2_f32_bf16 v151,v27,s29,v151\nv_dot2_f32_bf16 v144,v28,s28,v144\nv_dot2_f32_bf16 v145,v28,s29,v145\nv_dot2_f32_bf16 v146,v29,s28,v146\n"
"v_dot2_f32_bf16 v147,v29,s29,v147\nv_dot2_f32_bf16 v148,v30,s28,v148\nv_dot2_f32_bf16 v149,v30,s29,v149\nv_dot2_f32_bf16 v150,v31,s28,v150\n"
"v_dot2_f32_bf16 v151,v31,s29,v151\ns_waitcnt vmcnt(8)\nv_dot2_f32_bf16 v144,v32,s28,v144\nv_dot2_f32_bf16 v145,v32,s29,v145\n"
"v_dot2_f32_bf16 v146,v33,s28,v146\nv_dot2_f32_bf16 v147,v33,s29,v147\nv_dot2_f32_bf16 v148,v34,s28,v148\nv_dot2_f32_bf16 v149,v34,s29,v149\n"
"v_dot2_f32_bf16 v150,v35,s28,v150\nv_dot2_f32_bf16 v151,v35,s29,v151\nv_dot2_f32_bf16 v144,v36,s28,v144\nv_dot2_f32_bf16 v145,v36,s29,v145\n"
"v_dot2_f32_bf16 v146,v37,s28,v146\nv_dot2_f32_bf16 v147,v37,s29,v147\nv_dot2_f32_bf16 v148,v38,s28,v148\nv_dot2_f32_bf16 v149,v38,s29,v149\n"
"v_dot2_f32_bf16 v150,v39,s28,v150\nv_dot2_f32_bf16 v151,v39,s29,v151\nv_dot2_f32_bf16 v144,v40,s28,v144\nv_dot2_f32_bf16 v145,v40,s29,v145\n"
"v_dot2_f32_bf16 v146,v41,s28,v146\nv_dot2_f32_bf16 v147,v41,s29,v147\nv_dot2_f32_bf16 v148,v42,s28,v148\nv_dot2_f32_bf16 v149,v42,s29,v149\n"
"v_dot2_f32_bf16 v150,v43,s28,v150\nv_dot2_f32_bf16 v151,v43,s29,v151\nv_dot2_f32_bf16 v144,v44,s28,v144\nv_dot2_f32_bf16 v145,v44,s29,v145\n"
"v_dot2_f32_bf16 v146,v45,s28,v146\nv_dot2_f32_bf16 v147,v45,s29,v147\nv_dot2_f32_bf16 v148,v46,s28,v148\nv_dot2_f32_bf16 v149,v46,s29,v149\n"
"v_dot2_f32_bf16 v150,v47,s28,v150\nv_dot2_f32_bf16 v151,v47,s29,v151\ns_waitcnt vmcnt(4)\nv_dot2_f32_bf16 v144,v48,s28,v144\n"
"v_dot2_f32_bf16 v145,v48,s29,v145\nv_dot2_f32_bf16 v146,v49,s28,v146\nv_dot2_f32_bf16 v147,v49,s29,v147\nv_dot2_f32_bf16 v148,v50,s28,v148\n"
"v_dot2_f32_bf16 v149,v50,s29,v149\nv_dot2_f32_bf16 v150,v51,s28,v150\nv_dot2_f32_bf16 v151,v51,s29,v151\nv_dot2_f32_bf16 v144,v52,s28,v144\n"
"v_dot2_f32_bf16 v145,v52,s29,v145\nv_dot2_f32_bf16 v146,v53,s28,v146\nv_dot2_f32_bf16 v147,v53,s29,v147\nv_dot2_f32_bf16 v148,v54,s28,v148\n"
"v_dot2_f32_bf16 v149,v54,s29,v149\nv_dot2_f32_bf16 v150,v55,s28,v150\nv_dot2_f32_bf16 v151,v55,s29,v151\nv_dot2_f32_bf16 v144,v56,s28,v144\n"
"v_dot2_f32_bf16 v145,v56,s29,v145\nv_dot2_f32_bf16 v146,v57,s28,v146\nv_dot2_f32_bf16 v147,v57,s29,v147\nv_dot2_f32_bf16 v148,v58,s28,v148\n"
"v_dot2_f32_bf16 v149,v58,s29,v149\nv_dot2_f32_bf16 v150,v59,s28,v150\nv_dot2_f32_bf16 v151,v59,s29,v151\nv_dot2_f32_bf16 v144,v60,s28,v144\n"
"v_dot2_f32_bf16 v145,v60,s29,v145\nv_dot2_f32_bf16 v146,v61,s28,v146\nv_dot2_f32_bf16 v147,v61,s29,v147\nv_dot2_f32_bf16 v148,v62,s28,v148\n"
"v_dot2_f32_bf16 v149,v62,s29,v149\nv_dot2_f32_bf16 v150,v63,s28,v150\nv_dot2_f32_bf16 v151,v63,s29,v151\ns_waitcnt vmcnt(0)\n"
"v_dot2_f32_bf16 v144,v64,s28,v144\nv_dot2_f32_bf16 v145,v64,s29,v145\nv_dot2_f32_bf16 v146,v65,s28,v146\nv_dot2_f32_bf16 v147,v65,s29,v147\n"
"v_dot2_f32_bf16 v148,v66,s28,v148\nv_dot2_f32_bf16 v149,v66,s29,v149\nv_dot2_f32_bf16 v150,v67,s28,v150\nv_dot2_f32_bf16 v151,v67,s29,v151\n"
"v_dot2_f32_bf16 v144,v68,s28,v144\nv_dot2_f32_bf16 v145,v68,s29,v145\nv_dot2_f32_bf16 v146,v69,s28,v146\nv_dot2_f32_bf16 v147,v69,s29,v147\n"
"v_dot2_f32_bf16 v148,v70,s28,v148\nv_dot2_f32_bf16 v149,v70,s29,v149\nv_dot2_f32_bf16 v150,v71,s28,v150\nv_dot2_f32_bf16 v151,v71,s29,v151\n"
"v_dot2_f32_bf16 v144,v72,s28,v144\nv_dot2_f32_bf16 v145,v72,s29,v145\nv_dot2_f32_bf16 v146,v73,s28,v146\nv_dot2_f32_bf16 v147,v73,s29,v147\n"
"v_dot2_f32_bf16 v148,v74,s28,v148\nv_dot2_f32_bf16 v149,v74,s29,v149\nv_dot2_f32_bf16 v150,v75,s28,v150\nv_dot2_f32_bf16 v151,v75,s29,v151\n"
"v_dot2_f32_bf16 v144,v76,s28,v144\nv_dot2_f32_bf16 v145,v76,s29,v145\nv_dot2_f32_bf16 v146,v77,s28,v146\nv_dot2_f32_bf16 v147,v77,s29,v147\n"
"v_dot2_f32_bf16 v148,v78,s28,v148\nv_dot2_f32_bf16 v149,v78,s29,v149\nv_dot2_f32_bf16 v150,v79,s28,v150\nv_dot2_f32_bf16 v151,v79,s29,v151\ns_nop 2\n"
"CV 144,144,145\nCV 145,146,147\nCV 146,148,149\nCV 147,150,151\ns_nop 0\nv_permlane16_swap_b32 v144,v146\nv_permlane16_swap_b32 v145,v147\n"
"buffer_store_dwordx4 v[144:147],v12,s[16:19],s45 offen offset:896\ns_cmp_lg_u32 s31,0\ns_cbranch_scc1 fused_op_end\ns_mov_b32 s45,0\nv_mov_b32 v13,s65\n"
"s_mov_b64 exec,1\nbuffer_store_dword v13,off,s[24:27],s45 sc1\ns_mov_b64 exec,-1\ns_branch fused_op_end\nfused_op_ta:\ns_lshl_b32 s45,s10,13\n"
"s_add_u32 s45,s45,fused_op_end-32768-fused_op_pc\ns_min_u32 s45,s45,fused_op_end-fused_op_pc-8192\nv_lshlrev_b32 v12,3,v0\nv_add_u32 v12,s45,v12\n"
"global_load_dword a255,v12,s[54:55]\nNvaa 144,0,128,224\nRD 32,3,8192\nCV 208,176,177\nMvaa 144,4,132\nRD 36,3,9216\nCV 209,178,179\nMvaa 144,8,136\n"
"RD 40,3,10240\nv_add_u32 v1,0xffff8000,v1\nMvaa 144,12,140\nRD 44,3,11264\nCV 216,180,181\nMvaa 144,16,144\nRD 48,3,12288\nCV 217,182,183\n"
"Mvaa 144,20,148\nRD 52,3,13312\nv_add_f32 v234,v184,v234\nMvaa 144,24,152\nRD 56,3,14336\nv_add_f32 v234,v185,v234\nMvaa 144,28,156\n"
"s_add_u32 m0,s48,s44\nRD 60,3,15360\nCV 210,184,185\nNvaa 148,28,188,228\nv_exp_f32 v192,v192\nMvaa 148,24,184\nDS 1,20,36,0\nMvaa 148,20,180\n"
"v_exp_f32 v193,v193\nMvaa 148,16,176\nv_exp_f32 v194,v194\nMvaa 148,12,172\nDS 1,20,37,1024\nMvaa 148,8,168\nv_exp_f32 v195,v195\nMvaa 148,4,164\n"
"v_exp_f32 v196,v196\nMvaa 148,0,160\nWL 0\nDS 1,20,38,2048\nNvaa 152,32,128,224\nRD 0,3,16384\nv_add_f32 v234,v186,v234\nMvaa 152,36,132\nRD 4,3,17408\n"
"v_add_f32 v234,v187,v234\nMvaa 152,40,136\nRD 8,3,18432\nCV 211,186,187\nMvaa 152,44,140\nRD 12,3,19456\nv_add_f32 v235,v188,v235\nMvaa 152,48,144\n"
"RD 16,3,20480\nv_add_f32 v235,v189,v235\nMvaa 152,52,148\nRD 20,3,21504\nCV 218,188,189\nMvaa 152,56,152\nRD 24,3,22528\nv_add_f32 v235,v190,v235\n"
"Mvaa 152,60,156\nRD 28,3,23552\nv_add_f32 v235,v191,v235\nNvaa 156,60,188,228\nv_exp_f32 v197,v197\nMvaa 156,56,184\nDS 1,20,39,3072\nMvaa 156,52,180\n"
"s_add_u32 m0,m0,0x1000\nv_exp_f32 v198,v198\nMvaa 156,48,176\nv_exp_f32 v199,v199\nMvaa 156,44,172\nDS 1,20,40,0\nMvaa 156,40,168\nv_exp_f32 v200,v200\n"
"Mvaa 156,36,164\nv_exp_f32 v201,v201\nMvaa 156,32,160\nWL 0\nDS 1,20,41,1024\nNvaa 160,0,128,224\nRD 32,3,24576\nCV 219,190,191\nMvaa 160,4,132\n"
"s_mov_b32 s44,s47\nRD 36,3,25600\nv_add_f32 v234,v192,v234\nMvaa 160,8,136\ns_add_u32 s47,s47,0x8000\nRD 40,3,26624\nv_add_f32 v234,v193,v234\n"
"Mvaa 160,12,140\ns_cmp_eq_u32 s47,0x28000\nRD 44,3,27648\nCV 212,192,193\nMvaa 160,16,144\ns_cselect_b32 s47,0,s47\nRD 48,3,28672\n"
"v_add_f32 v234,v194,v234\nMvaa 160,20,148\nRD 52,3,29696\nv_add_f32 v234,v195,v234\nMvaa 160,24,152\nRD 56,3,30720\nCV 213,194,195\nMvaa 160,28,156\n"
"RD 60,3,31744\nv_add_f32 v235,v196,v235\nNvaa 164,28,188,228\nv_exp_f32 v202,v202\nMvaa 164,24,184\nDS 1,20,42,2048\nMvaa 164,20,180\n"
"v_exp_f32 v203,v203\nMvaa 164,16,176\nv_exp_f32 v204,v204\nMvaa 164,12,172\nDS 1,20,43,3072\nMvaa 164,8,168\ns_mul_i32 s45,s56,229376\n"
"v_lshrrev_b32 v13,7,v0\nv_add_f32 v235,v197,v235\nMvaa 164,4,164\ns_mul_i32 s49,s10,57344\nv_add_u32 v3,s47,v0\nv_and_b32 v1,6,v13\nMvaa 164,0,160\n"
"s_waitcnt vmcnt(25) lgkmcnt(0)\ns_barrier\nv_lshlrev_b32 v1,4,v1\nNvaa 168,32,128,224\ns_add_u32 s45,s45,s49\nRD 0,3,0\nv_xor_b32 v1,v1,v0\n"
"Mvaa 168,36,132\ns_add_u32 s20,s88,s45\ns_addc_u32 s21,s89,0\nRD 4,3,1024\nMvaa 168,40,136\ns_and_b32 s21,s21,0xffff\nRD 8,3,2048\n"
"v_and_b32 v1,0x70,v1\nMvaa 168,44,140\ns_sub_u32 s22,3670016,s45\nRD 12,3,3072\nv_mul_u32_u24 v13,896,v13\nMvaa 168,48,144\ns_mov_b32 s23,0x20000\n"
"RD 16,3,4096\nv_add_u32 v1,v1,v13\nMvaa 168,52,148\ns_mov_b32 s36,0\nRD 20,3,5120\nCV 220,196,197\nMvaa 168,56,152\ns_mov_b32 s37,6144\nRD 24,3,6144\n"
"v_add_f32 v235,v198,v235\nMvaa 168,60,156\ns_mov_b32 s38,12288\nRD 28,3,7168\nv_add_f32 v235,v199,v235\nNvaa 172,60,188,228\ns_mov_b32 s39,18432\n"
"v_exp_f32 v205,v205\nMvaa 172,56,184\ns_mov_b32 s40,28672\nv_exp_f32 v206,v206\nMvaa 172,52,180\ns_mov_b32 s41,34816\nv_exp_f32 v207,v207\n"
"Mvaa 172,48,176\ns_mov_b32 s42,40960\nCV 221,198,199\nv_add_f32 v234,v200,v234\nMvaa 172,44,172\ns_mov_b32 s43,47104\nv_add_f32 v234,v201,v234\n"
"CV 214,200,201\nMvaa 172,40,168\nv_add_f32 v234,v202,v234\nCV 215,202,203\nMvaa 172,36,164\nWL 0\nv_add_f32 v234,v203,v234\nv_add_f32 v235,v204,v235\n"
"Mvaa 172,32,160\nv_add_f32 v235,v205,v235\nCV 222,204,205\nv_add_f32 v235,v206,v235\nCV 223,206,207\nv_add_f32 v235,v207,v235\nMvav 16,0,208\n"
"RD 32,3,8192\nMvav 16,4,212\nRD 36,3,9216\nv_max3_f32 v238,v144,v145,v146\nMvav 20,4,220\nRD 40,3,10240\nv_max3_f32 v239,v147,v148,v149\nMvav 20,0,216\n"
"RD 44,3,11264\nv_max3_f32 v238,v238,v150,v151\nMvav 24,8,208\nRD 48,3,12288\nv_max3_f32 v239,v239,v152,v153\nMvav 24,12,212\nRD 52,3,13312\n"
"v_max3_f32 v238,v238,v154,v155\nMvav 28,12,220\nRD 56,3,14336\nv_max3_f32 v239,v239,v156,v157\nMvav 28,8,216\ns_add_u32 m0,s48,s44\nRD 60,3,15360\n"
"v_max3_f32 v238,v238,v158,v159\nMvav 32,16,208\nv_max3_f32 v239,v239,v160,v161\nv_max3_f32 v238,v238,v162,v163\nMvav 32,20,212\nDS 1,20,36,0\n"
"Mvav 36,20,220\nv_max3_f32 v239,v239,v164,v165\nv_max3_f32 v238,v238,v166,v167\nMvav 36,16,216\nv_max3_f32 v239,v239,v168,v169\n"
"v_max3_f32 v238,v238,v170,v171\nMvav 40,24,208\nDS 1,20,37,1024\nMvav 40,28,212\nv_max3_f32 v239,v239,v172,v173\nv_max3_f32 v238,v238,v174,v175\n"
"Mvav 44,28,220\nv_max_f32 v238,v238,v239\nMvav 44,24,216\nWL 0\nDS 1,20,38,2048\nMvav 48,32,208\nRD 0,3,16384\nv_cmp_lt_f32 vcc,s67,v238\n"
"Mvav 48,36,212\ns_cbranch_vccnz fused_op_rm24\nfused_op_rmb25:\nRD 4,3,17408\nMvav 52,36,220\nRD 8,3,18432\nMvav 52,32,216\nRD 12,3,19456\n"
"Mvav 56,40,208\nRD 16,3,20480\nMvav 56,44,212\nRD 20,3,21504\nMvav 60,44,220\nRD 24,3,22528\nMvav 60,40,216\nRD 28,3,23552\nMvav 64,48,208\n"
"v_exp_f32 v144,v144\nMvav 64,52,212\nDS 1,20,39,3072\nMvav 68,52,220\ns_add_u32 m0,m0,0x1000\nv_exp_f32 v145,v145\nMvav 68,48,216\nv_exp_f32 v146,v146\n"
"Mvav 72,56,208\nDS 1,20,40,0\nMvav 72,60,212\nv_exp_f32 v147,v147\nMvav 76,60,220\nv_exp_f32 v148,v148\nMvav 76,56,216\nWL 0\nDS 1,20,41,1024\n"
"Mvav 80,0,208\nRD 32,3,24576\nv_add_f32 v234,v144,v234\nMvav 80,4,212\ns_mov_b32 s44,s47\nRD 36,3,25600\nv_add_f32 v234,v145,v234\nMvav 84,4,220\n"
"s_add_u32 s47,s47,0x8000\nRD 40,3,26624\nv_add_f32 v234,v146,v234\nMvav 84,0,216\ns_cmp_eq_u32 s47,0x28000\nRD 44,3,27648\nv_add_f32 v234,v147,v234\n"
"Mvav 88,8,208\ns_cselect_b32 s47,0,s47\nRD 48,3,28672\nv_add_f32 v235,v148,v235\nMvav 88,12,212\nRD 52,3,29696\nMvav 92,12,220\nRD 56,3,30720\n"
"Mvav 92,8,216\nRD 60,3,31744\nMvav 96,16,208\nv_exp_f32 v149,v149\nMvav 96,20,212\nDS 1,20,42,2048\nMvav 100,20,220\nv_exp_f32 v150,v150\n"
"Mvav 100,16,216\nv_exp_f32 v151,v151\nMvav 104,24,208\nDS 1,20,43,3072\nMvav 104,28,212\nv_add_u32 v1,0x80,v1\nv_add_f32 v235,v149,v235\n"
"Mvav 108,28,220\nv_add_u32 v3,s47,v0\nv_add_f32 v235,v150,v235\nMvav 108,24,216\ns_waitcnt vmcnt(25) lgkmcnt(0)\ns_barrier\nv_add_f32 v235,v151,v235\n"
"Mvav 112,32,208\nRD 0,3,0\nMvav 112,36,212\nRD 4,3,1024\nMvav 116,36,220\nRD 8,3,2048\nMvav 116,32,216\nRD 12,3,3072\nMvav 120,40,208\nRD 16,3,4096\n"
"Mvav 120,44,212\nRD 20,3,5120\nMvav 124,44,220\nRD 24,3,6144\nMvav 124,40,216\nRD 28,3,7168\nMvav 128,48,208\nv_exp_f32 v152,v152\nMvav 128,52,212\n"
"v_exp_f32 v153,v153\nMvav 132,52,220\nv_exp_f32 v154,v154\nMvav 132,48,216\nv_exp_f32 v155,v155\nMvav 136,56,208\nv_exp_f32 v156,v156\nMvav 136,60,212\n"
"s_cmp_lg_u32 s64,0\nv_exp_f32 v157,v157\nMvav 140,60,220\nWL 0\nv_exp_f32 v158,v158\nMvav 140,56,216\ns_cbranch_scc1 fused_op_rs26\nfused_op_rsb27:\n"
"v_exp_f32 v159,v159\nfused_op_t2a:\nNvaa 176,0,128,224\nRD 32,3,8192\nCV 208,144,145\nMvaa 176,4,132\nRD 36,3,9216\nCV 209,146,147\nMvaa 176,8,136\n"
"RD 40,3,10240\nCV 216,148,149\nMvaa 176,12,140\nRD 44,3,11264\nCV 217,150,151\nMvaa 176,16,144\nRD 48,3,12288\nv_add_f32 v234,v152,v234\n"
"Mvaa 176,20,148\nRD 52,3,13312\nv_add_f32 v234,v153,v234\nMvaa 176,24,152\nRD 56,3,14336\nCV 210,152,153\nMvaa 176,28,156\ns_add_u32 m0,s48,s44\n"
"RD 60,3,15360\nv_add_f32 v234,v154,v234\nNvaa 180,28,188,228\nv_exp_f32 v160,v160\nMvaa 180,24,184\nDS 1,20,36,0\nMvaa 180,20,180\nv_exp_f32 v161,v161\n"
"Mvaa 180,16,176\nv_exp_f32 v162,v162\nMvaa 180,12,172\nDS 1,20,37,1024\nMvaa 180,8,168\nv_exp_f32 v163,v163\nMvaa 180,4,164\nv_exp_f32 v164,v164\n"
"Mvaa 180,0,160\nWL 0\nDS 1,20,38,2048\nNvaa 184,32,128,224\nRD 0,3,16384\nv_add_f32 v234,v155,v234\nMvaa 184,36,132\nRD 4,3,17408\nCV 211,154,155\n"
"Mvaa 184,40,136\nRD 8,3,18432\nv_add_f32 v235,v156,v235\nMvaa 184,44,140\nRD 12,3,19456\nv_add_f32 v235,v157,v235\nMvaa 184,48,144\nRD 16,3,20480\n"
"CV 218,156,157\nMvaa 184,52,148\nRD 20,3,21504\nv_add_f32 v235,v158,v235\nMvaa 184,56,152\nRD 24,3,22528\nv_add_f32 v235,v159,v235\nMvaa 184,60,156\n"
"RD 28,3,23552\nCV 219,158,159\nNvaa 188,60,188,228\nv_mov_b32 v15,0\nv_add_f32 v234,v160,v234\nMvaa 188,56,184\nDS 1,20,39,3072\nMvaa 188,52,180\n"
"s_add_u32 m0,m0,0x1000\nbuffer_load_dword v14,v15,s[24:27],s9 offen sc1\nMvaa 188,48,176\nv_exp_f32 v165,v165\nMvaa 188,44,172\nDS 1,20,40,0\n"
"Mvaa 188,40,168\nv_exp_f32 v166,v166\nMvaa 188,36,164\nv_exp_f32 v167,v167\nMvaa 188,32,160\nWL 0\nDS 1,20,41,1024\nNvaa 192,0,128,224\nRD 32,3,24576\n"
"v_add_f32 v234,v161,v234\nMvaa 192,4,132\ns_mov_b32 s44,s47\nRD 36,3,25600\nCV 212,160,161\nMvaa 192,8,136\ns_add_u32 s47,s47,0x8000\nRD 40,3,26624\n"
"v_add_f32 v234,v162,v234\nMvaa 192,12,140\ns_cmp_eq_u32 s47,0x28000\nRD 44,3,27648\nv_add_f32 v234,v163,v234\nMvaa 192,16,144\ns_cselect_b32 s47,0,s47\n"
"RD 48,3,28672\nCV 213,162,163\nMvaa 192,20,148\nRD 52,3,29696\nv_add_f32 v235,v164,v235\nMvaa 192,24,152\nRD 56,3,30720\nv_add_f32 v235,v165,v235\n"
"Mvaa 192,28,156\nRD 60,3,31744\nCV 220,164,165\nNvaa 196,28,188,228\nv_exp_f32 v168,v168\nMvaa 196,24,184\nDS 1,20,42,2048\nMvaa 196,20,180\n"
"v_exp_f32 v169,v169\nMvaa 196,16,176\nv_exp_f32 v170,v170\nMvaa 196,12,172\nDS 1,20,43,3072\nMvaa 196,8,168\nv_add_u32 v1,0x80,v1\n"
"v_add_f32 v235,v166,v235\nMvaa 196,4,164\nv_add_u32 v3,s47,v0\nv_add_f32 v235,v167,v235\nMvaa 196,0,160\ns_waitcnt vmcnt(26) lgkmcnt(0)\ns_barrier\n"
"CV 221,166,167\nNvaa 200,32,128,224\nRD 0,3,0\nv_add_f32 v234,v168,v234\nMvaa 200,36,132\nRD 4,3,1024\nv_add_f32 v234,v169,v234\nMvaa 200,40,136\n"
"RD 8,3,2048\nCV 214,168,169\nMvaa 200,44,140\nRD 12,3,3072\nv_add_f32 v234,v170,v234\nMvaa 200,48,144\nRD 16,3,4096\nMvaa 200,52,148\nRD 20,3,5120\n"
"Mvaa 200,56,152\nRD 24,3,6144\nMvaa 200,60,156\nRD 28,3,7168\nNvaa 204,60,188,228\nv_exp_f32 v171,v171\nMvaa 204,56,184\nv_exp_f32 v172,v172\n"
"Mvaa 204,52,180\nv_exp_f32 v173,v173\nMvaa 204,48,176\nv_exp_f32 v174,v174\nMvaa 204,44,172\nv_exp_f32 v175,v175\nMvaa 204,40,168\n"
"v_add_f32 v234,v171,v234\nCV 215,170,171\nMvaa 204,36,164\nWL 0\nMvaa 204,32,160\nv_add_f32 v235,v172,v235\nCV 222,172,173\nv_add_f32 v235,v173,v235\n"
"CV 223,174,175\nv_add_f32 v235,v174,v235\nv_add_f32 v235,v175,v235\ns_waitcnt vmcnt(4)\nv_cmp_ne_u32 vcc,s65,v14\ns_nop 0\n"
"s_cbranch_vccnz fused_op_sp28\nfused_op_spb29:\ns_add_u32 s45,s8,16384\nMvav 16,0,208\nRD 32,3,8192\nMvav 16,4,212\nRD 36,3,9216\n"
"v_max3_f32 v238,v176,v177,v178\nMvav 20,4,220\nRD 40,3,10240\nbuffer_load_dwordx4 v[248:251],v0,s[24:27],s45 offen sc1\nMvav 20,0,216\nRD 44,3,11264\n"
"v_max3_f32 v239,v179,v180,v181\nMvav 24,8,208\nRD 48,3,12288\nv_max3_f32 v238,v238,v182,v183\nMvav 24,12,212\nRD 52,3,13312\n"
"buffer_load_dwordx4 a[128:131],v0,s[24:27],s8 offen offset:0 sc1\nMvav 28,12,220\nRD 56,3,14336\nv_max3_f32 v239,v239,v184,v185\nMvav 28,8,216\n"
"s_add_u32 m0,s48,s44\nRD 60,3,15360\nv_max3_f32 v238,v238,v186,v187\nMvav 32,16,208\n"
"buffer_load_dwordx4 a[132:135],v0,s[24:27],s8 offen offset:1024 sc1\nMvav 32,20,212\nDS 1,20,36,0\nMvav 36,20,220\nv_max3_f32 v239,v239,v188,v189\n"
"v_max3_f32 v238,v238,v190,v191\nMvav 36,16,216\nbuffer_load_dwordx4 a[136:139],v0,s[24:27],s8 offen offset:2048 sc1\nMvav 40,24,208\nDS 1,20,37,1024\n"
"Mvav 40,28,212\nv_max3_f32 v239,v239,v192,v193\nv_max3_f32 v238,v238,v194,v195\nMvav 44,28,220\n"
"buffer_load_dwordx4 a[140:143],v0,s[24:27],s8 offen offset:3072 sc1\nMvav 44,24,216\nWL 0\nDS 1,20,38,2048\nMvav 48,32,208\ns_add_u32 s8,s8,4096\n"
"RD 0,3,16384\nv_max3_f32 v239,v239,v196,v197\nMvav 48,36,212\nRD 4,3,17408\nbuffer_load_dwordx4 a[144:147],v0,s[24:27],s8 offen offset:0 sc1\n"
"Mvav 52,36,220\nRD 8,3,18432\nv_max3_f32 v238,v238,v198,v199\nMvav 52,32,216\nRD 12,3,19456\nv_max3_f32 v239,v239,v200,v201\nMvav 56,40,208\n"
"RD 16,3,20480\nbuffer_load_dwordx4 a[148:151],v0,s[24:27],s8 offen offset:1024 sc1\nMvav 56,44,212\nRD 20,3,21504\nv_max3_f32 v238,v238,v202,v203\n"
"Mvav 60,44,220\nRD 24,3,22528\nv_max3_f32 v239,v239,v204,v205\nMvav 60,40,216\nRD 28,3,23552\nv_max3_f32 v238,v238,v206,v207\nMvav 64,48,208\n"
"buffer_load_dwordx4 a[152:155],v0,s[24:27],s8 offen offset:2048 sc1\nMvav 64,52,212\nDS 1,20,39,3072\nMvav 68,52,220\ns_add_u32 m0,m0,0x1000\n"
"buffer_load_dwordx4 a[156:159],v0,s[24:27],s8 offen offset:3072 sc1\nMvav 68,48,216\ns_add_u32 s8,s8,4096\nv_max_f32 v238,v238,v239\nMvav 72,56,208\n"
"DS 1,20,40,0\nMvav 72,60,212\nbuffer_load_dwordx4 a[160:163],v0,s[24:27],s8 offen offset:0 sc1\nMvav 76,60,220\nv_cmp_lt_f32 vcc,s67,v238\n"
"Mvav 76,56,216\nWL 0\nDS 1,20,41,1024\nMvav 80,0,208\ns_cbranch_vccnz fused_op_rm30\nfused_op_rmb31:\nRD 32,3,24576\n"
"buffer_load_dwordx4 a[164:167],v0,s[24:27],s8 offen offset:1024 sc1\nMvav 80,4,212\ns_mov_b32 s44,s47\nRD 36,3,25600\nMvav 84,4,220\n"
"s_add_u32 s47,s47,0x8000\nRD 40,3,26624\nMvav 84,0,216\ns_cmp_eq_u32 s47,0x28000\nRD 44,3,27648\n"
"buffer_load_dwordx4 a[168:171],v0,s[24:27],s8 offen offset:2048 sc1\nMvav 88,8,208\ns_cselect_b32 s47,0,s47\nRD 48,3,28672\nMvav 88,12,212\n"
"RD 52,3,29696\nMvav 92,12,220\nRD 56,3,30720\nMvav 92,8,216\nRD 60,3,31744\nMvav 96,16,208\n"
"buffer_load_dwordx4 a[172:175],v0,s[24:27],s8 offen offset:3072 sc1\nMvav 96,20,212\ns_add_u32 s8,s8,4096\nDS 1,20,42,2048\nMvav 100,20,220\n"
"buffer_load_dwordx4 a[176:179],v0,s[24:27],s8 offen offset:0 sc1\nMvav 100,16,216\nv_exp_f32 v176,v176\nMvav 104,24,208\nDS 1,20,43,3072\n"
"Mvav 104,28,212\nbuffer_load_dwordx4 a[180:183],v0,s[24:27],s8 offen offset:1024 sc1\nv_add_u32 v1,0x80,v1\nMvav 108,28,220\nv_add_u32 v3,s47,v0\n"
"v_add_f32 v234,v176,v234\nMvav 108,24,216\ns_waitcnt vmcnt(40) lgkmcnt(0)\ns_barrier\n"
"buffer_load_dwordx4 a[184:187],v0,s[24:27],s8 offen offset:2048 sc1\nMvav 112,32,208\nRD 0,3,0\nMvav 112,36,212\nRD 4,3,1024\nMvav 116,36,220\n"
"RD 8,3,2048\nbuffer_load_dwordx4 a[188:191],v0,s[24:27],s8 offen offset:3072 sc1\nMvav 116,32,216\ns_add_u32 s8,s8,4096\nRD 12,3,3072\nMvav 120,40,208\n"
"RD 16,3,4096\nMvav 120,44,212\nRD 20,3,5120\nMvav 124,44,220\nRD 24,3,6144\nMvav 124,40,216\nRD 28,3,7168\nMvav 128,48,208\nv_exp_f32 v177,v177\n"
"Mvav 128,52,212\nv_exp_f32 v178,v178\nMvav 132,52,220\nv_exp_f32 v179,v179\nMvav 132,48,216\nv_exp_f32 v180,v180\nMvav 136,56,208\nv_exp_f32 v181,v181\n"
"Mvav 136,60,212\ns_cmp_lg_u32 s64,0\nv_exp_f32 v182,v182\nMvav 140,60,220\nWL 0\nv_exp_f32 v183,v183\nMvav 140,56,216\ns_cbranch_scc1 fused_op_rs32\n"
"fused_op_rsb33:\nv_exp_f32 v184,v184\nv_exp_f32 v185,v185\nv_add_f32 v234,v177,v234\nCV 208,176,177\nv_exp_f32 v186,v186\nv_add_f32 v234,v178,v234\n"
"CV 209,178,179\nv_exp_f32 v187,v187\nv_add_f32 v234,v179,v234\nv_add_f32 v235,v180,v235\nv_exp_f32 v188,v188\nv_add_f32 v235,v181,v235\nCV 216,180,181\n"
"v_exp_f32 v189,v189\nv_add_f32 v235,v182,v235\nCV 217,182,183\nv_exp_f32 v190,v190\nv_add_f32 v235,v183,v235\nv_add_f32 v234,v184,v234\n"
"v_exp_f32 v191,v191\nv_add_f32 v234,v185,v234\nCV 210,184,185\nv_exp_f32 v192,v192\nv_add_f32 v234,v186,v234\nCV 211,186,187\nv_exp_f32 v193,v193\n"
"v_add_f32 v234,v187,v234\nv_add_f32 v235,v188,v235\nv_exp_f32 v194,v194\nv_add_f32 v235,v189,v235\nCV 218,188,189\nv_exp_f32 v195,v195\n"
"v_add_f32 v235,v190,v235\nCV 219,190,191\nv_exp_f32 v196,v196\nv_add_f32 v235,v191,v235\nv_add_f32 v234,v192,v234\nv_exp_f32 v197,v197\n"
"v_add_f32 v234,v193,v234\nCV 212,192,193\nv_exp_f32 v198,v198\nv_add_f32 v234,v194,v234\nCV 213,194,195\nv_exp_f32 v199,v199\nv_add_f32 v234,v195,v234\n"
"v_add_f32 v235,v196,v235\nv_exp_f32 v200,v200\nv_add_f32 v235,v197,v235\nCV 220,196,197\nv_exp_f32 v201,v201\nv_add_f32 v235,v198,v235\nCV 221,198,199\n"
"v_exp_f32 v202,v202\nv_add_f32 v235,v199,v235\nv_add_f32 v234,v200,v234\nv_exp_f32 v203,v203\nv_add_f32 v234,v201,v234\nCV 214,200,201\n"
"v_exp_f32 v204,v204\nv_add_f32 v234,v202,v234\nCV 215,202,203\nv_exp_f32 v205,v205\nv_add_f32 v234,v203,v234\nv_add_f32 v235,v204,v235\n"
"v_exp_f32 v206,v206\nv_add_f32 v235,v205,v235\nCV 222,204,205\nv_exp_f32 v207,v207\nv_add_f32 v235,v206,v235\nv_add_f32 v235,v207,v235\nCV 223,206,207\n"
"s_nop 1\nMvav 16,0,208\nRD 32,3,8192\nv_mov_b32 v238,v234\nMvav 16,4,212\nRD 36,3,9216\nv_mov_b32 v239,v235\nMvav 20,4,220\nRD 40,3,10240\n"
"v_permlane32_swap_b32 v234,v238\nMvav 20,0,216\nRD 44,3,11264\nv_permlane32_swap_b32 v235,v239\nMvav 24,8,208\nRD 48,3,12288\nv_add_f32 v234,v234,v238\n"
"Mvav 24,12,212\nRD 52,3,13312\nv_add_f32 v235,v235,v239\nMvav 28,12,220\nRD 56,3,14336\nv_mov_b32 v238,v234\nMvav 28,8,216\ns_add_u32 m0,s48,s44\n"
"RD 60,3,15360\nv_mov_b32 v239,v235\nMvav 32,16,208\nv_permlane16_swap_b32 v234,v238\nv_permlane16_swap_b32 v235,v239\nMvav 32,20,212\nDS 1,20,36,0\n"
"Mvav 36,20,220\nv_add_f32 v234,v234,v238\nv_add_f32 v235,v235,v239\nMvav 36,16,216\ns_waitcnt vmcnt(25)\nv_max_f32 v240,v232,v248\n"
"v_max_f32 v242,v233,v250\nMvav 40,24,208\nDS 1,20,37,1024\nMvav 40,28,212\nv_sub_f32 v241,v232,v240\nv_sub_f32 v243,v233,v242\nMvav 44,28,220\n"
"v_sub_f32 v240,v248,v240\nv_sub_f32 v242,v250,v242\nMvav 44,24,216\nWL 0\nDS 1,20,38,2048\nMvav 48,32,208\nRD 0,3,16384\ns_waitcnt vmcnt(26)\n"
"v_accvgpr_read_b32 v144,a128\nMvav 48,36,212\nRD 4,3,17408\nv_accvgpr_read_b32 v145,a129\nMvav 52,36,220\nRD 8,3,18432\nv_accvgpr_read_b32 v146,a130\n"
"Mvav 52,32,216\nRD 12,3,19456\nv_accvgpr_read_b32 v147,a131\nMvav 56,40,208\nRD 16,3,20480\ns_waitcnt vmcnt(25)\nv_accvgpr_read_b32 v148,a132\n"
"Mvav 56,44,212\nRD 20,3,21504\nv_accvgpr_read_b32 v149,a133\nMvav 60,44,220\nRD 24,3,22528\nv_accvgpr_read_b32 v150,a134\nMvav 60,40,216\n"
"RD 28,3,23552\nv_accvgpr_read_b32 v151,a135\nMvav 64,48,208\nv_exp_f32 v241,v241\nMvav 64,52,212\nDS 1,20,39,3072\nMvav 68,52,220\n"
"s_add_u32 m0,m0,0x1000\nv_exp_f32 v243,v243\nMvav 68,48,216\nv_exp_f32 v240,v240\nMvav 72,56,208\nDS 1,20,40,0\nMvav 72,60,212\nv_exp_f32 v242,v242\n"
"Mvav 76,60,220\nv_mul_f32 v240,v240,v249\nv_mul_f32 v242,v242,v251\nMvav 76,56,216\nWL 0\nDS 1,20,41,1024\nMvav 80,0,208\nRD 32,3,24576\n"
"v_lshrrev_b32 v12,4,v0\nMvav 80,4,212\ns_mov_b32 s44,s47\nRD 36,3,25600\nv_lshrrev_b32 v13,2,v12\nMvav 84,4,220\ns_add_u32 s47,s47,0x8000\n"
"RD 40,3,26624\nv_lshlrev_b32 v10,7,v13\nMvav 84,0,216\ns_cmp_eq_u32 s47,0x28000\nRD 44,3,27648\nv_bfe_u32 v13,v12,3,2\nMvav 88,8,208\n"
"s_cselect_b32 s47,0,s47\nRD 48,3,28672\nv_lshl_add_u32 v10,v13,5,v10\nMvav 88,12,212\nRD 52,3,29696\nv_and_b32 v12,3,v12\nMvav 92,12,220\n"
"RD 56,3,30720\nv_lshl_add_u32 v10,v12,3,v10\nMvav 92,8,216\nRD 60,3,31744\nv_fma_f32 v249,v234,v241,v240\nMvav 96,16,208\nv_rcp_f32 v249,v249\n"
"Mvav 96,20,212\nDS 1,20,42,2048\nMvav 100,20,220\nv_add_u32 v5,s47,v10\nv_xor_b32 v6,32,v5\nMvav 100,16,216\nv_xor_b32 v7,64,v5\nv_xor_b32 v8,96,v5\n"
"Mvav 104,24,208\nDS 1,20,43,3072\nMvav 104,28,212\nv_add_u32 v1,0x80,v1\nv_fma_f32 v251,v235,v243,v242\nMvav 108,28,220\nv_rcp_f32 v251,v251\n"
"Mvav 108,24,216\ns_waitcnt vmcnt(42) lgkmcnt(0)\ns_barrier\nv_mul_f32 v236,v241,v249\nMvav 112,32,208\nTR 0,5,0\nv_mul_f32 v237,v243,v251\n"
"Mvav 112,36,212\nTR 2,5,2048\nv_mul_f32 v248,v240,v249\nMvav 116,36,220\nTR 4,5,4096\nv_mul_f32 v250,v242,v251\nMvav 116,32,216\nTR 6,5,6144\n"
"s_waitcnt vmcnt(28)\nv_accvgpr_read_b32 v152,a136\nMvav 120,40,208\nTR 8,5,8192\nv_accvgpr_read_b32 v153,a137\nMvav 120,44,212\nTR 10,5,10240\n"
"v_accvgpr_read_b32 v154,a138\nMvav 124,44,220\nTR 12,5,12288\nv_accvgpr_read_b32 v155,a139\nMvav 124,40,216\nTR 14,5,14336\ns_waitcnt vmcnt(26)\n"
"v_accvgpr_read_b32 v156,a140\nMvav 128,48,208\nTR 16,5,16384\nv_accvgpr_read_b32 v157,a141\nMvav 128,52,212\nTR 18,5,18432\n"
"v_accvgpr_read_b32 v158,a142\nMvav 132,52,220\nTR 20,5,20480\nv_accvgpr_read_b32 v159,a143\nMvav 132,48,216\nTR 22,5,22528\ns_waitcnt vmcnt(24)\n"
"v_accvgpr_read_b32 v160,a144\nMvav 136,56,208\nTR 24,5,24576\nv_accvgpr_read_b32 v161,a145\nMvav 136,60,212\nTR 26,5,26624\n"
"v_accvgpr_read_b32 v162,a146\nMvav 140,60,220\nTR 28,5,28672\nv_accvgpr_read_b32 v163,a147\nMvav 140,56,216\nTR 30,5,30720\ns_waitcnt vmcnt(23)\n"
"v_accvgpr_read_b32 v164,a148\nv_accvgpr_read_b32 v165,a149\nv_accvgpr_read_b32 v166,a150\nv_accvgpr_read_b32 v167,a151\ns_waitcnt vmcnt(22)\n"
"v_accvgpr_read_b32 v168,a152\nv_accvgpr_read_b32 v169,a153\nv_accvgpr_read_b32 v170,a154\nv_accvgpr_read_b32 v171,a155\ns_waitcnt vmcnt(20)\n"
"v_accvgpr_read_b32 v172,a156\nv_accvgpr_read_b32 v173,a157\nv_accvgpr_read_b32 v174,a158\nv_accvgpr_read_b32 v175,a159\ns_waitcnt vmcnt(18)\n"
"v_accvgpr_read_b32 v176,a160\nv_accvgpr_read_b32 v177,a161\nv_accvgpr_read_b32 v178,a162\nv_accvgpr_read_b32 v179,a163\ns_waitcnt vmcnt(16)\n"
"v_accvgpr_read_b32 v180,a164\nv_accvgpr_read_b32 v181,a165\nv_accvgpr_read_b32 v182,a166\nv_accvgpr_read_b32 v183,a167\ns_waitcnt vmcnt(15)\n"
"v_accvgpr_read_b32 v184,a168\nv_accvgpr_read_b32 v185,a169\nv_accvgpr_read_b32 v186,a170\nv_accvgpr_read_b32 v187,a171\ns_waitcnt vmcnt(14)\n"
"v_accvgpr_read_b32 v188,a172\nv_accvgpr_read_b32 v189,a173\nv_accvgpr_read_b32 v190,a174\nv_accvgpr_read_b32 v191,a175\ns_waitcnt vmcnt(12)\n"
"v_accvgpr_read_b32 v192,a176\nv_accvgpr_read_b32 v193,a177\nv_accvgpr_read_b32 v194,a178\nv_accvgpr_read_b32 v195,a179\ns_waitcnt vmcnt(10)\n"
"v_accvgpr_read_b32 v196,a180\nv_accvgpr_read_b32 v197,a181\nv_accvgpr_read_b32 v198,a182\nv_accvgpr_read_b32 v199,a183\ns_waitcnt vmcnt(9)\n"
"v_accvgpr_read_b32 v200,a184\nv_accvgpr_read_b32 v201,a185\nv_accvgpr_read_b32 v202,a186\nv_accvgpr_read_b32 v203,a187\ns_waitcnt vmcnt(8)\n"
"v_accvgpr_read_b32 v204,a188\nv_accvgpr_read_b32 v205,a189\nv_accvgpr_read_b32 v206,a190\nv_accvgpr_read_b32 v207,a191\ns_nop 7\n"
"v_pk_mul_f32 v[16:17],v[16:17],v[236:237] op_sel:[0,0] op_sel_hi:[1,0]\nv_pk_mul_f32 v[20:21],v[20:21],v[236:237] op_sel:[0,1] op_sel_hi:[1,1]\n"
"v_pk_mul_f32 v[18:19],v[18:19],v[236:237] op_sel:[0,0] op_sel_hi:[1,0]\nv_pk_mul_f32 v[22:23],v[22:23],v[236:237] op_sel:[0,1] op_sel_hi:[1,1]\n"
"v_pk_mul_f32 v[24:25],v[24:25],v[236:237] op_sel:[0,0] op_sel_hi:[1,0]\nv_pk_mul_f32 v[28:29],v[28:29],v[236:237] op_sel:[0,1] op_sel_hi:[1,1]\n"
"v_pk_mul_f32 v[26:27],v[26:27],v[236:237] op_sel:[0,0] op_sel_hi:[1,0]\nv_pk_mul_f32 v[30:31],v[30:31],v[236:237] op_sel:[0,1] op_sel_hi:[1,1]\n"
"v_lshlrev_b32 v208,16,v144\nv_lshlrev_b32 v216,16,v176\nv_and_b32 v209,0xffff0000,v144\nv_and_b32 v217,0xffff0000,v176\nv_lshlrev_b32 v210,16,v145\n"
"v_lshlrev_b32 v218,16,v177\nv_and_b32 v211,0xffff0000,v145\nv_and_b32 v219,0xffff0000,v177\nv_lshlrev_b32 v212,16,v146\nv_lshlrev_b32 v220,16,v178\n"
"v_and_b32 v213,0xffff0000,v146\nv_and_b32 v221,0xffff0000,v178\nv_lshlrev_b32 v214,16,v147\nv_lshlrev_b32 v222,16,v179\nv_and_b32 v215,0xffff0000,v147\n"
"v_and_b32 v223,0xffff0000,v179\nv_pk_fma_f32 v[16:17],v[208:209],v[248:249],v[16:17] op_sel:[0,0,0] op_sel_hi:[1,0,1]\n"
"v_pk_fma_f32 v[20:21],v[216:217],v[250:251],v[20:21] op_sel:[0,0,0] op_sel_hi:[1,0,1]\n"
"v_pk_fma_f32 v[18:19],v[210:211],v[248:249],v[18:19] op_sel:[0,0,0] op_sel_hi:[1,0,1]\n"
"v_pk_fma_f32 v[22:23],v[218:219],v[250:251],v[22:23] op_sel:[0,0,0] op_sel_hi:[1,0,1]\n"
"v_pk_fma_f32 v[24:25],v[212:213],v[248:249],v[24:25] op_sel:[0,0,0] op_sel_hi:[1,0,1]\n"
"v_pk_fma_f32 v[28:29],v[220:221],v[250:251],v[28:29] op_sel:[0,0,0] op_sel_hi:[1,0,1]\n"
"v_pk_fma_f32 v[26:27],v[214:215],v[248:249],v[26:27] op_sel:[0,0,0] op_sel_hi:[1,0,1]\n"
"v_pk_fma_f32 v[30:31],v[222:223],v[250:251],v[30:31] op_sel:[0,0,0] op_sel_hi:[1,0,1]\nCV 16,16,17\nCV 20,20,21\nCV 17,18,19\nCV 21,22,23\nCV 18,24,25\n"
"CV 22,28,29\nCV 19,26,27\nCV 23,30,31\nv_pk_mul_f32 v[32:33],v[32:33],v[236:237] op_sel:[0,0] op_sel_hi:[1,0]\n"
"v_pk_mul_f32 v[36:37],v[36:37],v[236:237] op_sel:[0,1] op_sel_hi:[1,1]\nv_pk_mul_f32 v[34:35],v[34:35],v[236:237] op_sel:[0,0] op_sel_hi:[1,0]\n"
"v_pk_mul_f32 v[38:39],v[38:39],v[236:237] op_sel:[0,1] op_sel_hi:[1,1]\nv_pk_mul_f32 v[40:41],v[40:41],v[236:237] op_sel:[0,0] op_sel_hi:[1,0]\n"
"v_pk_mul_f32 v[44:45],v[44:45],v[236:237] op_sel:[0,1] op_sel_hi:[1,1]\nv_pk_mul_f32 v[42:43],v[42:43],v[236:237] op_sel:[0,0] op_sel_hi:[1,0]\n"
"v_pk_mul_f32 v[46:47],v[46:47],v[236:237] op_sel:[0,1] op_sel_hi:[1,1]\nv_lshlrev_b32 v208,16,v148\nv_lshlrev_b32 v216,16,v180\n"
"v_and_b32 v209,0xffff0000,v148\nv_and_b32 v217,0xffff0000,v180\nv_lshlrev_b32 v210,16,v149\nv_lshlrev_b32 v218,16,v181\nv_and_b32 v211,0xffff0000,v149\n"
"v_and_b32 v219,0xffff0000,v181\nv_lshlrev_b32 v212,16,v150\nv_lshlrev_b32 v220,16,v182\nv_and_b32 v213,0xffff0000,v150\nv_and_b32 v221,0xffff0000,v182\n"
"v_lshlrev_b32 v214,16,v151\nv_lshlrev_b32 v222,16,v183\nv_and_b32 v215,0xffff0000,v151\nv_and_b32 v223,0xffff0000,v183\n"
"v_pk_fma_f32 v[32:33],v[208:209],v[248:249],v[32:33] op_sel:[0,0,0] op_sel_hi:[1,0,1]\n"
"v_pk_fma_f32 v[36:37],v[216:217],v[250:251],v[36:37] op_sel:[0,0,0] op_sel_hi:[1,0,1]\n"
"v_pk_fma_f32 v[34:35],v[210:211],v[248:249],v[34:35] op_sel:[0,0,0] op_sel_hi:[1,0,1]\n"
"v_pk_fma_f32 v[38:39],v[218:219],v[250:251],v[38:39] op_sel:[0,0,0] op_sel_hi:[1,0,1]\n"
"v_pk_fma_f32 v[40:41],v[212:213],v[248:249],v[40:41] op_sel:[0,0,0] op_sel_hi:[1,0,1]\n"
"v_pk_fma_f32 v[44:45],v[220:221],v[250:251],v[44:45] op_sel:[0,0,0] op_sel_hi:[1,0,1]\n"
"v_pk_fma_f32 v[42:43],v[214:215],v[248:249],v[42:43] op_sel:[0,0,0] op_sel_hi:[1,0,1]\n"
"v_pk_fma_f32 v[46:47],v[222:223],v[250:251],v[46:47] op_sel:[0,0,0] op_sel_hi:[1,0,1]\nCV 32,32,33\nCV 36,36,37\nCV 33,34,35\nCV 37,38,39\nCV 34,40,41\n"
"CV 38,44,45\nCV 35,42,43\nCV 39,46,47\nv_pk_mul_f32 v[48:49],v[48:49],v[236:237] op_sel:[0,0] op_sel_hi:[1,0]\n"
"v_pk_mul_f32 v[52:53],v[52:53],v[236:237] op_sel:[0,1] op_sel_hi:[1,1]\nv_pk_mul_f32 v[50:51],v[50:51],v[236:237] op_sel:[0,0] op_sel_hi:[1,0]\n"
"v_pk_mul_f32 v[54:55],v[54:55],v[236:237] op_sel:[0,1] op_sel_hi:[1,1]\nv_pk_mul_f32 v[56:57],v[56:57],v[236:237] op_sel:[0,0] op_sel_hi:[1,0]\n"
"v_pk_mul_f32 v[60:61],v[60:61],v[236:237] op_sel:[0,1] op_sel_hi:[1,1]\nv_pk_mul_f32 v[58:59],v[58:59],v[236:237] op_sel:[0,0] op_sel_hi:[1,0]\n"
"v_pk_mul_f32 v[62:63],v[62:63],v[236:237] op_sel:[0,1] op_sel_hi:[1,1]\nv_lshlrev_b32 v208,16,v152\nv_lshlrev_b32 v216,16,v184\n"
"v_and_b32 v209,0xffff0000,v152\nv_and_b32 v217,0xffff0000,v184\nv_lshlrev_b32 v210,16,v153\nv_lshlrev_b32 v218,16,v185\nv_and_b32 v211,0xffff0000,v153\n"
"v_and_b32 v219,0xffff0000,v185\nv_lshlrev_b32 v212,16,v154\nv_lshlrev_b32 v220,16,v186\nv_and_b32 v213,0xffff0000,v154\nv_and_b32 v221,0xffff0000,v186\n"
"v_lshlrev_b32 v214,16,v155\nv_lshlrev_b32 v222,16,v187\nv_and_b32 v215,0xffff0000,v155\nv_and_b32 v223,0xffff0000,v187\n"
"v_pk_fma_f32 v[48:49],v[208:209],v[248:249],v[48:49] op_sel:[0,0,0] op_sel_hi:[1,0,1]\n"
"v_pk_fma_f32 v[52:53],v[216:217],v[250:251],v[52:53] op_sel:[0,0,0] op_sel_hi:[1,0,1]\n"
"v_pk_fma_f32 v[50:51],v[210:211],v[248:249],v[50:51] op_sel:[0,0,0] op_sel_hi:[1,0,1]\n"
"v_pk_fma_f32 v[54:55],v[218:219],v[250:251],v[54:55] op_sel:[0,0,0] op_sel_hi:[1,0,1]\n"
"v_pk_fma_f32 v[56:57],v[212:213],v[248:249],v[56:57] op_sel:[0,0,0] op_sel_hi:[1,0,1]\n"
"v_pk_fma_f32 v[60:61],v[220:221],v[250:251],v[60:61] op_sel:[0,0,0] op_sel_hi:[1,0,1]\n"
"v_pk_fma_f32 v[58:59],v[214:215],v[248:249],v[58:59] op_sel:[0,0,0] op_sel_hi:[1,0,1]\n"
"v_pk_fma_f32 v[62:63],v[222:223],v[250:251],v[62:63] op_sel:[0,0,0] op_sel_hi:[1,0,1]\nCV 48,48,49\nCV 52,52,53\nCV 49,50,51\nCV 53,54,55\nCV 50,56,57\n"
"CV 54,60,61\nCV 51,58,59\nCV 55,62,63\nv_pk_mul_f32 v[64:65],v[64:65],v[236:237] op_sel:[0,0] op_sel_hi:[1,0]\n"
"v_pk_mul_f32 v[68:69],v[68:69],v[236:237] op_sel:[0,1] op_sel_hi:[1,1]\nv_pk_mul_f32 v[66:67],v[66:67],v[236:237] op_sel:[0,0] op_sel_hi:[1,0]\n"
"v_pk_mul_f32 v[70:71],v[70:71],v[236:237] op_sel:[0,1] op_sel_hi:[1,1]\nv_pk_mul_f32 v[72:73],v[72:73],v[236:237] op_sel:[0,0] op_sel_hi:[1,0]\n"
"v_pk_mul_f32 v[76:77],v[76:77],v[236:237] op_sel:[0,1] op_sel_hi:[1,1]\nv_pk_mul_f32 v[74:75],v[74:75],v[236:237] op_sel:[0,0] op_sel_hi:[1,0]\n"
"v_pk_mul_f32 v[78:79],v[78:79],v[236:237] op_sel:[0,1] op_sel_hi:[1,1]\nv_lshlrev_b32 v208,16,v156\nv_lshlrev_b32 v216,16,v188\n"
"v_and_b32 v209,0xffff0000,v156\nv_and_b32 v217,0xffff0000,v188\nv_lshlrev_b32 v210,16,v157\nv_lshlrev_b32 v218,16,v189\nv_and_b32 v211,0xffff0000,v157\n"
"v_and_b32 v219,0xffff0000,v189\nv_lshlrev_b32 v212,16,v158\nv_lshlrev_b32 v220,16,v190\nv_and_b32 v213,0xffff0000,v158\nv_and_b32 v221,0xffff0000,v190\n"
"v_lshlrev_b32 v214,16,v159\nv_lshlrev_b32 v222,16,v191\nv_and_b32 v215,0xffff0000,v159\nv_and_b32 v223,0xffff0000,v191\n"
"v_pk_fma_f32 v[64:65],v[208:209],v[248:249],v[64:65] op_sel:[0,0,0] op_sel_hi:[1,0,1]\n"
"v_pk_fma_f32 v[68:69],v[216:217],v[250:251],v[68:69] op_sel:[0,0,0] op_sel_hi:[1,0,1]\n"
"v_pk_fma_f32 v[66:67],v[210:211],v[248:249],v[66:67] op_sel:[0,0,0] op_sel_hi:[1,0,1]\n"
"v_pk_fma_f32 v[70:71],v[218:219],v[250:251],v[70:71] op_sel:[0,0,0] op_sel_hi:[1,0,1]\n"
"v_pk_fma_f32 v[72:73],v[212:213],v[248:249],v[72:73] op_sel:[0,0,0] op_sel_hi:[1,0,1]\n"
"v_pk_fma_f32 v[76:77],v[220:221],v[250:251],v[76:77] op_sel:[0,0,0] op_sel_hi:[1,0,1]\n"
"v_pk_fma_f32 v[74:75],v[214:215],v[248:249],v[74:75] op_sel:[0,0,0] op_sel_hi:[1,0,1]\n"
"v_pk_fma_f32 v[78:79],v[222:223],v[250:251],v[78:79] op_sel:[0,0,0] op_sel_hi:[1,0,1]\nCV 64,64,65\nCV 68,68,69\nCV 65,66,67\nCV 69,70,71\nCV 66,72,73\n"
"CV 70,76,77\nCV 67,74,75\nCV 71,78,79\nv_pk_mul_f32 v[80:81],v[80:81],v[236:237] op_sel:[0,0] op_sel_hi:[1,0]\n"
"v_pk_mul_f32 v[84:85],v[84:85],v[236:237] op_sel:[0,1] op_sel_hi:[1,1]\nv_pk_mul_f32 v[82:83],v[82:83],v[236:237] op_sel:[0,0] op_sel_hi:[1,0]\n"
"v_pk_mul_f32 v[86:87],v[86:87],v[236:237] op_sel:[0,1] op_sel_hi:[1,1]\nv_pk_mul_f32 v[88:89],v[88:89],v[236:237] op_sel:[0,0] op_sel_hi:[1,0]\n"
"v_pk_mul_f32 v[92:93],v[92:93],v[236:237] op_sel:[0,1] op_sel_hi:[1,1]\nv_pk_mul_f32 v[90:91],v[90:91],v[236:237] op_sel:[0,0] op_sel_hi:[1,0]\n"
"v_pk_mul_f32 v[94:95],v[94:95],v[236:237] op_sel:[0,1] op_sel_hi:[1,1]\nv_lshlrev_b32 v208,16,v160\nv_lshlrev_b32 v216,16,v192\n"
"v_and_b32 v209,0xffff0000,v160\nv_and_b32 v217,0xffff0000,v192\nv_lshlrev_b32 v210,16,v161\nv_lshlrev_b32 v218,16,v193\nv_and_b32 v211,0xffff0000,v161\n"
"v_and_b32 v219,0xffff0000,v193\nv_lshlrev_b32 v212,16,v162\nv_lshlrev_b32 v220,16,v194\nv_and_b32 v213,0xffff0000,v162\nv_and_b32 v221,0xffff0000,v194\n"
"v_lshlrev_b32 v214,16,v163\nv_lshlrev_b32 v222,16,v195\nv_and_b32 v215,0xffff0000,v163\nv_and_b32 v223,0xffff0000,v195\n"
"v_pk_fma_f32 v[80:81],v[208:209],v[248:249],v[80:81] op_sel:[0,0,0] op_sel_hi:[1,0,1]\n"
"v_pk_fma_f32 v[84:85],v[216:217],v[250:251],v[84:85] op_sel:[0,0,0] op_sel_hi:[1,0,1]\n"
"v_pk_fma_f32 v[82:83],v[210:211],v[248:249],v[82:83] op_sel:[0,0,0] op_sel_hi:[1,0,1]\n"
"v_pk_fma_f32 v[86:87],v[218:219],v[250:251],v[86:87] op_sel:[0,0,0] op_sel_hi:[1,0,1]\n"
"v_pk_fma_f32 v[88:89],v[212:213],v[248:249],v[88:89] op_sel:[0,0,0] op_sel_hi:[1,0,1]\n"
"v_pk_fma_f32 v[92:93],v[220:221],v[250:251],v[92:93] op_sel:[0,0,0] op_sel_hi:[1,0,1]\n"
"v_pk_fma_f32 v[90:91],v[214:215],v[248:249],v[90:91] op_sel:[0,0,0] op_sel_hi:[1,0,1]\n"
"v_pk_fma_f32 v[94:95],v[222:223],v[250:251],v[94:95] op_sel:[0,0,0] op_sel_hi:[1,0,1]\nCV 80,80,81\nCV 84,84,85\nCV 81,82,83\nCV 85,86,87\nCV 82,88,89\n"
"CV 86,92,93\nCV 83,90,91\nCV 87,94,95\nv_pk_mul_f32 v[96:97],v[96:97],v[236:237] op_sel:[0,0] op_sel_hi:[1,0]\n"
"v_pk_mul_f32 v[100:101],v[100:101],v[236:237] op_sel:[0,1] op_sel_hi:[1,1]\nv_pk_mul_f32 v[98:99],v[98:99],v[236:237] op_sel:[0,0] op_sel_hi:[1,0]\n"
"v_pk_mul_f32 v[102:103],v[102:103],v[236:237] op_sel:[0,1] op_sel_hi:[1,1]\nv_pk_mul_f32 v[104:105],v[104:105],v[236:237] op_sel:[0,0] op_sel_hi:[1,0]\n"
"v_pk_mul_f32 v[108:109],v[108:109],v[236:237] op_sel:[0,1] op_sel_hi:[1,1]\nv_pk_mul_f32 v[106:107],v[106:107],v[236:237] op_sel:[0,0] op_sel_hi:[1,0]\n"
"v_pk_mul_f32 v[110:111],v[110:111],v[236:237] op_sel:[0,1] op_sel_hi:[1,1]\nv_lshlrev_b32 v208,16,v164\nv_lshlrev_b32 v216,16,v196\n"
"v_and_b32 v209,0xffff0000,v164\nv_and_b32 v217,0xffff0000,v196\nv_lshlrev_b32 v210,16,v165\nv_lshlrev_b32 v218,16,v197\nv_and_b32 v211,0xffff0000,v165\n"
"v_and_b32 v219,0xffff0000,v197\nv_lshlrev_b32 v212,16,v166\nv_lshlrev_b32 v220,16,v198\nv_and_b32 v213,0xffff0000,v166\nv_and_b32 v221,0xffff0000,v198\n"
"v_lshlrev_b32 v214,16,v167\nv_lshlrev_b32 v222,16,v199\nv_and_b32 v215,0xffff0000,v167\nv_and_b32 v223,0xffff0000,v199\n"
"v_pk_fma_f32 v[96:97],v[208:209],v[248:249],v[96:97] op_sel:[0,0,0] op_sel_hi:[1,0,1]\n"
"v_pk_fma_f32 v[100:101],v[216:217],v[250:251],v[100:101] op_sel:[0,0,0] op_sel_hi:[1,0,1]\n"
"v_pk_fma_f32 v[98:99],v[210:211],v[248:249],v[98:99] op_sel:[0,0,0] op_sel_hi:[1,0,1]\n"
"v_pk_fma_f32 v[102:103],v[218:219],v[250:251],v[102:103] op_sel:[0,0,0] op_sel_hi:[1,0,1]\n"
"v_pk_fma_f32 v[104:105],v[212:213],v[248:249],v[104:105] op_sel:[0,0,0] op_sel_hi:[1,0,1]\n"
"v_pk_fma_f32 v[108:109],v[220:221],v[250:251],v[108:109] op_sel:[0,0,0] op_sel_hi:[1,0,1]\n"
"v_pk_fma_f32 v[106:107],v[214:215],v[248:249],v[106:107] op_sel:[0,0,0] op_sel_hi:[1,0,1]\n"
"v_pk_fma_f32 v[110:111],v[222:223],v[250:251],v[110:111] op_sel:[0,0,0] op_sel_hi:[1,0,1]\nCV 96,96,97\nCV 100,100,101\nCV 97,98,99\nCV 101,102,103\n"
"CV 98,104,105\nCV 102,108,109\nCV 99,106,107\nCV 103,110,111\nv_pk_mul_f32 v[112:113],v[112:113],v[236:237] op_sel:[0,0] op_sel_hi:[1,0]\n"
"v_pk_mul_f32 v[116:117],v[116:117],v[236:237] op_sel:[0,1] op_sel_hi:[1,1]\nv_pk_mul_f32 v[114:115],v[114:115],v[236:237] op_sel:[0,0] op_sel_hi:[1,0]\n"
"v_pk_mul_f32 v[118:119],v[118:119],v[236:237] op_sel:[0,1] op_sel_hi:[1,1]\nv_pk_mul_f32 v[120:121],v[120:121],v[236:237] op_sel:[0,0] op_sel_hi:[1,0]\n"
"v_pk_mul_f32 v[124:125],v[124:125],v[236:237] op_sel:[0,1] op_sel_hi:[1,1]\nv_pk_mul_f32 v[122:123],v[122:123],v[236:237] op_sel:[0,0] op_sel_hi:[1,0]\n"
"v_pk_mul_f32 v[126:127],v[126:127],v[236:237] op_sel:[0,1] op_sel_hi:[1,1]\nv_lshlrev_b32 v208,16,v168\nv_lshlrev_b32 v216,16,v200\n"
"v_and_b32 v209,0xffff0000,v168\nv_and_b32 v217,0xffff0000,v200\nv_lshlrev_b32 v210,16,v169\nv_lshlrev_b32 v218,16,v201\nv_and_b32 v211,0xffff0000,v169\n"
"v_and_b32 v219,0xffff0000,v201\nv_lshlrev_b32 v212,16,v170\nv_lshlrev_b32 v220,16,v202\nv_and_b32 v213,0xffff0000,v170\nv_and_b32 v221,0xffff0000,v202\n"
"v_lshlrev_b32 v214,16,v171\nv_lshlrev_b32 v222,16,v203\nv_and_b32 v215,0xffff0000,v171\nv_and_b32 v223,0xffff0000,v203\n"
"v_pk_fma_f32 v[112:113],v[208:209],v[248:249],v[112:113] op_sel:[0,0,0] op_sel_hi:[1,0,1]\n"
"v_pk_fma_f32 v[116:117],v[216:217],v[250:251],v[116:117] op_sel:[0,0,0] op_sel_hi:[1,0,1]\n"
"v_pk_fma_f32 v[114:115],v[210:211],v[248:249],v[114:115] op_sel:[0,0,0] op_sel_hi:[1,0,1]\n"
"v_pk_fma_f32 v[118:119],v[218:219],v[250:251],v[118:119] op_sel:[0,0,0] op_sel_hi:[1,0,1]\n"
"v_pk_fma_f32 v[120:121],v[212:213],v[248:249],v[120:121] op_sel:[0,0,0] op_sel_hi:[1,0,1]\n"
"v_pk_fma_f32 v[124:125],v[220:221],v[250:251],v[124:125] op_sel:[0,0,0] op_sel_hi:[1,0,1]\n"
"v_pk_fma_f32 v[122:123],v[214:215],v[248:249],v[122:123] op_sel:[0,0,0] op_sel_hi:[1,0,1]\n"
"v_pk_fma_f32 v[126:127],v[222:223],v[250:251],v[126:127] op_sel:[0,0,0] op_sel_hi:[1,0,1]\nCV 112,112,113\nCV 116,116,117\nCV 113,114,115\n"
"CV 117,118,119\nCV 114,120,121\nCV 118,124,125\nCV 115,122,123\nCV 119,126,127\n"
"v_pk_mul_f32 v[128:129],v[128:129],v[236:237] op_sel:[0,0] op_sel_hi:[1,0]\nv_pk_mul_f32 v[132:133],v[132:133],v[236:237] op_sel:[0,1] op_sel_hi:[1,1]\n"
"v_pk_mul_f32 v[130:131],v[130:131],v[236:237] op_sel:[0,0] op_sel_hi:[1,0]\nv_pk_mul_f32 v[134:135],v[134:135],v[236:237] op_sel:[0,1] op_sel_hi:[1,1]\n"
"v_pk_mul_f32 v[136:137],v[136:137],v[236:237] op_sel:[0,0] op_sel_hi:[1,0]\nv_pk_mul_f32 v[140:141],v[140:141],v[236:237] op_sel:[0,1] op_sel_hi:[1,1]\n"
"v_pk_mul_f32 v[138:139],v[138:139],v[236:237] op_sel:[0,0] op_sel_hi:[1,0]\nv_pk_mul_f32 v[142:143],v[142:143],v[236:237] op_sel:[0,1] op_sel_hi:[1,1]\n"
"v_lshlrev_b32 v208,16,v172\nv_lshlrev_b32 v216,16,v204\nv_and_b32 v209,0xffff0000,v172\nv_and_b32 v217,0xffff0000,v204\nv_lshlrev_b32 v210,16,v173\n"
"v_lshlrev_b32 v218,16,v205\nv_and_b32 v211,0xffff0000,v173\nv_and_b32 v219,0xffff0000,v205\nv_lshlrev_b32 v212,16,v174\nv_lshlrev_b32 v220,16,v206\n"
"v_and_b32 v213,0xffff0000,v174\nv_and_b32 v221,0xffff0000,v206\nv_lshlrev_b32 v214,16,v175\nv_lshlrev_b32 v222,16,v207\nv_and_b32 v215,0xffff0000,v175\n"
"v_and_b32 v223,0xffff0000,v207\nv_pk_fma_f32 v[128:129],v[208:209],v[248:249],v[128:129] op_sel:[0,0,0] op_sel_hi:[1,0,1]\n"
"v_pk_fma_f32 v[132:133],v[216:217],v[250:251],v[132:133] op_sel:[0,0,0] op_sel_hi:[1,0,1]\n"
"v_pk_fma_f32 v[130:131],v[210:211],v[248:249],v[130:131] op_sel:[0,0,0] op_sel_hi:[1,0,1]\n"
"v_pk_fma_f32 v[134:135],v[218:219],v[250:251],v[134:135] op_sel:[0,0,0] op_sel_hi:[1,0,1]\n"
"v_pk_fma_f32 v[136:137],v[212:213],v[248:249],v[136:137] op_sel:[0,0,0] op_sel_hi:[1,0,1]\n"
"v_pk_fma_f32 v[140:141],v[220:221],v[250:251],v[140:141] op_sel:[0,0,0] op_sel_hi:[1,0,1]\n"
"v_pk_fma_f32 v[138:139],v[214:215],v[248:249],v[138:139] op_sel:[0,0,0] op_sel_hi:[1,0,1]\n"
"v_pk_fma_f32 v[142:143],v[222:223],v[250:251],v[142:143] op_sel:[0,0,0] op_sel_hi:[1,0,1]\nCV 128,128,129\nCV 132,132,133\nCV 129,130,131\n"
"CV 133,134,135\nCV 130,136,137\nCV 134,140,141\nCV 131,138,139\nCV 135,142,143\nfused_op_opa:\ns_mul_i32 s70,s68,131072\ns_mul_i32 s49,s10,32768\n"
"s_add_u32 s70,s70,s49\ns_mul_i32 s49,s58,-16777216\ns_add_u32 s70,s70,s49\ns_add_u32 s70,s70,0x3100000\ns_add_u32 s71,s70,14336\ns_mul_i32 s9,s59,8192\n"
"s_mul_i32 s49,s56,512\ns_add_u32 s9,s9,s49\ns_mul_i32 s49,s10,128\ns_add_u32 s9,s9,s49\ns_add_u32 s9,s9,0x50000\ns_add_u32 s63,s9,131072\nWL 0\n"
"Zvav 144,0,16\nTR 32,6,0\nTR 34,6,2048\nMvav 144,4,32\nTR 36,6,4096\nTR 38,6,6144\nMvav 144,8,48\nTR 40,6,8192\nTR 42,6,10240\nMvav 144,12,64\n"
"TR 44,6,12288\nMvav 144,16,80\nTR 46,6,14336\nMvav 144,20,96\nTR 48,6,16384\nMvav 144,24,112\nTR 50,6,18432\nMvav 144,28,128\ns_add_u32 m0,s48,s44\n"
"TR 52,6,20480\nZvav 148,28,132\nTR 54,6,22528\nMvav 148,24,116\nDP 1,20,36,0\nMvav 148,20,100\nTR 56,6,24576\nMvav 148,16,84\nTR 58,6,26624\n"
"Mvav 148,12,68\nDP 1,20,37,1024\nMvav 148,8,52\nTR 60,6,28672\nMvav 148,4,36\nTR 62,6,30720\nMvav 148,0,20\nWL 14\nDP 1,20,38,2048\nZvav 152,32,16\n"
"WL 12\nTR 0,7,0\nTR 2,7,2048\nMvav 152,36,32\nWL 12\nTR 4,7,4096\nTR 6,7,6144\nMvav 152,40,48\nWL 12\nTR 8,7,8192\nTR 10,7,10240\nMvav 152,44,64\n"
"WL 12\nTR 12,7,12288\nMvav 152,48,80\nWL 11\nTR 14,7,14336\nMvav 152,52,96\nWL 10\nTR 16,7,16384\nMvav 152,56,112\nWL 9\nTR 18,7,18432\n"
"Mvav 152,60,128\nTR 20,7,20480\nZvav 156,60,132\nTR 22,7,22528\nMvav 156,56,116\nDP 1,20,39,3072\nMvav 156,52,100\ns_add_u32 m0,m0,0x1000\n"
"TR 24,7,24576\nMvav 156,48,84\nTR 26,7,26624\nMvav 156,44,68\nDP 1,20,40,0\nMvav 156,40,52\nTR 28,7,28672\nMvav 156,36,36\nTR 30,7,30720\n"
"Mvav 156,32,20\nWL 14\nDP 1,20,41,1024\nZvav 160,0,16\nWL 12\nTR 32,8,0\nTR 34,8,2048\nMvav 160,4,32\nWL 12\ns_mov_b32 s44,s47\nTR 36,8,4096\n"
"TR 38,8,6144\nMvav 160,8,48\nWL 12\ns_add_u32 s47,s47,0x8000\nTR 40,8,8192\nTR 42,8,10240\nMvav 160,12,64\nWL 12\ns_cmp_eq_u32 s47,0x28000\n"
"TR 44,8,12288\nTR 46,8,14336\nMvav 160,16,80\nWL 12\ns_cselect_b32 s47,0,s47\nTR 48,8,16384\nTR 50,8,18432\nMvav 160,20,96\nWL 12\nTR 52,8,20480\n"
"TR 54,8,22528\nMvav 160,24,112\nWL 12\nTR 56,8,24576\nTR 58,8,26624\nMvav 160,28,128\nTR 60,8,28672\nZvav 164,28,132\nTR 62,8,30720\nMvav 164,24,116\n"
"DP 1,20,42,2048\nMvav 164,20,100\nv_add_u32 v5,s47,v10\nv_xor_b32 v6,32,v5\nMvav 164,16,84\nv_xor_b32 v7,64,v5\nv_xor_b32 v8,96,v5\nMvav 164,12,68\n"
"DP 1,20,43,3072\nMvav 164,8,52\nv_add_u32 v1,0x80,v1\nMvav 164,4,36\nMvav 164,0,20\ns_waitcnt vmcnt(24) lgkmcnt(0)\ns_barrier\nZvav 168,32,16\n"
"TR 0,5,0\nMvav 168,36,32\nTR 2,5,2048\nMvav 168,40,48\nTR 4,5,4096\nMvav 168,44,64\nTR 6,5,6144\nMvav 168,48,80\nTR 8,5,8192\nMvav 168,52,96\n"
"TR 10,5,10240\nMvav 168,56,112\nTR 12,5,12288\nMvav 168,60,128\nTR 14,5,14336\nZvav 172,60,132\nTR 16,5,16384\nMvav 172,56,116\nTR 18,5,18432\n"
"Mvav 172,52,100\nTR 20,5,20480\nMvav 172,48,84\nTR 22,5,22528\nMvav 172,44,68\nTR 24,5,24576\nMvav 172,40,52\nTR 26,5,26624\nMvav 172,36,36\n"
"TR 28,5,28672\nMvav 172,32,20\nWL 13\nTR 30,5,30720\nZvav 176,0,16\nWL 12\nTR 32,6,0\nTR 34,6,2048\nMvav 176,4,32\nWL 12\nTR 36,6,4096\nTR 38,6,6144\n"
"Mvav 176,8,48\nWL 12\nTR 40,6,8192\nTR 42,6,10240\nMvav 176,12,64\nWL 12\nTR 44,6,12288\nMvav 176,16,80\nWL 11\nTR 46,6,14336\nCV 144,144,145\n"
"Mvav 176,20,96\nWL 10\nTR 48,6,16384\nCV 145,146,147\nMvav 176,24,112\nWL 9\nTR 50,6,18432\nCV 146,152,153\nMvav 176,28,128\ns_add_u32 m0,s48,s44\n"
"TR 52,6,20480\nCV 147,154,155\nZvav 180,28,132\nTR 54,6,22528\nbuffer_store_dwordx4 v[144:147],v0,s[24:27],s70 offen sc1\nMvav 180,24,116\n"
"s_add_u32 s70,s70,1024\nDP 1,20,36,0\nMvav 180,20,100\nTR 56,6,24576\nCV 148,148,149\nMvav 180,16,84\nTR 58,6,26624\nCV 149,150,151\nMvav 180,12,68\n"
"DP 1,20,37,1024\nMvav 180,8,52\nTR 60,6,28672\nCV 150,156,157\nMvav 180,4,36\nTR 62,6,30720\nCV 151,158,159\nMvav 180,0,20\nWL 14\nDP 1,20,38,2048\n"
"Zvav 184,32,16\nWL 12\nTR 0,7,0\nTR 2,7,2048\nbuffer_store_dwordx4 v[148:151],v0,s[24:27],s71 offen sc1\nMvav 184,36,32\nWL 12\ns_add_u32 s71,s71,1024\n"
"TR 4,7,4096\nTR 6,7,6144\nMvav 184,40,48\nWL 12\nTR 8,7,8192\nTR 10,7,10240\nMvav 184,44,64\nWL 12\nTR 12,7,12288\nCV 160,160,161\nMvav 184,48,80\n"
"WL 11\nTR 14,7,14336\nCV 161,162,163\nMvav 184,52,96\nWL 10\nTR 16,7,16384\nCV 162,168,169\nMvav 184,56,112\nWL 9\nTR 18,7,18432\nCV 163,170,171\n"
"Mvav 184,60,128\nTR 20,7,20480\nbuffer_store_dwordx4 v[160:163],v0,s[24:27],s70 offen sc1\nZvav 188,60,132\ns_add_u32 s70,s70,1024\nTR 22,7,22528\n"
"CV 164,164,165\nMvav 188,56,116\nDP 1,20,39,3072\nMvav 188,52,100\ns_add_u32 m0,m0,0x1000\nTR 24,7,24576\nCV 165,166,167\nMvav 188,48,84\n"
"TR 26,7,26624\nCV 166,172,173\nMvav 188,44,68\nDP 1,20,40,0\nMvav 188,40,52\nTR 28,7,28672\nCV 167,174,175\nMvav 188,36,36\nTR 30,7,30720\n"
"buffer_store_dwordx4 v[164:167],v0,s[24:27],s71 offen sc1\nMvav 188,32,20\nWL 14\ns_add_u32 s71,s71,1024\nDP 1,20,41,1024\nZvav 192,0,16\nWL 12\n"
"TR 32,8,0\nTR 34,8,2048\nMvav 192,4,32\nWL 12\ns_mov_b32 s44,s47\nTR 36,8,4096\nTR 38,8,6144\nMvav 192,8,48\nWL 12\ns_add_u32 s47,s47,0x8000\n"
"TR 40,8,8192\nTR 42,8,10240\nMvav 192,12,64\nWL 12\ns_cmp_eq_u32 s47,0x28000\nTR 44,8,12288\nTR 46,8,14336\nMvav 192,16,80\nWL 12\n"
"s_cselect_b32 s47,0,s47\nTR 48,8,16384\nTR 50,8,18432\nMvav 192,20,96\nWL 12\nTR 52,8,20480\nTR 54,8,22528\nMvav 192,24,112\nWL 12\nTR 56,8,24576\n"
"TR 58,8,26624\nMvav 192,28,128\nTR 60,8,28672\nZvav 196,28,132\nTR 62,8,30720\nMvav 196,24,116\nDP 1,20,42,2048\nMvav 196,20,100\nv_add_u32 v5,s47,v10\n"
"v_xor_b32 v6,32,v5\nMvav 196,16,84\nv_xor_b32 v7,64,v5\nv_xor_b32 v8,96,v5\nMvav 196,12,68\nDP 1,20,43,3072\nMvav 196,8,52\nv_add_u32 v1,0x80,v1\n"
"Mvav 196,4,36\nMvav 196,0,20\ns_waitcnt vmcnt(28) lgkmcnt(0)\ns_barrier\nZvav 200,32,16\nTR 0,5,0\nMvav 200,36,32\nTR 2,5,2048\nMvav 200,40,48\n"
"TR 4,5,4096\nMvav 200,44,64\nTR 6,5,6144\nMvav 200,48,80\nTR 8,5,8192\nMvav 200,52,96\nTR 10,5,10240\nMvav 200,56,112\nTR 12,5,12288\nMvav 200,60,128\n"
"TR 14,5,14336\nZvav 204,60,132\nTR 16,5,16384\nMvav 204,56,116\nTR 18,5,18432\nMvav 204,52,100\nTR 20,5,20480\nMvav 204,48,84\nTR 22,5,22528\n"
"Mvav 204,44,68\nTR 24,5,24576\nMvav 204,40,52\nTR 26,5,26624\nMvav 204,36,36\nTR 28,5,28672\nMvav 204,32,20\nWL 13\nTR 30,5,30720\nZvav 144,0,16\n"
"WL 12\nTR 32,6,0\nTR 34,6,2048\nMvav 144,4,32\nWL 12\nTR 36,6,4096\nTR 38,6,6144\nMvav 144,8,48\nWL 12\nTR 40,6,8192\nTR 42,6,10240\nMvav 144,12,64\n"
"WL 12\nTR 44,6,12288\nMvav 144,16,80\nWL 11\nTR 46,6,14336\nCV 176,176,177\nMvav 144,20,96\nWL 10\nTR 48,6,16384\nCV 177,178,179\nMvav 144,24,112\n"
"WL 9\nTR 50,6,18432\nCV 178,184,185\nMvav 144,28,128\ns_add_u32 m0,s48,s44\nTR 52,6,20480\nCV 179,186,187\nZvav 148,28,132\nTR 54,6,22528\n"
"buffer_store_dwordx4 v[176:179],v0,s[24:27],s70 offen sc1\nMvav 148,24,116\ns_add_u32 s70,s70,1024\nDP 1,20,36,0\nMvav 148,20,100\nTR 56,6,24576\n"
"CV 180,180,181\nMvav 148,16,84\nTR 58,6,26624\nCV 181,182,183\nMvav 148,12,68\nDP 1,20,37,1024\nMvav 148,8,52\nTR 60,6,28672\nCV 182,188,189\n"
"Mvav 148,4,36\nTR 62,6,30720\nCV 183,190,191\nMvav 148,0,20\nWL 14\nDP 1,20,38,2048\nZvav 152,32,16\nWL 12\nTR 0,7,0\nTR 2,7,2048\n"
"buffer_store_dwordx4 v[180:183],v0,s[24:27],s71 offen sc1\nMvav 152,36,32\nWL 12\ns_add_u32 s71,s71,1024\nTR 4,7,4096\nTR 6,7,6144\nMvav 152,40,48\n"
"WL 12\nTR 8,7,8192\nTR 10,7,10240\nMvav 152,44,64\nWL 12\nTR 12,7,12288\nCV 192,192,193\nMvav 152,48,80\nWL 11\nTR 14,7,14336\nCV 193,194,195\n"
"Mvav 152,52,96\nWL 10\nTR 16,7,16384\nCV 194,200,201\nMvav 152,56,112\nWL 9\nTR 18,7,18432\nCV 195,202,203\nMvav 152,60,128\nTR 20,7,20480\n"
"buffer_store_dwordx4 v[192:195],v0,s[24:27],s70 offen sc1\nZvav 156,60,132\ns_add_u32 s70,s70,1024\nTR 22,7,22528\nCV 196,196,197\nMvav 156,56,116\n"
"DP 1,20,39,3072\nMvav 156,52,100\ns_add_u32 m0,m0,0x1000\nTR 24,7,24576\nCV 197,198,199\nMvav 156,48,84\nTR 26,7,26624\nCV 198,204,205\nMvav 156,44,68\n"
"DP 1,20,40,0\nMvav 156,40,52\nTR 28,7,28672\nCV 199,206,207\nMvav 156,36,36\nTR 30,7,30720\nbuffer_store_dwordx4 v[196:199],v0,s[24:27],s71 offen sc1\n"
"Mvav 156,32,20\nWL 14\ns_add_u32 s71,s71,1024\nDP 1,20,41,1024\nZvav 160,0,16\nWL 12\nTR 32,8,0\nTR 34,8,2048\nMvav 160,4,32\nWL 12\ns_mov_b32 s44,s47\n"
"TR 36,8,4096\nTR 38,8,6144\nMvav 160,8,48\nWL 12\ns_add_u32 s47,s47,0x8000\nTR 40,8,8192\nTR 42,8,10240\nMvav 160,12,64\nWL 12\n"
"s_cmp_eq_u32 s47,0x28000\nTR 44,8,12288\nTR 46,8,14336\nMvav 160,16,80\nWL 12\ns_cselect_b32 s47,0,s47\nTR 48,8,16384\nMvav 160,20,96\nWL 11\n"
"TR 50,8,18432\nMvav 160,24,112\nWL 10\nTR 52,8,20480\nMvav 160,28,128\nTR 54,8,22528\nZvav 164,28,132\nTR 56,8,24576\nMvav 164,24,116\nDP 1,20,42,2048\n"
"Mvav 164,20,100\nTR 58,8,26624\nMvav 164,16,84\nTR 60,8,28672\nMvav 164,12,68\nDP 1,20,43,3072\nMvav 164,8,52\nTR 62,8,30720\nMvav 164,4,36\n"
"s_waitcnt vmcnt(32)\nv_add_u32 v5,s47,v10\nv_xor_b32 v6,32,v5\nMvav 164,0,20\ns_barrier\nv_xor_b32 v7,64,v5\nv_xor_b32 v8,96,v5\nWL 14\nZvav 168,32,16\n"
"WL 12\nTR 0,5,0\nMvav 168,36,32\nWL 11\nTR 2,5,2048\nMvav 168,40,48\nWL 10\nTR 4,5,4096\nMvav 168,44,64\nWL 9\nTR 6,5,6144\nMvav 168,48,80\nWL 8\n"
"TR 8,5,8192\nMvav 168,52,96\nWL 7\nTR 10,5,10240\nMvav 168,56,112\nWL 6\nTR 12,5,12288\nMvav 168,60,128\nTR 14,5,14336\nZvav 172,60,132\nTR 16,5,16384\n"
"Mvav 172,56,116\nTR 18,5,18432\nMvav 172,52,100\nTR 20,5,20480\nMvav 172,48,84\nTR 22,5,22528\nMvav 172,44,68\nTR 24,5,24576\nMvav 172,40,52\n"
"TR 26,5,26624\nMvav 172,36,36\nTR 28,5,28672\nMvav 172,32,20\nWL 13\nTR 30,5,30720\nZvav 176,0,16\nWL 12\nTR 32,6,0\nMvav 176,4,32\nWL 11\n"
"TR 34,6,2048\nMvav 176,8,48\nWL 10\nTR 36,6,4096\nMvav 176,12,64\nWL 9\nTR 38,6,6144\nMvav 176,16,80\nWL 8\nTR 40,6,8192\nCV 144,144,145\n"
"Mvav 176,20,96\nWL 7\nTR 42,6,10240\nCV 145,146,147\nMvav 176,24,112\nWL 6\nTR 44,6,12288\nCV 146,152,153\nMvav 176,28,128\nTR 46,6,14336\n"
"CV 147,154,155\nZvav 180,28,132\nTR 48,6,16384\nbuffer_store_dwordx4 v[144:147],v0,s[24:27],s70 offen sc1\nMvav 180,24,116\ns_add_u32 s70,s70,1024\n"
"TR 50,6,18432\nCV 148,148,149\nMvav 180,20,100\nTR 52,6,20480\nCV 149,150,151\nMvav 180,16,84\nTR 54,6,22528\nCV 150,156,157\nMvav 180,12,68\n"
"TR 56,6,24576\nCV 151,158,159\nMvav 180,8,52\nTR 58,6,26624\nbuffer_store_dwordx4 v[148:151],v0,s[24:27],s71 offen sc1\nMvav 180,4,36\n"
"s_add_u32 s71,s71,1024\nTR 60,6,28672\nCV 160,160,161\nMvav 180,0,20\nWL 13\nTR 62,6,30720\nCV 161,162,163\nZvav 184,32,16\nWL 12\nTR 0,7,0\n"
"CV 162,168,169\nMvav 184,36,32\nWL 11\nTR 2,7,2048\nCV 163,170,171\nMvav 184,40,48\nWL 10\nTR 4,7,4096\n"
"buffer_store_dwordx4 v[160:163],v0,s[24:27],s70 offen sc1\nMvav 184,44,64\nWL 9\ns_add_u32 s70,s70,1024\nTR 6,7,6144\nCV 164,164,165\nMvav 184,48,80\n"
"WL 8\nTR 8,7,8192\nCV 165,166,167\nMvav 184,52,96\nWL 7\nTR 10,7,10240\nCV 166,172,173\nMvav 184,56,112\nWL 6\nTR 12,7,12288\nCV 167,174,175\n"
"Mvav 184,60,128\nTR 14,7,14336\nbuffer_store_dwordx4 v[164:167],v0,s[24:27],s71 offen sc1\nZvav 188,60,132\ns_add_u32 s71,s71,1024\nTR 16,7,16384\n"
"Mvav 188,56,116\nTR 18,7,18432\nMvav 188,52,100\nTR 20,7,20480\nMvav 188,48,84\nTR 22,7,22528\nMvav 188,44,68\nTR 24,7,24576\nMvav 188,40,52\n"
"TR 26,7,26624\nMvav 188,36,36\nTR 28,7,28672\nMvav 188,32,20\nWL 13\nTR 30,7,30720\nZvav 192,0,16\nWL 12\nTR 32,8,0\nTR 34,8,2048\nMvav 192,4,32\n"
"WL 12\ns_mov_b32 s44,s47\nTR 36,8,4096\nTR 38,8,6144\nMvav 192,8,48\nWL 12\ns_add_u32 s47,s47,0x8000\nTR 40,8,8192\nMvav 192,12,64\nWL 11\n"
"s_cmp_eq_u32 s47,0x28000\nTR 42,8,10240\nMvav 192,16,80\nWL 10\ns_cselect_b32 s47,0,s47\nTR 44,8,12288\nMvav 192,20,96\nWL 9\nTR 46,8,14336\n"
"Mvav 192,24,112\nWL 8\nTR 48,8,16384\nMvav 192,28,128\nTR 50,8,18432\nZvav 196,28,132\nTR 52,8,20480\nMvav 196,24,116\nTR 54,8,22528\nMvav 196,20,100\n"
"TR 56,8,24576\nMvav 196,16,84\nTR 58,8,26624\nMvav 196,12,68\nTR 60,8,28672\nMvav 196,8,52\nTR 62,8,30720\nMvav 196,4,36\ns_waitcnt vmcnt(4)\n"
"v_add_u32 v5,s47,v10\nv_xor_b32 v6,32,v5\nMvav 196,0,20\ns_barrier\nv_xor_b32 v7,64,v5\nv_xor_b32 v8,96,v5\nWL 14\nZvav 200,32,16\nWL 12\nTR 0,5,0\n"
"Mvav 200,36,32\nWL 11\nTR 2,5,2048\nMvav 200,40,48\nWL 10\nTR 4,5,4096\nMvav 200,44,64\nWL 9\nTR 6,5,6144\nMvav 200,48,80\nWL 8\nTR 8,5,8192\n"
"Mvav 200,52,96\nWL 7\nTR 10,5,10240\nMvav 200,56,112\nWL 6\nTR 12,5,12288\nMvav 200,60,128\nTR 14,5,14336\nZvav 204,60,132\nTR 16,5,16384\n"
"Mvav 204,56,116\nTR 18,5,18432\nMvav 204,52,100\nTR 20,5,20480\nMvav 204,48,84\nTR 22,5,22528\nMvav 204,44,68\nTR 24,5,24576\nMvav 204,40,52\n"
"TR 26,5,26624\nMvav 204,36,36\nTR 28,5,28672\nMvav 204,32,20\nWL 13\nTR 30,5,30720\nZvav 144,0,16\nWL 12\nTR 32,6,0\nMvav 144,4,32\nWL 11\n"
"TR 34,6,2048\nMvav 144,8,48\nWL 10\nTR 36,6,4096\nMvav 144,12,64\nWL 9\nTR 38,6,6144\nMvav 144,16,80\nWL 8\nTR 40,6,8192\nCV 176,176,177\n"
"Mvav 144,20,96\nWL 7\nTR 42,6,10240\nCV 177,178,179\nMvav 144,24,112\nWL 6\nTR 44,6,12288\nCV 178,184,185\nMvav 144,28,128\nTR 46,6,14336\n"
"CV 179,186,187\nZvav 148,28,132\nTR 48,6,16384\nbuffer_store_dwordx4 v[176:179],v0,s[24:27],s70 offen sc1\nMvav 148,24,116\ns_add_u32 s70,s70,1024\n"
"TR 50,6,18432\nCV 180,180,181\nMvav 148,20,100\nTR 52,6,20480\nCV 181,182,183\nMvav 148,16,84\nTR 54,6,22528\nCV 182,188,189\nMvav 148,12,68\n"
"TR 56,6,24576\nCV 183,190,191\nMvav 148,8,52\nTR 58,6,26624\nbuffer_store_dwordx4 v[180:183],v0,s[24:27],s71 offen sc1\nMvav 148,4,36\n"
"s_add_u32 s71,s71,1024\nTR 60,6,28672\nCV 192,192,193\nMvav 148,0,20\nWL 13\nTR 62,6,30720\nCV 193,194,195\nZvav 152,32,16\nWL 12\nTR 0,7,0\n"
"CV 194,200,201\nMvav 152,36,32\nWL 11\nTR 2,7,2048\nCV 195,202,203\nMvav 152,40,48\nWL 10\nTR 4,7,4096\n"
"buffer_store_dwordx4 v[192:195],v0,s[24:27],s70 offen sc1\nMvav 152,44,64\nWL 9\ns_add_u32 s70,s70,1024\nTR 6,7,6144\nCV 196,196,197\nMvav 152,48,80\n"
"WL 8\nTR 8,7,8192\nCV 197,198,199\nMvav 152,52,96\nWL 7\nTR 10,7,10240\nCV 198,204,205\nMvav 152,56,112\nWL 6\nTR 12,7,12288\nCV 199,206,207\n"
"Mvav 152,60,128\nTR 14,7,14336\nbuffer_store_dwordx4 v[196:199],v0,s[24:27],s71 offen sc1\nZvav 156,60,132\ns_add_u32 s71,s71,1024\nTR 16,7,16384\n"
"Mvav 156,56,116\nTR 18,7,18432\nMvav 156,52,100\nTR 20,7,20480\nMvav 156,48,84\nTR 22,7,22528\nMvav 156,44,68\nTR 24,7,24576\nMvav 156,40,52\n"
"TR 26,7,26624\nMvav 156,36,36\nTR 28,7,28672\nMvav 156,32,20\nWL 13\nTR 30,7,30720\nZvav 160,0,16\nWL 12\nTR 32,8,0\nTR 34,8,2048\nMvav 160,4,32\n"
"WL 12\ns_mov_b32 s44,s47\nTR 36,8,4096\nTR 38,8,6144\nMvav 160,8,48\nWL 12\ns_add_u32 s47,s47,0x8000\nTR 40,8,8192\nMvav 160,12,64\nWL 11\n"
"s_cmp_eq_u32 s47,0x28000\nTR 42,8,10240\nMvav 160,16,80\nWL 10\ns_cselect_b32 s47,0,s47\nTR 44,8,12288\nMvav 160,20,96\nWL 9\nTR 46,8,14336\n"
"Mvav 160,24,112\nWL 8\nTR 48,8,16384\nMvav 160,28,128\nTR 50,8,18432\nZvav 164,28,132\nTR 52,8,20480\nMvav 164,24,116\nTR 54,8,22528\nMvav 164,20,100\n"
"TR 56,8,24576\nMvav 164,16,84\nTR 58,8,26624\nMvav 164,12,68\nTR 60,8,28672\nMvav 164,8,52\nTR 62,8,30720\nMvav 164,4,36\nv_add_u32 v5,s47,v10\n"
"v_xor_b32 v6,32,v5\nMvav 164,0,20\nWL 14\nv_xor_b32 v7,64,v5\nv_xor_b32 v8,96,v5\nZvav 168,32,16\nWL 12\nTR 0,5,0\nMvav 168,36,32\nWL 11\nTR 2,5,2048\n"
"Mvav 168,40,48\nWL 10\nTR 4,5,4096\nMvav 168,44,64\nWL 9\nTR 6,5,6144\nMvav 168,48,80\nWL 8\nTR 8,5,8192\nMvav 168,52,96\nWL 7\nTR 10,5,10240\n"
"Mvav 168,56,112\nWL 6\nTR 12,5,12288\nMvav 168,60,128\nTR 14,5,14336\nZvav 172,60,132\nTR 16,5,16384\nMvav 172,56,116\nTR 18,5,18432\nMvav 172,52,100\n"
"TR 20,5,20480\nMvav 172,48,84\nTR 22,5,22528\nMvav 172,44,68\nTR 24,5,24576\nMvav 172,40,52\nTR 26,5,26624\nMvav 172,36,36\nTR 28,5,28672\n"
"Mvav 172,32,20\nTR 30,5,30720\ns_nop 0\nWL 14\nZvav 176,0,16\nWL 12\nTR 32,6,0\nMvav 176,4,32\nWL 11\nTR 34,6,2048\nMvav 176,8,48\nWL 10\nTR 36,6,4096\n"
"Mvav 176,12,64\nWL 9\nTR 38,6,6144\nMvav 176,16,80\nWL 8\nTR 40,6,8192\nCV 144,144,145\nMvav 176,20,96\nWL 7\nTR 42,6,10240\nCV 145,146,147\n"
"Mvav 176,24,112\nWL 6\nTR 44,6,12288\nCV 146,152,153\nMvav 176,28,128\nTR 46,6,14336\nCV 147,154,155\nZvav 180,28,132\nTR 48,6,16384\n"
"buffer_store_dwordx4 v[144:147],v0,s[24:27],s70 offen sc1\nMvav 180,24,116\ns_add_u32 s70,s70,1024\nTR 50,6,18432\nCV 148,148,149\nMvav 180,20,100\n"
"TR 52,6,20480\nCV 149,150,151\nMvav 180,16,84\nTR 54,6,22528\nCV 150,156,157\nMvav 180,12,68\nTR 56,6,24576\nCV 151,158,159\nMvav 180,8,52\n"
"TR 58,6,26624\nbuffer_store_dwordx4 v[148:151],v0,s[24:27],s71 offen sc1\nMvav 180,4,36\ns_add_u32 s71,s71,1024\nTR 60,6,28672\nCV 160,160,161\n"
"Mvav 180,0,20\nWL 13\nTR 62,6,30720\nCV 161,162,163\nZvav 184,32,16\nWL 12\nTR 0,7,0\nCV 162,168,169\nMvav 184,36,32\nWL 11\nTR 2,7,2048\n"
"CV 163,170,171\nMvav 184,40,48\nWL 10\nTR 4,7,4096\nbuffer_store_dwordx4 v[160:163],v0,s[24:27],s70 offen sc1\nMvav 184,44,64\nWL 9\n"
"s_add_u32 s70,s70,1024\nTR 6,7,6144\nCV 164,164,165\nMvav 184,48,80\nWL 8\nTR 8,7,8192\nCV 165,166,167\nMvav 184,52,96\nWL 7\nTR 10,7,10240\n"
"CV 166,172,173\nMvav 184,56,112\nWL 6\nTR 12,7,12288\nCV 167,174,175\nMvav 184,60,128\nTR 14,7,14336\n"
"buffer_store_dwordx4 v[164:167],v0,s[24:27],s71 offen sc1\nZvav 188,60,132\ns_add_u32 s71,s71,1024\nTR 16,7,16384\nMvav 188,56,116\nTR 18,7,18432\n"
"Mvav 188,52,100\nTR 20,7,20480\nMvav 188,48,84\nTR 22,7,22528\nMvav 188,44,68\nTR 24,7,24576\nMvav 188,40,52\nTR 26,7,26624\nMvav 188,36,36\n"
"TR 28,7,28672\nMvav 188,32,20\nWL 13\nTR 30,7,30720\nZvav 192,0,16\nWL 12\nTR 32,8,0\nTR 34,8,2048\nMvav 192,4,32\nWL 12\ns_mov_b32 s44,s47\n"
"TR 36,8,4096\nTR 38,8,6144\nMvav 192,8,48\nWL 12\ns_add_u32 s47,s47,0x8000\nTR 40,8,8192\nMvav 192,12,64\nWL 11\ns_cmp_eq_u32 s47,0x28000\n"
"TR 42,8,10240\nMvav 192,16,80\nWL 10\ns_cselect_b32 s47,0,s47\nTR 44,8,12288\nMvav 192,20,96\nWL 9\nTR 46,8,14336\nMvav 192,24,112\nWL 8\n"
"TR 48,8,16384\nMvav 192,28,128\nTR 50,8,18432\nZvav 196,28,132\nTR 52,8,20480\nMvav 196,24,116\nTR 54,8,22528\nMvav 196,20,100\nTR 56,8,24576\n"
"Mvav 196,16,84\nTR 58,8,26624\nMvav 196,12,68\nTR 60,8,28672\nMvav 196,8,52\nTR 62,8,30720\nMvav 196,4,36\nv_add_u32 v5,s47,v10\nv_xor_b32 v6,32,v5\n"
"Mvav 196,0,20\nWL 14\nv_xor_b32 v7,64,v5\nv_xor_b32 v8,96,v5\nZvav 200,32,16\nWL 12\nTR 0,5,0\nMvav 200,36,32\nWL 11\nTR 2,5,2048\nMvav 200,40,48\n"
"WL 10\nTR 4,5,4096\nMvav 200,44,64\nWL 9\nTR 6,5,6144\nMvav 200,48,80\nWL 8\nTR 8,5,8192\nMvav 200,52,96\nWL 7\nTR 10,5,10240\nMvav 200,56,112\n"
"s_waitcnt vmcnt(4)\nTR 12,5,12288\nv_mov_b32 v13,s65\ns_mov_b64 exec,1\nbuffer_store_dword v13,off,s[24:27],s63 sc1\ns_mov_b64 exec,-1\nWL 7\n"
"Mvav 200,60,128\nTR 14,5,14336\nZvav 204,60,132\nTR 16,5,16384\nMvav 204,56,116\nTR 18,5,18432\nMvav 204,52,100\nTR 20,5,20480\nMvav 204,48,84\n"
"TR 22,5,22528\nMvav 204,44,68\nTR 24,5,24576\nMvav 204,40,52\nTR 26,5,26624\nMvav 204,36,36\nTR 28,5,28672\nMvav 204,32,20\nWL 13\nTR 30,5,30720\n"
"Zvav 144,0,16\nWL 12\nTR 32,6,0\nMvav 144,4,32\nWL 11\nTR 34,6,2048\nMvav 144,8,48\nWL 10\nTR 36,6,4096\nMvav 144,12,64\nWL 9\nTR 38,6,6144\n"
"Mvav 144,16,80\nWL 8\nTR 40,6,8192\nCV 176,176,177\nMvav 144,20,96\nWL 7\nTR 42,6,10240\nCV 177,178,179\nMvav 144,24,112\nWL 6\nTR 44,6,12288\n"
"CV 178,184,185\nMvav 144,28,128\nTR 46,6,14336\nCV 179,186,187\nZvav 148,28,132\nTR 48,6,16384\n"
"buffer_store_dwordx4 v[176:179],v0,s[24:27],s70 offen sc1\nMvav 148,24,116\ns_add_u32 s70,s70,1024\nTR 50,6,18432\nCV 180,180,181\nMvav 148,20,100\n"
"TR 52,6,20480\nCV 181,182,183\nMvav 148,16,84\nTR 54,6,22528\nCV 182,188,189\nMvav 148,12,68\nTR 56,6,24576\nCV 183,190,191\nMvav 148,8,52\n"
"TR 58,6,26624\nbuffer_store_dwordx4 v[180:183],v0,s[24:27],s71 offen sc1\nMvav 148,4,36\ns_add_u32 s71,s71,1024\nTR 60,6,28672\nCV 192,192,193\n"
"Mvav 148,0,20\nWL 13\nTR 62,6,30720\nCV 193,194,195\nZvav 152,32,16\nWL 12\nTR 0,7,0\nCV 194,200,201\nMvav 152,36,32\nWL 11\nTR 2,7,2048\n"
"CV 195,202,203\nMvav 152,40,48\nWL 10\nTR 4,7,4096\nbuffer_store_dwordx4 v[192:195],v0,s[24:27],s70 offen sc1\nMvav 152,44,64\nWL 9\n"
"s_add_u32 s70,s70,1024\nTR 6,7,6144\nCV 196,196,197\nMvav 152,48,80\nWL 8\nTR 8,7,8192\nCV 197,198,199\nMvav 152,52,96\nWL 7\nTR 10,7,10240\n"
"CV 198,204,205\nMvav 152,56,112\nWL 6\nTR 12,7,12288\nCV 199,206,207\nMvav 152,60,128\nTR 14,7,14336\n"
"buffer_store_dwordx4 v[196:199],v0,s[24:27],s71 offen sc1\nZvav 156,60,132\ns_add_u32 s71,s71,1024\nTR 16,7,16384\nMvav 156,56,116\nTR 18,7,18432\n"
"Mvav 156,52,100\nTR 20,7,20480\nMvav 156,48,84\nTR 22,7,22528\nMvav 156,44,68\nTR 24,7,24576\nMvav 156,40,52\nTR 26,7,26624\nMvav 156,36,36\n"
"TR 28,7,28672\nMvav 156,32,20\nWL 13\nTR 30,7,30720\nZvav 160,0,16\nWL 12\nTR 32,8,0\nTR 34,8,2048\nMvav 160,4,32\nWL 12\nTR 36,8,4096\nTR 38,8,6144\n"
"Mvav 160,8,48\nWL 12\nTR 40,8,8192\nMvav 160,12,64\nWL 11\nTR 42,8,10240\nMvav 160,16,80\nWL 10\nTR 44,8,12288\nMvav 160,20,96\nWL 9\nTR 46,8,14336\n"
"Mvav 160,24,112\nWL 8\nTR 48,8,16384\nMvav 160,28,128\nTR 50,8,18432\nZvav 164,28,132\nTR 52,8,20480\nMvav 164,24,116\nTR 54,8,22528\nMvav 164,20,100\n"
"TR 56,8,24576\nMvav 164,16,84\nTR 58,8,26624\nMvav 164,12,68\nTR 60,8,28672\nMvav 164,8,52\nTR 62,8,30720\nMvav 164,4,36\nMvav 164,0,20\nWL 14\n"
"Zvav 168,32,16\nWL 12\nMvav 168,36,32\nWL 10\nMvav 168,40,48\nWL 8\nMvav 168,44,64\nWL 6\nMvav 168,48,80\nWL 4\nMvav 168,52,96\nWL 2\nMvav 168,56,112\n"
"WL 0\nMvav 168,60,128\nZvav 172,60,132\nMvav 172,56,116\nMvav 172,52,100\nMvav 172,48,84\nMvav 172,44,68\nMvav 172,40,52\nMvav 172,36,36\n"
"Mvav 172,32,20\ns_nop 7\nCV 144,144,145\nCV 145,146,147\nCV 146,152,153\nCV 147,154,155\nbuffer_store_dwordx4 v[144:147],v0,s[24:27],s70 offen sc1\n"
"s_add_u32 s70,s70,1024\nCV 148,148,149\nCV 149,150,151\nCV 150,156,157\nCV 151,158,159\nbuffer_store_dwordx4 v[148:151],v0,s[24:27],s71 offen sc1\n"
"s_add_u32 s71,s71,1024\nCV 160,160,161\nCV 161,162,163\nCV 162,168,169\nCV 163,170,171\nbuffer_store_dwordx4 v[160:163],v0,s[24:27],s70 offen sc1\n"
"s_add_u32 s70,s70,1024\nCV 164,164,165\nCV 165,166,167\nCV 166,172,173\nCV 167,174,175\nbuffer_store_dwordx4 v[164:167],v0,s[24:27],s71 offen sc1\n"
"s_add_u32 s71,s71,1024\nv_lshrrev_b32 v12,4,v0\nv_and_b32 v13,15,v12\nv_mul_u32_u24 v13,1792,v13\nv_bfe_u32 v11,v12,4,1\nv_lshl_add_u32 v13,v11,5,v13\n"
"v_lshrrev_b32 v12,5,v12\nv_lshl_add_u32 v12,v12,4,v13\ns_mov_b32 s16,s92\ns_and_b32 s17,s93,0xffff\ns_mov_b32 s18,3670016\ns_mov_b32 s19,0x20000\n"
"s_mov_b32 s28,0x3f80\ns_mov_b32 s29,0x3f800000\ns_mul_i32 s31,s11,2\ns_mul_i32 s49,s10,1\ns_add_u32 s31,s31,s49\ns_mul_hi_u32 s94,s31,0x2aaaaaab\n"
"s_mul_i32 s95,s94,6\ns_sub_u32 s95,s31,s95\ns_add_u32 s94,s94,64\ns_add_u32 s95,s95,8\ns_lshr_b32 s8,s94,3\ns_lshl_b32 s8,s8,13\n"
"s_bfe_u32 s49,s94,0x20001\ns_lshl_b32 s49,s49,7\ns_add_u32 s8,s8,s49\ns_add_u32 s8,s8,327680\ns_waitcnt vmcnt(0)\nv_mov_b32 v13,s65\ns_mov_b64 exec,1\n"
"buffer_store_dword v13,off,s[24:27],s9 sc1\ns_mov_b64 exec,-1\ns_sleep 12\ns_cmp_lt_u32 s10,2\ns_cbranch_scc1 fused_op_item\ns_branch fused_op_end\n"
"fused_op_sp8:\ns_sub_u32 s14,s14,1\ns_cmp_eq_u32 s14,0\ns_cbranch_scc1 fused_op_giveup1\ns_sleep 4\nbuffer_load_dword v14,v15,s[24:27],s8 offen sc1\n"
"s_waitcnt vmcnt(0)\nv_cmp_ne_u32 vcc,s65,v14\ns_nop 1\ns_cbranch_vccnz fused_op_sp8\ns_branch fused_op_spb9\nfused_op_sp10:\ns_sub_u32 s14,s14,1\n"
"s_cmp_eq_u32 s14,0\ns_cbranch_scc1 fused_op_giveup1\ns_sleep 4\nbuffer_load_dword v14,v15,s[24:27],s8 offen sc1\ns_waitcnt vmcnt(0)\n"
"v_cmp_ne_u32 vcc,s65,v14\ns_nop 1\ns_cbranch_vccnz fused_op_sp10\ns_branch fused_op_spb11\nfused_op_ev12:\ns_mul_i32 s45,s56,8192\n"
"s_add_u32 s45,s45,0x11000\nv_bfe_u32 v12,v0,4,5\nv_lshlrev_b32 v12,7,v12\nfused_op_ev12s:\ns_sub_u32 s14,s14,1\ns_cmp_eq_u32 s14,0\n"
"s_cbranch_scc1 fused_op_giveup1\nbuffer_load_dword v13,v12,s[24:27],s45 offen sc1\ns_waitcnt vmcnt(0)\nv_cmp_ne_u32 vcc,s65,v13\ns_nop 1\n"
"s_cbranch_vccnz fused_op_ev12s\ns_mov_b32 s46,0x7fffffff\ns_branch fused_op_evb13\nfused_op_ev14:\ns_mul_i32 s45,s56,8192\ns_add_u32 s45,s45,0x11000\n"
"v_bfe_u32 v12,v0,4,5\nv_lshlrev_b32 v12,7,v12\nfused_op_ev14s:\ns_sub_u32 s14,s14,1\ns_cmp_eq_u32 s14,0\ns_cbranch_scc1 fused_op_giveup1\n"
"buffer_load_dword v13,v12,s[24:27],s45 offen sc1\ns_waitcnt vmcnt(0)\nv_cmp_ne_u32 vcc,s65,v13\ns_nop 1\ns_cbranch_vccnz fused_op_ev14s\n"
"s_mov_b32 s46,0x7fffffff\ns_branch fused_op_evb15\nfused_op_rm16:\ns_nop 7\nv_max3_f32 v238,v144,v145,v146\nv_max3_f32 v240,v148,v149,v150\n"
"v_max3_f32 v238,v238,v147,v152\nv_max3_f32 v240,v240,v151,v156\nv_max3_f32 v238,v238,v153,v154\nv_max3_f32 v240,v240,v157,v158\n"
"v_max3_f32 v238,v238,v155,v160\nv_max3_f32 v240,v240,v159,v164\nv_max3_f32 v238,v238,v161,v162\nv_max3_f32 v240,v240,v165,v166\n"
"v_max3_f32 v238,v238,v163,v168\nv_max3_f32 v240,v240,v167,v172\nv_max3_f32 v238,v238,v169,v170\nv_max3_f32 v240,v240,v173,v174\n"
"v_max_f32 v238,v238,v171\nv_max_f32 v240,v240,v175\nv_mov_b32 v239,v238\nv_mov_b32 v241,v240\ns_nop 0\nv_permlane32_swap_b32 v238,v239\n"
"v_permlane32_swap_b32 v240,v241\nv_max_f32 v238,v238,v239\nv_max_f32 v240,v240,v241\nv_mov_b32 v239,v238\nv_mov_b32 v241,v240\ns_nop 0\n"
"v_permlane16_swap_b32 v238,v239\nv_permlane16_swap_b32 v240,v241\nv_max_f32 v238,v238,v239\nv_max_f32 v240,v240,v241\nv_max_f32 v238,0,v238\n"
"v_max_f32 v240,0,v240\nv_add_f32 v232,v238,v232\nv_add_f32 v233,v240,v233\nv_exp_f32 v236,-v238\nv_exp_f32 v237,-v240\nv_mul_f32 v234,v236,v234\n"
"v_mul_f32 v235,v237,v235\nv_sub_f32 v224,0,v232\nv_sub_f32 v228,0,v233\nv_sub_f32 v225,0,v232\nv_sub_f32 v229,0,v233\nv_sub_f32 v226,0,v232\n"
"v_sub_f32 v230,0,v233\nv_sub_f32 v227,0,v232\nv_sub_f32 v231,0,v233\nv_sub_f32 v144,v144,v238\nv_sub_f32 v148,v148,v240\nv_sub_f32 v145,v145,v238\n"
"v_sub_f32 v149,v149,v240\nv_sub_f32 v146,v146,v238\nv_sub_f32 v150,v150,v240\nv_sub_f32 v147,v147,v238\nv_sub_f32 v151,v151,v240\n"
"v_sub_f32 v152,v152,v238\nv_sub_f32 v156,v156,v240\nv_sub_f32 v153,v153,v238\nv_sub_f32 v157,v157,v240\nv_sub_f32 v154,v154,v238\n"
"v_sub_f32 v158,v158,v240\nv_sub_f32 v155,v155,v238\nv_sub_f32 v159,v159,v240\nv_sub_f32 v160,v160,v238\nv_sub_f32 v164,v164,v240\n"
"v_sub_f32 v161,v161,v238\nv_sub_f32 v165,v165,v240\nv_sub_f32 v162,v162,v238\nv_sub_f32 v166,v166,v240\nv_sub_f32 v163,v163,v238\n"
"v_sub_f32 v167,v167,v240\nv_sub_f32 v168,v168,v238\nv_sub_f32 v172,v172,v240\nv_sub_f32 v169,v169,v238\nv_sub_f32 v173,v173,v240\n"
"v_sub_f32 v170,v170,v238\nv_sub_f32 v174,v174,v240\nv_sub_f32 v171,v171,v238\nv_sub_f32 v175,v175,v240\ns_mov_b32 s64,1\ns_nop 1\n"
"s_branch fused_op_rmb17\nfused_op_rs18:\ns_nop 7\nv_mul_f32 v16,v236,v16\nv_mul_f32 v17,v236,v17\nv_mul_f32 v18,v236,v18\nv_mul_f32 v19,v236,v19\n"
"v_mul_f32 v20,v237,v20\nv_mul_f32 v21,v237,v21\nv_mul_f32 v22,v237,v22\nv_mul_f32 v23,v237,v23\nv_mul_f32 v24,v236,v24\nv_mul_f32 v25,v236,v25\n"
"v_mul_f32 v26,v236,v26\nv_mul_f32 v27,v236,v27\nv_mul_f32 v28,v237,v28\nv_mul_f32 v29,v237,v29\nv_mul_f32 v30,v237,v30\nv_mul_f32 v31,v237,v31\n"
"v_mul_f32 v32,v236,v32\nv_mul_f32 v33,v236,v33\nv_mul_f32 v34,v236,v34\nv_mul_f32 v35,v236,v35\nv_mul_f32 v36,v237,v36\nv_mul_f32 v37,v237,v37\n"
"v_mul_f32 v38,v237,v38\nv_mul_f32 v39,v237,v39\nv_mul_f32 v40,v236,v40\nv_mul_f32 v41,v236,v41\nv_mul_f32 v42,v236,v42\nv_mul_f32 v43,v236,v43\n"
"v_mul_f32 v44,v237,v44\nv_mul_f32 v45,v237,v45\nv_mul_f32 v46,v237,v46\nv_mul_f32 v47,v237,v47\nv_mul_f32 v48,v236,v48\nv_mul_f32 v49,v236,v49\n"
"v_mul_f32 v50,v236,v50\nv_mul_f32 v51,v236,v51\nv_mul_f32 v52,v237,v52\nv_mul_f32 v53,v237,v53\nv_mul_f32 v54,v237,v54\nv_mul_f32 v55,v237,v55\n"
"v_mul_f32 v56,v236,v56\nv_mul_f32 v57,v236,v57\nv_mul_f32 v58,v236,v58\nv_mul_f32 v59,v236,v59\nv_mul_f32 v60,v237,v60\nv_mul_f32 v61,v237,v61\n"
"v_mul_f32 v62,v237,v62\nv_mul_f32 v63,v237,v63\nv_mul_f32 v64,v236,v64\nv_mul_f32 v65,v236,v65\nv_mul_f32 v66,v236,v66\nv_mul_f32 v67,v236,v67\n"
"v_mul_f32 v68,v237,v68\nv_mul_f32 v69,v237,v69\nv_mul_f32 v70,v237,v70\nv_mul_f32 v71,v237,v71\nv_mul_f32 v72,v236,v72\nv_mul_f32 v73,v236,v73\n"
"v_mul_f32 v74,v236,v74\nv_mul_f32 v75,v236,v75\nv_mul_f32 v76,v237,v76\nv_mul_f32 v77,v237,v77\nv_mul_f32 v78,v237,v78\nv_mul_f32 v79,v237,v79\n"
"v_mul_f32 v80,v236,v80\nv_mul_f32 v81,v236,v81\nv_mul_f32 v82,v236,v82\nv_mul_f32 v83,v236,v83\nv_mul_f32 v84,v237,v84\nv_mul_f32 v85,v237,v85\n"
"v_mul_f32 v86,v237,v86\nv_mul_f32 v87,v237,v87\nv_mul_f32 v88,v236,v88\nv_mul_f32 v89,v236,v89\nv_mul_f32 v90,v236,v90\nv_mul_f32 v91,v236,v91\n"
"v_mul_f32 v92,v237,v92\nv_mul_f32 v93,v237,v93\nv_mul_f32 v94,v237,v94\nv_mul_f32 v95,v237,v95\nv_mul_f32 v96,v236,v96\nv_mul_f32 v97,v236,v97\n"
"v_mul_f32 v98,v236,v98\nv_mul_f32 v99,v236,v99\nv_mul_f32 v100,v237,v100\nv_mul_f32 v101,v237,v101\nv_mul_f32 v102,v237,v102\nv_mul_f32 v103,v237,v103\n"
"v_mul_f32 v104,v236,v104\nv_mul_f32 v105,v236,v105\nv_mul_f32 v106,v236,v106\nv_mul_f32 v107,v236,v107\nv_mul_f32 v108,v237,v108\n"
"v_mul_f32 v109,v237,v109\nv_mul_f32 v110,v237,v110\nv_mul_f32 v111,v237,v111\nv_mul_f32 v112,v236,v112\nv_mul_f32 v113,v236,v113\n"
"v_mul_f32 v114,v236,v114\nv_mul_f32 v115,v236,v115\nv_mul_f32 v116,v237,v116\nv_mul_f32 v117,v237,v117\nv_mul_f32 v118,v237,v118\n"
"v_mul_f32 v119,v237,v119\nv_mul_f32 v120,v236,v120\nv_mul_f32 v121,v236,v121\nv_mul_f32 v122,v236,v122\nv_mul_f32 v123,v236,v123\n"
"v_mul_f32 v124,v237,v124\nv_mul_f32 v125,v237,v125\nv_mul_f32 v126,v237,v126\nv_mul_f32 v127,v237,v127\nv_mul_f32 v128,v236,v128\n"
"v_mul_f32 v129,v236,v129\nv_mul_f32 v130,v236,v130\nv_mul_f32 v131,v236,v131\nv_mul_f32 v132,v237,v132\nv_mul_f32 v133,v237,v133\n"
"v_mul_f32 v134,v237,v134\nv_mul_f32 v135,v237,v135\nv_mul_f32 v136,v236,v136\nv_mul_f32 v137,v236,v137\nv_mul_f32 v138,v236,v138\n"
"v_mul_f32 v139,v236,v139\nv_mul_f32 v140,v237,v140\nv_mul_f32 v141,v237,v141\nv_mul_f32 v142,v237,v142\nv_mul_f32 v143,v237,v143\ns_mov_b32 s64,0\n"
"s_nop 1\ns_branch fused_op_rsb19\nfused_op_rm20:\ns_nop 7\nv_max3_f32 v238,v176,v177,v178\nv_max3_f32 v240,v180,v181,v182\n"
"v_max3_f32 v238,v238,v179,v184\nv_max3_f32 v240,v240,v183,v188\nv_max3_f32 v238,v238,v185,v186\nv_max3_f32 v240,v240,v189,v190\n"
"v_max3_f32 v238,v238,v187,v192\nv_max3_f32 v240,v240,v191,v196\nv_max3_f32 v238,v238,v193,v194\nv_max3_f32 v240,v240,v197,v198\n"
"v_max3_f32 v238,v238,v195,v200\nv_max3_f32 v240,v240,v199,v204\nv_max3_f32 v238,v238,v201,v202\nv_max3_f32 v240,v240,v205,v206\n"
"v_max_f32 v238,v238,v203\nv_max_f32 v240,v240,v207\nv_mov_b32 v239,v238\nv_mov_b32 v241,v240\ns_nop 0\nv_permlane32_swap_b32 v238,v239\n"
"v_permlane32_swap_b32 v240,v241\nv_max_f32 v238,v238,v239\nv_max_f32 v240,v240,v241\nv_mov_b32 v239,v238\nv_mov_b32 v241,v240\ns_nop 0\n"
"v_permlane16_swap_b32 v238,v239\nv_permlane16_swap_b32 v240,v241\nv_max_f32 v238,v238,v239\nv_max_f32 v240,v240,v241\nv_max_f32 v238,0,v238\n"
"v_max_f32 v240,0,v240\nv_add_f32 v232,v238,v232\nv_add_f32 v233,v240,v233\nv_exp_f32 v236,-v238\nv_exp_f32 v237,-v240\nv_mul_f32 v234,v236,v234\n"
"v_mul_f32 v235,v237,v235\nv_sub_f32 v224,0,v232\nv_sub_f32 v228,0,v233\nv_sub_f32 v225,0,v232\nv_sub_f32 v229,0,v233\nv_sub_f32 v226,0,v232\n"
"v_sub_f32 v230,0,v233\nv_sub_f32 v227,0,v232\nv_sub_f32 v231,0,v233\nv_sub_f32 v176,v176,v238\nv_sub_f32 v180,v180,v240\nv_sub_f32 v177,v177,v238\n"
"v_sub_f32 v181,v181,v240\nv_sub_f32 v178,v178,v238\nv_sub_f32 v182,v182,v240\nv_sub_f32 v179,v179,v238\nv_sub_f32 v183,v183,v240\n"
"v_sub_f32 v184,v184,v238\nv_sub_f32 v188,v188,v240\nv_sub_f32 v185,v185,v238\nv_sub_f32 v189,v189,v240\nv_sub_f32 v186,v186,v238\n"
"v_sub_f32 v190,v190,v240\nv_sub_f32 v187,v187,v238\nv_sub_f32 v191,v191,v240\nv_sub_f32 v192,v192,v238\nv_sub_f32 v196,v196,v240\n"
"v_sub_f32 v193,v193,v238\nv_sub_f32 v197,v197,v240\nv_sub_f32 v194,v194,v238\nv_sub_f32 v198,v198,v240\nv_sub_f32 v195,v195,v238\n"
"v_sub_f32 v199,v199,v240\nv_sub_f32 v200,v200,v238\nv_sub_f32 v204,v204,v240\nv_sub_f32 v201,v201,v238\nv_sub_f32 v205,v205,v240\n"
"v_sub_f32 v202,v202,v238\nv_sub_f32 v206,v206,v240\nv_sub_f32 v203,v203,v238\nv_sub_f32 v207,v207,v240\ns_mov_b32 s64,1\ns_nop 1\n"
"s_branch fused_op_rmb21\nfused_op_rs22:\ns_nop 7\nv_mul_f32 v16,v236,v16\nv_mul_f32 v17,v236,v17\nv_mul_f32 v18,v236,v18\nv_mul_f32 v19,v236,v19\n"
"v_mul_f32 v20,v237,v20\nv_mul_f32 v21,v237,v21\nv_mul_f32 v22,v237,v22\nv_mul_f32 v23,v237,v23\nv_mul_f32 v24,v236,v24\nv_mul_f32 v25,v236,v25\n"
"v_mul_f32 v26,v236,v26\nv_mul_f32 v27,v236,v27\nv_mul_f32 v28,v237,v28\nv_mul_f32 v29,v237,v29\nv_mul_f32 v30,v237,v30\nv_mul_f32 v31,v237,v31\n"
"v_mul_f32 v32,v236,v32\nv_mul_f32 v33,v236,v33\nv_mul_f32 v34,v236,v34\nv_mul_f32 v35,v236,v35\nv_mul_f32 v36,v237,v36\nv_mul_f32 v37,v237,v37\n"
"v_mul_f32 v38,v237,v38\nv_mul_f32 v39,v237,v39\nv_mul_f32 v40,v236,v40\nv_mul_f32 v41,v236,v41\nv_mul_f32 v42,v236,v42\nv_mul_f32 v43,v236,v43\n"
"v_mul_f32 v44,v237,v44\nv_mul_f32 v45,v237,v45\nv_mul_f32 v46,v237,v46\nv_mul_f32 v47,v237,v47\nv_mul_f32 v48,v236,v48\nv_mul_f32 v49,v236,v49\n"
"v_mul_f32 v50,v236,v50\nv_mul_f32 v51,v236,v51\nv_mul_f32 v52,v237,v52\nv_mul_f32 v53,v237,v53\nv_mul_f32 v54,v237,v54\nv_mul_f32 v55,v237,v55\n"
"v_mul_f32 v56,v236,v56\nv_mul_f32 v57,v236,v57\nv_mul_f32 v58,v236,v58\nv_mul_f32 v59,v236,v59\nv_mul_f32 v60,v237,v60\nv_mul_f32 v61,v237,v61\n"
"v_mul_f32 v62,v237,v62\nv_mul_f32 v63,v237,v63\nv_mul_f32 v64,v236,v64\nv_mul_f32 v65,v236,v65\nv_mul_f32 v66,v236,v66\nv_mul_f32 v67,v236,v67\n"
"v_mul_f32 v68,v237,v68\nv_mul_f32 v69,v237,v69\nv_mul_f32 v70,v237,v70\nv_mul_f32 v71,v237,v71\nv_mul_f32 v72,v236,v72\nv_mul_f32 v73,v236,v73\n"
"v_mul_f32 v74,v236,v74\nv_mul_f32 v75,v236,v75\nv_mul_f32 v76,v237,v76\nv_mul_f32 v77,v237,v77\nv_mul_f32 v78,v237,v78\nv_mul_f32 v79,v237,v79\n"
"v_mul_f32 v80,v236,v80\nv_mul_f32 v81,v236,v81\nv_mul_f32 v82,v236,v82\nv_mul_f32 v83,v236,v83\nv_mul_f32 v84,v237,v84\nv_mul_f32 v85,v237,v85\n"
"v_mul_f32 v86,v237,v86\nv_mul_f32 v87,v237,v87\nv_mul_f32 v88,v236,v88\nv_mul_f32 v89,v236,v89\nv_mul_f32 v90,v236,v90\nv_mul_f32 v91,v236,v91\n"
"v_mul_f32 v92,v237,v92\nv_mul_f32 v93,v237,v93\nv_mul_f32 v94,v237,v94\nv_mul_f32 v95,v237,v95\nv_mul_f32 v96,v236,v96\nv_mul_f32 v97,v236,v97\n"
"v_mul_f32 v98,v236,v98\nv_mul_f32 v99,v236,v99\nv_mul_f32 v100,v237,v100\nv_mul_f32 v101,v237,v101\nv_mul_f32 v102,v237,v102\nv_mul_f32 v103,v237,v103\n"
"v_mul_f32 v104,v236,v104\nv_mul_f32 v105,v236,v105\nv_mul_f32 v106,v236,v106\nv_mul_f32 v107,v236,v107\nv_mul_f32 v108,v237,v108\n"
"v_mul_f32 v109,v237,v109\nv_mul_f32 v110,v237,v110\nv_mul_f32 v111,v237,v111\nv_mul_f32 v112,v236,v112\nv_mul_f32 v113,v236,v113\n"
"v_mul_f32 v114,v236,v114\nv_mul_f32 v115,v236,v115\nv_mul_f32 v116,v237,v116\nv_mul_f32 v117,v237,v117\nv_mul_f32 v118,v237,v118\n"
"v_mul_f32 v119,v237,v119\nv_mul_f32 v120,v236,v120\nv_mul_f32 v121,v236,v121\nv_mul_f32 v122,v236,v122\nv_mul_f32 v123,v236,v123\n"
"v_mul_f32 v124,v237,v124\nv_mul_f32 v125,v237,v125\nv_mul_f32 v126,v237,v126\nv_mul_f32 v127,v237,v127\nv_mul_f32 v128,v236,v128\n"
"v_mul_f32 v129,v236,v129\nv_mul_f32 v130,v236,v130\nv_mul_f32 v131,v236,v131\nv_mul_f32 v132,v237,v132\nv_mul_f32 v133,v237,v133\n"
"v_mul_f32 v134,v237,v134\nv_mul_f32 v135,v237,v135\nv_mul_f32 v136,v236,v136\nv_mul_f32 v137,v236,v137\nv_mul_f32 v138,v236,v138\n"
"v_mul_f32 v139,v236,v139\nv_mul_f32 v140,v237,v140\nv_mul_f32 v141,v237,v141\nv_mul_f32 v142,v237,v142\nv_mul_f32 v143,v237,v143\ns_mov_b32 s64,0\n"
"s_nop 1\ns_branch fused_op_rsb23\nfused_op_rm24:\ns_nop 7\nv_max3_f32 v238,v144,v145,v146\nv_max3_f32 v240,v148,v149,v150\n"
"v_max3_f32 v238,v238,v147,v152\nv_max3_f32 v240,v240,v151,v156\nv_max3_f32 v238,v238,v153,v154\nv_max3_f32 v240,v240,v157,v158\n"
"v_max3_f32 v238,v238,v155,v160\nv_max3_f32 v240,v240,v159,v164\nv_max3_f32 v238,v238,v161,v162\nv_max3_f32 v240,v240,v165,v166\n"
"v_max3_f32 v238,v238,v163,v168\nv_max3_f32 v240,v240,v167,v172\nv_max3_f32 v238,v238,v169,v170\nv_max3_f32 v240,v240,v173,v174\n"
"v_max_f32 v238,v238,v171\nv_max_f32 v240,v240,v175\nv_mov_b32 v239,v238\nv_mov_b32 v241,v240\ns_nop 0\nv_permlane32_swap_b32 v238,v239\n"
"v_permlane32_swap_b32 v240,v241\nv_max_f32 v238,v238,v239\nv_max_f32 v240,v240,v241\nv_mov_b32 v239,v238\nv_mov_b32 v241,v240\ns_nop 0\n"
"v_permlane16_swap_b32 v238,v239\nv_permlane16_swap_b32 v240,v241\nv_max_f32 v238,v238,v239\nv_max_f32 v240,v240,v241\nv_max_f32 v238,0,v238\n"
"v_max_f32 v240,0,v240\nv_add_f32 v232,v238,v232\nv_add_f32 v233,v240,v233\nv_exp_f32 v236,-v238\nv_exp_f32 v237,-v240\nv_mul_f32 v234,v236,v234\n"
"v_mul_f32 v235,v237,v235\nv_sub_f32 v224,0,v232\nv_sub_f32 v228,0,v233\nv_sub_f32 v225,0,v232\nv_sub_f32 v229,0,v233\nv_sub_f32 v226,0,v232\n"
"v_sub_f32 v230,0,v233\nv_sub_f32 v227,0,v232\nv_sub_f32 v231,0,v233\nv_sub_f32 v144,v144,v238\nv_sub_f32 v148,v148,v240\nv_sub_f32 v145,v145,v238\n"
"v_sub_f32 v149,v149,v240\nv_sub_f32 v146,v146,v238\nv_sub_f32 v150,v150,v240\nv_sub_f32 v147,v147,v238\nv_sub_f32 v151,v151,v240\n"
"v_sub_f32 v152,v152,v238\nv_sub_f32 v156,v156,v240\nv_sub_f32 v153,v153,v238\nv_sub_f32 v157,v157,v240\nv_sub_f32 v154,v154,v238\n"
"v_sub_f32 v158,v158,v240\nv_sub_f32 v155,v155,v238\nv_sub_f32 v159,v159,v240\nv_sub_f32 v160,v160,v238\nv_sub_f32 v164,v164,v240\n"
"v_sub_f32 v161,v161,v238\nv_sub_f32 v165,v165,v240\nv_sub_f32 v162,v162,v238\nv_sub_f32 v166,v166,v240\nv_sub_f32 v163,v163,v238\n"
"v_sub_f32 v167,v167,v240\nv_sub_f32 v168,v168,v238\nv_sub_f32 v172,v172,v240\nv_sub_f32 v169,v169,v238\nv_sub_f32 v173,v173,v240\n"
"v_sub_f32 v170,v170,v238\nv_sub_f32 v174,v174,v240\nv_sub_f32 v171,v171,v238\nv_sub_f32 v175,v175,v240\ns_mov_b32 s64,1\ns_nop 1\n"
"s_branch fused_op_rmb25\nfused_op_rs26:\ns_nop 7\nv_mul_f32 v16,v236,v16\nv_mul_f32 v17,v236,v17\nv_mul_f32 v18,v236,v18\nv_mul_f32 v19,v236,v19\n"
"v_mul_f32 v20,v237,v20\nv_mul_f32 v21,v237,v21\nv_mul_f32 v22,v237,v22\nv_mul_f32 v23,v237,v23\nv_mul_f32 v24,v236,v24\nv_mul_f32 v25,v236,v25\n"
"v_mul_f32 v26,v236,v26\nv_mul_f32 v27,v236,v27\nv_mul_f32 v28,v237,v28\nv_mul_f32 v29,v237,v29\nv_mul_f32 v30,v237,v30\nv_mul_f32 v31,v237,v31\n"
"v_mul_f32 v32,v236,v32\nv_mul_f32 v33,v236,v33\nv_mul_f32 v34,v236,v34\nv_mul_f32 v35,v236,v35\nv_mul_f32 v36,v237,v36\nv_mul_f32 v37,v237,v37\n"
"v_mul_f32 v38,v237,v38\nv_mul_f32 v39,v237,v39\nv_mul_f32 v40,v236,v40\nv_mul_f32 v41,v236,v41\nv_mul_f32 v42,v236,v42\nv_mul_f32 v43,v236,v43\n"
"v_mul_f32 v44,v237,v44\nv_mul_f32 v45,v237,v45\nv_mul_f32 v46,v237,v46\nv_mul_f32 v47,v237,v47\nv_mul_f32 v48,v236,v48\nv_mul_f32 v49,v236,v49\n"
"v_mul_f32 v50,v236,v50\nv_mul_f32 v51,v236,v51\nv_mul_f32 v52,v237,v52\nv_mul_f32 v53,v237,v53\nv_mul_f32 v54,v237,v54\nv_mul_f32 v55,v237,v55\n"
"v_mul_f32 v56,v236,v56\nv_mul_f32 v57,v236,v57\nv_mul_f32 v58,v236,v58\nv_mul_f32 v59,v236,v59\nv_mul_f32 v60,v237,v60\nv_mul_f32 v61,v237,v61\n"
"v_mul_f32 v62,v237,v62\nv_mul_f32 v63,v237,v63\nv_mul_f32 v64,v236,v64\nv_mul_f32 v65,v236,v65\nv_mul_f32 v66,v236,v66\nv_mul_f32 v67,v236,v67\n"
"v_mul_f32 v68,v237,v68\nv_mul_f32 v69,v237,v69\nv_mul_f32 v70,v237,v70\nv_mul_f32 v71,v237,v71\nv_mul_f32 v72,v236,v72\nv_mul_f32 v73,v236,v73\n"
"v_mul_f32 v74,v236,v74\nv_mul_f32 v75,v236,v75\nv_mul_f32 v76,v237,v76\nv_mul_f32 v77,v237,v77\nv_mul_f32 v78,v237,v78\nv_mul_f32 v79,v237,v79\n"
"v_mul_f32 v80,v236,v80\nv_mul_f32 v81,v236,v81\nv_mul_f32 v82,v236,v82\nv_mul_f32 v83,v236,v83\nv_mul_f32 v84,v237,v84\nv_mul_f32 v85,v237,v85\n"
"v_mul_f32 v86,v237,v86\nv_mul_f32 v87,v237,v87\nv_mul_f32 v88,v236,v88\nv_mul_f32 v89,v236,v89\nv_mul_f32 v90,v236,v90\nv_mul_f32 v91,v236,v91\n"
"v_mul_f32 v92,v237,v92\nv_mul_f32 v93,v237,v93\nv_mul_f32 v94,v237,v94\nv_mul_f32 v95,v237,v95\nv_mul_f32 v96,v236,v96\nv_mul_f32 v97,v236,v97\n"
"v_mul_f32 v98,v236,v98\nv_mul_f32 v99,v236,v99\nv_mul_f32 v100,v237,v100\nv_mul_f32 v101,v237,v101\nv_mul_f32 v102,v237,v102\nv_mul_f32 v103,v237,v103\n"
"v_mul_f32 v104,v236,v104\nv_mul_f32 v105,v236,v105\nv_mul_f32 v106,v236,v106\nv_mul_f32 v107,v236,v107\nv_mul_f32 v108,v237,v108\n"
"v_mul_f32 v109,v237,v109\nv_mul_f32 v110,v237,v110\nv_mul_f32 v111,v237,v111\nv_mul_f32 v112,v236,v112\nv_mul_f32 v113,v236,v113\n"
"v_mul_f32 v114,v236,v114\nv_mul_f32 v115,v236,v115\nv_mul_f32 v116,v237,v116\nv_mul_f32 v117,v237,v117\nv_mul_f32 v118,v237,v118\n"
"v_mul_f32 v119,v237,v119\nv_mul_f32 v120,v236,v120\nv_mul_f32 v121,v236,v121\nv_mul_f32 v122,v236,v122\nv_mul_f32 v123,v236,v123\n"
"v_mul_f32 v124,v237,v124\nv_mul_f32 v125,v237,v125\nv_mul_f32 v126,v237,v126\nv_mul_f32 v127,v237,v127\nv_mul_f32 v128,v236,v128\n"
"v_mul_f32 v129,v236,v129\nv_mul_f32 v130,v236,v130\nv_mul_f32 v131,v236,v131\nv_mul_f32 v132,v237,v132\nv_mul_f32 v133,v237,v133\n"
"v_mul_f32 v134,v237,v134\nv_mul_f32 v135,v237,v135\nv_mul_f32 v136,v236,v136\nv_mul_f32 v137,v236,v137\nv_mul_f32 v138,v236,v138\n"
"v_mul_f32 v139,v236,v139\nv_mul_f32 v140,v237,v140\nv_mul_f32 v141,v237,v141\nv_mul_f32 v142,v237,v142\nv_mul_f32 v143,v237,v143\ns_mov_b32 s64,0\n"
"s_nop 1\ns_branch fused_op_rsb27\nfused_op_sp28:\ns_sub_u32 s14,s14,1\ns_cmp_eq_u32 s14,0\ns_cbranch_scc1 fused_op_giveup1\ns_sleep 4\n"
"buffer_load_dword v14,v15,s[24:27],s9 offen sc1\ns_waitcnt vmcnt(0)\nv_cmp_ne_u32 vcc,s65,v14\ns_nop 1\ns_cbranch_vccnz fused_op_sp28\n"
"s_branch fused_op_spb29\nfused_op_rm30:\ns_nop 7\nv_max3_f32 v238,v176,v177,v178\nv_max3_f32 v240,v180,v181,v182\nv_max3_f32 v238,v238,v179,v184\n"
"v_max3_f32 v240,v240,v183,v188\nv_max3_f32 v238,v238,v185,v186\nv_max3_f32 v240,v240,v189,v190\nv_max3_f32 v238,v238,v187,v192\n"
"v_max3_f32 v240,v240,v191,v196\nv_max3_f32 v238,v238,v193,v194\nv_max3_f32 v240,v240,v197,v198\nv_max3_f32 v238,v238,v195,v200\n"
"v_max3_f32 v240,v240,v199,v204\nv_max3_f32 v238,v238,v201,v202\nv_max3_f32 v240,v240,v205,v206\nv_max_f32 v238,v238,v203\nv_max_f32 v240,v240,v207\n"
"v_mov_b32 v239,v238\nv_mov_b32 v241,v240\ns_nop 0\nv_permlane32_swap_b32 v238,v239\nv_permlane32_swap_b32 v240,v241\nv_max_f32 v238,v238,v239\n"
"v_max_f32 v240,v240,v241\nv_mov_b32 v239,v238\nv_mov_b32 v241,v240\ns_nop 0\nv_permlane16_swap_b32 v238,v239\nv_permlane16_swap_b32 v240,v241\n"
"v_max_f32 v238,v238,v239\nv_max_f32 v240,v240,v241\nv_max_f32 v238,0,v238\nv_max_f32 v240,0,v240\nv_add_f32 v232,v238,v232\nv_add_f32 v233,v240,v233\n"
"v_exp_f32 v236,-v238\nv_exp_f32 v237,-v240\nv_mul_f32 v234,v236,v234\nv_mul_f32 v235,v237,v235\nv_sub_f32 v224,0,v232\nv_sub_f32 v228,0,v233\n"
"v_sub_f32 v225,0,v232\nv_sub_f32 v229,0,v233\nv_sub_f32 v226,0,v232\nv_sub_f32 v230,0,v233\nv_sub_f32 v227,0,v232\nv_sub_f32 v231,0,v233\n"
"v_sub_f32 v176,v176,v238\nv_sub_f32 v180,v180,v240\nv_sub_f32 v177,v177,v238\nv_sub_f32 v181,v181,v240\nv_sub_f32 v178,v178,v238\n"
"v_sub_f32 v182,v182,v240\nv_sub_f32 v179,v179,v238\nv_sub_f32 v183,v183,v240\nv_sub_f32 v184,v184,v238\nv_sub_f32 v188,v188,v240\n"
"v_sub_f32 v185,v185,v238\nv_sub_f32 v189,v189,v240\nv_sub_f32 v186,v186,v238\nv_sub_f32 v190,v190,v240\nv_sub_f32 v187,v187,v238\n"
"v_sub_f32 v191,v191,v240\nv_sub_f32 v192,v192,v238\nv_sub_f32 v196,v196,v240\nv_sub_f32 v193,v193,v238\nv_sub_f32 v197,v197,v240\n"
"v_sub_f32 v194,v194,v238\nv_sub_f32 v198,v198,v240\nv_sub_f32 v195,v195,v238\nv_sub_f32 v199,v199,v240\nv_sub_f32 v200,v200,v238\n"
"v_sub_f32 v204,v204,v240\nv_sub_f32 v201,v201,v238\nv_sub_f32 v205,v205,v240\nv_sub_f32 v202,v202,v238\nv_sub_f32 v206,v206,v240\n"
"v_sub_f32 v203,v203,v238\nv_sub_f32 v207,v207,v240\ns_mov_b32 s64,1\ns_nop 1\ns_branch fused_op_rmb31\nfused_op_rs32:\ns_nop 7\nv_mul_f32 v16,v236,v16\n"
"v_mul_f32 v17,v236,v17\nv_mul_f32 v18,v236,v18\nv_mul_f32 v19,v236,v19\nv_mul_f32 v20,v237,v20\nv_mul_f32 v21,v237,v21\nv_mul_f32 v22,v237,v22\n"
"v_mul_f32 v23,v237,v23\nv_mul_f32 v24,v236,v24\nv_mul_f32 v25,v236,v25\nv_mul_f32 v26,v236,v26\nv_mul_f32 v27,v236,v27\nv_mul_f32 v28,v237,v28\n"
"v_mul_f32 v29,v237,v29\nv_mul_f32 v30,v237,v30\nv_mul_f32 v31,v237,v31\nv_mul_f32 v32,v236,v32\nv_mul_f32 v33,v236,v33\nv_mul_f32 v34,v236,v34\n"
"v_mul_f32 v35,v236,v35\nv_mul_f32 v36,v237,v36\nv_mul_f32 v37,v237,v37\nv_mul_f32 v38,v237,v38\nv_mul_f32 v39,v237,v39\nv_mul_f32 v40,v236,v40\n"
"v_mul_f32 v41,v236,v41\nv_mul_f32 v42,v236,v42\nv_mul_f32 v43,v236,v43\nv_mul_f32 v44,v237,v44\nv_mul_f32 v45,v237,v45\nv_mul_f32 v46,v237,v46\n"
"v_mul_f32 v47,v237,v47\nv_mul_f32 v48,v236,v48\nv_mul_f32 v49,v236,v49\nv_mul_f32 v50,v236,v50\nv_mul_f32 v51,v236,v51\nv_mul_f32 v52,v237,v52\n"
"v_mul_f32 v53,v237,v53\nv_mul_f32 v54,v237,v54\nv_mul_f32 v55,v237,v55\nv_mul_f32 v56,v236,v56\nv_mul_f32 v57,v236,v57\nv_mul_f32 v58,v236,v58\n"
"v_mul_f32 v59,v236,v59\nv_mul_f32 v60,v237,v60\nv_mul_f32 v61,v237,v61\nv_mul_f32 v62,v237,v62\nv_mul_f32 v63,v237,v63\nv_mul_f32 v64,v236,v64\n"
"v_mul_f32 v65,v236,v65\nv_mul_f32 v66,v236,v66\nv_mul_f32 v67,v236,v67\nv_mul_f32 v68,v237,v68\nv_mul_f32 v69,v237,v69\nv_mul_f32 v70,v237,v70\n"
"v_mul_f32 v71,v237,v71\nv_mul_f32 v72,v236,v72\nv_mul_f32 v73,v236,v73\nv_mul_f32 v74,v236,v74\nv_mul_f32 v75,v236,v75\nv_mul_f32 v76,v237,v76\n"
"v_mul_f32 v77,v237,v77\nv_mul_f32 v78,v237,v78\nv_mul_f32 v79,v237,v79\nv_mul_f32 v80,v236,v80\nv_mul_f32 v81,v236,v81\nv_mul_f32 v82,v236,v82\n"
"v_mul_f32 v83,v236,v83\nv_mul_f32 v84,v237,v84\nv_mul_f32 v85,v237,v85\nv_mul_f32 v86,v237,v86\nv_mul_f32 v87,v237,v87\nv_mul_f32 v88,v236,v88\n"
"v_mul_f32 v89,v236,v89\nv_mul_f32 v90,v236,v90\nv_mul_f32 v91,v236,v91\nv_mul_f32 v92,v237,v92\nv_mul_f32 v93,v237,v93\nv_mul_f32 v94,v237,v94\n"
"v_mul_f32 v95,v237,v95\nv_mul_f32 v96,v236,v96\nv_mul_f32 v97,v236,v97\nv_mul_f32 v98,v236,v98\nv_mul_f32 v99,v236,v99\nv_mul_f32 v100,v237,v100\n"
"v_mul_f32 v101,v237,v101\nv_mul_f32 v102,v237,v102\nv_mul_f32 v103,v237,v103\nv_mul_f32 v104,v236,v104\nv_mul_f32 v105,v236,v105\n"
"v_mul_f32 v106,v236,v106\nv_mul_f32 v107,v236,v107\nv_mul_f32 v108,v237,v108\nv_mul_f32 v109,v237,v109\nv_mul_f32 v110,v237,v110\n"
"v_mul_f32 v111,v237,v111\nv_mul_f32 v112,v236,v112\nv_mul_f32 v113,v236,v113\nv_mul_f32 v114,v236,v114\nv_mul_f32 v115,v236,v115\n"
"v_mul_f32 v116,v237,v116\nv_mul_f32 v117,v237,v117\nv_mul_f32 v118,v237,v118\nv_mul_f32 v119,v237,v119\nv_mul_f32 v120,v236,v120\n"
"v_mul_f32 v121,v236,v121\nv_mul_f32 v122,v236,v122\nv_mul_f32 v123,v236,v123\nv_mul_f32 v124,v237,v124\nv_mul_f32 v125,v237,v125\n"
"v_mul_f32 v126,v237,v126\nv_mul_f32 v127,v237,v127\nv_mul_f32 v128,v236,v128\nv_mul_f32 v129,v236,v129\nv_mul_f32 v130,v236,v130\n"
"v_mul_f32 v131,v236,v131\nv_mul_f32 v132,v237,v132\nv_mul_f32 v133,v237,v133\nv_mul_f32 v134,v237,v134\nv_mul_f32 v135,v237,v135\n"
"v_mul_f32 v136,v236,v136\nv_mul_f32 v137,v236,v137\nv_mul_f32 v138,v236,v138\nv_mul_f32 v139,v236,v139\nv_mul_f32 v140,v237,v140\n"
"v_mul_f32 v141,v237,v141\nv_mul_f32 v142,v237,v142\nv_mul_f32 v143,v237,v143\ns_mov_b32 s64,0\ns_nop 1\ns_branch fused_op_rsb33\nfused_op_rm34:\n"
"s_nop 7\nv_max3_f32 v238,v144,v145,v146\nv_max3_f32 v240,v148,v149,v150\nv_max3_f32 v238,v238,v147,v152\nv_max3_f32 v240,v240,v151,v156\n"
"v_max3_f32 v238,v238,v153,v154\nv_max3_f32 v240,v240,v157,v158\nv_max3_f32 v238,v238,v155,v160\nv_max3_f32 v240,v240,v159,v164\n"
"v_max3_f32 v238,v238,v161,v162\nv_max3_f32 v240,v240,v165,v166\nv_max3_f32 v238,v238,v163,v168\nv_max3_f32 v240,v240,v167,v172\n"
"v_max3_f32 v238,v238,v169,v170\nv_max3_f32 v240,v240,v173,v174\nv_max_f32 v238,v238,v171\nv_max_f32 v240,v240,v175\nv_mov_b32 v239,v238\n"
"v_mov_b32 v241,v240\ns_nop 0\nv_permlane32_swap_b32 v238,v239\nv_permlane32_swap_b32 v240,v241\nv_max_f32 v238,v238,v239\nv_max_f32 v240,v240,v241\n"
"v_mov_b32 v239,v238\nv_mov_b32 v241,v240\ns_nop 0\nv_permlane16_swap_b32 v238,v239\nv_permlane16_swap_b32 v240,v241\nv_max_f32 v238,v238,v239\n"
"v_max_f32 v240,v240,v241\nv_max_f32 v238,0,v238\nv_max_f32 v240,0,v240\nv_add_f32 v232,v238,v232\nv_add_f32 v233,v240,v233\nv_exp_f32 v236,-v238\n"
"v_exp_f32 v237,-v240\nv_mul_f32 v234,v236,v234\nv_mul_f32 v235,v237,v235\nv_sub_f32 v224,0,v232\nv_sub_f32 v228,0,v233\nv_sub_f32 v225,0,v232\n"
"v_sub_f32 v229,0,v233\nv_sub_f32 v226,0,v232\nv_sub_f32 v230,0,v233\nv_sub_f32 v227,0,v232\nv_sub_f32 v231,0,v233\nv_sub_f32 v144,v144,v238\n"
"v_sub_f32 v148,v148,v240\nv_sub_f32 v145,v145,v238\nv_sub_f32 v149,v149,v240\nv_sub_f32 v146,v146,v238\nv_sub_f32 v150,v150,v240\n"
"v_sub_f32 v147,v147,v238\nv_sub_f32 v151,v151,v240\nv_sub_f32 v152,v152,v238\nv_sub_f32 v156,v156,v240\nv_sub_f32 v153,v153,v238\n"
"v_sub_f32 v157,v157,v240\nv_sub_f32 v154,v154,v238\nv_sub_f32 v158,v158,v240\nv_sub_f32 v155,v155,v238\nv_sub_f32 v159,v159,v240\n"
"v_sub_f32 v160,v160,v238\nv_sub_f32 v164,v164,v240\nv_sub_f32 v161,v161,v238\nv_sub_f32 v165,v165,v240\nv_sub_f32 v162,v162,v238\n"
"v_sub_f32 v166,v166,v240\nv_sub_f32 v163,v163,v238\nv_sub_f32 v167,v167,v240\nv_sub_f32 v168,v168,v238\nv_sub_f32 v172,v172,v240\n"
"v_sub_f32 v169,v169,v238\nv_sub_f32 v173,v173,v240\nv_sub_f32 v170,v170,v238\nv_sub_f32 v174,v174,v240\nv_sub_f32 v171,v171,v238\n"
"v_sub_f32 v175,v175,v240\ns_mov_b32 s64,1\ns_nop 1\ns_branch fused_op_rmb35\nfused_op_rs36:\ns_nop 7\nv_mul_f32 v16,v236,v16\nv_mul_f32 v17,v236,v17\n"
"v_mul_f32 v18,v236,v18\nv_mul_f32 v19,v236,v19\nv_mul_f32 v20,v237,v20\nv_mul_f32 v21,v237,v21\nv_mul_f32 v22,v237,v22\nv_mul_f32 v23,v237,v23\n"
"v_mul_f32 v24,v236,v24\nv_mul_f32 v25,v236,v25\nv_mul_f32 v26,v236,v26\nv_mul_f32 v27,v236,v27\nv_mul_f32 v28,v237,v28\nv_mul_f32 v29,v237,v29\n"
"v_mul_f32 v30,v237,v30\nv_mul_f32 v31,v237,v31\nv_mul_f32 v32,v236,v32\nv_mul_f32 v33,v236,v33\nv_mul_f32 v34,v236,v34\nv_mul_f32 v35,v236,v35\n"
"v_mul_f32 v36,v237,v36\nv_mul_f32 v37,v237,v37\nv_mul_f32 v38,v237,v38\nv_mul_f32 v39,v237,v39\nv_mul_f32 v40,v236,v40\nv_mul_f32 v41,v236,v41\n"
"v_mul_f32 v42,v236,v42\nv_mul_f32 v43,v236,v43\nv_mul_f32 v44,v237,v44\nv_mul_f32 v45,v237,v45\nv_mul_f32 v46,v237,v46\nv_mul_f32 v47,v237,v47\n"
"v_mul_f32 v48,v236,v48\nv_mul_f32 v49,v236,v49\nv_mul_f32 v50,v236,v50\nv_mul_f32 v51,v236,v51\nv_mul_f32 v52,v237,v52\nv_mul_f32 v53,v237,v53\n"
"v_mul_f32 v54,v237,v54\nv_mul_f32 v55,v237,v55\nv_mul_f32 v56,v236,v56\nv_mul_f32 v57,v236,v57\nv_mul_f32 v58,v236,v58\nv_mul_f32 v59,v236,v59\n"
"v_mul_f32 v60,v237,v60\nv_mul_f32 v61,v237,v61\nv_mul_f32 v62,v237,v62\nv_mul_f32 v63,v237,v63\nv_mul_f32 v64,v236,v64\nv_mul_f32 v65,v236,v65\n"
"v_mul_f32 v66,v236,v66\nv_mul_f32 v67,v236,v67\nv_mul_f32 v68,v237,v68\nv_mul_f32 v69,v237,v69\nv_mul_f32 v70,v237,v70\nv_mul_f32 v71,v237,v71\n"
"v_mul_f32 v72,v236,v72\nv_mul_f32 v73,v236,v73\nv_mul_f32 v74,v236,v74\nv_mul_f32 v75,v236,v75\nv_mul_f32 v76,v237,v76\nv_mul_f32 v77,v237,v77\n"
"v_mul_f32 v78,v237,v78\nv_mul_f32 v79,v237,v79\nv_mul_f32 v80,v236,v80\nv_mul_f32 v81,v236,v81\nv_mul_f32 v82,v236,v82\nv_mul_f32 v83,v236,v83\n"
"v_mul_f32 v84,v237,v84\nv_mul_f32 v85,v237,v85\nv_mul_f32 v86,v237,v86\nv_mul_f32 v87,v237,v87\nv_mul_f32 v88,v236,v88\nv_mul_f32 v89,v236,v89\n"
"v_mul_f32 v90,v236,v90\nv_mul_f32 v91,v236,v91\nv_mul_f32 v92,v237,v92\nv_mul_f32 v93,v237,v93\nv_mul_f32 v94,v237,v94\nv_mul_f32 v95,v237,v95\n"
"v_mul_f32 v96,v236,v96\nv_mul_f32 v97,v236,v97\nv_mul_f32 v98,v236,v98\nv_mul_f32 v99,v236,v99\nv_mul_f32 v100,v237,v100\nv_mul_f32 v101,v237,v101\n"
"v_mul_f32 v102,v237,v102\nv_mul_f32 v103,v237,v103\nv_mul_f32 v104,v236,v104\nv_mul_f32 v105,v236,v105\nv_mul_f32 v106,v236,v106\n"
"v_mul_f32 v107,v236,v107\nv_mul_f32 v108,v237,v108\nv_mul_f32 v109,v237,v109\nv_mul_f32 v110,v237,v110\nv_mul_f32 v111,v237,v111\n"
"v_mul_f32 v112,v236,v112\nv_mul_f32 v113,v236,v113\nv_mul_f32 v114,v236,v114\nv_mul_f32 v115,v236,v115\nv_mul_f32 v116,v237,v116\n"
"v_mul_f32 v117,v237,v117\nv_mul_f32 v118,v237,v118\nv_mul_f32 v119,v237,v119\nv_mul_f32 v120,v236,v120\nv_mul_f32 v121,v236,v121\n"
"v_mul_f32 v122,v236,v122\nv_mul_f32 v123,v236,v123\nv_mul_f32 v124,v237,v124\nv_mul_f32 v125,v237,v125\nv_mul_f32 v126,v237,v126\n"
"v_mul_f32 v127,v237,v127\nv_mul_f32 v128,v236,v128\nv_mul_f32 v129,v236,v129\nv_mul_f32 v130,v236,v130\nv_mul_f32 v131,v236,v131\n"
"v_mul_f32 v132,v237,v132\nv_mul_f32 v133,v237,v133\nv_mul_f32 v134,v237,v134\nv_mul_f32 v135,v237,v135\nv_mul_f32 v136,v236,v136\n"
"v_mul_f32 v137,v236,v137\nv_mul_f32 v138,v236,v138\nv_mul_f32 v139,v236,v139\nv_mul_f32 v140,v237,v140\nv_mul_f32 v141,v237,v141\n"
"v_mul_f32 v142,v237,v142\nv_mul_f32 v143,v237,v143\ns_mov_b32 s64,0\ns_nop 1\ns_branch fused_op_rsb37\nfused_op_rm38:\ns_nop 7\n"
"v_max3_f32 v238,v176,v177,v178\nv_max3_f32 v240,v180,v181,v182\nv_max3_f32 v238,v238,v179,v184\nv_max3_f32 v240,v240,v183,v188\n"
"v_max3_f32 v238,v238,v185,v186\nv_max3_f32 v240,v240,v189,v190\nv_max3_f32 v238,v238,v187,v192\nv_max3_f32 v240,v240,v191,v196\n"
"v_max3_f32 v238,v238,v193,v194\nv_max3_f32 v240,v240,v197,v198\nv_max3_f32 v238,v238,v195,v200\nv_max3_f32 v240,v240,v199,v204\n"
"v_max3_f32 v238,v238,v201,v202\nv_max3_f32 v240,v240,v205,v206\nv_max_f32 v238,v238,v203\nv_max_f32 v240,v240,v207\nv_mov_b32 v239,v238\n"
"v_mov_b32 v241,v240\ns_nop 0\nv_permlane32_swap_b32 v238,v239\nv_permlane32_swap_b32 v240,v241\nv_max_f32 v238,v238,v239\nv_max_f32 v240,v240,v241\n"
"v_mov_b32 v239,v238\nv_mov_b32 v241,v240\ns_nop 0\nv_permlane16_swap_b32 v238,v239\nv_permlane16_swap_b32 v240,v241\nv_max_f32 v238,v238,v239\n"
"v_max_f32 v240,v240,v241\nv_max_f32 v238,0,v238\nv_max_f32 v240,0,v240\nv_add_f32 v232,v238,v232\nv_add_f32 v233,v240,v233\nv_exp_f32 v236,-v238\n"
"v_exp_f32 v237,-v240\nv_mul_f32 v234,v236,v234\nv_mul_f32 v235,v237,v235\nv_sub_f32 v224,0,v232\nv_sub_f32 v228,0,v233\nv_sub_f32 v225,0,v232\n"
"v_sub_f32 v229,0,v233\nv_sub_f32 v226,0,v232\nv_sub_f32 v230,0,v233\nv_sub_f32 v227,0,v232\nv_sub_f32 v231,0,v233\nv_sub_f32 v176,v176,v238\n"
"v_sub_f32 v180,v180,v240\nv_sub_f32 v177,v177,v238\nv_sub_f32 v181,v181,v240\nv_sub_f32 v178,v178,v238\nv_sub_f32 v182,v182,v240\n"
"v_sub_f32 v179,v179,v238\nv_sub_f32 v183,v183,v240\nv_sub_f32 v184,v184,v238\nv_sub_f32 v188,v188,v240\nv_sub_f32 v185,v185,v238\n"
"v_sub_f32 v189,v189,v240\nv_sub_f32 v186,v186,v238\nv_sub_f32 v190,v190,v240\nv_sub_f32 v187,v187,v238\nv_sub_f32 v191,v191,v240\n"
"v_sub_f32 v192,v192,v238\nv_sub_f32 v196,v196,v240\nv_sub_f32 v193,v193,v238\nv_sub_f32 v197,v197,v240\nv_sub_f32 v194,v194,v238\n"
"v_sub_f32 v198,v198,v240\nv_sub_f32 v195,v195,v238\nv_sub_f32 v199,v199,v240\nv_sub_f32 v200,v200,v238\nv_sub_f32 v204,v204,v240\n"
"v_sub_f32 v201,v201,v238\nv_sub_f32 v205,v205,v240\nv_sub_f32 v202,v202,v238\nv_sub_f32 v206,v206,v240\nv_sub_f32 v203,v203,v238\n"
"v_sub_f32 v207,v207,v240\ns_mov_b32 s64,1\ns_nop 1\ns_branch fused_op_rmb39\nfused_op_rs40:\ns_nop 7\nv_mul_f32 v16,v236,v16\nv_mul_f32 v17,v236,v17\n"
"v_mul_f32 v18,v236,v18\nv_mul_f32 v19,v236,v19\nv_mul_f32 v20,v237,v20\nv_mul_f32 v21,v237,v21\nv_mul_f32 v22,v237,v22\nv_mul_f32 v23,v237,v23\n"
"v_mul_f32 v24,v236,v24\nv_mul_f32 v25,v236,v25\nv_mul_f32 v26,v236,v26\nv_mul_f32 v27,v236,v27\nv_mul_f32 v28,v237,v28\nv_mul_f32 v29,v237,v29\n"
"v_mul_f32 v30,v237,v30\nv_mul_f32 v31,v237,v31\nv_mul_f32 v32,v236,v32\nv_mul_f32 v33,v236,v33\nv_mul_f32 v34,v236,v34\nv_mul_f32 v35,v236,v35\n"
"v_mul_f32 v36,v237,v36\nv_mul_f32 v37,v237,v37\nv_mul_f32 v38,v237,v38\nv_mul_f32 v39,v237,v39\nv_mul_f32 v40,v236,v40\nv_mul_f32 v41,v236,v41\n"
"v_mul_f32 v42,v236,v42\nv_mul_f32 v43,v236,v43\nv_mul_f32 v44,v237,v44\nv_mul_f32 v45,v237,v45\nv_mul_f32 v46,v237,v46\nv_mul_f32 v47,v237,v47\n"
"v_mul_f32 v48,v236,v48\nv_mul_f32 v49,v236,v49\nv_mul_f32 v50,v236,v50\nv_mul_f32 v51,v236,v51\nv_mul_f32 v52,v237,v52\nv_mul_f32 v53,v237,v53\n"
"v_mul_f32 v54,v237,v54\nv_mul_f32 v55,v237,v55\nv_mul_f32 v56,v236,v56\nv_mul_f32 v57,v236,v57\nv_mul_f32 v58,v236,v58\nv_mul_f32 v59,v236,v59\n"
"v_mul_f32 v60,v237,v60\nv_mul_f32 v61,v237,v61\nv_mul_f32 v62,v237,v62\nv_mul_f32 v63,v237,v63\nv_mul_f32 v64,v236,v64\nv_mul_f32 v65,v236,v65\n"
"v_mul_f32 v66,v236,v66\nv_mul_f32 v67,v236,v67\nv_mul_f32 v68,v237,v68\nv_mul_f32 v69,v237,v69\nv_mul_f32 v70,v237,v70\nv_mul_f32 v71,v237,v71\n"
"v_mul_f32 v72,v236,v72\nv_mul_f32 v73,v236,v73\nv_mul_f32 v74,v236,v74\nv_mul_f32 v75,v236,v75\nv_mul_f32 v76,v237,v76\nv_mul_f32 v77,v237,v77\n"
"v_mul_f32 v78,v237,v78\nv_mul_f32 v79,v237,v79\nv_mul_f32 v80,v236,v80\nv_mul_f32 v81,v236,v81\nv_mul_f32 v82,v236,v82\nv_mul_f32 v83,v236,v83\n"
"v_mul_f32 v84,v237,v84\nv_mul_f32 v85,v237,v85\nv_mul_f32 v86,v237,v86\nv_mul_f32 v87,v237,v87\nv_mul_f32 v88,v236,v88\nv_mul_f32 v89,v236,v89\n"
"v_mul_f32 v90,v236,v90\nv_mul_f32 v91,v236,v91\nv_mul_f32 v92,v237,v92\nv_mul_f32 v93,v237,v93\nv_mul_f32 v94,v237,v94\nv_mul_f32 v95,v237,v95\n"
"v_mul_f32 v96,v236,v96\nv_mul_f32 v97,v236,v97\nv_mul_f32 v98,v236,v98\nv_mul_f32 v99,v236,v99\nv_mul_f32 v100,v237,v100\nv_mul_f32 v101,v237,v101\n"
"v_mul_f32 v102,v237,v102\nv_mul_f32 v103,v237,v103\nv_mul_f32 v104,v236,v104\nv_mul_f32 v105,v236,v105\nv_mul_f32 v106,v236,v106\n"
"v_mul_f32 v107,v236,v107\nv_mul_f32 v108,v237,v108\nv_mul_f32 v109,v237,v109\nv_mul_f32 v110,v237,v110\nv_mul_f32 v111,v237,v111\n"
"v_mul_f32 v112,v236,v112\nv_mul_f32 v113,v236,v113\nv_mul_f32 v114,v236,v114\nv_mul_f32 v115,v236,v115\nv_mul_f32 v116,v237,v116\n"
"v_mul_f32 v117,v237,v117\nv_mul_f32 v118,v237,v118\nv_mul_f32 v119,v237,v119\nv_mul_f32 v120,v236,v120\nv_mul_f32 v121,v236,v121\n"
"v_mul_f32 v122,v236,v122\nv_mul_f32 v123,v236,v123\nv_mul_f32 v124,v237,v124\nv_mul_f32 v125,v237,v125\nv_mul_f32 v126,v237,v126\n"
"v_mul_f32 v127,v237,v127\nv_mul_f32 v128,v236,v128\nv_mul_f32 v129,v236,v129\nv_mul_f32 v130,v236,v130\nv_mul_f32 v131,v236,v131\n"
"v_mul_f32 v132,v237,v132\nv_mul_f32 v133,v237,v133\nv_mul_f32 v134,v237,v134\nv_mul_f32 v135,v237,v135\nv_mul_f32 v136,v236,v136\n"
"v_mul_f32 v137,v236,v137\nv_mul_f32 v138,v236,v138\nv_mul_f32 v139,v236,v139\nv_mul_f32 v140,v237,v140\nv_mul_f32 v141,v237,v141\n"
"v_mul_f32 v142,v237,v142\nv_mul_f32 v143,v237,v143\ns_mov_b32 s64,0\ns_nop 1\ns_branch fused_op_rsb41\nfused_op_tsw:\ns_add_u32 m0,s48,s44\ns_nop 0\n"
"DS 1,20,36,0\nDS 1,20,37,1024\nDS 1,20,38,2048\nDS 1,20,39,3072\ns_add_u32 m0,m0,0x1000\ns_nop 0\nDS 1,20,40,0\nDS 1,20,41,1024\nDS 1,20,42,2048\n"
"DS 1,20,43,3072\nv_add_u32 v1,0x80,v1\ns_mov_b32 s44,s47\ns_add_u32 s47,s47,0x8000\ns_cmp_eq_u32 s47,0x28000\ns_cselect_b32 s47,0,s47\n"
"v_add_u32 v3,s47,v0\ns_waitcnt vmcnt(25) lgkmcnt(0)\ns_barrier\nRD 0,3,0\nRD 4,3,1024\nRD 8,3,2048\nRD 12,3,3072\nRD 16,3,4096\nRD 20,3,5120\n"
"RD 24,3,6144\nRD 28,3,7168\nv_exp_f32 v155,v155\nCV 208,144,145\nv_add_f32 v234,v147,v234\nv_exp_f32 v156,v156\nCV 209,146,147\n"
"v_add_f32 v235,v148,v235\nv_exp_f32 v157,v157\nv_add_f32 v235,v149,v235\nCV 216,148,149\nv_exp_f32 v158,v158\nv_add_f32 v235,v150,v235\nCV 217,150,151\n"
"v_exp_f32 v159,v159\nv_add_f32 v235,v151,v235\nv_add_f32 v234,v152,v234\nv_exp_f32 v160,v160\nv_add_f32 v234,v153,v234\nCV 210,152,153\n"
"v_exp_f32 v161,v161\nv_add_f32 v234,v154,v234\nCV 211,154,155\nv_exp_f32 v162,v162\nv_add_f32 v234,v155,v234\nv_add_f32 v235,v156,v235\n"
"v_exp_f32 v163,v163\nv_add_f32 v235,v157,v235\nCV 218,156,157\nv_exp_f32 v164,v164\nv_add_f32 v235,v158,v235\nCV 219,158,159\nv_exp_f32 v165,v165\n"
"v_add_f32 v235,v159,v235\nv_add_f32 v234,v160,v234\nv_exp_f32 v166,v166\nv_add_f32 v234,v161,v234\nCV 212,160,161\nv_exp_f32 v167,v167\n"
"v_add_f32 v234,v162,v234\nCV 213,162,163\nv_exp_f32 v168,v168\nv_add_f32 v234,v163,v234\nv_add_f32 v235,v164,v235\nv_exp_f32 v169,v169\n"
"v_add_f32 v235,v165,v235\nCV 220,164,165\nv_exp_f32 v170,v170\nv_add_f32 v235,v166,v235\nCV 221,166,167\nv_exp_f32 v171,v171\nv_add_f32 v235,v167,v235\n"
"v_add_f32 v234,v168,v234\nv_exp_f32 v172,v172\nv_add_f32 v234,v169,v234\nCV 214,168,169\nv_exp_f32 v173,v173\nv_add_f32 v234,v170,v234\nCV 215,170,171\n"
"v_exp_f32 v174,v174\nv_add_f32 v234,v171,v234\nv_add_f32 v235,v172,v235\nv_exp_f32 v175,v175\nv_add_f32 v235,v173,v235\nCV 222,172,173\n"
"v_add_f32 v235,v174,v235\nCV 223,174,175\nv_add_f32 v235,v175,v235\nWL 0\nMvav 16,0,208\nRD 32,3,8192\nMvav 16,4,212\nRD 36,3,9216\nMvav 20,4,220\n"
"RD 40,3,10240\nMvav 20,0,216\nRD 44,3,11264\nMvav 24,8,208\nRD 48,3,12288\nMvav 24,12,212\nRD 52,3,13312\nMvav 28,12,220\nRD 56,3,14336\nMvav 28,8,216\n"
"s_add_u32 m0,s48,s44\nRD 60,3,15360\nMvav 32,16,208\nMvav 32,20,212\nDS 1,20,36,0\nMvav 36,20,220\nMvav 36,16,216\nMvav 40,24,208\nDS 1,20,37,1024\n"
"Mvav 40,28,212\nMvav 44,28,220\nMvav 44,24,216\nWL 0\nDS 1,20,38,2048\nMvav 48,32,208\nRD 0,3,16384\nMvav 48,36,212\nRD 4,3,17408\nMvav 52,36,220\n"
"RD 8,3,18432\nMvav 52,32,216\nRD 12,3,19456\nMvav 56,40,208\nRD 16,3,20480\nMvav 56,44,212\nRD 20,3,21504\nMvav 60,44,220\nRD 24,3,22528\n"
"Mvav 60,40,216\nRD 28,3,23552\nMvav 64,48,208\nMvav 64,52,212\nDS 1,20,39,3072\nMvav 68,52,220\ns_add_u32 m0,m0,0x1000\nMvav 68,48,216\nMvav 72,56,208\n"
"DS 1,20,40,0\nMvav 72,60,212\nMvav 76,60,220\nMvav 76,56,216\nWL 0\nDS 1,20,41,1024\nMvav 80,0,208\nRD 32,3,24576\nMvav 80,4,212\ns_mov_b32 s44,s47\n"
"RD 36,3,25600\nMvav 84,4,220\ns_add_u32 s47,s47,0x8000\nRD 40,3,26624\nMvav 84,0,216\ns_cmp_eq_u32 s47,0x28000\nRD 44,3,27648\nMvav 88,8,208\n"
"s_cselect_b32 s47,0,s47\nRD 48,3,28672\nMvav 88,12,212\nRD 52,3,29696\nMvav 92,12,220\nRD 56,3,30720\nMvav 92,8,216\nRD 60,3,31744\nMvav 96,16,208\n"
"Mvav 96,20,212\nDS 1,20,42,2048\nMvav 100,20,220\nMvav 100,16,216\nMvav 104,24,208\nDS 1,20,43,3072\nMvav 104,28,212\nv_add_u32 v1,0x80,v1\n"
"Mvav 108,28,220\nMvav 108,24,216\ns_waitcnt vmcnt(24) lgkmcnt(0)\ns_barrier\nMvav 112,32,208\nMvav 112,36,212\nMvav 116,36,220\nMvav 116,32,216\n"
"Mvav 120,40,208\nMvav 120,44,212\nMvav 124,44,220\nMvav 124,40,216\nMvav 128,48,208\nMvav 128,52,212\nMvav 132,52,220\nMvav 132,48,216\n"
"Mvav 136,56,208\nMvav 136,60,212\nMvav 140,60,220\nMvav 140,56,216\ns_add_u32 m0,s48,s44\ns_nop 0\nDS 1,20,36,0\nDS 1,20,37,1024\nDS 1,20,38,2048\n"
"DS 1,20,39,3072\ns_add_u32 m0,m0,0x1000\ns_nop 0\nDS 1,20,40,0\nDS 1,20,41,1024\nDS 1,20,42,2048\nDS 1,20,43,3072\nv_add_u32 v1,0x80,v1\n"
"v_lshrrev_b32 v12,4,v0\nv_lshrrev_b32 v13,2,v12\nv_lshlrev_b32 v10,7,v13\nv_bfe_u32 v13,v12,3,2\nv_lshl_add_u32 v10,v13,5,v10\nv_and_b32 v12,3,v12\n"
"v_lshl_add_u32 v10,v12,3,v10\ns_mov_b32 s44,s47\ns_add_u32 s47,s47,0x8000\ns_cmp_eq_u32 s47,0x28000\ns_cselect_b32 s47,0,s47\nv_add_u32 v5,s47,v10\n"
"v_xor_b32 v6,32,v5\nv_xor_b32 v7,64,v5\nv_xor_b32 v8,96,v5\ns_waitcnt vmcnt(24) lgkmcnt(0)\ns_barrier\nTR 0,5,0\nTR 2,5,2048\nTR 4,5,4096\nTR 6,5,6144\n"
"TR 8,5,8192\nTR 10,5,10240\nTR 12,5,12288\nTR 14,5,14336\nTR 16,5,16384\nTR 18,5,18432\nTR 20,5,20480\nTR 22,5,22528\nTR 24,5,24576\nTR 26,5,26624\n"
"TR 28,5,28672\nTR 30,5,30720\nWL 0\ns_branch fused_op_tew\nfused_op_rm42:\ns_nop 7\nv_max3_f32 v238,v144,v145,v146\nv_max3_f32 v240,v148,v149,v150\n"
"v_max3_f32 v238,v238,v147,v152\nv_max3_f32 v240,v240,v151,v156\nv_max3_f32 v238,v238,v153,v154\nv_max3_f32 v240,v240,v157,v158\n"
"v_max3_f32 v238,v238,v155,v160\nv_max3_f32 v240,v240,v159,v164\nv_max3_f32 v238,v238,v161,v162\nv_max3_f32 v240,v240,v165,v166\n"
"v_max3_f32 v238,v238,v163,v168\nv_max3_f32 v240,v240,v167,v172\nv_max3_f32 v238,v238,v169,v170\nv_max3_f32 v240,v240,v173,v174\n"
"v_max_f32 v238,v238,v171\nv_max_f32 v240,v240,v175\nv_mov_b32 v239,v238\nv_mov_b32 v241,v240\ns_nop 0\nv_permlane32_swap_b32 v238,v239\n"
"v_permlane32_swap_b32 v240,v241\nv_max_f32 v238,v238,v239\nv_max_f32 v240,v240,v241\nv_mov_b32 v239,v238\nv_mov_b32 v241,v240\ns_nop 0\n"
"v_permlane16_swap_b32 v238,v239\nv_permlane16_swap_b32 v240,v241\nv_max_f32 v238,v238,v239\nv_max_f32 v240,v240,v241\nv_max_f32 v238,0,v238\n"
"v_max_f32 v240,0,v240\nv_add_f32 v232,v238,v232\nv_add_f32 v233,v240,v233\nv_exp_f32 v236,-v238\nv_exp_f32 v237,-v240\nv_mul_f32 v234,v236,v234\n"
"v_mul_f32 v235,v237,v235\nv_sub_f32 v224,0,v232\nv_sub_f32 v228,0,v233\nv_sub_f32 v225,0,v232\nv_sub_f32 v229,0,v233\nv_sub_f32 v226,0,v232\n"
"v_sub_f32 v230,0,v233\nv_sub_f32 v227,0,v232\nv_sub_f32 v231,0,v233\nv_sub_f32 v144,v144,v238\nv_sub_f32 v148,v148,v240\nv_sub_f32 v145,v145,v238\n"
"v_sub_f32 v149,v149,v240\nv_sub_f32 v146,v146,v238\nv_sub_f32 v150,v150,v240\nv_sub_f32 v147,v147,v238\nv_sub_f32 v151,v151,v240\n"
"v_sub_f32 v152,v152,v238\nv_sub_f32 v156,v156,v240\nv_sub_f32 v153,v153,v238\nv_sub_f32 v157,v157,v240\nv_sub_f32 v154,v154,v238\n"
"v_sub_f32 v158,v158,v240\nv_sub_f32 v155,v155,v238\nv_sub_f32 v159,v159,v240\nv_sub_f32 v160,v160,v238\nv_sub_f32 v164,v164,v240\n"
"v_sub_f32 v161,v161,v238\nv_sub_f32 v165,v165,v240\nv_sub_f32 v162,v162,v238\nv_sub_f32 v166,v166,v240\nv_sub_f32 v163,v163,v238\n"
"v_sub_f32 v167,v167,v240\nv_sub_f32 v168,v168,v238\nv_sub_f32 v172,v172,v240\nv_sub_f32 v169,v169,v238\nv_sub_f32 v173,v173,v240\n"
"v_sub_f32 v170,v170,v238\nv_sub_f32 v174,v174,v240\nv_sub_f32 v171,v171,v238\nv_sub_f32 v175,v175,v240\ns_mov_b32 s64,1\ns_nop 1\n"
"s_branch fused_op_rmb43\nfused_op_rs44:\ns_nop 7\nv_mul_f32 v16,v236,v16\nv_mul_f32 v17,v236,v17\nv_mul_f32 v18,v236,v18\nv_mul_f32 v19,v236,v19\n"
"v_mul_f32 v20,v237,v20\nv_mul_f32 v21,v237,v21\nv_mul_f32 v22,v237,v22\nv_mul_f32 v23,v237,v23\nv_mul_f32 v24,v236,v24\nv_mul_f32 v25,v236,v25\n"
"v_mul_f32 v26,v236,v26\nv_mul_f32 v27,v236,v27\nv_mul_f32 v28,v237,v28\nv_mul_f32 v29,v237,v29\nv_mul_f32 v30,v237,v30\nv_mul_f32 v31,v237,v31\n"
"v_mul_f32 v32,v236,v32\nv_mul_f32 v33,v236,v33\nv_mul_f32 v34,v236,v34\nv_mul_f32 v35,v236,v35\nv_mul_f32 v36,v237,v36\nv_mul_f32 v37,v237,v37\n"
"v_mul_f32 v38,v237,v38\nv_mul_f32 v39,v237,v39\nv_mul_f32 v40,v236,v40\nv_mul_f32 v41,v236,v41\nv_mul_f32 v42,v236,v42\nv_mul_f32 v43,v236,v43\n"
"v_mul_f32 v44,v237,v44\nv_mul_f32 v45,v237,v45\nv_mul_f32 v46,v237,v46\nv_mul_f32 v47,v237,v47\nv_mul_f32 v48,v236,v48\nv_mul_f32 v49,v236,v49\n"
"v_mul_f32 v50,v236,v50\nv_mul_f32 v51,v236,v51\nv_mul_f32 v52,v237,v52\nv_mul_f32 v53,v237,v53\nv_mul_f32 v54,v237,v54\nv_mul_f32 v55,v237,v55\n"
"v_mul_f32 v56,v236,v56\nv_mul_f32 v57,v236,v57\nv_mul_f32 v58,v236,v58\nv_mul_f32 v59,v236,v59\nv_mul_f32 v60,v237,v60\nv_mul_f32 v61,v237,v61\n"
"v_mul_f32 v62,v237,v62\nv_mul_f32 v63,v237,v63\nv_mul_f32 v64,v236,v64\nv_mul_f32 v65,v236,v65\nv_mul_f32 v66,v236,v66\nv_mul_f32 v67,v236,v67\n"
"v_mul_f32 v68,v237,v68\nv_mul_f32 v69,v237,v69\nv_mul_f32 v70,v237,v70\nv_mul_f32 v71,v237,v71\nv_mul_f32 v72,v236,v72\nv_mul_f32 v73,v236,v73\n"
"v_mul_f32 v74,v236,v74\nv_mul_f32 v75,v236,v75\nv_mul_f32 v76,v237,v76\nv_mul_f32 v77,v237,v77\nv_mul_f32 v78,v237,v78\nv_mul_f32 v79,v237,v79\n"
"v_mul_f32 v80,v236,v80\nv_mul_f32 v81,v236,v81\nv_mul_f32 v82,v236,v82\nv_mul_f32 v83,v236,v83\nv_mul_f32 v84,v237,v84\nv_mul_f32 v85,v237,v85\n"
"v_mul_f32 v86,v237,v86\nv_mul_f32 v87,v237,v87\nv_mul_f32 v88,v236,v88\nv_mul_f32 v89,v236,v89\nv_mul_f32 v90,v236,v90\nv_mul_f32 v91,v236,v91\n"
"v_mul_f32 v92,v237,v92\nv_mul_f32 v93,v237,v93\nv_mul_f32 v94,v237,v94\nv_mul_f32 v95,v237,v95\nv_mul_f32 v96,v236,v96\nv_mul_f32 v97,v236,v97\n"
"v_mul_f32 v98,v236,v98\nv_mul_f32 v99,v236,v99\nv_mul_f32 v100,v237,v100\nv_mul_f32 v101,v237,v101\nv_mul_f32 v102,v237,v102\nv_mul_f32 v103,v237,v103\n"
"v_mul_f32 v104,v236,v104\nv_mul_f32 v105,v236,v105\nv_mul_f32 v106,v236,v106\nv_mul_f32 v107,v236,v107\nv_mul_f32 v108,v237,v108\n"
"v_mul_f32 v109,v237,v109\nv_mul_f32 v110,v237,v110\nv_mul_f32 v111,v237,v111\nv_mul_f32 v112,v236,v112\nv_mul_f32 v113,v236,v113\n"
"v_mul_f32 v114,v236,v114\nv_mul_f32 v115,v236,v115\nv_mul_f32 v116,v237,v116\nv_mul_f32 v117,v237,v117\nv_mul_f32 v118,v237,v118\n"
"v_mul_f32 v119,v237,v119\nv_mul_f32 v120,v236,v120\nv_mul_f32 v121,v236,v121\nv_mul_f32 v122,v236,v122\nv_mul_f32 v123,v236,v123\n"
"v_mul_f32 v124,v237,v124\nv_mul_f32 v125,v237,v125\nv_mul_f32 v126,v237,v126\nv_mul_f32 v127,v237,v127\nv_mul_f32 v128,v236,v128\n"
"v_mul_f32 v129,v236,v129\nv_mul_f32 v130,v236,v130\nv_mul_f32 v131,v236,v131\nv_mul_f32 v132,v237,v132\nv_mul_f32 v133,v237,v133\n"
"v_mul_f32 v134,v237,v134\nv_mul_f32 v135,v237,v135\nv_mul_f32 v136,v236,v136\nv_mul_f32 v137,v236,v137\nv_mul_f32 v138,v236,v138\n"
"v_mul_f32 v139,v236,v139\nv_mul_f32 v140,v237,v140\nv_mul_f32 v141,v237,v141\nv_mul_f32 v142,v237,v142\nv_mul_f32 v143,v237,v143\ns_mov_b32 s64,0\n"
"s_nop 1\ns_branch fused_op_rsb45\nfused_op_sp46:\ns_sub_u32 s14,s14,1\ns_cmp_eq_u32 s14,0\ns_cbranch_scc1 fused_op_giveup1\ns_sleep 4\n"
"buffer_load_dword v14,v15,s[24:27],s9 offen sc1\ns_waitcnt vmcnt(0)\nv_cmp_ne_u32 vcc,s65,v14\ns_nop 1\ns_cbranch_vccnz fused_op_sp46\n"
"s_branch fused_op_spb47\nfused_op_rm48:\ns_nop 7\nv_max3_f32 v238,v176,v177,v178\nv_max3_f32 v240,v180,v181,v182\nv_max3_f32 v238,v238,v179,v184\n"
"v_max3_f32 v240,v240,v183,v188\nv_max3_f32 v238,v238,v185,v186\nv_max3_f32 v240,v240,v189,v190\nv_max3_f32 v238,v238,v187,v192\n"
"v_max3_f32 v240,v240,v191,v196\nv_max3_f32 v238,v238,v193,v194\nv_max3_f32 v240,v240,v197,v198\nv_max3_f32 v238,v238,v195,v200\n"
"v_max3_f32 v240,v240,v199,v204\nv_max3_f32 v238,v238,v201,v202\nv_max3_f32 v240,v240,v205,v206\nv_max_f32 v238,v238,v203\nv_max_f32 v240,v240,v207\n"
"v_mov_b32 v239,v238\nv_mov_b32 v241,v240\ns_nop 0\nv_permlane32_swap_b32 v238,v239\nv_permlane32_swap_b32 v240,v241\nv_max_f32 v238,v238,v239\n"
"v_max_f32 v240,v240,v241\nv_mov_b32 v239,v238\nv_mov_b32 v241,v240\ns_nop 0\nv_permlane16_swap_b32 v238,v239\nv_permlane16_swap_b32 v240,v241\n"
"v_max_f32 v238,v238,v239\nv_max_f32 v240,v240,v241\nv_max_f32 v238,0,v238\nv_max_f32 v240,0,v240\nv_add_f32 v232,v238,v232\nv_add_f32 v233,v240,v233\n"
"v_exp_f32 v236,-v238\nv_exp_f32 v237,-v240\nv_mul_f32 v234,v236,v234\nv_mul_f32 v235,v237,v235\nv_sub_f32 v224,0,v232\nv_sub_f32 v228,0,v233\n"
"v_sub_f32 v225,0,v232\nv_sub_f32 v229,0,v233\nv_sub_f32 v226,0,v232\nv_sub_f32 v230,0,v233\nv_sub_f32 v227,0,v232\nv_sub_f32 v231,0,v233\n"
"v_sub_f32 v176,v176,v238\nv_sub_f32 v180,v180,v240\nv_sub_f32 v177,v177,v238\nv_sub_f32 v181,v181,v240\nv_sub_f32 v178,v178,v238\n"
"v_sub_f32 v182,v182,v240\nv_sub_f32 v179,v179,v238\nv_sub_f32 v183,v183,v240\nv_sub_f32 v184,v184,v238\nv_sub_f32 v188,v188,v240\n"
"v_sub_f32 v185,v185,v238\nv_sub_f32 v189,v189,v240\nv_sub_f32 v186,v186,v238\nv_sub_f32 v190,v190,v240\nv_sub_f32 v187,v187,v238\n"
"v_sub_f32 v191,v191,v240\nv_sub_f32 v192,v192,v238\nv_sub_f32 v196,v196,v240\nv_sub_f32 v193,v193,v238\nv_sub_f32 v197,v197,v240\n"
"v_sub_f32 v194,v194,v238\nv_sub_f32 v198,v198,v240\nv_sub_f32 v195,v195,v238\nv_sub_f32 v199,v199,v240\nv_sub_f32 v200,v200,v238\n"
"v_sub_f32 v204,v204,v240\nv_sub_f32 v201,v201,v238\nv_sub_f32 v205,v205,v240\nv_sub_f32 v202,v202,v238\nv_sub_f32 v206,v206,v240\n"
"v_sub_f32 v203,v203,v238\nv_sub_f32 v207,v207,v240\ns_mov_b32 s64,1\ns_nop 1\ns_branch fused_op_rmb49\nfused_op_rs50:\ns_nop 7\nv_mul_f32 v16,v236,v16\n"
"v_mul_f32 v17,v236,v17\nv_mul_f32 v18,v236,v18\nv_mul_f32 v19,v236,v19\nv_mul_f32 v20,v237,v20\nv_mul_f32 v21,v237,v21\nv_mul_f32 v22,v237,v22\n"
"v_mul_f32 v23,v237,v23\nv_mul_f32 v24,v236,v24\nv_mul_f32 v25,v236,v25\nv_mul_f32 v26,v236,v26\nv_mul_f32 v27,v236,v27\nv_mul_f32 v28,v237,v28\n"
"v_mul_f32 v29,v237,v29\nv_mul_f32 v30,v237,v30\nv_mul_f32 v31,v237,v31\nv_mul_f32 v32,v236,v32\nv_mul_f32 v33,v236,v33\nv_mul_f32 v34,v236,v34\n"
"v_mul_f32 v35,v236,v35\nv_mul_f32 v36,v237,v36\nv_mul_f32 v37,v237,v37\nv_mul_f32 v38,v237,v38\nv_mul_f32 v39,v237,v39\nv_mul_f32 v40,v236,v40\n"
"v_mul_f32 v41,v236,v41\nv_mul_f32 v42,v236,v42\nv_mul_f32 v43,v236,v43\nv_mul_f32 v44,v237,v44\nv_mul_f32 v45,v237,v45\nv_mul_f32 v46,v237,v46\n"
"v_mul_f32 v47,v237,v47\nv_mul_f32 v48,v236,v48\nv_mul_f32 v49,v236,v49\nv_mul_f32 v50,v236,v50\nv_mul_f32 v51,v236,v51\nv_mul_f32 v52,v237,v52\n"
"v_mul_f32 v53,v237,v53\nv_mul_f32 v54,v237,v54\nv_mul_f32 v55,v237,v55\nv_mul_f32 v56,v236,v56\nv_mul_f32 v57,v236,v57\nv_mul_f32 v58,v236,v58\n"
"v_mul_f32 v59,v236,v59\nv_mul_f32 v60,v237,v60\nv_mul_f32 v61,v237,v61\nv_mul_f32 v62,v237,v62\nv_mul_f32 v63,v237,v63\nv_mul_f32 v64,v236,v64\n"
"v_mul_f32 v65,v236,v65\nv_mul_f32 v66,v236,v66\nv_mul_f32 v67,v236,v67\nv_mul_f32 v68,v237,v68\nv_mul_f32 v69,v237,v69\nv_mul_f32 v70,v237,v70\n"
"v_mul_f32 v71,v237,v71\nv_mul_f32 v72,v236,v72\nv_mul_f32 v73,v236,v73\nv_mul_f32 v74,v236,v74\nv_mul_f32 v75,v236,v75\nv_mul_f32 v76,v237,v76\n"
"v_mul_f32 v77,v237,v77\nv_mul_f32 v78,v237,v78\nv_mul_f32 v79,v237,v79\nv_mul_f32 v80,v236,v80\nv_mul_f32 v81,v236,v81\nv_mul_f32 v82,v236,v82\n"
"v_mul_f32 v83,v236,v83\nv_mul_f32 v84,v237,v84\nv_mul_f32 v85,v237,v85\nv_mul_f32 v86,v237,v86\nv_mul_f32 v87,v237,v87\nv_mul_f32 v88,v236,v88\n"
"v_mul_f32 v89,v236,v89\nv_mul_f32 v90,v236,v90\nv_mul_f32 v91,v236,v91\nv_mul_f32 v92,v237,v92\nv_mul_f32 v93,v237,v93\nv_mul_f32 v94,v237,v94\n"
"v_mul_f32 v95,v237,v95\nv_mul_f32 v96,v236,v96\nv_mul_f32 v97,v236,v97\nv_mul_f32 v98,v236,v98\nv_mul_f32 v99,v236,v99\nv_mul_f32 v100,v237,v100\n"
"v_mul_f32 v101,v237,v101\nv_mul_f32 v102,v237,v102\nv_mul_f32 v103,v237,v103\nv_mul_f32 v104,v236,v104\nv_mul_f32 v105,v236,v105\n"
"v_mul_f32 v106,v236,v106\nv_mul_f32 v107,v236,v107\nv_mul_f32 v108,v237,v108\nv_mul_f32 v109,v237,v109\nv_mul_f32 v110,v237,v110\n"
"v_mul_f32 v111,v237,v111\nv_mul_f32 v112,v236,v112\nv_mul_f32 v113,v236,v113\nv_mul_f32 v114,v236,v114\nv_mul_f32 v115,v236,v115\n"
"v_mul_f32 v116,v237,v116\nv_mul_f32 v117,v237,v117\nv_mul_f32 v118,v237,v118\nv_mul_f32 v119,v237,v119\nv_mul_f32 v120,v236,v120\n"
"v_mul_f32 v121,v236,v121\nv_mul_f32 v122,v236,v122\nv_mul_f32 v123,v236,v123\nv_mul_f32 v124,v237,v124\nv_mul_f32 v125,v237,v125\n"
"v_mul_f32 v126,v237,v126\nv_mul_f32 v127,v237,v127\nv_mul_f32 v128,v236,v128\nv_mul_f32 v129,v236,v129\nv_mul_f32 v130,v236,v130\n"
"v_mul_f32 v131,v236,v131\nv_mul_f32 v132,v237,v132\nv_mul_f32 v133,v237,v133\nv_mul_f32 v134,v237,v134\nv_mul_f32 v135,v237,v135\n"
"v_mul_f32 v136,v236,v136\nv_mul_f32 v137,v236,v137\nv_mul_f32 v138,v236,v138\nv_mul_f32 v139,v236,v139\nv_mul_f32 v140,v237,v140\n"
"v_mul_f32 v141,v237,v141\nv_mul_f32 v142,v237,v142\nv_mul_f32 v143,v237,v143\ns_mov_b32 s64,0\ns_nop 1\ns_branch fused_op_rsb51\nfused_op_sp52:\n"
"s_sub_u32 s14,s14,1\ns_cmp_eq_u32 s14,0\ns_cbranch_scc1 fused_op_giveup1\ns_sleep 4\nbuffer_load_dword v14,v15,s[24:27],s9 offen sc1\n"
"s_waitcnt vmcnt(0)\nv_cmp_ne_u32 vcc,s65,v14\ns_nop 1\ns_cbranch_vccnz fused_op_sp52\ns_branch fused_op_spb53\nfused_op_tsk:\ns_add_u32 m0,s48,s44\n"
"s_nop 0\nDS 1,20,36,0\nDS 1,20,37,1024\nDS 1,20,38,2048\nDS 1,20,39,3072\ns_add_u32 m0,m0,0x1000\ns_nop 0\nDS 1,20,40,0\nDS 1,20,41,1024\n"
"DS 1,20,42,2048\nDS 1,20,43,3072\nv_add_u32 v1,0xffff8000,v1\nv_mov_b32 v15,0\nbuffer_load_dword v14,v15,s[24:27],s9 offen sc1\ns_mov_b32 s44,s47\n"
"s_add_u32 s47,s47,0x8000\ns_cmp_eq_u32 s47,0x28000\ns_cselect_b32 s47,0,s47\nv_add_u32 v3,s47,v0\ns_waitcnt vmcnt(25) lgkmcnt(0)\ns_barrier\nRD 0,3,0\n"
"RD 4,3,1024\nRD 8,3,2048\nRD 12,3,3072\nRD 16,3,4096\nRD 20,3,5120\nRD 24,3,6144\nRD 28,3,7168\nv_exp_f32 v155,v155\nCV 208,144,145\n"
"v_add_f32 v234,v147,v234\nv_exp_f32 v156,v156\nCV 209,146,147\nv_add_f32 v235,v148,v235\nv_exp_f32 v157,v157\nv_add_f32 v235,v149,v235\nCV 216,148,149\n"
"v_exp_f32 v158,v158\nv_add_f32 v235,v150,v235\nCV 217,150,151\nv_exp_f32 v159,v159\nv_add_f32 v235,v151,v235\nv_add_f32 v234,v152,v234\n"
"v_exp_f32 v160,v160\nv_add_f32 v234,v153,v234\nCV 210,152,153\nv_exp_f32 v161,v161\nv_add_f32 v234,v154,v234\nCV 211,154,155\nv_exp_f32 v162,v162\n"
"v_add_f32 v234,v155,v234\nv_add_f32 v235,v156,v235\nv_exp_f32 v163,v163\nv_add_f32 v235,v157,v235\nCV 218,156,157\nv_exp_f32 v164,v164\n"
"v_add_f32 v235,v158,v235\nCV 219,158,159\nv_exp_f32 v165,v165\nv_add_f32 v235,v159,v235\nv_add_f32 v234,v160,v234\nv_exp_f32 v166,v166\n"
"v_add_f32 v234,v161,v234\nCV 212,160,161\nv_exp_f32 v167,v167\nv_add_f32 v234,v162,v234\nCV 213,162,163\nv_exp_f32 v168,v168\nv_add_f32 v234,v163,v234\n"
"v_add_f32 v235,v164,v235\nv_exp_f32 v169,v169\nv_add_f32 v235,v165,v235\nCV 220,164,165\nv_exp_f32 v170,v170\nv_add_f32 v235,v166,v235\nCV 221,166,167\n"
"v_exp_f32 v171,v171\nv_add_f32 v235,v167,v235\nv_add_f32 v234,v168,v234\nv_exp_f32 v172,v172\nv_add_f32 v234,v169,v234\nCV 214,168,169\n"
"v_exp_f32 v173,v173\nv_add_f32 v234,v170,v234\nCV 215,170,171\nv_exp_f32 v174,v174\nv_add_f32 v234,v171,v234\nv_add_f32 v235,v172,v235\n"
"v_exp_f32 v175,v175\nv_add_f32 v235,v173,v235\nCV 222,172,173\nv_add_f32 v235,v174,v235\nCV 223,174,175\nv_add_f32 v235,v175,v235\ns_waitcnt vmcnt(0)\n"
"v_cmp_ne_u32 vcc,s65,v14\ns_nop 0\ns_cbranch_vccnz fused_op_sp52\nfused_op_spb53:\ns_add_u32 s45,s8,16384\nWL 0\nMvav 16,0,208\nRD 32,3,8192\n"
"Mvav 16,4,212\nRD 36,3,9216\nMvav 20,4,220\nRD 40,3,10240\nbuffer_load_dwordx4 a[128:131],v0,s[24:27],s8 offen offset:0 sc1\nMvav 20,0,216\n"
"RD 44,3,11264\nMvav 24,8,208\nRD 48,3,12288\nMvav 24,12,212\nRD 52,3,13312\nbuffer_load_dwordx4 a[132:135],v0,s[24:27],s8 offen offset:1024 sc1\n"
"Mvav 28,12,220\nRD 56,3,14336\nMvav 28,8,216\ns_add_u32 m0,s48,s44\nRD 60,3,15360\nMvav 32,16,208\n"
"buffer_load_dwordx4 a[136:139],v0,s[24:27],s8 offen offset:2048 sc1\nMvav 32,20,212\nDS 1,20,36,0\nMvav 36,20,220\nMvav 36,16,216\n"
"buffer_load_dwordx4 a[140:143],v0,s[24:27],s8 offen offset:3072 sc1\nMvav 40,24,208\ns_add_u32 s8,s8,4096\nDS 1,20,37,1024\nMvav 40,28,212\n"
"Mvav 44,28,220\nbuffer_load_dwordx4 a[144:147],v0,s[24:27],s8 offen offset:0 sc1\nMvav 44,24,216\nWL 0\nDS 1,20,38,2048\nMvav 48,32,208\nRD 0,3,16384\n"
"Mvav 48,36,212\nRD 4,3,17408\nbuffer_load_dwordx4 a[148:151],v0,s[24:27],s8 offen offset:1024 sc1\nMvav 52,36,220\nRD 8,3,18432\nMvav 52,32,216\n"
"RD 12,3,19456\nMvav 56,40,208\nRD 16,3,20480\nbuffer_load_dwordx4 a[152:155],v0,s[24:27],s8 offen offset:2048 sc1\nMvav 56,44,212\nRD 20,3,21504\n"
"Mvav 60,44,220\nRD 24,3,22528\nMvav 60,40,216\nRD 28,3,23552\nMvav 64,48,208\nbuffer_load_dwordx4 a[156:159],v0,s[24:27],s8 offen offset:3072 sc1\n"
"Mvav 64,52,212\ns_add_u32 s8,s8,4096\nDS 1,20,39,3072\nMvav 68,52,220\ns_add_u32 m0,m0,0x1000\n"
"buffer_load_dwordx4 a[160:163],v0,s[24:27],s8 offen offset:0 sc1\nMvav 68,48,216\nMvav 72,56,208\nDS 1,20,40,0\nMvav 72,60,212\n"
"buffer_load_dwordx4 a[164:167],v0,s[24:27],s8 offen offset:1024 sc1\nMvav 76,60,220\nMvav 76,56,216\nWL 0\nDS 1,20,41,1024\nMvav 80,0,208\n"
"RD 32,3,24576\nbuffer_load_dwordx4 a[168:171],v0,s[24:27],s8 offen offset:2048 sc1\nMvav 80,4,212\ns_mov_b32 s44,s47\nRD 36,3,25600\nMvav 84,4,220\n"
"s_add_u32 s47,s47,0x8000\nRD 40,3,26624\nMvav 84,0,216\ns_cmp_eq_u32 s47,0x28000\nRD 44,3,27648\n"
"buffer_load_dwordx4 a[172:175],v0,s[24:27],s8 offen offset:3072 sc1\nMvav 88,8,208\ns_cselect_b32 s47,0,s47\nRD 48,3,28672\nMvav 88,12,212\n"
"s_add_u32 s8,s8,4096\nRD 52,3,29696\nMvav 92,12,220\nRD 56,3,30720\nMvav 92,8,216\nRD 60,3,31744\nMvav 96,16,208\n"
"buffer_load_dwordx4 a[176:179],v0,s[24:27],s8 offen offset:0 sc1\nMvav 96,20,212\nDS 1,20,42,2048\nMvav 100,20,220\n"
"buffer_load_dwordx4 a[180:183],v0,s[24:27],s8 offen offset:1024 sc1\nMvav 100,16,216\nMvav 104,24,208\nDS 1,20,43,3072\nMvav 104,28,212\n"
"v_add_u32 v1,s96,v1\nMvav 108,28,220\nbuffer_load_dwordx4 a[184:187],v0,s[24:27],s8 offen offset:2048 sc1\nMvav 108,24,216\n"
"s_waitcnt vmcnt(40) lgkmcnt(0)\ns_barrier\nMvav 112,32,208\nbuffer_load_dwordx4 a[188:191],v0,s[24:27],s8 offen offset:3072 sc1\nMvav 112,36,212\n"
"s_add_u32 s8,s8,4096\nMvav 116,36,220\nMvav 116,32,216\nMvav 120,40,208\nMvav 120,44,212\nMvav 124,44,220\nMvav 124,40,216\nMvav 128,48,208\n"
"Mvav 128,52,212\nMvav 132,52,220\nMvav 132,48,216\nMvav 136,56,208\nMvav 136,60,212\nMvav 140,60,220\nMvav 140,56,216\ns_add_u32 m0,s48,s44\ns_nop 0\n"
"DS 1,20,36,0\nDS 1,20,37,1024\nDS 1,20,38,2048\nDS 1,20,39,3072\ns_add_u32 m0,m0,0x1000\ns_nop 0\nDS 1,20,40,0\nDS 1,20,41,1024\nDS 1,20,42,2048\n"
"DS 1,20,43,3072\nv_add_u32 v1,0xffff8000,v1\ns_mov_b32 s44,s47\ns_add_u32 s47,s47,0x8000\ns_cmp_eq_u32 s47,0x28000\ns_cselect_b32 s47,0,s47\n"
"v_add_u32 v3,s47,v0\ns_waitcnt vmcnt(41) lgkmcnt(0)\ns_barrier\nRD 0,3,0\nRD 4,3,1024\nRD 8,3,2048\nRD 12,3,3072\nRD 16,3,4096\nRD 20,3,5120\n"
"RD 24,3,6144\nRD 28,3,7168\nWL 0\ns_branch fused_op_tek\nfused_op_sp54:\ns_sub_u32 s14,s14,1\ns_cmp_eq_u32 s14,0\ns_cbranch_scc1 fused_op_giveup1\n"
"s_sleep 4\nbuffer_load_dword v14,v15,s[24:27],s8 offen sc1\ns_waitcnt vmcnt(0)\nv_cmp_ne_u32 vcc,s65,v14\ns_nop 1\ns_cbranch_vccnz fused_op_sp54\n"
"s_branch fused_op_spb55\nfused_op_sp56:\ns_sub_u32 s14,s14,1\ns_cmp_eq_u32 s14,0\ns_cbranch_scc1 fused_op_giveup1\ns_sleep 4\n"
"buffer_load_dword v14,v15,s[24:27],s8 offen sc1\ns_waitcnt vmcnt(0)\nv_cmp_ne_u32 vcc,s65,v14\ns_nop 1\ns_cbranch_vccnz fused_op_sp56\n"
"s_branch fused_op_spb57\nfused_op_sp58:\ns_sub_u32 s14,s14,1\ns_cmp_eq_u32 s14,0\ns_cbranch_scc1 fused_op_giveup1\ns_sleep 2\n"
"buffer_load_dword v14,v15,s[24:27],s8 offen sc1\ns_waitcnt vmcnt(0)\nv_cmp_ne_u32 vcc,s65,v14\ns_nop 1\ns_cbranch_vccnz fused_op_sp58\n"
"s_branch fused_op_spb59\nfused_op_sp60:\ns_sub_u32 s14,s14,1\ns_cmp_eq_u32 s14,0\ns_cbranch_scc1 fused_op_giveup1\ns_sleep 2\n"
"buffer_load_dword v14,v15,s[24:27],s8 offen sc1\ns_waitcnt vmcnt(0)\nv_cmp_ne_u32 vcc,s65,v14\ns_nop 1\ns_cbranch_vccnz fused_op_sp60\n"
"s_branch fused_op_spb61\nfused_op_giveup1:\ns_endpgm\nfused_op_end:\ns_waitcnt vmcnt(0) lgkmcnt(0)\ns_waitcnt vmcnt(0)\n.purgem CV\n.purgem DP\n"
".purgem DS\n.purgem Maaa\n.purgem Mvaa\n.purgem Mvav\n.purgem Nvaa\n.purgem RD\n.purgem TR\n.purgem WL\n.purgem Zaaa\n.purgem Zvaa\n.purgem Zvav\n"
".purgem Zvvv\n"
  :: "s"(__builtin_amdgcn_kernarg_segment_ptr()), "s"(__builtin_amdgcn_readfirstlane(threadIdx.x >> 6)), "s"(blockIdx.x), "s"((unsigned)(size_t)lds)
  : "v0","v1","v2","v3","v4","v5","v6","v7","v8","v9","v10","v11","v12","v13","v14","v15","v16","v17","v18","v19","v20","v21","v22","v23","v24","v25","v26","v27","v28","v29","v30","v31","v32","v33","v34","v35","v36","v37","v38","v39","v40","v41","v42","v43","v44","v45","v46","v47","v48","v49","v50","v51","v52","v53","v54","v55","v56","v57","v58","v59","v60","v61","v62","v63","v64","v65","v66","v67","v68","v69","v70","v71","v72","v73","v74","v75","v76","v77","v78","v79","v80","v81","v82","v83","v84","v85","v86","v87","v88","v89","v90","v91","v92","v93","v94","v95","v96","v97","v98","v99","v100","v101","v102","v103","v104","v105","v106","v107","v108","v109","v110","v111","v112","v113","v114","v115","v116","v117","v118","v119","v120","v121","v122","v123","v124","v125","v126","v127","v128","v129","v130","v131","v132","v133","v134","v135","v136","v137","v138","v139","v140","v141","v142","v143","v144","v145","v146","v147","v148","v149","v150","v151","v152","v153","v154","v155","v156","v157","v158","v159","v160","v161","v162","v163","v164","v165","v166","v167","v168","v169","v170","v171","v172","v173","v174","v175","v176","v177","v178","v179","v180","v181","v182","v183","v184","v185","v186","v187","v188","v189","v190","v191","v192","v193","v194","v195","v196","v197","v198","v199","v200","v201","v202","v203","v204","v205","v206","v207","v208","v209","v210","v211","v212","v213","v214","v215","v216","v217","v218","v219","v220","v221","v222","v223","v224","v225","v226","v227","v228","v229","v230","v231","v232","v233","v234","v235","v236","v237","v238","v239","v240","v241","v242","v243","v244","v245","v246","v247","v248","v249","v250","v251","v252","v253","v254","v255","a0","a1","a2","a3","a4","a5","a6","a7","a8","a9","a10","a11","a12","a13","a14","a15","a16","a17","a18","a19","a20","a21","a22","a23","a24","a25","a26","a27","a28","a29","a30","a31","a32","a33","a34","a35","a36","a37","a38","a39","a40","a41","a42","a43","a44","a45","a46","a47","a48","a49","a50","a51","a52","a53","a54","a55","a56","a57","a58","a59","a60","a61","a62","a63","a64","a65","a66","a67","a68","a69","a70","a71","a72","a73","a74","a75","a76","a77","a78","a79","a80","a81","a82","a83","a84","a85","a86","a87","a88","a89","a90","a91","a92","a93","a94","a95","a96","a97","a98","a99","a100","a101","a102","a103","a104","a105","a106","a107","a108","a109","a110","a111","a112","a113","a114","a115","a116","a117","a118","a119","a120","a121","a122","a123","a124","a125","a126","a127","a128","a129","a130","a131","a132","a133","a134","a135","a136","a137","a138","a139","a140","a141","a142","a143","a144","a145","a146","a147","a148","a149","a150","a151","a152","a153","a154","a155","a156","a157","a158","a159","a160","a161","a162","a163","a164","a165","a166","a167","a168","a169","a170","a171","a172","a173","a174","a175","a176","a177","a178","a179","a180","a181","a182","a183","a184","a185","a186","a187","a188","a189","a190","a191","a192","a193","a194","a195","a196","a197","a198","a199","a200","a201","a202","a203","a204","a205","a206","a207","a208","a209","a210","a211","a212","a213","a214","a215","a216","a217","a218","a219","a220","a221","a222","a223","a224","a225","a226","a227","a228","a229","a230","a231","a232","a233","a234","a235","a236","a237","a238","a239","a240","a241","a242","a243","a244","a245","a246","a247","a248","a249","a250","a251","a252","a253","a254","a255","s5","s6","s7","s8","s9","s10","s11","s12","s13","s14","s15","s16","s17","s18","s19","s20","s21","s22","s23","s24","s25","s26","s27","s28","s29","s30","s31","s34","s35","s36","s37","s38","s39","s40","s41","s42","s43","s44","s45","s46","s47","s48","s49","s50","s51","s52","s53","s54","s55","s56","s57","s58","s59","s60","s61","s62","s63","s64","s65","s66","s67","s68","s69","s70","s71","s72","s73","s74","s75","s76","s77","s78","s79","s80","s81","s82","s83","s84","s85","s86","s87","s88","s89","s90","s91","s92","s93","s94","s95","s96","s97","s98","s99","vcc","scc","memory");
}

extern "C" void kernel_launch(void* const* d_in, const int* in_sizes, int n_in, void* d_out, int out_size, void* d_ws,
                              size_t ws_size, hipStream_t stream) {
  (void)in_sizes; (void)n_in; (void)out_size; (void)ws_size;
  fused_op<<<256, 256, 0, stream>>>(d_in[4], d_in[6], d_in[7], d_in[3], d_in[5], d_in[0], d_in[1], d_ws, d_in[8], d_in[2], d_out);
}
```
